```python
import torch

def _softmax(x, axis=-1):
    return torch.softmax(x, dim=axis)
def _gelu(x):
    return torch.nn.functional.gelu(x, approximate="tanh")
def _silu(x):
    return torch.nn.functional.silu(x)
def _sigmoid(x):
    return torch.sigmoid(x)
def _layernorm(x, axis=-1, eps=1e-5):
    m = x.mean(dim=axis, keepdim=True)
    v = x.var(dim=axis, keepdim=True, unbiased=False)
    return (x - m) / torch.sqrt(v + eps)
def _scatter_add(tgt, idx, src, axis=0):
    return tgt.clone().index_add_(axis, idx, src)
def _segment_sum(src, seg, n):
    out = torch.zeros((n,) + src.shape[1:], dtype=src.dtype)
    return out.index_add_(0, seg, src)
def _pad(x, pw):
    flat = tuple(v for pair in reversed(pw) for v in pair)
    return torch.nn.functional.pad(x, flat)

kernel_name = 'gated_mix__sliding_local_dilated__compressed_kv_cla__prompt'

def setup_inputs():

    _w = (lambda a: (lambda t: t.to(torch.bfloat16) if t.is_floating_point() else t)(torch.as_tensor(a)))
    def _A_setup():
        B, S, D = 1, 2048, 512
        return {
            "x": _w((lambda *s: torch.randn(*s, dtype=torch.bfloat16) * 0.05)(B, S, D)),
            "wqkv": _w((lambda *s: torch.randn(*s, dtype=torch.bfloat16) * 1.6)(D, 3 * D)),
            "wo": _w((lambda *s: torch.randn(*s, dtype=torch.bfloat16) * 0.05)(D, D)),
        }
    _A_in = _A_setup()

    def _B_setup():
        B, H, Sq, Sk, D, M = 1, 32, 2048, 2048, 128, 512
        Dc = 4 * D
        return {
            "x": _w((lambda *s: torch.randn(*s, dtype=torch.bfloat16) * 0.05)(B, Sq, M)),
            "c_kv": _w((lambda *s: torch.randn(*s, dtype=torch.bfloat16) * 1.6)(B, Sk, Dc)),
            "w_q": _w((lambda *s: torch.randn(*s, dtype=torch.bfloat16) * 6.4)(M, H, D) * (M ** -0.5)),
            "w_uk": _w((lambda *s: torch.randn(*s, dtype=torch.bfloat16) * 6.4)(Dc, H, D) * (Dc ** -0.5)),
            "w_uv": _w((lambda *s: torch.randn(*s, dtype=torch.bfloat16) * 1.6)(Dc, H, D) * (Dc ** -0.5)),
            "w_o": _w((lambda *s: torch.randn(*s, dtype=torch.bfloat16) * 1.6)(H, D, M) * ((H * D) ** -0.5)),
        }
    _B_in = _B_setup()

    return {
        "A_x": _A_in["x"],
        "A_wqkv": _A_in["wqkv"],
        "A_wo": _A_in["wo"],
        "B_x": _B_in["x"],
        "B_c_kv": _B_in["c_kv"],
        "B_w_q": _B_in["w_q"],
        "B_w_uk": _B_in["w_uk"],
        "B_w_uv": _B_in["w_uv"],
        "B_w_o": _B_in["w_o"],
        "gate_w": _w((lambda *s: torch.randn(*s, dtype=torch.bfloat16) * 0.05)(512) * 16.0),
    }

def reference(A_x, A_wqkv, A_wo, B_x, B_c_kv, B_w_q, B_w_uk, B_w_uv, B_w_o, gate_w):
    def _A_ref(x, wqkv, wo):
        B, S, D = x.shape
        H = 32; Dh = D // H
        W, dil = 8, 2
        qkv = (lambda x, *d: x.permute(*d))((x @ wqkv).reshape(B, S, 3, H, Dh), 2, 0, 3, 1, 4)
        q, k, v = qkv[0], qkv[1], qkv[2]
        scale = Dh ** -0.5
        pos = torch.arange(S)
        off = torch.arange(W) * dil
        idx = pos[:, None] - off[None, :]
        mask = idx >= 0
        idx_c = torch.where(mask, idx, 0).reshape(-1)
        kg = k[:, :, idx_c, :].reshape(B, H, S, W, Dh)
        vg = v[:, :, idx_c, :].reshape(B, H, S, W, Dh)
        logits = torch.einsum("bhsd,bhswd->bhsw", q, kg) * scale
        logits = torch.where(mask[None, None, :, :], logits, -1e9)
        attn = _softmax(logits, axis=-1)
        o = torch.einsum("bhsw,bhswd->bhsd", attn, vg)
        return (lambda x, *d: x.permute(*d))(o, 0, 2, 1, 3).reshape(B, S, D) @ wo

    def _B_ref(x, c_kv, w_q, w_uk, w_uv, w_o):
        B, Sq, M = x.shape[0], x.shape[1], x.shape[2]
        Sk = c_kv.shape[1]
        H, D = w_q.shape[1], w_q.shape[2]
        q = torch.einsum("bqm,mhd->bhqd", x, w_q)
        k = torch.einsum("bkc,chd->bhkd", c_kv, w_uk)
        v = torch.einsum("bkc,chd->bhkd", c_kv, w_uv)
        scores = torch.einsum("bhqd,bhkd->bhqk", q, k) * (D ** -0.5)
        off = Sk - Sq
        qi = torch.arange(Sq)[:, None]
        kj = torch.arange(Sk)[None, :]
        cmask = kj <= (qi + off)
        scores = torch.where(cmask[None, None], scores, -1e9)
        p = _softmax(scores, axis=-1)
        ctx = torch.einsum("bhqk,bhkd->bhqd", p, v)
        return torch.einsum("bhqd,hdm->bqm", ctx, w_o)

    x = A_x
    g = _sigmoid((x * gate_w).sum(-1, keepdims=True))
    a = _A_ref(x, A_wqkv, A_wo)
    b = _B_ref(x, B_c_kv, B_w_q, B_w_uk, B_w_uv, B_w_o)
    return g * a + (1 - g) * b
```

```cpp
#include <hip/hip_runtime.h>
#include <hip/hip_bf16.h>

extern "C" __global__ __launch_bounds__(256, 1) void gated_mix_fused(const void* c, const void* wk, const void* wv, const void* x, const void* gw,
                                                                     const void* wq, const void* wqkv, const void* wob, const void* woa, void* ws, void* out) {
  const unsigned wid = __builtin_amdgcn_readfirstlane(threadIdx.x >> 6);
  const auto ka = __builtin_amdgcn_kernarg_segment_ptr();
  asm volatile(
    "s_mov_b32 s33, %[wid]\n\t"
    "s_load_dwordx2 s[12:13], %[ka], 0\n\t"
    "s_load_dwordx4 s[92:95], %[ka], 8\n\t"
    "s_load_dwordx2 s[8:9], %[ka], 24\n\t"
    "s_load_dwordx2 s[72:73], %[ka], 32\n\t"
    "s_load_dwordx2 s[84:85], %[ka], 40\n\t"
    "s_load_dwordx2 s[88:89], %[ka], 48\n\t"
    "s_load_dwordx2 s[20:21], %[ka], 72\n\t"
    "v_mbcnt_lo_u32_b32 v217, -1, 0\n\t"
    "v_mbcnt_hi_u32_b32 v217, -1, v217\n\t"
    "v_lshlrev_b32 v216, 4, v217\n\t"
    "v_lshrrev_b32 v218, 3, v217\n\t"
    "v_lshlrev_b32 v218, 10, v218\n\t"
    "v_lshrrev_b32 v219, 4, v217\n\t"
    "v_and_b32 v220, 7, v217\n\t"
    "v_xor_b32 v221, v220, v219\n\t"
    "v_lshl_add_u32 v138, v221, 4, v218\n\t"
    "v_xor_b32 v221, 4, v221\n\t"
    "v_lshl_add_u32 v139, v221, 4, v218\n\t"
    "v_lshlrev_b32 v218, 13, v219\n\t"
    "v_and_b32 v220, 1, v217\n\t"
    "v_lshl_add_u32 v218, v220, 4, v218\n\t"
    "v_bfe_u32 v220, v217, 1, 3\n\t"
    "v_xor_b32 v221, v220, v219\n\t"
    "v_lshl_add_u32 v140, v221, 5, v218\n\t"
    "v_xor_b32 v221, 4, v221\n\t"
    "v_lshl_add_u32 v141, v221, 5, v218\n\t"
    "s_and_b32 s34, %[bid], 7\n\t"
    "s_lshl_b32 s34, s34, 2\n\t"
    "s_lshr_b32 s37, %[bid], 6\n\t"
    "s_add_u32 s34, s34, s37\n\t"
    "s_lshr_b32 s35, %[bid], 3\n\t"
    "s_and_b32 s35, s35, 7\n\t"
    "s_lshl_b32 s36, s34, 3\n\t"
    "s_add_u32 s36, s36, s35\n\t"
    "s_lshl_b32 s42, s35, 18\n\t"
    "s_lshl_b32 s37, s33, 16\n\t"
    "s_add_u32 s42, s42, s37\n\t"
    "s_and_b32 s37, s33, 1\n\t"
    "s_lshl_b32 s43, s37, 18\n\t"
    "s_lshl_b32 s38, s34, 8\n\t"
    "s_add_u32 s43, s43, s38\n\t"
    "s_lshl_b32 s44, s33, 13\n\t"
    "s_mov_b32 s45, 0\n\t"
    "s_mov_b32 s46, 0\n\t"
    "s_mov_b32 s47, 0\n\t"
    "s_mov_b32 s48, 0x10000\n\t"
    "s_waitcnt lgkmcnt(0)\n\t"
    "s_cmp_ge_u32 s33, 2\n\t"
    "s_cselect_b64 s[16:17], s[94:95], s[92:93]\n\t"
    "s_and_b32 s13, s13, 0xffff\n\t"
    "s_mov_b32 s14, 0x200000\n\t"
    "s_mov_b32 s15, 0x20000\n\t"
    "s_and_b32 s17, s17, 0xffff\n\t"
    "s_mov_b32 s18, 0x400000\n\t"
    "s_mov_b32 s19, 0x20000\n\t"
    "s_add_u32 m0, s44, s48\n\t"
    "s_add_u32 s39, s42, s45\n\t"
    "buffer_load_dwordx4 v138, s[12:15], s39 offen lds\n\t"
    "s_and_b32 s9, s9, 0xffff\n\t"
    "s_mov_b32 s10, 0x200000\n\t"
    "s_mov_b32 s11, 0x20000\n\t"
    "s_and_b32 s73, s73, 0xffff\n\t"
    "s_add_u32 m0, m0, 0x400\n\t"
    "s_add_u32 s39, s39, 0x2000\n\t"
    "buffer_load_dwordx4 v139, s[12:15], s39 offen lds\n\t"
    "s_mov_b32 s74, 0x400\n\t"
    "s_mov_b32 s75, 0x20000\n\t"
    "s_add_u32 s20, s20, 0x0\n\t"
    "s_addc_u32 s21, s21, 0\n\t"
    "s_add_u32 s24, s20, 0x2000000\n\t"
    "s_addc_u32 s25, s21, 0\n\t"
    "s_add_u32 m0, m0, 0x400\n\t"
    "s_add_u32 s39, s39, 0x2000\n\t"
    "buffer_load_dwordx4 v138, s[12:15], s39 offen lds\n\t"
    "s_add_u32 s28, s20, 0x3000000\n\t"
    "s_addc_u32 s29, s21, 0\n\t"
    "s_lshl_b32 s37, s34, 20\n\t"
    "s_add_u32 s20, s20, s37\n\t"
    "s_addc_u32 s21, s21, 0\n\t"
    "s_and_b32 s29, s29, 0xffff\n\t"
    "s_add_u32 m0, m0, 0x400\n\t"
    "s_add_u32 s39, s39, 0x2000\n\t"
    "buffer_load_dwordx4 v139, s[12:15], s39 offen lds\n\t"
    "s_mov_b32 s30, 0x400000\n\t"
    "s_mov_b32 s31, 0x20000\n\t"
    "s_and_b32 s21, s21, 0xffff\n\t"
    "s_mov_b32 s22, 0x100000\n\t"
    "s_add_u32 m0, m0, 0x400\n\t"
    "s_add_u32 s39, s39, 0x2000\n\t"
    "buffer_load_dwordx4 v138, s[12:15], s39 offen lds\n\t"
    "s_mov_b32 s23, 0x20000\n\t"
    "s_and_b32 s25, s25, 0xffff\n\t"
    "s_mov_b32 s26, 0x1000000\n\t"
    "s_mov_b32 s27, 0x20000\n\t"
    "s_add_u32 m0, m0, 0x400\n\t"
    "s_add_u32 s39, s39, 0x2000\n\t"
    "buffer_load_dwordx4 v139, s[12:15], s39 offen lds\n\t"
    "s_and_b32 s85, s85, 0xffff\n\t"
    "s_mov_b32 s86, 0x400000\n\t"
    "s_mov_b32 s87, 0x20000\n\t"
    "s_and_b32 s89, s89, 0xffff\n\t"
    "s_add_u32 m0, m0, 0x400\n\t"
    "s_add_u32 s39, s39, 0x2000\n\t"
    "buffer_load_dwordx4 v138, s[12:15], s39 offen lds\n\t"
    "s_mov_b32 s90, 0x180000\n\t"
    "s_mov_b32 s91, 0x20000\n\t"
    "s_and_b32 s37, s33, 1\n\t"
    "s_lshl_b32 s53, s35, 17\n\t"
    "s_add_u32 m0, m0, 0x400\n\t"
    "s_add_u32 s39, s39, 0x2000\n\t"
    "buffer_load_dwordx4 v139, s[12:15], s39 offen lds\n\t"
    "s_lshl_b32 s38, s37, 16\n\t"
    "s_add_u32 s53, s53, s38\n\t"
    "s_lshr_b32 s38, s33, 1\n\t"
    "s_lshl_b32 s38, s38, 14\n\t"
    "s_add_u32 s45, s45, 0x80\n\t"
    "s_add_u32 s48, s48, 0x8000\n\t"
    "s_cmp_eq_u32 s48, 0x28000\n\t"
    "s_cselect_b32 s48, 0x10000, s48\n\t"
    "s_add_u32 s53, s53, s38\n\t"
    "s_mov_b32 s49, 0x10000\n\t"
    "s_mov_b32 s51, 0xffff0000\n\t"
    "s_mov_b32 s52, 0x8000\n\t"
    "s_lshl_b32 s41, s33, 12\n\t"
    "s_add_u32 m0, s44, s47\n\t"
    "s_add_u32 s39, s43, s46\n\t"
    "buffer_load_dwordx4 v140, s[16:19], s39 offen lds\n\t"
    "v_lshrrev_b32 v219, 4, v217\n\t"
    "v_and_b32 v218, 15, v217\n\t"
    "v_lshrrev_b32 v220, 1, v218\n\t"
    "v_xor_b32 v220, v220, v219\n\t"
    "s_add_u32 m0, m0, 0x400\n\t"
    "s_add_u32 s39, s39, 0x8000\n\t"
    "buffer_load_dwordx4 v140, s[16:19], s39 offen lds\n\t"
    "v_lshlrev_b32 v220, 4, v220\n\t"
    "v_lshl_add_u32 v128, v218, 7, v220\n\t"
    "s_and_b32 s37, s33, 1\n\t"
    "s_lshl_b32 s37, s37, 14\n\t"
    "s_add_u32 m0, m0, 0x400\n\t"
    "s_add_u32 s39, s39, 0x8000\n\t"
    "buffer_load_dwordx4 v141, s[16:19], s39 offen lds\n\t"
    "s_add_u32 s37, s37, 0x10000\n\t"
    "v_add_u32 v128, s37, v128\n\t"
    "v_xor_b32 v129, 64, v128\n\t"
    "v_bfe_u32 v218, v217, 2, 2\n\t"
    "s_add_u32 m0, m0, 0x400\n\t"
    "s_add_u32 s39, s39, 0x8000\n\t"
    "buffer_load_dwordx4 v141, s[16:19], s39 offen lds\n\t"
    "v_lshl_add_u32 v220, v219, 3, v218\n\t"
    "v_lshlrev_b32 v220, 8, v220\n\t"
    "v_and_b32 v221, 3, v217\n\t"
    "v_lshl_add_u32 v220, v221, 3, v220\n\t"
    "s_add_u32 m0, m0, 0x400\n\t"
    "s_add_u32 s39, s39, 0x8000\n\t"
    "buffer_load_dwordx4 v140, s[16:19], s39 offen lds\n\t"
    "s_lshr_b32 s37, s33, 1\n\t"
    "s_lshl_b32 s37, s37, 14\n\t"
    "v_add_u32 v220, s37, v220\n\t"
    "v_and_b32 v221, 1, v219\n\t"
    "s_add_u32 m0, m0, 0x400\n\t"
    "s_add_u32 s39, s39, 0x8000\n\t"
    "buffer_load_dwordx4 v140, s[16:19], s39 offen lds\n\t"
    "v_lshl_or_b32 v218, v221, 2, v218\n\t"
    "v_xor_b32 v221, 0, v218\n\t"
    "v_lshl_add_u32 v130, v221, 5, v220\n\t"
    "v_xor_b32 v221, 1, v218\n\t"
    "s_add_u32 m0, m0, 0x400\n\t"
    "s_add_u32 s39, s39, 0x8000\n\t"
    "buffer_load_dwordx4 v141, s[16:19], s39 offen lds\n\t"
    "v_lshl_add_u32 v131, v221, 5, v220\n\t"
    "v_xor_b32 v221, 2, v218\n\t"
    "v_lshl_add_u32 v132, v221, 5, v220\n\t"
    "v_xor_b32 v221, 3, v218\n\t"
    "s_add_u32 m0, m0, 0x400\n\t"
    "s_add_u32 s39, s39, 0x8000\n\t"
    "buffer_load_dwordx4 v141, s[16:19], s39 offen lds\n\t"
    "v_lshl_add_u32 v133, v221, 5, v220\n\t"
    "v_xor_b32 v221, 4, v218\n\t"
    "v_lshl_add_u32 v134, v221, 5, v220\n\t"
    "v_xor_b32 v221, 5, v218\n\t"
    "s_add_u32 s46, s46, 0x80000\n\t"
    "s_xor_b32 s47, s47, 0x8000\n\t"
    "v_lshl_add_u32 v135, v221, 5, v220\n\t"
    "v_xor_b32 v221, 6, v218\n\t"
    "v_lshl_add_u32 v136, v221, 5, v220\n\t"
    "v_xor_b32 v221, 7, v218\n\t"
    "v_lshl_add_u32 v137, v221, 5, v220\n\t"
    "s_lshl_b32 s37, s34, 3\n\t"
    "s_lshl_b32 s38, s33, 1\n\t"
    "s_add_u32 s37, s37, s38\n\t"
    "s_lshl_b32 s96, s35, 7\n\t"
    "s_sub_u32 s38, 1792, s96\n\t"
    "s_cmp_lt_u32 s37, 128\n\t"
    "s_cselect_b32 s96, s96, s38\n\t"
    "s_add_u32 s96, s96, s37\n\t"
    "s_lshl_b32 s37, s96, 10\n\t"
    "buffer_load_dwordx4 v[156:159], v216, s[8:11], s37 offen\n\t"
    "buffer_load_dwordx4 v[160:163], v216, s[8:11], s37 offen offset:1024\n\t"
    "buffer_load_dwordx4 v[164:167], v216, s[72:75], 0 offen\n\t"
    "s_lshl_b32 s96, s96, 2\n\t"
    "s_add_u32 s96, s96, 0x200000\n\t"
    "s_getpc_b64 s[92:93]\n\t"
    "L_pc:\n\t"
    "s_and_b32 s93, s93, 0xffff\n\t"
    "s_mov_b32 s94, L_code_end-L_pc\n\t"
    "s_mov_b32 s95, 0x20000\n\t"
    "s_lshr_b32 s37, %[bid], 3\n\t"
    "s_lshl_b32 s37, s37, 2\n\t"
    "s_add_u32 s37, s37, s33\n\t"
    "s_lshl_b32 s37, s37, 10\n\t"
    "buffer_load_dwordx4 v[236:239], v216, s[92:95], s37 offen\n\t"
    "s_lshl_b32 s37, s36, 6\n\t"
    "s_add_u32 s37, s37, 0x210000\n\t"
    "v_mov_b32 v170, s37\n\t"
    "s_cmp_lg_u32 s33, 0\n\t"
    "s_cbranch_scc1 L_n0\n\t"
    "buffer_load_dwordx2 v[172:173], v170, s[28:31], 0 offen sc1\n\t"
    "L_n0:\n\t"
    "s_add_u32 m0, s44, s48\n\t"
    "s_add_u32 s39, s42, s45\n\t"
    "buffer_load_dwordx4 v138, s[12:15], s39 offen lds\n\t"
    "s_lshl_b32 s60, s35, 3\n\t"
    "s_lshl_b32 s37, s33, 1\n\t"
    "s_add_u32 s60, s60, s37\n\t"
    "s_sub_u32 s61, 15, s35\n\t"
    "s_lshl_b32 s61, s61, 3\n\t"
    "s_add_u32 s61, s61, s37\n\t"
    "s_lshl_b32 s54, s60, 14\n\t"
    "s_add_u32 m0, m0, 0x400\n\t"
    "s_add_u32 s39, s39, 0x2000\n\t"
    "buffer_load_dwordx4 v139, s[12:15], s39 offen lds\n\t"
    "s_lshl_b32 s55, s61, 14\n\t"
    "s_sub_u32 s37, 15, s35\n\t"
    "s_cmp_lt_u32 s33, 2\n\t"
    "s_cselect_b32 s37, s35, s37\n\t"
    "s_lshl_b32 s37, s37, 7\n\t"
    "s_and_b32 s38, s33, 1\n\t"
    "s_lshl_b32 s38, s38, 3\n\t"
    "s_add_u32 s37, s37, s38\n\t"
    "s_sub_i32 s37, s37, 16\n\t"
    "s_add_u32 m0, m0, 0x400\n\t"
    "s_add_u32 s39, s39, 0x2000\n\t"
    "buffer_load_dwordx4 v138, s[12:15], s39 offen lds\n\t"
    "s_max_i32 s56, s37, 0\n\t"
    "s_lshl_b32 s56, s56, 10\n\t"
    "s_lshl_b32 s57, s33, 17\n\t"
    "s_lshl_b32 s37, s34, 8\n\t"
    "s_add_u32 s57, s57, s37\n\t"
    "s_mul_i32 s58, s33, 49152\n\t"
    "s_lshl_b32 s37, s34, 5\n\t"
    "s_add_u32 s58, s58, s37\n\t"
    "s_add_u32 m0, m0, 0x400\n\t"
    "s_add_u32 s39, s39, 0x2000\n\t"
    "buffer_load_dwordx4 v139, s[12:15], s39 offen lds\n\t"
    "s_lshl_b32 s59, s33, 10\n\t"
    "s_mov_b32 s64, 0x10000\n\t"
    "s_mov_b32 s65, 0xffff0000\n\t"
    "s_mov_b32 s70, s41\n\t"
    "s_add_u32 s71, s41, 0x4000\n\t"
    "s_mov_b32 s68, s41\n\t"
    "s_add_u32 s69, s41, 0x4000\n\t"
    "s_sub_u32 s66, s60, s33\n\t"
    "s_add_u32 m0, m0, 0x400\n\t"
    "s_add_u32 s39, s39, 0x2000\n\t"
    "buffer_load_dwordx4 v138, s[12:15], s39 offen lds\n\t"
    "s_lshl_b32 s66, s66, 17\n\t"
    "s_lshl_b32 s37, s34, 12\n\t"
    "s_add_u32 s66, s66, s37\n\t"
    "s_sub_u32 s67, s61, s33\n\t"
    "s_lshl_b32 s67, s67, 17\n\t"
    "s_add_u32 s67, s67, s37\n\t"
    "v_mov_b32 v209, v138\n\t"
    "v_mov_b32 v210, v139\n\t"
    "s_add_u32 m0, m0, 0x400\n\t"
    "s_add_u32 s39, s39, 0x2000\n\t"
    "buffer_load_dwordx4 v139, s[12:15], s39 offen lds\n\t"
    "v_mov_b32 v212, v140\n\t"
    "v_mov_b32 v213, v141\n\t"
    "s_and_b32 s37, s33, 1\n\t"
    "s_cmp_eq_u32 s37, 1\n\t"
    "s_cselect_b64 vcc, -1, 0\n\t"
    "s_nop 1\n\t"
    "v_cndmask_b32 v211, v138, v139, vcc\n\t"
    "v_lshrrev_b32 v218, 3, v217\n\t"
    "v_mul_u32_u24 v218, 3072, v218\n\t"
    "s_add_u32 m0, m0, 0x400\n\t"
    "s_add_u32 s39, s39, 0x2000\n\t"
    "buffer_load_dwordx4 v138, s[12:15], s39 offen lds\n\t"
    "v_and_b32 v220, 1, v217\n\t"
    "v_lshl_add_u32 v218, v220, 4, v218\n\t"
    "v_bfe_u32 v220, v217, 1, 2\n\t"
    "v_bfe_u32 v221, v217, 4, 1\n\t"
    "v_xor_b32 v220, v220, v221\n\t"
    "v_xor_b32 v221, 0, v220\n\t"
    "v_min_u32 v221, 2, v221\n\t"
    "s_add_u32 m0, m0, 0x400\n\t"
    "s_add_u32 s39, s39, 0x2000\n\t"
    "buffer_load_dwordx4 v139, s[12:15], s39 offen lds\n\t"
    "v_lshl_add_u32 v214, v221, 10, v218\n\t"
    "v_xor_b32 v221, 2, v220\n\t"
    "v_min_u32 v221, 2, v221\n\t"
    "v_lshl_add_u32 v215, v221, 10, v218\n\t"
    "v_lshrrev_b32 v219, 4, v217\n\t"
    "v_and_b32 v218, 15, v217\n\t"
    "v_lshrrev_b32 v220, 1, v218\n\t"
    "v_xor_b32 v220, v220, v219\n\t"
    "s_add_u32 s45, s45, 0x80\n\t"
    "s_add_u32 s48, s48, 0x8000\n\t"
    "s_cmp_eq_u32 s48, 0x28000\n\t"
    "s_cselect_b32 s48, 0x10000, s48\n\t"
    "v_lshlrev_b32 v220, 4, v220\n\t"
    "v_lshl_add_u32 v220, v218, 7, v220\n\t"
    "s_lshl_b32 s37, s33, 11\n\t"
    "s_add_u32 s37, s37, 0x20000\n\t"
    "v_add_u32 v234, s37, v220\n\t"
    "v_xor_b32 v235, 64, v234\n\t"
    "s_add_u32 s37, s41, 0x20000\n\t"
    "v_add_u32 v192, s37, v220\n\t"
    "s_add_u32 m0, s44, s47\n\t"
    "s_add_u32 s39, s43, s46\n\t"
    "buffer_load_dwordx4 v140, s[16:19], s39 offen lds\n\t"
    "v_xor_b32 v193, 64, v192\n\t"
    "s_sub_u32 s38, s37, 2048\n\t"
    "s_cmp_eq_u32 s33, 0\n\t"
    "s_cselect_b32 s38, 0xe000, s38\n\t"
    "v_add_u32 v194, s38, v220\n\t"
    "v_xor_b32 v195, 64, v194\n\t"
    "s_add_u32 s38, s37, 14336\n\t"
    "s_cmp_eq_u32 s33, 0\n\t"
    "s_cselect_b32 s38, 0xe800, s38\n\t"
    "v_add_u32 v196, s38, v220\n\t"
    "s_add_u32 m0, m0, 0x400\n\t"
    "s_add_u32 s39, s39, 0x8000\n\t"
    "buffer_load_dwordx4 v140, s[16:19], s39 offen lds\n\t"
    "v_xor_b32 v197, 64, v196\n\t"
    "v_bfe_u32 v218, v217, 2, 2\n\t"
    "v_lshl_add_u32 v220, v219, 3, v218\n\t"
    "v_and_b32 v221, 3, v217\n\t"
    "v_lshlrev_b32 v221, 3, v221\n\t"
    "v_lshl_add_u32 v222, v220, 8, v221\n\t"
    "v_add_u32 v222, 0x8000, v222\n\t"
    "v_lshl_add_u32 v223, v220, 7, v221\n\t"
    "s_add_u32 m0, m0, 0x400\n\t"
    "s_add_u32 s39, s39, 0x8000\n\t"
    "buffer_load_dwordx4 v141, s[16:19], s39 offen lds\n\t"
    "v_add_u32 v223, 0xc000, v223\n\t"
    "v_and_b32 v221, 1, v219\n\t"
    "v_lshl_or_b32 v224, v221, 2, v218\n\t"
    "v_lshrrev_b32 v218, 1, v218\n\t"
    "v_lshl_or_b32 v225, v221, 1, v218\n\t"
    "v_xor_b32 v221, 0, v224\n\t"
    "v_lshl_add_u32 v198, v221, 5, v222\n\t"
    "v_xor_b32 v221, 1, v224\n\t"
    "s_add_u32 m0, m0, 0x400\n\t"
    "s_add_u32 s39, s39, 0x8000\n\t"
    "buffer_load_dwordx4 v141, s[16:19], s39 offen lds\n\t"
    "v_lshl_add_u32 v199, v221, 5, v222\n\t"
    "v_xor_b32 v221, 2, v224\n\t"
    "v_lshl_add_u32 v200, v221, 5, v222\n\t"
    "v_xor_b32 v221, 3, v224\n\t"
    "v_lshl_add_u32 v201, v221, 5, v222\n\t"
    "v_xor_b32 v221, 4, v224\n\t"
    "v_lshl_add_u32 v202, v221, 5, v222\n\t"
    "s_add_u32 m0, m0, 0x400\n\t"
    "s_add_u32 s39, s39, 0x8000\n\t"
    "buffer_load_dwordx4 v140, s[16:19], s39 offen lds\n\t"
    "v_xor_b32 v221, 5, v224\n\t"
    "v_lshl_add_u32 v203, v221, 5, v222\n\t"
    "v_xor_b32 v221, 6, v224\n\t"
    "v_lshl_add_u32 v204, v221, 5, v222\n\t"
    "v_xor_b32 v221, 7, v224\n\t"
    "v_lshl_add_u32 v205, v221, 5, v222\n\t"
    "v_xor_b32 v221, 0, v225\n\t"
    "v_lshl_add_u32 v206, v221, 5, v223\n\t"
    "s_add_u32 m0, m0, 0x400\n\t"
    "s_add_u32 s39, s39, 0x8000\n\t"
    "buffer_load_dwordx4 v140, s[16:19], s39 offen lds\n\t"
    "v_xor_b32 v221, 1, v225\n\t"
    "v_lshl_add_u32 v207, v221, 5, v223\n\t"
    "v_xor_b32 v221, 2, v225\n\t"
    "v_lshl_add_u32 v208, v221, 5, v223\n\t"
    "v_and_b32 v218, 7, v217\n\t"
    "s_lshl_b32 s37, s34, 3\n\t"
    "v_add_u32 v219, s37, v218\n\t"
    "v_lshlrev_b32 v252, 6, v219\n\t"
    "s_add_u32 m0, m0, 0x400\n\t"
    "s_add_u32 s39, s39, 0x8000\n\t"
    "buffer_load_dwordx4 v141, s[16:19], s39 offen lds\n\t"
    "v_add_u32 v252, 0x210000, v252\n\t"
    "v_lshlrev_b32 v254, 7, v219\n\t"
    "s_lshl_b32 s37, s35, 4\n\t"
    "s_add_u32 s37, s37, 0x214000\n\t"
    "v_add_u32 v254, s37, v254\n\t"
    "v_lshlrev_b32 v255, 4, v218\n\t"
    "s_lshl_b32 s37, s36, 7\n\t"
    "s_add_u32 s37, s37, 0x214000\n\t"
    "s_add_u32 m0, m0, 0x400\n\t"
    "s_add_u32 s39, s39, 0x8000\n\t"
    "buffer_load_dwordx4 v141, s[16:19], s39 offen lds\n\t"
    "v_add_u32 v255, s37, v255\n\t"
    "s_lshl_b32 s81, s36, 2\n\t"
    "s_add_u32 s81, s81, s33\n\t"
    "s_lshl_b32 s81, s81, 11\n\t"
    "s_lshr_b32 s82, %[bid], 3\n\t"
    "s_lshl_b32 s80, s82, 1\n\t"
    "s_lshl_b32 s82, s82, 2\n\t"
    "s_add_u32 s82, s82, s33\n\t"
    "s_add_u32 s46, s46, 0x80000\n\t"
    "s_xor_b32 s47, s47, 0x8000\n\t"
    "s_lshl_b32 s82, s82, 15\n\t"
    "s_and_b32 s83, s33, 1\n\t"
    "s_add_u32 s80, s80, s83\n\t"
    "s_lshl_b32 s80, s80, 16\n\t"
    "s_and_b32 s83, %[bid], 7\n\t"
    "s_lshl_b32 s83, s83, 10\n\t"
    "s_add_u32 s82, s82, s83\n\t"
    "s_add_u32 s80, s80, s83\n\t"
    "s_cmp_lg_u32 s33, 0\n\t"
    "s_cbranch_scc1 L_touch\n\t"
    "buffer_load_dwordx4 v[236:239], v255, s[28:31], 0 offen sc1\n\t"
    "v_and_b32 v218, 31, v217\n\t"
    "v_lshlrev_b32 v218, 4, v218\n\t"
    "s_lshl_b32 s37, s36, 9\n\t"
    "s_add_u32 s37, s37, 0x220000\n\t"
    "buffer_load_dwordx4 v[236:239], v218, s[28:31], s37 offen sc1\n\t"
    "v_and_b32 v218, 48, v218\n\t"
    "s_lshl_b32 s37, s36, 6\n\t"
    "s_add_u32 s37, s37, 0x240000\n\t"
    "buffer_load_dwordx4 v[236:239], v218, s[28:31], s37 offen sc1\n\t"
    "L_touch:\n\t"
    "s_waitcnt vmcnt(16)\n\t"
    "s_cmp_lg_u32 s33, 0\n\t"
    "s_cbranch_scc1 L_n1\n\t"
    "v_readfirstlane_b32 s76, v172\n\t"
    "v_readfirstlane_b32 s77, v173\n\t"
    "s_nop 1\n\t"
    "s_add_u32 s76, s76, 0x7f4a7c15\n\t"
    "s_addc_u32 s77, s77, 0x9e3779b9\n\t"
    "v_mov_b32 v172, s76\n\t"
    "v_mov_b32 v173, s77\n\t"
    "buffer_store_dwordx2 v[172:173], v170, s[28:31], 0 offen sc1\n\t"
    "L_n1:\n\t"
    "s_barrier\n\t"
    "ds_read_b128 a[128:131], v128 offset:0\n\t"
    "ds_read_b64_tr_b16 a[160:161], v130 offset:0\n\t"
    "ds_read_b64_tr_b16 a[162:163], v130 offset:1024\n\t"
    "ds_read_b64_tr_b16 a[164:165], v131 offset:0\n\t"
    "ds_read_b64_tr_b16 a[166:167], v131 offset:1024\n\t"
    "ds_read_b64_tr_b16 a[168:169], v132 offset:0\n\t"
    "ds_read_b64_tr_b16 a[170:171], v132 offset:1024\n\t"
    "ds_read_b64_tr_b16 a[172:173], v133 offset:0\n\t"
    "ds_read_b64_tr_b16 a[174:175], v133 offset:1024\n\t"
    "ds_read_b64_tr_b16 a[176:177], v134 offset:0\n\t"
    "ds_read_b64_tr_b16 a[178:179], v134 offset:1024\n\t"
    "ds_read_b64_tr_b16 a[180:181], v135 offset:0\n\t"
    "ds_read_b64_tr_b16 a[182:183], v135 offset:1024\n\t"
    "ds_read_b64_tr_b16 a[184:185], v136 offset:0\n\t"
    "ds_read_b64_tr_b16 a[186:187], v136 offset:1024\n\t"
    "ds_read_b64_tr_b16 a[188:189], v137 offset:0\n\t"
    "ds_read_b64_tr_b16 a[190:191], v137 offset:1024\n\t"
    "ds_read_b128 a[132:135], v128 offset:2048\n\t"
    "ds_read_b128 a[136:139], v128 offset:4096\n\t"
    "ds_read_b128 a[140:143], v128 offset:6144\n\t"
    "ds_read_b128 a[144:147], v128 offset:8192\n\t"
    "ds_read_b128 a[148:151], v128 offset:10240\n\t"
    "ds_read_b128 a[152:155], v128 offset:12288\n\t"
    "ds_read_b128 a[156:159], v128 offset:14336\n\t"
    "s_mov_b32 s40, 5\n\t"
    "s_cmp_ge_u32 s33, 2\n\t"
    "s_cbranch_scc1 L_kv_v\n\t"
    "L_kv_k:\n\t"
    "s_waitcnt lgkmcnt(15)\n\t"
    "v_mfma_f32_16x16x32_bf16 v[0:3], a[160:163], a[128:131], 0\n\t"
    "s_waitcnt lgkmcnt(15)\n\t"
    "ds_read_b128 a[192:195], v129 offset:0\n\t"
    "v_mfma_f32_16x16x32_bf16 v[4:7], a[164:167], a[128:131], 0\n\t"
    "s_waitcnt lgkmcnt(15)\n\t"
    "s_add_u32 m0, s44, s48\n\t"
    "v_lshlrev_b32 v142, 16, v164\n\t"
    "v_and_b32 v143, 0xffff0000, v164\n\t"
    "v_mfma_f32_16x16x32_bf16 v[8:11], a[168:171], a[128:131], 0\n\t"
    "s_waitcnt lgkmcnt(15)\n\t"
    "s_add_u32 s39, s42, s45\n\t"
    "ds_read_b64_tr_b16 a[224:225], v130 offset:8192\n\t"
    "v_lshlrev_b32 v144, 16, v165\n\t"
    "v_mfma_f32_16x16x32_bf16 v[12:15], a[172:175], a[128:131], 0\n\t"
    "s_waitcnt lgkmcnt(15)\n\t"
    "buffer_load_dwordx4 v138, s[12:15], s39 offen lds\n\t"
    "v_and_b32 v145, 0xffff0000, v165\n\t"
    "v_mfma_f32_16x16x32_bf16 v[16:19], a[176:179], a[128:131], 0\n\t"
    "s_waitcnt lgkmcnt(13)\n\t"
    "ds_read_b64_tr_b16 a[226:227], v130 offset:9216\n\t"
    "v_lshlrev_b32 v146, 16, v166\n\t"
    "v_mfma_f32_16x16x32_bf16 v[20:23], a[180:183], a[128:131], 0\n\t"
    "s_waitcnt lgkmcnt(12)\n\t"
    "buffer_load_dwordx4 v[236:239], v216, s[12:15], s81 offen offset:0\n\t"
    "v_and_b32 v147, 0xffff0000, v166\n\t"
    "v_mfma_f32_16x16x32_bf16 v[24:27], a[184:187], a[128:131], 0\n\t"
    "s_waitcnt lgkmcnt(10)\n\t"
    "ds_read_b64_tr_b16 a[228:229], v131 offset:8192\n\t"
    "v_lshlrev_b32 v148, 16, v167\n\t"
    "v_mfma_f32_16x16x32_bf16 v[28:31], a[188:191], a[128:131], 0\n\t"
    "s_waitcnt lgkmcnt(10)\n\t"
    "s_add_u32 m0, m0, 0x400\n\t"
    "v_and_b32 v149, 0xffff0000, v167\n\t"
    "v_lshlrev_b32 v150, 16, v156\n\t"
    "v_mfma_f32_16x16x32_bf16 v[32:35], a[160:163], a[132:135], 0\n\t"
    "s_add_u32 s39, s39, 0x2000\n\t"
    "ds_read_b64_tr_b16 a[230:231], v131 offset:9216\n\t"
    "v_and_b32 v151, 0xffff0000, v156\n\t"
    "v_mfma_f32_16x16x32_bf16 v[36:39], a[164:167], a[132:135], 0\n\t"
    "buffer_load_dwordx4 v139, s[12:15], s39 offen lds\n\t"
    "buffer_load_dwordx4 v[236:239], v216, s[12:15], s81 offen offset:1024\n\t"
    "v_mfma_f32_16x16x32_bf16 v[40:43], a[168:171], a[132:135], 0\n\t"
    "ds_read_b64_tr_b16 a[232:233], v132 offset:8192\n\t"
    "v_mul_f32 v168, v150, v142\n\t"
    "v_mfma_f32_16x16x32_bf16 v[44:47], a[172:175], a[132:135], 0\n\t"
    "v_fmac_f32 v168, v151, v143\n\t"
    "v_lshlrev_b32 v150, 16, v157\n\t"
    "v_mfma_f32_16x16x32_bf16 v[48:51], a[176:179], a[132:135], 0\n\t"
    "s_add_u32 s83, s80, 0x0\n\t"
    "ds_read_b64_tr_b16 a[234:235], v132 offset:9216\n\t"
    "v_and_b32 v151, 0xffff0000, v157\n\t"
    "v_mfma_f32_16x16x32_bf16 v[52:55], a[180:183], a[132:135], 0\n\t"
    "s_add_u32 m0, m0, 0x400\n\t"
    "buffer_load_dwordx4 v[236:239], v216, s[16:19], s83 offen\n\t"
    "v_fmac_f32 v168, v150, v144\n\t"
    "v_mfma_f32_16x16x32_bf16 v[56:59], a[184:187], a[132:135], 0\n\t"
    "s_add_u32 s39, s39, 0x2000\n\t"
    "ds_read_b64_tr_b16 a[236:237], v133 offset:8192\n\t"
    "v_fmac_f32 v168, v151, v145\n\t"
    "v_mfma_f32_16x16x32_bf16 v[60:63], a[188:191], a[132:135], 0\n\t"
    "s_waitcnt lgkmcnt(13)\n\t"
    "buffer_load_dwordx4 v138, s[12:15], s39 offen lds\n\t"
    "v_lshlrev_b32 v150, 16, v158\n\t"
    "v_mfma_f32_16x16x32_bf16 v[64:67], a[160:163], a[136:139], 0\n\t"
    "s_add_u32 s83, s80, 0x2000\n\t"
    "ds_read_b64_tr_b16 a[238:239], v133 offset:9216\n\t"
    "v_and_b32 v151, 0xffff0000, v158\n\t"
    "v_mfma_f32_16x16x32_bf16 v[68:71], a[164:167], a[136:139], 0\n\t"
    "buffer_load_dwordx4 v[236:239], v216, s[16:19], s83 offen\n\t"
    "v_fmac_f32 v168, v150, v146\n\t"
    "v_mfma_f32_16x16x32_bf16 v[72:75], a[168:171], a[136:139], 0\n\t"
    "ds_read_b64_tr_b16 a[240:241], v134 offset:8192\n\t"
    "v_fmac_f32 v168, v151, v147\n\t"
    "v_mfma_f32_16x16x32_bf16 v[76:79], a[172:175], a[136:139], 0\n\t"
    "s_add_u32 m0, m0, 0x400\n\t"
    "v_lshlrev_b32 v150, 16, v159\n\t"
    "v_and_b32 v151, 0xffff0000, v159\n\t"
    "v_mfma_f32_16x16x32_bf16 v[80:83], a[176:179], a[136:139], 0\n\t"
    "s_add_u32 s39, s39, 0x2000\n\t"
    "s_add_u32 s83, s80, 0x4000\n\t"
    "ds_read_b64_tr_b16 a[242:243], v134 offset:9216\n\t"
    "v_fmac_f32 v168, v150, v148\n\t"
    "v_mfma_f32_16x16x32_bf16 v[84:87], a[180:183], a[136:139], 0\n\t"
    "buffer_load_dwordx4 v139, s[12:15], s39 offen lds\n\t"
    "buffer_load_dwordx4 v[236:239], v216, s[16:19], s83 offen\n\t"
    "v_mfma_f32_16x16x32_bf16 v[88:91], a[184:187], a[136:139], 0\n\t"
    "ds_read_b64_tr_b16 a[244:245], v135 offset:8192\n\t"
    "v_fmac_f32 v168, v151, v149\n\t"
    "v_mfma_f32_16x16x32_bf16 v[92:95], a[188:191], a[136:139], 0\n\t"
    "s_waitcnt lgkmcnt(15)\n\t"
    "v_lshlrev_b32 v150, 16, v160\n\t"
    "v_and_b32 v151, 0xffff0000, v160\n\t"
    "v_mfma_f32_16x16x32_bf16 v[96:99], a[160:163], a[140:143], 0\n\t"
    "s_add_u32 s83, s80, 0x6000\n\t"
    "ds_read_b64_tr_b16 a[246:247], v135 offset:9216\n\t"
    "v_mul_f32 v169, v150, v142\n\t"
    "v_mfma_f32_16x16x32_bf16 v[100:103], a[164:167], a[140:143], 0\n\t"
    "s_add_u32 m0, m0, 0x400\n\t"
    "buffer_load_dwordx4 v[236:239], v216, s[16:19], s83 offen\n\t"
    "v_fmac_f32 v169, v151, v143\n\t"
    "v_mfma_f32_16x16x32_bf16 v[104:107], a[168:171], a[140:143], 0\n\t"
    "s_add_u32 s39, s39, 0x2000\n\t"
    "ds_read_b64_tr_b16 a[248:249], v136 offset:8192\n\t"
    "v_lshlrev_b32 v150, 16, v161\n\t"
    "v_mfma_f32_16x16x32_bf16 v[108:111], a[172:175], a[140:143], 0\n\t"
    "buffer_load_dwordx4 v138, s[12:15], s39 offen lds\n\t"
    "v_and_b32 v151, 0xffff0000, v161\n\t"
    "v_mfma_f32_16x16x32_bf16 v[112:115], a[176:179], a[140:143], 0\n\t"
    "s_add_u32 s83, s80, 0x8000\n\t"
    "ds_read_b64_tr_b16 a[250:251], v136 offset:9216\n\t"
    "v_fmac_f32 v169, v150, v144\n\t"
    "v_mfma_f32_16x16x32_bf16 v[116:119], a[180:183], a[140:143], 0\n\t"
    "buffer_load_dwordx4 v[236:239], v216, s[16:19], s83 offen\n\t"
    "v_fmac_f32 v169, v151, v145\n\t"
    "v_mfma_f32_16x16x32_bf16 v[120:123], a[184:187], a[140:143], 0\n\t"
    "ds_read_b64_tr_b16 a[252:253], v137 offset:8192\n\t"
    "v_lshlrev_b32 v150, 16, v162\n\t"
    "v_mfma_f32_16x16x32_bf16 v[124:127], a[188:191], a[140:143], 0\n\t"
    "s_waitcnt lgkmcnt(15)\n\t"
    "s_add_u32 m0, m0, 0x400\n\t"
    "v_and_b32 v151, 0xffff0000, v162\n\t"
    "v_fmac_f32 v169, v150, v146\n\t"
    "v_mfma_f32_16x16x32_bf16 a[0:3], a[160:163], a[144:147], 0\n\t"
    "s_add_u32 s39, s39, 0x2000\n\t"
    "s_add_u32 s83, s80, 0xa000\n\t"
    "ds_read_b64_tr_b16 a[254:255], v137 offset:9216\n\t"
    "v_fmac_f32 v169, v151, v147\n\t"
    "v_mfma_f32_16x16x32_bf16 a[4:7], a[164:167], a[144:147], 0\n\t"
    "buffer_load_dwordx4 v139, s[12:15], s39 offen lds\n\t"
    "buffer_load_dwordx4 v[236:239], v216, s[16:19], s83 offen\n\t"
    "v_mfma_f32_16x16x32_bf16 a[8:11], a[168:171], a[144:147], 0\n\t"
    "ds_read_b128 a[196:199], v129 offset:2048\n\t"
    "v_lshlrev_b32 v150, 16, v163\n\t"
    "v_mfma_f32_16x16x32_bf16 a[12:15], a[172:175], a[144:147], 0\n\t"
    "v_and_b32 v151, 0xffff0000, v163\n\t"
    "v_fmac_f32 v169, v150, v148\n\t"
    "v_mfma_f32_16x16x32_bf16 a[16:19], a[176:179], a[144:147], 0\n\t"
    "s_add_u32 s83, s80, 0xc000\n\t"
    "ds_read_b128 a[200:203], v129 offset:4096\n\t"
    "v_fmac_f32 v169, v151, v149\n\t"
    "v_mfma_f32_16x16x32_bf16 a[20:23], a[180:183], a[144:147], 0\n\t"
    "s_add_u32 m0, m0, 0x400\n\t"
    "buffer_load_dwordx4 v[236:239], v216, s[16:19], s83 offen\n\t"
    "v_add_f32_dpp v168, v168, v168 quad_perm:[1,0,3,2] row_mask:0xf bank_mask:0xf\n\t"
    "v_mfma_f32_16x16x32_bf16 a[24:27], a[184:187], a[144:147], 0\n\t"
    "s_add_u32 s39, s39, 0x2000\n\t"
    "ds_read_b128 a[204:207], v129 offset:6144\n\t"
    "v_add_f32_dpp v169, v169, v169 quad_perm:[1,0,3,2] row_mask:0xf bank_mask:0xf\n\t"
    "v_mfma_f32_16x16x32_bf16 a[28:31], a[188:191], a[144:147], 0\n\t"
    "s_waitcnt lgkmcnt(15)\n\t"
    "buffer_load_dwordx4 v138, s[12:15], s39 offen lds\n\t"
    "v_add_f32_dpp v168, v168, v168 quad_perm:[2,3,0,1] row_mask:0xf bank_mask:0xf\n\t"
    "v_mfma_f32_16x16x32_bf16 a[32:35], a[160:163], a[148:151], 0\n\t"
    "s_add_u32 s83, s80, 0xe000\n\t"
    "ds_read_b128 a[208:211], v129 offset:8192\n\t"
    "v_add_f32_dpp v169, v169, v169 quad_perm:[2,3,0,1] row_mask:0xf bank_mask:0xf\n\t"
    "v_mfma_f32_16x16x32_bf16 a[36:39], a[164:167], a[148:151], 0\n\t"
    "buffer_load_dwordx4 v[236:239], v216, s[16:19], s83 offen\n\t"
    "v_add_f32_dpp v168, v168, v168 row_half_mirror row_mask:0xf bank_mask:0xf\n\t"
    "v_mfma_f32_16x16x32_bf16 a[40:43], a[168:171], a[148:151], 0\n\t"
    "ds_read_b128 a[212:215], v129 offset:10240\n\t"
    "v_add_f32_dpp v169, v169, v169 row_half_mirror row_mask:0xf bank_mask:0xf\n\t"
    "v_mfma_f32_16x16x32_bf16 a[44:47], a[172:175], a[148:151], 0\n\t"
    "s_add_u32 m0, m0, 0x400\n\t"
    "v_add_f32_dpp v168, v168, v168 row_mirror row_mask:0xf bank_mask:0xf\n\t"
    "v_mfma_f32_16x16x32_bf16 a[48:51], a[176:179], a[148:151], 0\n\t"
    "s_add_u32 s39, s39, 0x2000\n\t"
    "ds_read_b128 a[216:219], v129 offset:12288\n\t"
    "v_add_f32_dpp v169, v169, v169 row_mirror row_mask:0xf bank_mask:0xf\n\t"
    "v_mfma_f32_16x16x32_bf16 a[52:55], a[180:183], a[148:151], 0\n\t"
    "buffer_load_dwordx4 v139, s[12:15], s39 offen lds\n\t"
    "buffer_load_dwordx4 v[236:239], v216, s[8:11], s81 offen\n\t"
    "v_mfma_f32_16x16x32_bf16 a[56:59], a[184:187], a[148:151], 0\n\t"
    "s_add_u32 s45, s45, 0x80\n\t"
    "ds_read_b128 a[220:223], v129 offset:14336\n\t"
    "v_mov_b32 v152, v168\n\t"
    "v_mfma_f32_16x16x32_bf16 a[60:63], a[188:191], a[148:151], 0\n\t"
    "s_waitcnt lgkmcnt(15)\n\t"
    "s_add_u32 s48, s48, 0x8000\n\t"
    "v_mov_b32 v153, v169\n\t"
    "v_mfma_f32_16x16x32_bf16 a[64:67], a[160:163], a[152:155], 0\n\t"
    "s_cmp_eq_u32 s48, 0x28000\n\t"
    "s_cselect_b32 s48, 0x10000, s48\n\t"
    "v_permlane16_swap_b32 v168, v152\n\t"
    "v_mfma_f32_16x16x32_bf16 a[68:71], a[164:167], a[152:155], 0\n\t"
    "buffer_load_dwordx4 v[236:239], v216, s[8:11], s81 offen offset:1024\n\t"
    "v_permlane16_swap_b32 v169, v153\n\t"
    "v_mfma_f32_16x16x32_bf16 a[72:75], a[168:171], a[152:155], 0\n\t"
    "v_add_f32 v168, v168, v152\n\t"
    "v_add_f32 v169, v169, v153\n\t"
    "v_mfma_f32_16x16x32_bf16 a[76:79], a[172:175], a[152:155], 0\n\t"
    "v_mov_b32 v152, v168\n\t"
    "v_mov_b32 v153, v169\n\t"
    "v_mfma_f32_16x16x32_bf16 a[80:83], a[176:179], a[152:155], 0\n\t"
    "v_permlane32_swap_b32 v168, v152\n\t"
    "v_mfma_f32_16x16x32_bf16 a[84:87], a[180:183], a[152:155], 0\n\t"
    "buffer_load_dwordx4 v[236:239], v216, s[88:91], s81 offen\n\t"
    "v_permlane32_swap_b32 v169, v153\n\t"
    "v_mfma_f32_16x16x32_bf16 a[88:91], a[184:187], a[152:155], 0\n\t"
    "v_add_f32 v168, v168, v152\n\t"
    "v_add_f32 v169, v169, v153\n\t"
    "v_mfma_f32_16x16x32_bf16 a[92:95], a[188:191], a[152:155], 0\n\t"
    "s_waitcnt lgkmcnt(15)\n\t"
    "v_mul_f32 v168, 0xbfb8aa3b, v168\n\t"
    "v_mul_f32 v169, 0xbfb8aa3b, v169\n\t"
    "v_mfma_f32_16x16x32_bf16 a[96:99], a[160:163], a[156:159], 0\n\t"
    "v_exp_f32 v168, v168\n\t"
    "v_mfma_f32_16x16x32_bf16 a[100:103], a[164:167], a[156:159], 0\n\t"
    "buffer_load_dwordx4 v[236:239], v216, s[88:91], s81 offen offset:1024\n\t"
    "v_exp_f32 v169, v169\n\t"
    "v_mfma_f32_16x16x32_bf16 a[104:107], a[168:171], a[156:159], 0\n\t"
    "v_add_f32 v168, 1.0, v168\n\t"
    "v_add_f32 v169, 1.0, v169\n\t"
    "v_mfma_f32_16x16x32_bf16 a[108:111], a[172:175], a[156:159], 0\n\t"
    "v_rcp_f32 v168, v168\n\t"
    "v_mfma_f32_16x16x32_bf16 a[112:115], a[176:179], a[156:159], 0\n\t"
    "v_rcp_f32 v169, v169\n\t"
    "v_mfma_f32_16x16x32_bf16 a[116:119], a[180:183], a[156:159], 0\n\t"
    "v_mov_b32 v142, 0\n\t"
    "v_mfma_f32_16x16x32_bf16 a[120:123], a[184:187], a[156:159], 0\n\t"
    "v_mfma_f32_16x16x32_bf16 a[124:127], a[188:191], a[156:159], 0\n\t"
    "s_waitcnt vmcnt(22) lgkmcnt(0)\n\t"
    "s_barrier\n\t"
    "v_mfma_f32_16x16x32_bf16 v[0:3], a[224:227], a[192:195], v[0:3]\n\t"
    "s_cmp_eq_u32 s49, 0x20000\n\t"
    "s_cselect_b32 s50, s51, s52\n\t"
    "v_xor_b32 v130, 0x8000, v130\n\t"
    "v_xor_b32 v131, 0x8000, v131\n\t"
    "v_mfma_f32_16x16x32_bf16 v[4:7], a[228:231], a[192:195], v[4:7]\n\t"
    "s_add_u32 s49, s49, s50\n\t"
    "s_add_u32 m0, s44, s47\n\t"
    "buffer_store_dword v168, v142, s[28:31], s96 offen sc1\n\t"
    "v_xor_b32 v132, 0x8000, v132\n\t"
    "v_xor_b32 v133, 0x8000, v133\n\t"
    "v_mfma_f32_16x16x32_bf16 v[8:11], a[232:235], a[192:195], v[8:11]\n\t"
    "s_add_u32 s39, s43, s46\n\t"
    "buffer_store_dword v169, v142, s[28:31], s96 offen offset:4 sc1\n\t"
    "v_xor_b32 v134, 0x8000, v134\n\t"
    "v_xor_b32 v135, 0x8000, v135\n\t"
    "v_mfma_f32_16x16x32_bf16 v[12:15], a[236:239], a[192:195], v[12:15]\n\t"
    "buffer_load_dwordx4 v140, s[16:19], s39 offen lds\n\t"
    "v_xor_b32 v136, 0x8000, v136\n\t"
    "v_xor_b32 v137, 0x8000, v137\n\t"
    "v_mfma_f32_16x16x32_bf16 v[16:19], a[240:243], a[192:195], v[16:19]\n\t"
    "v_add_u32 v128, s50, v128\n\t"
    "v_add_u32 v129, s50, v129\n\t"
    "v_mfma_f32_16x16x32_bf16 v[20:23], a[244:247], a[192:195], v[20:23]\n\t"
    "v_mfma_f32_16x16x32_bf16 v[24:27], a[248:251], a[192:195], v[24:27]\n\t"
    "ds_read_b128 a[128:131], v128 offset:0\n\t"
    "v_mfma_f32_16x16x32_bf16 v[28:31], a[252:255], a[192:195], v[28:31]\n\t"
    "s_add_u32 m0, m0, 0x400\n\t"
    "v_mfma_f32_16x16x32_bf16 v[32:35], a[224:227], a[196:199], v[32:35]\n\t"
    "s_add_u32 s39, s39, 0x8000\n\t"
    "ds_read_b64_tr_b16 a[160:161], v130 offset:0\n\t"
    "v_mfma_f32_16x16x32_bf16 v[36:39], a[228:231], a[196:199], v[36:39]\n\t"
    "buffer_load_dwordx4 v140, s[16:19], s39 offen lds\n\t"
    "v_mfma_f32_16x16x32_bf16 v[40:43], a[232:235], a[196:199], v[40:43]\n\t"
    "ds_read_b64_tr_b16 a[162:163], v130 offset:1024\n\t"
    "v_mfma_f32_16x16x32_bf16 v[44:47], a[236:239], a[196:199], v[44:47]\n\t"
    "v_mfma_f32_16x16x32_bf16 v[48:51], a[240:243], a[196:199], v[48:51]\n\t"
    "ds_read_b64_tr_b16 a[164:165], v131 offset:0\n\t"
    "v_mfma_f32_16x16x32_bf16 v[52:55], a[244:247], a[196:199], v[52:55]\n\t"
    "s_add_u32 m0, m0, 0x400\n\t"
    "v_mfma_f32_16x16x32_bf16 v[56:59], a[248:251], a[196:199], v[56:59]\n\t"
    "s_add_u32 s39, s39, 0x8000\n\t"
    "ds_read_b64_tr_b16 a[166:167], v131 offset:1024\n\t"
    "v_mfma_f32_16x16x32_bf16 v[60:63], a[252:255], a[196:199], v[60:63]\n\t"
    "buffer_load_dwordx4 v141, s[16:19], s39 offen lds\n\t"
    "v_mfma_f32_16x16x32_bf16 v[64:67], a[224:227], a[200:203], v[64:67]\n\t"
    "ds_read_b64_tr_b16 a[168:169], v132 offset:0\n\t"
    "v_mfma_f32_16x16x32_bf16 v[68:71], a[228:231], a[200:203], v[68:71]\n\t"
    "v_mfma_f32_16x16x32_bf16 v[72:75], a[232:235], a[200:203], v[72:75]\n\t"
    "ds_read_b64_tr_b16 a[170:171], v132 offset:1024\n\t"
    "v_mfma_f32_16x16x32_bf16 v[76:79], a[236:239], a[200:203], v[76:79]\n\t"
    "s_add_u32 m0, m0, 0x400\n\t"
    "v_mfma_f32_16x16x32_bf16 v[80:83], a[240:243], a[200:203], v[80:83]\n\t"
    "s_add_u32 s39, s39, 0x8000\n\t"
    "ds_read_b64_tr_b16 a[172:173], v133 offset:0\n\t"
    "v_mfma_f32_16x16x32_bf16 v[84:87], a[244:247], a[200:203], v[84:87]\n\t"
    "buffer_load_dwordx4 v141, s[16:19], s39 offen lds\n\t"
    "v_mfma_f32_16x16x32_bf16 v[88:91], a[248:251], a[200:203], v[88:91]\n\t"
    "ds_read_b64_tr_b16 a[174:175], v133 offset:1024\n\t"
    "v_mfma_f32_16x16x32_bf16 v[92:95], a[252:255], a[200:203], v[92:95]\n\t"
    "v_mfma_f32_16x16x32_bf16 v[96:99], a[224:227], a[204:207], v[96:99]\n\t"
    "ds_read_b64_tr_b16 a[176:177], v134 offset:0\n\t"
    "v_mfma_f32_16x16x32_bf16 v[100:103], a[228:231], a[204:207], v[100:103]\n\t"
    "s_add_u32 m0, m0, 0x400\n\t"
    "v_mfma_f32_16x16x32_bf16 v[104:107], a[232:235], a[204:207], v[104:107]\n\t"
    "s_add_u32 s39, s39, 0x8000\n\t"
    "ds_read_b64_tr_b16 a[178:179], v134 offset:1024\n\t"
    "v_mfma_f32_16x16x32_bf16 v[108:111], a[236:239], a[204:207], v[108:111]\n\t"
    "buffer_load_dwordx4 v140, s[16:19], s39 offen lds\n\t"
    "v_mfma_f32_16x16x32_bf16 v[112:115], a[240:243], a[204:207], v[112:115]\n\t"
    "ds_read_b64_tr_b16 a[180:181], v135 offset:0\n\t"
    "v_mfma_f32_16x16x32_bf16 v[116:119], a[244:247], a[204:207], v[116:119]\n\t"
    "v_mfma_f32_16x16x32_bf16 v[120:123], a[248:251], a[204:207], v[120:123]\n\t"
    "ds_read_b64_tr_b16 a[182:183], v135 offset:1024\n\t"
    "v_mfma_f32_16x16x32_bf16 v[124:127], a[252:255], a[204:207], v[124:127]\n\t"
    "s_add_u32 m0, m0, 0x400\n\t"
    "v_mfma_f32_16x16x32_bf16 a[0:3], a[224:227], a[208:211], a[0:3]\n\t"
    "s_add_u32 s39, s39, 0x8000\n\t"
    "ds_read_b64_tr_b16 a[184:185], v136 offset:0\n\t"
    "v_mfma_f32_16x16x32_bf16 a[4:7], a[228:231], a[208:211], a[4:7]\n\t"
    "buffer_load_dwordx4 v140, s[16:19], s39 offen lds\n\t"
    "v_mfma_f32_16x16x32_bf16 a[8:11], a[232:235], a[208:211], a[8:11]\n\t"
    "ds_read_b64_tr_b16 a[186:187], v136 offset:1024\n\t"
    "v_mfma_f32_16x16x32_bf16 a[12:15], a[236:239], a[208:211], a[12:15]\n\t"
    "v_mfma_f32_16x16x32_bf16 a[16:19], a[240:243], a[208:211], a[16:19]\n\t"
    "ds_read_b64_tr_b16 a[188:189], v137 offset:0\n\t"
    "v_mfma_f32_16x16x32_bf16 a[20:23], a[244:247], a[208:211], a[20:23]\n\t"
    "s_add_u32 m0, m0, 0x400\n\t"
    "v_mfma_f32_16x16x32_bf16 a[24:27], a[248:251], a[208:211], a[24:27]\n\t"
    "s_add_u32 s39, s39, 0x8000\n\t"
    "ds_read_b64_tr_b16 a[190:191], v137 offset:1024\n\t"
    "v_mfma_f32_16x16x32_bf16 a[28:31], a[252:255], a[208:211], a[28:31]\n\t"
    "buffer_load_dwordx4 v141, s[16:19], s39 offen lds\n\t"
    "v_mfma_f32_16x16x32_bf16 a[32:35], a[224:227], a[212:215], a[32:35]\n\t"
    "ds_read_b128 a[132:135], v128 offset:2048\n\t"
    "v_mfma_f32_16x16x32_bf16 a[36:39], a[228:231], a[212:215], a[36:39]\n\t"
    "v_mfma_f32_16x16x32_bf16 a[40:43], a[232:235], a[212:215], a[40:43]\n\t"
    "ds_read_b128 a[136:139], v128 offset:4096\n\t"
    "v_mfma_f32_16x16x32_bf16 a[44:47], a[236:239], a[212:215], a[44:47]\n\t"
    "s_add_u32 m0, m0, 0x400\n\t"
    "v_mfma_f32_16x16x32_bf16 a[48:51], a[240:243], a[212:215], a[48:51]\n\t"
    "s_add_u32 s39, s39, 0x8000\n\t"
    "ds_read_b128 a[140:143], v128 offset:6144\n\t"
    "v_mfma_f32_16x16x32_bf16 a[52:55], a[244:247], a[212:215], a[52:55]\n\t"
    "buffer_load_dwordx4 v141, s[16:19], s39 offen lds\n\t"
    "v_mfma_f32_16x16x32_bf16 a[56:59], a[248:251], a[212:215], a[56:59]\n\t"
    "s_add_u32 s46, s46, 0x80000\n\t"
    "ds_read_b128 a[144:147], v128 offset:8192\n\t"
    "v_mfma_f32_16x16x32_bf16 a[60:63], a[252:255], a[212:215], a[60:63]\n\t"
    "s_xor_b32 s47, s47, 0x8000\n\t"
    "v_mfma_f32_16x16x32_bf16 a[64:67], a[224:227], a[216:219], a[64:67]\n\t"
    "ds_read_b128 a[148:151], v128 offset:10240\n\t"
    "v_mfma_f32_16x16x32_bf16 a[68:71], a[228:231], a[216:219], a[68:71]\n\t"
    "v_mfma_f32_16x16x32_bf16 a[72:75], a[232:235], a[216:219], a[72:75]\n\t"
    "ds_read_b128 a[152:155], v128 offset:12288\n\t"
    "v_mfma_f32_16x16x32_bf16 a[76:79], a[236:239], a[216:219], a[76:79]\n\t"
    "v_mfma_f32_16x16x32_bf16 a[80:83], a[240:243], a[216:219], a[80:83]\n\t"
    "ds_read_b128 a[156:159], v128 offset:14336\n\t"
    "v_mfma_f32_16x16x32_bf16 a[84:87], a[244:247], a[216:219], a[84:87]\n\t"
    "v_mfma_f32_16x16x32_bf16 a[88:91], a[248:251], a[216:219], a[88:91]\n\t"
    "v_mfma_f32_16x16x32_bf16 a[92:95], a[252:255], a[216:219], a[92:95]\n\t"
    "v_mfma_f32_16x16x32_bf16 a[96:99], a[224:227], a[220:223], a[96:99]\n\t"
    "v_mfma_f32_16x16x32_bf16 a[100:103], a[228:231], a[220:223], a[100:103]\n\t"
    "v_mfma_f32_16x16x32_bf16 a[104:107], a[232:235], a[220:223], a[104:107]\n\t"
    "v_mfma_f32_16x16x32_bf16 a[108:111], a[236:239], a[220:223], a[108:111]\n\t"
    "v_mfma_f32_16x16x32_bf16 a[112:115], a[240:243], a[220:223], a[112:115]\n\t"
    "v_mfma_f32_16x16x32_bf16 a[116:119], a[244:247], a[220:223], a[116:119]\n\t"
    "v_mfma_f32_16x16x32_bf16 a[120:123], a[248:251], a[220:223], a[120:123]\n\t"
    "v_mfma_f32_16x16x32_bf16 a[124:127], a[252:255], a[220:223], a[124:127]\n\t"
    "L_kv_kloop:\n\t"
    "s_waitcnt lgkmcnt(15)\n\t"
    "v_mfma_f32_16x16x32_bf16 v[0:3], a[160:163], a[128:131], v[0:3]\n\t"
    "s_waitcnt lgkmcnt(15)\n\t"
    "ds_read_b128 a[192:195], v129 offset:0\n\t"
    "v_mfma_f32_16x16x32_bf16 v[4:7], a[164:167], a[128:131], v[4:7]\n\t"
    "s_waitcnt lgkmcnt(15)\n\t"
    "s_add_u32 m0, s44, s48\n\t"
    "v_mfma_f32_16x16x32_bf16 v[8:11], a[168:171], a[128:131], v[8:11]\n\t"
    "s_waitcnt lgkmcnt(15)\n\t"
    "s_add_u32 s39, s42, s45\n\t"
    "ds_read_b64_tr_b16 a[224:225], v130 offset:8192\n\t"
    "v_mfma_f32_16x16x32_bf16 v[12:15], a[172:175], a[128:131], v[12:15]\n\t"
    "s_waitcnt lgkmcnt(15)\n\t"
    "buffer_load_dwordx4 v138, s[12:15], s39 offen lds\n\t"
    "v_mfma_f32_16x16x32_bf16 v[16:19], a[176:179], a[128:131], v[16:19]\n\t"
    "s_waitcnt lgkmcnt(13)\n\t"
    "ds_read_b64_tr_b16 a[226:227], v130 offset:9216\n\t"
    "v_mfma_f32_16x16x32_bf16 v[20:23], a[180:183], a[128:131], v[20:23]\n\t"
    "s_waitcnt lgkmcnt(12)\n\t"
    "v_mfma_f32_16x16x32_bf16 v[24:27], a[184:187], a[128:131], v[24:27]\n\t"
    "s_waitcnt lgkmcnt(10)\n\t"
    "ds_read_b64_tr_b16 a[228:229], v131 offset:8192\n\t"
    "v_mfma_f32_16x16x32_bf16 v[28:31], a[188:191], a[128:131], v[28:31]\n\t"
    "s_waitcnt lgkmcnt(10)\n\t"
    "s_add_u32 m0, m0, 0x400\n\t"
    "v_mfma_f32_16x16x32_bf16 v[32:35], a[160:163], a[132:135], v[32:35]\n\t"
    "s_add_u32 s39, s39, 0x2000\n\t"
    "ds_read_b64_tr_b16 a[230:231], v131 offset:9216\n\t"
    "v_mfma_f32_16x16x32_bf16 v[36:39], a[164:167], a[132:135], v[36:39]\n\t"
    "buffer_load_dwordx4 v139, s[12:15], s39 offen lds\n\t"
    "v_mfma_f32_16x16x32_bf16 v[40:43], a[168:171], a[132:135], v[40:43]\n\t"
    "ds_read_b64_tr_b16 a[232:233], v132 offset:8192\n\t"
    "v_mfma_f32_16x16x32_bf16 v[44:47], a[172:175], a[132:135], v[44:47]\n\t"
    "v_mfma_f32_16x16x32_bf16 v[48:51], a[176:179], a[132:135], v[48:51]\n\t"
    "ds_read_b64_tr_b16 a[234:235], v132 offset:9216\n\t"
    "v_mfma_f32_16x16x32_bf16 v[52:55], a[180:183], a[132:135], v[52:55]\n\t"
    "s_add_u32 m0, m0, 0x400\n\t"
    "v_mfma_f32_16x16x32_bf16 v[56:59], a[184:187], a[132:135], v[56:59]\n\t"
    "s_add_u32 s39, s39, 0x2000\n\t"
    "ds_read_b64_tr_b16 a[236:237], v133 offset:8192\n\t"
    "v_mfma_f32_16x16x32_bf16 v[60:63], a[188:191], a[132:135], v[60:63]\n\t"
    "s_waitcnt lgkmcnt(13)\n\t"
    "buffer_load_dwordx4 v138, s[12:15], s39 offen lds\n\t"
    "v_mfma_f32_16x16x32_bf16 v[64:67], a[160:163], a[136:139], v[64:67]\n\t"
    "ds_read_b64_tr_b16 a[238:239], v133 offset:9216\n\t"
    "v_mfma_f32_16x16x32_bf16 v[68:71], a[164:167], a[136:139], v[68:71]\n\t"
    "v_mfma_f32_16x16x32_bf16 v[72:75], a[168:171], a[136:139], v[72:75]\n\t"
    "ds_read_b64_tr_b16 a[240:241], v134 offset:8192\n\t"
    "v_mfma_f32_16x16x32_bf16 v[76:79], a[172:175], a[136:139], v[76:79]\n\t"
    "s_add_u32 m0, m0, 0x400\n\t"
    "v_mfma_f32_16x16x32_bf16 v[80:83], a[176:179], a[136:139], v[80:83]\n\t"
    "s_add_u32 s39, s39, 0x2000\n\t"
    "ds_read_b64_tr_b16 a[242:243], v134 offset:9216\n\t"
    "v_mfma_f32_16x16x32_bf16 v[84:87], a[180:183], a[136:139], v[84:87]\n\t"
    "buffer_load_dwordx4 v139, s[12:15], s39 offen lds\n\t"
    "v_mfma_f32_16x16x32_bf16 v[88:91], a[184:187], a[136:139], v[88:91]\n\t"
    "ds_read_b64_tr_b16 a[244:245], v135 offset:8192\n\t"
    "v_mfma_f32_16x16x32_bf16 v[92:95], a[188:191], a[136:139], v[92:95]\n\t"
    "s_waitcnt lgkmcnt(15)\n\t"
    "v_mfma_f32_16x16x32_bf16 v[96:99], a[160:163], a[140:143], v[96:99]\n\t"
    "ds_read_b64_tr_b16 a[246:247], v135 offset:9216\n\t"
    "v_mfma_f32_16x16x32_bf16 v[100:103], a[164:167], a[140:143], v[100:103]\n\t"
    "s_add_u32 m0, m0, 0x400\n\t"
    "v_mfma_f32_16x16x32_bf16 v[104:107], a[168:171], a[140:143], v[104:107]\n\t"
    "s_add_u32 s39, s39, 0x2000\n\t"
    "ds_read_b64_tr_b16 a[248:249], v136 offset:8192\n\t"
    "v_mfma_f32_16x16x32_bf16 v[108:111], a[172:175], a[140:143], v[108:111]\n\t"
    "buffer_load_dwordx4 v138, s[12:15], s39 offen lds\n\t"
    "v_mfma_f32_16x16x32_bf16 v[112:115], a[176:179], a[140:143], v[112:115]\n\t"
    "ds_read_b64_tr_b16 a[250:251], v136 offset:9216\n\t"
    "v_mfma_f32_16x16x32_bf16 v[116:119], a[180:183], a[140:143], v[116:119]\n\t"
    "v_mfma_f32_16x16x32_bf16 v[120:123], a[184:187], a[140:143], v[120:123]\n\t"
    "ds_read_b64_tr_b16 a[252:253], v137 offset:8192\n\t"
    "v_mfma_f32_16x16x32_bf16 v[124:127], a[188:191], a[140:143], v[124:127]\n\t"
    "s_waitcnt lgkmcnt(15)\n\t"
    "s_add_u32 m0, m0, 0x400\n\t"
    "v_mfma_f32_16x16x32_bf16 a[0:3], a[160:163], a[144:147], a[0:3]\n\t"
    "s_add_u32 s39, s39, 0x2000\n\t"
    "ds_read_b64_tr_b16 a[254:255], v137 offset:9216\n\t"
    "v_mfma_f32_16x16x32_bf16 a[4:7], a[164:167], a[144:147], a[4:7]\n\t"
    "buffer_load_dwordx4 v139, s[12:15], s39 offen lds\n\t"
    "v_mfma_f32_16x16x32_bf16 a[8:11], a[168:171], a[144:147], a[8:11]\n\t"
    "ds_read_b128 a[196:199], v129 offset:2048\n\t"
    "v_mfma_f32_16x16x32_bf16 a[12:15], a[172:175], a[144:147], a[12:15]\n\t"
    "v_mfma_f32_16x16x32_bf16 a[16:19], a[176:179], a[144:147], a[16:19]\n\t"
    "ds_read_b128 a[200:203], v129 offset:4096\n\t"
    "v_mfma_f32_16x16x32_bf16 a[20:23], a[180:183], a[144:147], a[20:23]\n\t"
    "s_add_u32 m0, m0, 0x400\n\t"
    "v_mfma_f32_16x16x32_bf16 a[24:27], a[184:187], a[144:147], a[24:27]\n\t"
    "s_add_u32 s39, s39, 0x2000\n\t"
    "ds_read_b128 a[204:207], v129 offset:6144\n\t"
    "v_mfma_f32_16x16x32_bf16 a[28:31], a[188:191], a[144:147], a[28:31]\n\t"
    "s_waitcnt lgkmcnt(15)\n\t"
    "buffer_load_dwordx4 v138, s[12:15], s39 offen lds\n\t"
    "v_mfma_f32_16x16x32_bf16 a[32:35], a[160:163], a[148:151], a[32:35]\n\t"
    "ds_read_b128 a[208:211], v129 offset:8192\n\t"
    "v_mfma_f32_16x16x32_bf16 a[36:39], a[164:167], a[148:151], a[36:39]\n\t"
    "v_mfma_f32_16x16x32_bf16 a[40:43], a[168:171], a[148:151], a[40:43]\n\t"
    "ds_read_b128 a[212:215], v129 offset:10240\n\t"
    "v_mfma_f32_16x16x32_bf16 a[44:47], a[172:175], a[148:151], a[44:47]\n\t"
    "s_add_u32 m0, m0, 0x400\n\t"
    "v_mfma_f32_16x16x32_bf16 a[48:51], a[176:179], a[148:151], a[48:51]\n\t"
    "s_add_u32 s39, s39, 0x2000\n\t"
    "ds_read_b128 a[216:219], v129 offset:12288\n\t"
    "v_mfma_f32_16x16x32_bf16 a[52:55], a[180:183], a[148:151], a[52:55]\n\t"
    "buffer_load_dwordx4 v139, s[12:15], s39 offen lds\n\t"
    "v_mfma_f32_16x16x32_bf16 a[56:59], a[184:187], a[148:151], a[56:59]\n\t"
    "s_add_u32 s45, s45, 0x80\n\t"
    "ds_read_b128 a[220:223], v129 offset:14336\n\t"
    "v_mfma_f32_16x16x32_bf16 a[60:63], a[188:191], a[148:151], a[60:63]\n\t"
    "s_waitcnt lgkmcnt(15)\n\t"
    "s_add_u32 s48, s48, 0x8000\n\t"
    "buffer_load_dwordx4 v[236:239], v216, s[84:87], s82 offen\n\t"
    "v_mfma_f32_16x16x32_bf16 a[64:67], a[160:163], a[152:155], a[64:67]\n\t"
    "s_cmp_eq_u32 s48, 0x28000\n\t"
    "s_cselect_b32 s48, 0x10000, s48\n\t"
    "v_mfma_f32_16x16x32_bf16 a[68:71], a[164:167], a[152:155], a[68:71]\n\t"
    "v_mfma_f32_16x16x32_bf16 a[72:75], a[168:171], a[152:155], a[72:75]\n\t"
    "s_add_u32 s82, s82, 0x2000\n\t"
    "v_mfma_f32_16x16x32_bf16 a[76:79], a[172:175], a[152:155], a[76:79]\n\t"
    "v_mfma_f32_16x16x32_bf16 a[80:83], a[176:179], a[152:155], a[80:83]\n\t"
    "v_mfma_f32_16x16x32_bf16 a[84:87], a[180:183], a[152:155], a[84:87]\n\t"
    "v_mfma_f32_16x16x32_bf16 a[88:91], a[184:187], a[152:155], a[88:91]\n\t"
    "v_mfma_f32_16x16x32_bf16 a[92:95], a[188:191], a[152:155], a[92:95]\n\t"
    "s_waitcnt lgkmcnt(15)\n\t"
    "v_mfma_f32_16x16x32_bf16 a[96:99], a[160:163], a[156:159], a[96:99]\n\t"
    "v_mfma_f32_16x16x32_bf16 a[100:103], a[164:167], a[156:159], a[100:103]\n\t"
    "v_mfma_f32_16x16x32_bf16 a[104:107], a[168:171], a[156:159], a[104:107]\n\t"
    "v_mfma_f32_16x16x32_bf16 a[108:111], a[172:175], a[156:159], a[108:111]\n\t"
    "v_mfma_f32_16x16x32_bf16 a[112:115], a[176:179], a[156:159], a[112:115]\n\t"
    "v_mfma_f32_16x16x32_bf16 a[116:119], a[180:183], a[156:159], a[116:119]\n\t"
    "v_mfma_f32_16x16x32_bf16 a[120:123], a[184:187], a[156:159], a[120:123]\n\t"
    "v_mfma_f32_16x16x32_bf16 a[124:127], a[188:191], a[156:159], a[124:127]\n\t"
    "s_waitcnt vmcnt(9) lgkmcnt(0)\n\t"
    "s_barrier\n\t"
    "v_mfma_f32_16x16x32_bf16 v[0:3], a[224:227], a[192:195], v[0:3]\n\t"
    "s_cmp_eq_u32 s49, 0x20000\n\t"
    "s_cselect_b32 s50, s51, s52\n\t"
    "v_xor_b32 v130, 0x8000, v130\n\t"
    "v_xor_b32 v131, 0x8000, v131\n\t"
    "v_mfma_f32_16x16x32_bf16 v[4:7], a[228:231], a[192:195], v[4:7]\n\t"
    "s_add_u32 s49, s49, s50\n\t"
    "s_add_u32 m0, s44, s47\n\t"
    "v_xor_b32 v132, 0x8000, v132\n\t"
    "v_xor_b32 v133, 0x8000, v133\n\t"
    "v_mfma_f32_16x16x32_bf16 v[8:11], a[232:235], a[192:195], v[8:11]\n\t"
    "s_add_u32 s39, s43, s46\n\t"
    "v_xor_b32 v134, 0x8000, v134\n\t"
    "v_xor_b32 v135, 0x8000, v135\n\t"
    "v_mfma_f32_16x16x32_bf16 v[12:15], a[236:239], a[192:195], v[12:15]\n\t"
    "buffer_load_dwordx4 v140, s[16:19], s39 offen lds\n\t"
    "v_xor_b32 v136, 0x8000, v136\n\t"
    "v_xor_b32 v137, 0x8000, v137\n\t"
    "v_mfma_f32_16x16x32_bf16 v[16:19], a[240:243], a[192:195], v[16:19]\n\t"
    "v_add_u32 v128, s50, v128\n\t"
    "v_add_u32 v129, s50, v129\n\t"
    "v_mfma_f32_16x16x32_bf16 v[20:23], a[244:247], a[192:195], v[20:23]\n\t"
    "v_mfma_f32_16x16x32_bf16 v[24:27], a[248:251], a[192:195], v[24:27]\n\t"
    "ds_read_b128 a[128:131], v128 offset:0\n\t"
    "v_mfma_f32_16x16x32_bf16 v[28:31], a[252:255], a[192:195], v[28:31]\n\t"
    "s_add_u32 m0, m0, 0x400\n\t"
    "v_mfma_f32_16x16x32_bf16 v[32:35], a[224:227], a[196:199], v[32:35]\n\t"
    "s_add_u32 s39, s39, 0x8000\n\t"
    "ds_read_b64_tr_b16 a[160:161], v130 offset:0\n\t"
    "v_mfma_f32_16x16x32_bf16 v[36:39], a[228:231], a[196:199], v[36:39]\n\t"
    "buffer_load_dwordx4 v140, s[16:19], s39 offen lds\n\t"
    "v_mfma_f32_16x16x32_bf16 v[40:43], a[232:235], a[196:199], v[40:43]\n\t"
    "ds_read_b64_tr_b16 a[162:163], v130 offset:1024\n\t"
    "v_mfma_f32_16x16x32_bf16 v[44:47], a[236:239], a[196:199], v[44:47]\n\t"
    "v_mfma_f32_16x16x32_bf16 v[48:51], a[240:243], a[196:199], v[48:51]\n\t"
    "ds_read_b64_tr_b16 a[164:165], v131 offset:0\n\t"
    "v_mfma_f32_16x16x32_bf16 v[52:55], a[244:247], a[196:199], v[52:55]\n\t"
    "s_add_u32 m0, m0, 0x400\n\t"
    "v_mfma_f32_16x16x32_bf16 v[56:59], a[248:251], a[196:199], v[56:59]\n\t"
    "s_add_u32 s39, s39, 0x8000\n\t"
    "ds_read_b64_tr_b16 a[166:167], v131 offset:1024\n\t"
    "v_mfma_f32_16x16x32_bf16 v[60:63], a[252:255], a[196:199], v[60:63]\n\t"
    "buffer_load_dwordx4 v141, s[16:19], s39 offen lds\n\t"
    "v_mfma_f32_16x16x32_bf16 v[64:67], a[224:227], a[200:203], v[64:67]\n\t"
    "ds_read_b64_tr_b16 a[168:169], v132 offset:0\n\t"
    "v_mfma_f32_16x16x32_bf16 v[68:71], a[228:231], a[200:203], v[68:71]\n\t"
    "v_mfma_f32_16x16x32_bf16 v[72:75], a[232:235], a[200:203], v[72:75]\n\t"
    "ds_read_b64_tr_b16 a[170:171], v132 offset:1024\n\t"
    "v_mfma_f32_16x16x32_bf16 v[76:79], a[236:239], a[200:203], v[76:79]\n\t"
    "s_add_u32 m0, m0, 0x400\n\t"
    "v_mfma_f32_16x16x32_bf16 v[80:83], a[240:243], a[200:203], v[80:83]\n\t"
    "s_add_u32 s39, s39, 0x8000\n\t"
    "ds_read_b64_tr_b16 a[172:173], v133 offset:0\n\t"
    "v_mfma_f32_16x16x32_bf16 v[84:87], a[244:247], a[200:203], v[84:87]\n\t"
    "buffer_load_dwordx4 v141, s[16:19], s39 offen lds\n\t"
    "v_mfma_f32_16x16x32_bf16 v[88:91], a[248:251], a[200:203], v[88:91]\n\t"
    "ds_read_b64_tr_b16 a[174:175], v133 offset:1024\n\t"
    "v_mfma_f32_16x16x32_bf16 v[92:95], a[252:255], a[200:203], v[92:95]\n\t"
    "v_mfma_f32_16x16x32_bf16 v[96:99], a[224:227], a[204:207], v[96:99]\n\t"
    "ds_read_b64_tr_b16 a[176:177], v134 offset:0\n\t"
    "v_mfma_f32_16x16x32_bf16 v[100:103], a[228:231], a[204:207], v[100:103]\n\t"
    "s_add_u32 m0, m0, 0x400\n\t"
    "v_mfma_f32_16x16x32_bf16 v[104:107], a[232:235], a[204:207], v[104:107]\n\t"
    "s_add_u32 s39, s39, 0x8000\n\t"
    "ds_read_b64_tr_b16 a[178:179], v134 offset:1024\n\t"
    "v_mfma_f32_16x16x32_bf16 v[108:111], a[236:239], a[204:207], v[108:111]\n\t"
    "buffer_load_dwordx4 v140, s[16:19], s39 offen lds\n\t"
    "v_mfma_f32_16x16x32_bf16 v[112:115], a[240:243], a[204:207], v[112:115]\n\t"
    "ds_read_b64_tr_b16 a[180:181], v135 offset:0\n\t"
    "v_mfma_f32_16x16x32_bf16 v[116:119], a[244:247], a[204:207], v[116:119]\n\t"
    "v_mfma_f32_16x16x32_bf16 v[120:123], a[248:251], a[204:207], v[120:123]\n\t"
    "ds_read_b64_tr_b16 a[182:183], v135 offset:1024\n\t"
    "v_mfma_f32_16x16x32_bf16 v[124:127], a[252:255], a[204:207], v[124:127]\n\t"
    "s_add_u32 m0, m0, 0x400\n\t"
    "v_mfma_f32_16x16x32_bf16 a[0:3], a[224:227], a[208:211], a[0:3]\n\t"
    "s_add_u32 s39, s39, 0x8000\n\t"
    "ds_read_b64_tr_b16 a[184:185], v136 offset:0\n\t"
    "v_mfma_f32_16x16x32_bf16 a[4:7], a[228:231], a[208:211], a[4:7]\n\t"
    "buffer_load_dwordx4 v140, s[16:19], s39 offen lds\n\t"
    "v_mfma_f32_16x16x32_bf16 a[8:11], a[232:235], a[208:211], a[8:11]\n\t"
    "ds_read_b64_tr_b16 a[186:187], v136 offset:1024\n\t"
    "v_mfma_f32_16x16x32_bf16 a[12:15], a[236:239], a[208:211], a[12:15]\n\t"
    "v_mfma_f32_16x16x32_bf16 a[16:19], a[240:243], a[208:211], a[16:19]\n\t"
    "ds_read_b64_tr_b16 a[188:189], v137 offset:0\n\t"
    "v_mfma_f32_16x16x32_bf16 a[20:23], a[244:247], a[208:211], a[20:23]\n\t"
    "s_add_u32 m0, m0, 0x400\n\t"
    "v_mfma_f32_16x16x32_bf16 a[24:27], a[248:251], a[208:211], a[24:27]\n\t"
    "s_add_u32 s39, s39, 0x8000\n\t"
    "ds_read_b64_tr_b16 a[190:191], v137 offset:1024\n\t"
    "v_mfma_f32_16x16x32_bf16 a[28:31], a[252:255], a[208:211], a[28:31]\n\t"
    "buffer_load_dwordx4 v141, s[16:19], s39 offen lds\n\t"
    "v_mfma_f32_16x16x32_bf16 a[32:35], a[224:227], a[212:215], a[32:35]\n\t"
    "ds_read_b128 a[132:135], v128 offset:2048\n\t"
    "v_mfma_f32_16x16x32_bf16 a[36:39], a[228:231], a[212:215], a[36:39]\n\t"
    "v_mfma_f32_16x16x32_bf16 a[40:43], a[232:235], a[212:215], a[40:43]\n\t"
    "ds_read_b128 a[136:139], v128 offset:4096\n\t"
    "v_mfma_f32_16x16x32_bf16 a[44:47], a[236:239], a[212:215], a[44:47]\n\t"
    "s_add_u32 m0, m0, 0x400\n\t"
    "v_mfma_f32_16x16x32_bf16 a[48:51], a[240:243], a[212:215], a[48:51]\n\t"
    "s_add_u32 s39, s39, 0x8000\n\t"
    "ds_read_b128 a[140:143], v128 offset:6144\n\t"
    "v_mfma_f32_16x16x32_bf16 a[52:55], a[244:247], a[212:215], a[52:55]\n\t"
    "buffer_load_dwordx4 v141, s[16:19], s39 offen lds\n\t"
    "v_mfma_f32_16x16x32_bf16 a[56:59], a[248:251], a[212:215], a[56:59]\n\t"
    "s_add_u32 s46, s46, 0x80000\n\t"
    "ds_read_b128 a[144:147], v128 offset:8192\n\t"
    "v_mfma_f32_16x16x32_bf16 a[60:63], a[252:255], a[212:215], a[60:63]\n\t"
    "s_xor_b32 s47, s47, 0x8000\n\t"
    "v_mfma_f32_16x16x32_bf16 a[64:67], a[224:227], a[216:219], a[64:67]\n\t"
    "ds_read_b128 a[148:151], v128 offset:10240\n\t"
    "v_mfma_f32_16x16x32_bf16 a[68:71], a[228:231], a[216:219], a[68:71]\n\t"
    "v_mfma_f32_16x16x32_bf16 a[72:75], a[232:235], a[216:219], a[72:75]\n\t"
    "ds_read_b128 a[152:155], v128 offset:12288\n\t"
    "v_mfma_f32_16x16x32_bf16 a[76:79], a[236:239], a[216:219], a[76:79]\n\t"
    "v_mfma_f32_16x16x32_bf16 a[80:83], a[240:243], a[216:219], a[80:83]\n\t"
    "ds_read_b128 a[156:159], v128 offset:14336\n\t"
    "v_mfma_f32_16x16x32_bf16 a[84:87], a[244:247], a[216:219], a[84:87]\n\t"
    "v_mfma_f32_16x16x32_bf16 a[88:91], a[248:251], a[216:219], a[88:91]\n\t"
    "v_mfma_f32_16x16x32_bf16 a[92:95], a[252:255], a[216:219], a[92:95]\n\t"
    "v_mfma_f32_16x16x32_bf16 a[96:99], a[224:227], a[220:223], a[96:99]\n\t"
    "v_mfma_f32_16x16x32_bf16 a[100:103], a[228:231], a[220:223], a[100:103]\n\t"
    "v_mfma_f32_16x16x32_bf16 a[104:107], a[232:235], a[220:223], a[104:107]\n\t"
    "v_mfma_f32_16x16x32_bf16 a[108:111], a[236:239], a[220:223], a[108:111]\n\t"
    "v_mfma_f32_16x16x32_bf16 a[112:115], a[240:243], a[220:223], a[112:115]\n\t"
    "v_mfma_f32_16x16x32_bf16 a[116:119], a[244:247], a[220:223], a[116:119]\n\t"
    "v_mfma_f32_16x16x32_bf16 a[120:123], a[248:251], a[220:223], a[120:123]\n\t"
    "v_mfma_f32_16x16x32_bf16 a[124:127], a[252:255], a[220:223], a[124:127]\n\t"
    "s_sub_u32 s40, s40, 1\n\t"
    "s_cmp_lg_u32 s40, 0\n\t"
    "s_cbranch_scc1 L_kv_kloop\n\t"
    "s_waitcnt lgkmcnt(15)\n\t"
    "v_mfma_f32_16x16x32_bf16 v[0:3], a[160:163], a[128:131], v[0:3]\n\t"
    "s_waitcnt lgkmcnt(15)\n\t"
    "ds_read_b128 a[192:195], v129 offset:0\n\t"
    "v_mfma_f32_16x16x32_bf16 v[4:7], a[164:167], a[128:131], v[4:7]\n\t"
    "s_waitcnt lgkmcnt(15)\n\t"
    "s_add_u32 m0, s41, 0x20000\n\t"
    "v_mfma_f32_16x16x32_bf16 v[8:11], a[168:171], a[128:131], v[8:11]\n\t"
    "s_waitcnt lgkmcnt(15)\n\t"
    "s_add_u32 s39, s54, 0x0\n\t"
    "ds_read_b64_tr_b16 a[224:225], v130 offset:8192\n\t"
    "v_mfma_f32_16x16x32_bf16 v[12:15], a[172:175], a[128:131], v[12:15]\n\t"
    "s_waitcnt lgkmcnt(15)\n\t"
    "buffer_load_dwordx4 v209, s[8:11], s39 offen lds\n\t"
    "v_mfma_f32_16x16x32_bf16 v[16:19], a[176:179], a[128:131], v[16:19]\n\t"
    "s_waitcnt lgkmcnt(13)\n\t"
    "ds_read_b64_tr_b16 a[226:227], v130 offset:9216\n\t"
    "v_mfma_f32_16x16x32_bf16 v[20:23], a[180:183], a[128:131], v[20:23]\n\t"
    "s_waitcnt lgkmcnt(12)\n\t"
    "v_mfma_f32_16x16x32_bf16 v[24:27], a[184:187], a[128:131], v[24:27]\n\t"
    "s_waitcnt lgkmcnt(10)\n\t"
    "ds_read_b64_tr_b16 a[228:229], v131 offset:8192\n\t"
    "v_mfma_f32_16x16x32_bf16 v[28:31], a[188:191], a[128:131], v[28:31]\n\t"
    "s_waitcnt lgkmcnt(10)\n\t"
    "s_add_u32 m0, m0, 0x400\n\t"
    "v_mfma_f32_16x16x32_bf16 v[32:35], a[160:163], a[132:135], v[32:35]\n\t"
    "s_add_u32 s39, s39, 0x2000\n\t"
    "ds_read_b64_tr_b16 a[230:231], v131 offset:9216\n\t"
    "v_mfma_f32_16x16x32_bf16 v[36:39], a[164:167], a[132:135], v[36:39]\n\t"
    "buffer_load_dwordx4 v210, s[8:11], s39 offen lds\n\t"
    "v_mfma_f32_16x16x32_bf16 v[40:43], a[168:171], a[132:135], v[40:43]\n\t"
    "ds_read_b64_tr_b16 a[232:233], v132 offset:8192\n\t"
    "v_mfma_f32_16x16x32_bf16 v[44:47], a[172:175], a[132:135], v[44:47]\n\t"
    "v_mfma_f32_16x16x32_bf16 v[48:51], a[176:179], a[132:135], v[48:51]\n\t"
    "ds_read_b64_tr_b16 a[234:235], v132 offset:9216\n\t"
    "v_mfma_f32_16x16x32_bf16 v[52:55], a[180:183], a[132:135], v[52:55]\n\t"
    "s_add_u32 m0, m0, 0x400\n\t"
    "v_mfma_f32_16x16x32_bf16 v[56:59], a[184:187], a[132:135], v[56:59]\n\t"
    "s_add_u32 s39, s39, 0x2000\n\t"
    "ds_read_b64_tr_b16 a[236:237], v133 offset:8192\n\t"
    "v_mfma_f32_16x16x32_bf16 v[60:63], a[188:191], a[132:135], v[60:63]\n\t"
    "s_waitcnt lgkmcnt(13)\n\t"
    "buffer_load_dwordx4 v209, s[8:11], s39 offen lds\n\t"
    "v_mfma_f32_16x16x32_bf16 v[64:67], a[160:163], a[136:139], v[64:67]\n\t"
    "ds_read_b64_tr_b16 a[238:239], v133 offset:9216\n\t"
    "v_mfma_f32_16x16x32_bf16 v[68:71], a[164:167], a[136:139], v[68:71]\n\t"
    "v_mfma_f32_16x16x32_bf16 v[72:75], a[168:171], a[136:139], v[72:75]\n\t"
    "ds_read_b64_tr_b16 a[240:241], v134 offset:8192\n\t"
    "v_mfma_f32_16x16x32_bf16 v[76:79], a[172:175], a[136:139], v[76:79]\n\t"
    "s_add_u32 m0, m0, 0x400\n\t"
    "v_mfma_f32_16x16x32_bf16 v[80:83], a[176:179], a[136:139], v[80:83]\n\t"
    "s_add_u32 s39, s39, 0x2000\n\t"
    "ds_read_b64_tr_b16 a[242:243], v134 offset:9216\n\t"
    "v_mfma_f32_16x16x32_bf16 v[84:87], a[180:183], a[136:139], v[84:87]\n\t"
    "buffer_load_dwordx4 v210, s[8:11], s39 offen lds\n\t"
    "v_mfma_f32_16x16x32_bf16 v[88:91], a[184:187], a[136:139], v[88:91]\n\t"
    "ds_read_b64_tr_b16 a[244:245], v135 offset:8192\n\t"
    "v_mfma_f32_16x16x32_bf16 v[92:95], a[188:191], a[136:139], v[92:95]\n\t"
    "s_waitcnt lgkmcnt(15)\n\t"
    "v_mfma_f32_16x16x32_bf16 v[96:99], a[160:163], a[140:143], v[96:99]\n\t"
    "ds_read_b64_tr_b16 a[246:247], v135 offset:9216\n\t"
    "v_mfma_f32_16x16x32_bf16 v[100:103], a[164:167], a[140:143], v[100:103]\n\t"
    "s_add_u32 m0, s41, 0x24000\n\t"
    "v_mfma_f32_16x16x32_bf16 v[104:107], a[168:171], a[140:143], v[104:107]\n\t"
    "s_add_u32 s39, s55, 0x0\n\t"
    "ds_read_b64_tr_b16 a[248:249], v136 offset:8192\n\t"
    "v_mfma_f32_16x16x32_bf16 v[108:111], a[172:175], a[140:143], v[108:111]\n\t"
    "buffer_load_dwordx4 v209, s[8:11], s39 offen lds\n\t"
    "v_mfma_f32_16x16x32_bf16 v[112:115], a[176:179], a[140:143], v[112:115]\n\t"
    "ds_read_b64_tr_b16 a[250:251], v136 offset:9216\n\t"
    "v_mfma_f32_16x16x32_bf16 v[116:119], a[180:183], a[140:143], v[116:119]\n\t"
    "v_mfma_f32_16x16x32_bf16 v[120:123], a[184:187], a[140:143], v[120:123]\n\t"
    "ds_read_b64_tr_b16 a[252:253], v137 offset:8192\n\t"
    "v_mfma_f32_16x16x32_bf16 v[124:127], a[188:191], a[140:143], v[124:127]\n\t"
    "s_waitcnt lgkmcnt(15)\n\t"
    "s_add_u32 m0, m0, 0x400\n\t"
    "v_mfma_f32_16x16x32_bf16 a[0:3], a[160:163], a[144:147], a[0:3]\n\t"
    "s_add_u32 s39, s39, 0x2000\n\t"
    "ds_read_b64_tr_b16 a[254:255], v137 offset:9216\n\t"
    "v_mfma_f32_16x16x32_bf16 a[4:7], a[164:167], a[144:147], a[4:7]\n\t"
    "buffer_load_dwordx4 v210, s[8:11], s39 offen lds\n\t"
    "v_mfma_f32_16x16x32_bf16 a[8:11], a[168:171], a[144:147], a[8:11]\n\t"
    "ds_read_b128 a[196:199], v129 offset:2048\n\t"
    "v_mfma_f32_16x16x32_bf16 a[12:15], a[172:175], a[144:147], a[12:15]\n\t"
    "v_mfma_f32_16x16x32_bf16 a[16:19], a[176:179], a[144:147], a[16:19]\n\t"
    "ds_read_b128 a[200:203], v129 offset:4096\n\t"
    "v_mfma_f32_16x16x32_bf16 a[20:23], a[180:183], a[144:147], a[20:23]\n\t"
    "s_add_u32 m0, m0, 0x400\n\t"
    "v_mfma_f32_16x16x32_bf16 a[24:27], a[184:187], a[144:147], a[24:27]\n\t"
    "s_add_u32 s39, s39, 0x2000\n\t"
    "ds_read_b128 a[204:207], v129 offset:6144\n\t"
    "v_mfma_f32_16x16x32_bf16 a[28:31], a[188:191], a[144:147], a[28:31]\n\t"
    "s_waitcnt lgkmcnt(15)\n\t"
    "buffer_load_dwordx4 v209, s[8:11], s39 offen lds\n\t"
    "v_mfma_f32_16x16x32_bf16 a[32:35], a[160:163], a[148:151], a[32:35]\n\t"
    "ds_read_b128 a[208:211], v129 offset:8192\n\t"
    "v_mfma_f32_16x16x32_bf16 a[36:39], a[164:167], a[148:151], a[36:39]\n\t"
    "v_mfma_f32_16x16x32_bf16 a[40:43], a[168:171], a[148:151], a[40:43]\n\t"
    "ds_read_b128 a[212:215], v129 offset:10240\n\t"
    "v_mfma_f32_16x16x32_bf16 a[44:47], a[172:175], a[148:151], a[44:47]\n\t"
    "s_add_u32 m0, m0, 0x400\n\t"
    "v_mfma_f32_16x16x32_bf16 a[48:51], a[176:179], a[148:151], a[48:51]\n\t"
    "s_add_u32 s39, s39, 0x2000\n\t"
    "ds_read_b128 a[216:219], v129 offset:12288\n\t"
    "v_mfma_f32_16x16x32_bf16 a[52:55], a[180:183], a[148:151], a[52:55]\n\t"
    "buffer_load_dwordx4 v210, s[8:11], s39 offen lds\n\t"
    "v_mfma_f32_16x16x32_bf16 a[56:59], a[184:187], a[148:151], a[56:59]\n\t"
    "ds_read_b128 a[220:223], v129 offset:14336\n\t"
    "v_mfma_f32_16x16x32_bf16 a[60:63], a[188:191], a[148:151], a[60:63]\n\t"
    "s_waitcnt lgkmcnt(15)\n\t"
    "v_mfma_f32_16x16x32_bf16 a[64:67], a[160:163], a[152:155], a[64:67]\n\t"
    "v_mfma_f32_16x16x32_bf16 a[68:71], a[164:167], a[152:155], a[68:71]\n\t"
    "v_mfma_f32_16x16x32_bf16 a[72:75], a[168:171], a[152:155], a[72:75]\n\t"
    "v_mfma_f32_16x16x32_bf16 a[76:79], a[172:175], a[152:155], a[76:79]\n\t"
    "v_mfma_f32_16x16x32_bf16 a[80:83], a[176:179], a[152:155], a[80:83]\n\t"
    "v_mfma_f32_16x16x32_bf16 a[84:87], a[180:183], a[152:155], a[84:87]\n\t"
    "v_mfma_f32_16x16x32_bf16 a[88:91], a[184:187], a[152:155], a[88:91]\n\t"
    "v_mfma_f32_16x16x32_bf16 a[92:95], a[188:191], a[152:155], a[92:95]\n\t"
    "s_waitcnt lgkmcnt(15)\n\t"
    "v_mfma_f32_16x16x32_bf16 a[96:99], a[160:163], a[156:159], a[96:99]\n\t"
    "v_mfma_f32_16x16x32_bf16 a[100:103], a[164:167], a[156:159], a[100:103]\n\t"
    "v_mfma_f32_16x16x32_bf16 a[104:107], a[168:171], a[156:159], a[104:107]\n\t"
    "v_mfma_f32_16x16x32_bf16 a[108:111], a[172:175], a[156:159], a[108:111]\n\t"
    "v_mfma_f32_16x16x32_bf16 a[112:115], a[176:179], a[156:159], a[112:115]\n\t"
    "v_mfma_f32_16x16x32_bf16 a[116:119], a[180:183], a[156:159], a[116:119]\n\t"
    "v_mfma_f32_16x16x32_bf16 a[120:123], a[184:187], a[156:159], a[120:123]\n\t"
    "v_mfma_f32_16x16x32_bf16 a[124:127], a[188:191], a[156:159], a[124:127]\n\t"
    "s_waitcnt vmcnt(8) lgkmcnt(0)\n\t"
    "s_barrier\n\t"
    "v_mfma_f32_16x16x32_bf16 v[0:3], a[224:227], a[192:195], v[0:3]\n\t"
    "s_cmp_eq_u32 s49, 0x20000\n\t"
    "s_cselect_b32 s50, s51, s52\n\t"
    "v_xor_b32 v130, 0x8000, v130\n\t"
    "v_xor_b32 v131, 0x8000, v131\n\t"
    "v_mfma_f32_16x16x32_bf16 v[4:7], a[228:231], a[192:195], v[4:7]\n\t"
    "s_add_u32 s49, s49, s50\n\t"
    "v_xor_b32 v132, 0x8000, v132\n\t"
    "v_xor_b32 v133, 0x8000, v133\n\t"
    "v_mfma_f32_16x16x32_bf16 v[8:11], a[232:235], a[192:195], v[8:11]\n\t"
    "v_xor_b32 v134, 0x8000, v134\n\t"
    "v_xor_b32 v135, 0x8000, v135\n\t"
    "v_mfma_f32_16x16x32_bf16 v[12:15], a[236:239], a[192:195], v[12:15]\n\t"
    "v_xor_b32 v136, 0x8000, v136\n\t"
    "v_xor_b32 v137, 0x8000, v137\n\t"
    "v_mfma_f32_16x16x32_bf16 v[16:19], a[240:243], a[192:195], v[16:19]\n\t"
    "v_add_u32 v128, s50, v128\n\t"
    "v_add_u32 v129, s50, v129\n\t"
    "v_mfma_f32_16x16x32_bf16 v[20:23], a[244:247], a[192:195], v[20:23]\n\t"
    "v_mfma_f32_16x16x32_bf16 v[24:27], a[248:251], a[192:195], v[24:27]\n\t"
    "ds_read_b128 a[128:131], v128 offset:0\n\t"
    "v_mfma_f32_16x16x32_bf16 v[28:31], a[252:255], a[192:195], v[28:31]\n\t"
    "v_mfma_f32_16x16x32_bf16 v[32:35], a[224:227], a[196:199], v[32:35]\n\t"
    "ds_read_b64_tr_b16 a[160:161], v130 offset:0\n\t"
    "v_mfma_f32_16x16x32_bf16 v[36:39], a[228:231], a[196:199], v[36:39]\n\t"
    "v_mfma_f32_16x16x32_bf16 v[40:43], a[232:235], a[196:199], v[40:43]\n\t"
    "ds_read_b64_tr_b16 a[162:163], v130 offset:1024\n\t"
    "v_mfma_f32_16x16x32_bf16 v[44:47], a[236:239], a[196:199], v[44:47]\n\t"
    "v_mfma_f32_16x16x32_bf16 v[48:51], a[240:243], a[196:199], v[48:51]\n\t"
    "ds_read_b64_tr_b16 a[164:165], v131 offset:0\n\t"
    "v_mfma_f32_16x16x32_bf16 v[52:55], a[244:247], a[196:199], v[52:55]\n\t"
    "v_mfma_f32_16x16x32_bf16 v[56:59], a[248:251], a[196:199], v[56:59]\n\t"
    "ds_read_b64_tr_b16 a[166:167], v131 offset:1024\n\t"
    "v_mfma_f32_16x16x32_bf16 v[60:63], a[252:255], a[196:199], v[60:63]\n\t"
    "v_mfma_f32_16x16x32_bf16 v[64:67], a[224:227], a[200:203], v[64:67]\n\t"
    "ds_read_b64_tr_b16 a[168:169], v132 offset:0\n\t"
    "v_mfma_f32_16x16x32_bf16 v[68:71], a[228:231], a[200:203], v[68:71]\n\t"
    "v_mfma_f32_16x16x32_bf16 v[72:75], a[232:235], a[200:203], v[72:75]\n\t"
    "ds_read_b64_tr_b16 a[170:171], v132 offset:1024\n\t"
    "v_mfma_f32_16x16x32_bf16 v[76:79], a[236:239], a[200:203], v[76:79]\n\t"
    "v_mfma_f32_16x16x32_bf16 v[80:83], a[240:243], a[200:203], v[80:83]\n\t"
    "ds_read_b64_tr_b16 a[172:173], v133 offset:0\n\t"
    "v_mfma_f32_16x16x32_bf16 v[84:87], a[244:247], a[200:203], v[84:87]\n\t"
    "v_mfma_f32_16x16x32_bf16 v[88:91], a[248:251], a[200:203], v[88:91]\n\t"
    "ds_read_b64_tr_b16 a[174:175], v133 offset:1024\n\t"
    "v_mfma_f32_16x16x32_bf16 v[92:95], a[252:255], a[200:203], v[92:95]\n\t"
    "v_mfma_f32_16x16x32_bf16 v[96:99], a[224:227], a[204:207], v[96:99]\n\t"
    "ds_read_b64_tr_b16 a[176:177], v134 offset:0\n\t"
    "v_mfma_f32_16x16x32_bf16 v[100:103], a[228:231], a[204:207], v[100:103]\n\t"
    "v_mfma_f32_16x16x32_bf16 v[104:107], a[232:235], a[204:207], v[104:107]\n\t"
    "ds_read_b64_tr_b16 a[178:179], v134 offset:1024\n\t"
    "v_mfma_f32_16x16x32_bf16 v[108:111], a[236:239], a[204:207], v[108:111]\n\t"
    "v_mfma_f32_16x16x32_bf16 v[112:115], a[240:243], a[204:207], v[112:115]\n\t"
    "ds_read_b64_tr_b16 a[180:181], v135 offset:0\n\t"
    "v_mfma_f32_16x16x32_bf16 v[116:119], a[244:247], a[204:207], v[116:119]\n\t"
    "v_mfma_f32_16x16x32_bf16 v[120:123], a[248:251], a[204:207], v[120:123]\n\t"
    "ds_read_b64_tr_b16 a[182:183], v135 offset:1024\n\t"
    "v_mfma_f32_16x16x32_bf16 v[124:127], a[252:255], a[204:207], v[124:127]\n\t"
    "v_mfma_f32_16x16x32_bf16 a[0:3], a[224:227], a[208:211], a[0:3]\n\t"
    "ds_read_b64_tr_b16 a[184:185], v136 offset:0\n\t"
    "v_mfma_f32_16x16x32_bf16 a[4:7], a[228:231], a[208:211], a[4:7]\n\t"
    "v_mfma_f32_16x16x32_bf16 a[8:11], a[232:235], a[208:211], a[8:11]\n\t"
    "ds_read_b64_tr_b16 a[186:187], v136 offset:1024\n\t"
    "v_mfma_f32_16x16x32_bf16 a[12:15], a[236:239], a[208:211], a[12:15]\n\t"
    "v_mfma_f32_16x16x32_bf16 a[16:19], a[240:243], a[208:211], a[16:19]\n\t"
    "ds_read_b64_tr_b16 a[188:189], v137 offset:0\n\t"
    "v_mfma_f32_16x16x32_bf16 a[20:23], a[244:247], a[208:211], a[20:23]\n\t"
    "v_mfma_f32_16x16x32_bf16 a[24:27], a[248:251], a[208:211], a[24:27]\n\t"
    "ds_read_b64_tr_b16 a[190:191], v137 offset:1024\n\t"
    "v_mfma_f32_16x16x32_bf16 a[28:31], a[252:255], a[208:211], a[28:31]\n\t"
    "v_mfma_f32_16x16x32_bf16 a[32:35], a[224:227], a[212:215], a[32:35]\n\t"
    "ds_read_b128 a[132:135], v128 offset:2048\n\t"
    "v_mfma_f32_16x16x32_bf16 a[36:39], a[228:231], a[212:215], a[36:39]\n\t"
    "v_mfma_f32_16x16x32_bf16 a[40:43], a[232:235], a[212:215], a[40:43]\n\t"
    "ds_read_b128 a[136:139], v128 offset:4096\n\t"
    "v_mfma_f32_16x16x32_bf16 a[44:47], a[236:239], a[212:215], a[44:47]\n\t"
    "v_mfma_f32_16x16x32_bf16 a[48:51], a[240:243], a[212:215], a[48:51]\n\t"
    "ds_read_b128 a[140:143], v128 offset:6144\n\t"
    "v_mfma_f32_16x16x32_bf16 a[52:55], a[244:247], a[212:215], a[52:55]\n\t"
    "v_mfma_f32_16x16x32_bf16 a[56:59], a[248:251], a[212:215], a[56:59]\n\t"
    "ds_read_b128 a[144:147], v128 offset:8192\n\t"
    "v_mfma_f32_16x16x32_bf16 a[60:63], a[252:255], a[212:215], a[60:63]\n\t"
    "v_mfma_f32_16x16x32_bf16 a[64:67], a[224:227], a[216:219], a[64:67]\n\t"
    "ds_read_b128 a[148:151], v128 offset:10240\n\t"
    "v_mfma_f32_16x16x32_bf16 a[68:71], a[228:231], a[216:219], a[68:71]\n\t"
    "v_mfma_f32_16x16x32_bf16 a[72:75], a[232:235], a[216:219], a[72:75]\n\t"
    "ds_read_b128 a[152:155], v128 offset:12288\n\t"
    "v_mfma_f32_16x16x32_bf16 a[76:79], a[236:239], a[216:219], a[76:79]\n\t"
    "v_mfma_f32_16x16x32_bf16 a[80:83], a[240:243], a[216:219], a[80:83]\n\t"
    "ds_read_b128 a[156:159], v128 offset:14336\n\t"
    "v_mfma_f32_16x16x32_bf16 a[84:87], a[244:247], a[216:219], a[84:87]\n\t"
    "v_mfma_f32_16x16x32_bf16 a[88:91], a[248:251], a[216:219], a[88:91]\n\t"
    "v_mfma_f32_16x16x32_bf16 a[92:95], a[252:255], a[216:219], a[92:95]\n\t"
    "v_mfma_f32_16x16x32_bf16 a[96:99], a[224:227], a[220:223], a[96:99]\n\t"
    "v_mfma_f32_16x16x32_bf16 a[100:103], a[228:231], a[220:223], a[100:103]\n\t"
    "v_mfma_f32_16x16x32_bf16 a[104:107], a[232:235], a[220:223], a[104:107]\n\t"
    "v_mfma_f32_16x16x32_bf16 a[108:111], a[236:239], a[220:223], a[108:111]\n\t"
    "v_mfma_f32_16x16x32_bf16 a[112:115], a[240:243], a[220:223], a[112:115]\n\t"
    "v_mfma_f32_16x16x32_bf16 a[116:119], a[244:247], a[220:223], a[116:119]\n\t"
    "v_mfma_f32_16x16x32_bf16 a[120:123], a[248:251], a[220:223], a[120:123]\n\t"
    "v_mfma_f32_16x16x32_bf16 a[124:127], a[252:255], a[220:223], a[124:127]\n\t"
    "s_waitcnt lgkmcnt(15)\n\t"
    "v_mfma_f32_16x16x32_bf16 v[0:3], a[160:163], a[128:131], v[0:3]\n\t"
    "s_waitcnt lgkmcnt(15)\n\t"
    "ds_read_b128 a[192:195], v129 offset:0\n\t"
    "v_mfma_f32_16x16x32_bf16 v[4:7], a[164:167], a[128:131], v[4:7]\n\t"
    "s_waitcnt lgkmcnt(15)\n\t"
    "s_add_u32 m0, s41, 0x10000\n\t"
    "v_mfma_f32_16x16x32_bf16 v[8:11], a[168:171], a[128:131], v[8:11]\n\t"
    "s_waitcnt lgkmcnt(15)\n\t"
    "s_add_u32 s39, s54, 0x80\n\t"
    "ds_read_b64_tr_b16 a[224:225], v130 offset:8192\n\t"
    "v_mfma_f32_16x16x32_bf16 v[12:15], a[172:175], a[128:131], v[12:15]\n\t"
    "s_waitcnt lgkmcnt(15)\n\t"
    "buffer_load_dwordx4 v209, s[8:11], s39 offen lds\n\t"
    "v_mfma_f32_16x16x32_bf16 v[16:19], a[176:179], a[128:131], v[16:19]\n\t"
    "s_waitcnt lgkmcnt(13)\n\t"
    "ds_read_b64_tr_b16 a[226:227], v130 offset:9216\n\t"
    "v_mfma_f32_16x16x32_bf16 v[20:23], a[180:183], a[128:131], v[20:23]\n\t"
    "s_waitcnt lgkmcnt(12)\n\t"
    "v_mfma_f32_16x16x32_bf16 v[24:27], a[184:187], a[128:131], v[24:27]\n\t"
    "s_waitcnt lgkmcnt(10)\n\t"
    "ds_read_b64_tr_b16 a[228:229], v131 offset:8192\n\t"
    "v_mfma_f32_16x16x32_bf16 v[28:31], a[188:191], a[128:131], v[28:31]\n\t"
    "s_waitcnt lgkmcnt(10)\n\t"
    "s_add_u32 m0, m0, 0x400\n\t"
    "v_mfma_f32_16x16x32_bf16 v[32:35], a[160:163], a[132:135], v[32:35]\n\t"
    "s_add_u32 s39, s39, 0x2000\n\t"
    "ds_read_b64_tr_b16 a[230:231], v131 offset:9216\n\t"
    "v_mfma_f32_16x16x32_bf16 v[36:39], a[164:167], a[132:135], v[36:39]\n\t"
    "buffer_load_dwordx4 v210, s[8:11], s39 offen lds\n\t"
    "v_mfma_f32_16x16x32_bf16 v[40:43], a[168:171], a[132:135], v[40:43]\n\t"
    "ds_read_b64_tr_b16 a[232:233], v132 offset:8192\n\t"
    "v_mfma_f32_16x16x32_bf16 v[44:47], a[172:175], a[132:135], v[44:47]\n\t"
    "v_mfma_f32_16x16x32_bf16 v[48:51], a[176:179], a[132:135], v[48:51]\n\t"
    "ds_read_b64_tr_b16 a[234:235], v132 offset:9216\n\t"
    "v_mfma_f32_16x16x32_bf16 v[52:55], a[180:183], a[132:135], v[52:55]\n\t"
    "s_add_u32 m0, m0, 0x400\n\t"
    "v_mfma_f32_16x16x32_bf16 v[56:59], a[184:187], a[132:135], v[56:59]\n\t"
    "s_add_u32 s39, s39, 0x2000\n\t"
    "ds_read_b64_tr_b16 a[236:237], v133 offset:8192\n\t"
    "v_mfma_f32_16x16x32_bf16 v[60:63], a[188:191], a[132:135], v[60:63]\n\t"
    "s_waitcnt lgkmcnt(13)\n\t"
    "buffer_load_dwordx4 v209, s[8:11], s39 offen lds\n\t"
    "v_mfma_f32_16x16x32_bf16 v[64:67], a[160:163], a[136:139], v[64:67]\n\t"
    "ds_read_b64_tr_b16 a[238:239], v133 offset:9216\n\t"
    "v_mfma_f32_16x16x32_bf16 v[68:71], a[164:167], a[136:139], v[68:71]\n\t"
    "v_mfma_f32_16x16x32_bf16 v[72:75], a[168:171], a[136:139], v[72:75]\n\t"
    "ds_read_b64_tr_b16 a[240:241], v134 offset:8192\n\t"
    "v_mfma_f32_16x16x32_bf16 v[76:79], a[172:175], a[136:139], v[76:79]\n\t"
    "s_add_u32 m0, m0, 0x400\n\t"
    "v_mfma_f32_16x16x32_bf16 v[80:83], a[176:179], a[136:139], v[80:83]\n\t"
    "s_add_u32 s39, s39, 0x2000\n\t"
    "ds_read_b64_tr_b16 a[242:243], v134 offset:9216\n\t"
    "v_mfma_f32_16x16x32_bf16 v[84:87], a[180:183], a[136:139], v[84:87]\n\t"
    "buffer_load_dwordx4 v210, s[8:11], s39 offen lds\n\t"
    "v_mfma_f32_16x16x32_bf16 v[88:91], a[184:187], a[136:139], v[88:91]\n\t"
    "ds_read_b64_tr_b16 a[244:245], v135 offset:8192\n\t"
    "v_mfma_f32_16x16x32_bf16 v[92:95], a[188:191], a[136:139], v[92:95]\n\t"
    "s_waitcnt lgkmcnt(15)\n\t"
    "v_mfma_f32_16x16x32_bf16 v[96:99], a[160:163], a[140:143], v[96:99]\n\t"
    "ds_read_b64_tr_b16 a[246:247], v135 offset:9216\n\t"
    "v_mfma_f32_16x16x32_bf16 v[100:103], a[164:167], a[140:143], v[100:103]\n\t"
    "s_add_u32 m0, s41, 0x14000\n\t"
    "v_mfma_f32_16x16x32_bf16 v[104:107], a[168:171], a[140:143], v[104:107]\n\t"
    "s_add_u32 s39, s55, 0x80\n\t"
    "ds_read_b64_tr_b16 a[248:249], v136 offset:8192\n\t"
    "v_mfma_f32_16x16x32_bf16 v[108:111], a[172:175], a[140:143], v[108:111]\n\t"
    "buffer_load_dwordx4 v209, s[8:11], s39 offen lds\n\t"
    "v_mfma_f32_16x16x32_bf16 v[112:115], a[176:179], a[140:143], v[112:115]\n\t"
    "ds_read_b64_tr_b16 a[250:251], v136 offset:9216\n\t"
    "v_mfma_f32_16x16x32_bf16 v[116:119], a[180:183], a[140:143], v[116:119]\n\t"
    "v_mfma_f32_16x16x32_bf16 v[120:123], a[184:187], a[140:143], v[120:123]\n\t"
    "ds_read_b64_tr_b16 a[252:253], v137 offset:8192\n\t"
    "v_mfma_f32_16x16x32_bf16 v[124:127], a[188:191], a[140:143], v[124:127]\n\t"
    "s_waitcnt lgkmcnt(15)\n\t"
    "s_add_u32 m0, m0, 0x400\n\t"
    "v_mfma_f32_16x16x32_bf16 a[0:3], a[160:163], a[144:147], a[0:3]\n\t"
    "s_add_u32 s39, s39, 0x2000\n\t"
    "ds_read_b64_tr_b16 a[254:255], v137 offset:9216\n\t"
    "v_mfma_f32_16x16x32_bf16 a[4:7], a[164:167], a[144:147], a[4:7]\n\t"
    "buffer_load_dwordx4 v210, s[8:11], s39 offen lds\n\t"
    "v_mfma_f32_16x16x32_bf16 a[8:11], a[168:171], a[144:147], a[8:11]\n\t"
    "ds_read_b128 a[196:199], v129 offset:2048\n\t"
    "v_mfma_f32_16x16x32_bf16 a[12:15], a[172:175], a[144:147], a[12:15]\n\t"
    "v_mfma_f32_16x16x32_bf16 a[16:19], a[176:179], a[144:147], a[16:19]\n\t"
    "ds_read_b128 a[200:203], v129 offset:4096\n\t"
    "v_mfma_f32_16x16x32_bf16 a[20:23], a[180:183], a[144:147], a[20:23]\n\t"
    "s_add_u32 m0, m0, 0x400\n\t"
    "v_mfma_f32_16x16x32_bf16 a[24:27], a[184:187], a[144:147], a[24:27]\n\t"
    "s_add_u32 s39, s39, 0x2000\n\t"
    "ds_read_b128 a[204:207], v129 offset:6144\n\t"
    "v_mfma_f32_16x16x32_bf16 a[28:31], a[188:191], a[144:147], a[28:31]\n\t"
    "s_waitcnt lgkmcnt(15)\n\t"
    "buffer_load_dwordx4 v209, s[8:11], s39 offen lds\n\t"
    "v_mfma_f32_16x16x32_bf16 a[32:35], a[160:163], a[148:151], a[32:35]\n\t"
    "ds_read_b128 a[208:211], v129 offset:8192\n\t"
    "v_mfma_f32_16x16x32_bf16 a[36:39], a[164:167], a[148:151], a[36:39]\n\t"
    "v_mfma_f32_16x16x32_bf16 a[40:43], a[168:171], a[148:151], a[40:43]\n\t"
    "ds_read_b128 a[212:215], v129 offset:10240\n\t"
    "v_mfma_f32_16x16x32_bf16 a[44:47], a[172:175], a[148:151], a[44:47]\n\t"
    "s_add_u32 m0, m0, 0x400\n\t"
    "v_mfma_f32_16x16x32_bf16 a[48:51], a[176:179], a[148:151], a[48:51]\n\t"
    "s_add_u32 s39, s39, 0x2000\n\t"
    "ds_read_b128 a[216:219], v129 offset:12288\n\t"
    "v_mfma_f32_16x16x32_bf16 a[52:55], a[180:183], a[148:151], a[52:55]\n\t"
    "buffer_load_dwordx4 v210, s[8:11], s39 offen lds\n\t"
    "v_mfma_f32_16x16x32_bf16 a[56:59], a[184:187], a[148:151], a[56:59]\n\t"
    "ds_read_b128 a[220:223], v129 offset:14336\n\t"
    "v_mfma_f32_16x16x32_bf16 a[60:63], a[188:191], a[148:151], a[60:63]\n\t"
    "s_waitcnt lgkmcnt(15)\n\t"
    "v_mfma_f32_16x16x32_bf16 a[64:67], a[160:163], a[152:155], a[64:67]\n\t"
    "v_mfma_f32_16x16x32_bf16 a[68:71], a[164:167], a[152:155], a[68:71]\n\t"
    "v_mfma_f32_16x16x32_bf16 a[72:75], a[168:171], a[152:155], a[72:75]\n\t"
    "v_mfma_f32_16x16x32_bf16 a[76:79], a[172:175], a[152:155], a[76:79]\n\t"
    "v_mfma_f32_16x16x32_bf16 a[80:83], a[176:179], a[152:155], a[80:83]\n\t"
    "v_mfma_f32_16x16x32_bf16 a[84:87], a[180:183], a[152:155], a[84:87]\n\t"
    "v_mfma_f32_16x16x32_bf16 a[88:91], a[184:187], a[152:155], a[88:91]\n\t"
    "v_mfma_f32_16x16x32_bf16 a[92:95], a[188:191], a[152:155], a[92:95]\n\t"
    "s_waitcnt lgkmcnt(15)\n\t"
    "v_mfma_f32_16x16x32_bf16 a[96:99], a[160:163], a[156:159], a[96:99]\n\t"
    "v_mfma_f32_16x16x32_bf16 a[100:103], a[164:167], a[156:159], a[100:103]\n\t"
    "v_mfma_f32_16x16x32_bf16 a[104:107], a[168:171], a[156:159], a[104:107]\n\t"
    "v_mfma_f32_16x16x32_bf16 a[108:111], a[172:175], a[156:159], a[108:111]\n\t"
    "v_mfma_f32_16x16x32_bf16 a[112:115], a[176:179], a[156:159], a[112:115]\n\t"
    "v_mfma_f32_16x16x32_bf16 a[116:119], a[180:183], a[156:159], a[116:119]\n\t"
    "v_mfma_f32_16x16x32_bf16 a[120:123], a[184:187], a[156:159], a[120:123]\n\t"
    "v_mfma_f32_16x16x32_bf16 a[124:127], a[188:191], a[156:159], a[124:127]\n\t"
    "s_waitcnt lgkmcnt(0)\n\t"
    "s_barrier\n\t"
    "v_mfma_f32_16x16x32_bf16 v[0:3], a[224:227], a[192:195], v[0:3]\n\t"
    "s_add_u32 m0, s41, 0x8000\n\t"
    "v_mfma_f32_16x16x32_bf16 v[4:7], a[228:231], a[192:195], v[4:7]\n\t"
    "s_add_u32 s39, s57, 0x0\n\t"
    "v_mfma_f32_16x16x32_bf16 v[8:11], a[232:235], a[192:195], v[8:11]\n\t"
    "buffer_load_dwordx4 v212, s[84:87], s39 offen lds\n\t"
    "v_mfma_f32_16x16x32_bf16 v[12:15], a[236:239], a[192:195], v[12:15]\n\t"
    "v_mfma_f32_16x16x32_bf16 v[16:19], a[240:243], a[192:195], v[16:19]\n\t"
    "s_add_u32 m0, m0, 0x400\n\t"
    "v_mfma_f32_16x16x32_bf16 v[20:23], a[244:247], a[192:195], v[20:23]\n\t"
    "s_add_u32 s39, s39, 0x8000\n\t"
    "v_mfma_f32_16x16x32_bf16 v[24:27], a[248:251], a[192:195], v[24:27]\n\t"
    "buffer_load_dwordx4 v212, s[84:87], s39 offen lds\n\t"
    "v_mfma_f32_16x16x32_bf16 v[28:31], a[252:255], a[192:195], v[28:31]\n\t"
    "v_cvt_pk_bf16_f32 v0, v0, v1\n\t"
    "v_cvt_pk_bf16_f32 v1, v2, v3\n\t"
    "v_mfma_f32_16x16x32_bf16 v[32:35], a[224:227], a[196:199], v[32:35]\n\t"
    "s_add_u32 m0, m0, 0x400\n\t"
    "v_cvt_pk_bf16_f32 v2, v4, v5\n\t"
    "v_cvt_pk_bf16_f32 v3, v6, v7\n\t"
    "v_mfma_f32_16x16x32_bf16 v[36:39], a[228:231], a[196:199], v[36:39]\n\t"
    "s_add_u32 s39, s39, 0x8000\n\t"
    "v_cvt_pk_bf16_f32 v4, v8, v9\n\t"
    "v_cvt_pk_bf16_f32 v5, v10, v11\n\t"
    "v_mfma_f32_16x16x32_bf16 v[40:43], a[232:235], a[196:199], v[40:43]\n\t"
    "buffer_load_dwordx4 v213, s[84:87], s39 offen lds\n\t"
    "v_cvt_pk_bf16_f32 v6, v12, v13\n\t"
    "v_mfma_f32_16x16x32_bf16 v[44:47], a[236:239], a[196:199], v[44:47]\n\t"
    "v_cvt_pk_bf16_f32 v7, v14, v15\n\t"
    "v_cvt_pk_bf16_f32 v8, v16, v17\n\t"
    "v_mfma_f32_16x16x32_bf16 v[48:51], a[240:243], a[196:199], v[48:51]\n\t"
    "s_add_u32 m0, m0, 0x400\n\t"
    "v_cvt_pk_bf16_f32 v9, v18, v19\n\t"
    "v_cvt_pk_bf16_f32 v10, v20, v21\n\t"
    "v_mfma_f32_16x16x32_bf16 v[52:55], a[244:247], a[196:199], v[52:55]\n\t"
    "s_add_u32 s39, s39, 0x8000\n\t"
    "v_cvt_pk_bf16_f32 v11, v22, v23\n\t"
    "v_cvt_pk_bf16_f32 v12, v24, v25\n\t"
    "v_mfma_f32_16x16x32_bf16 v[56:59], a[248:251], a[196:199], v[56:59]\n\t"
    "buffer_load_dwordx4 v213, s[84:87], s39 offen lds\n\t"
    "v_cvt_pk_bf16_f32 v13, v26, v27\n\t"
    "v_mfma_f32_16x16x32_bf16 v[60:63], a[252:255], a[196:199], v[60:63]\n\t"
    "v_cvt_pk_bf16_f32 v14, v28, v29\n\t"
    "v_cvt_pk_bf16_f32 v15, v30, v31\n\t"
    "v_mfma_f32_16x16x32_bf16 v[64:67], a[224:227], a[200:203], v[64:67]\n\t"
    "s_lshr_b32 s37, s41, 1\n\t"
    "s_add_u32 m0, s37, 0xc000\n\t"
    "v_cvt_pk_bf16_f32 v16, v32, v33\n\t"
    "v_cvt_pk_bf16_f32 v17, v34, v35\n\t"
    "v_mfma_f32_16x16x32_bf16 v[68:71], a[228:231], a[200:203], v[68:71]\n\t"
    "s_add_u32 s39, s58, 0x0\n\t"
    "v_cvt_pk_bf16_f32 v18, v36, v37\n\t"
    "v_cvt_pk_bf16_f32 v19, v38, v39\n\t"
    "v_mfma_f32_16x16x32_bf16 v[72:75], a[232:235], a[200:203], v[72:75]\n\t"
    "buffer_load_dwordx4 v214, s[88:91], s39 offen lds\n\t"
    "v_cvt_pk_bf16_f32 v20, v40, v41\n\t"
    "v_mfma_f32_16x16x32_bf16 v[76:79], a[236:239], a[200:203], v[76:79]\n\t"
    "v_cvt_pk_bf16_f32 v21, v42, v43\n\t"
    "v_cvt_pk_bf16_f32 v22, v44, v45\n\t"
    "v_mfma_f32_16x16x32_bf16 v[80:83], a[240:243], a[200:203], v[80:83]\n\t"
    "s_add_u32 m0, m0, 0x400\n\t"
    "v_cvt_pk_bf16_f32 v23, v46, v47\n\t"
    "v_cvt_pk_bf16_f32 v24, v48, v49\n\t"
    "v_mfma_f32_16x16x32_bf16 v[84:87], a[244:247], a[200:203], v[84:87]\n\t"
    "s_add_u32 s39, s39, 0x6000\n\t"
    "v_cvt_pk_bf16_f32 v25, v50, v51\n\t"
    "v_cvt_pk_bf16_f32 v26, v52, v53\n\t"
    "v_mfma_f32_16x16x32_bf16 v[88:91], a[248:251], a[200:203], v[88:91]\n\t"
    "buffer_load_dwordx4 v215, s[88:91], s39 offen lds\n\t"
    "v_cvt_pk_bf16_f32 v27, v54, v55\n\t"
    "v_mfma_f32_16x16x32_bf16 v[92:95], a[252:255], a[200:203], v[92:95]\n\t"
    "v_cvt_pk_bf16_f32 v28, v56, v57\n\t"
    "v_cvt_pk_bf16_f32 v29, v58, v59\n\t"
    "v_mfma_f32_16x16x32_bf16 v[96:99], a[224:227], a[204:207], v[96:99]\n\t"
    "s_add_u32 m0, s59, 0xe000\n\t"
    "v_cvt_pk_bf16_f32 v30, v60, v61\n\t"
    "v_cvt_pk_bf16_f32 v31, v62, v63\n\t"
    "v_mfma_f32_16x16x32_bf16 v[100:103], a[228:231], a[204:207], v[100:103]\n\t"
    "s_add_u32 s39, s56, 0x0\n\t"
    "v_cvt_pk_bf16_f32 v32, v64, v65\n\t"
    "v_cvt_pk_bf16_f32 v33, v66, v67\n\t"
    "v_mfma_f32_16x16x32_bf16 v[104:107], a[232:235], a[204:207], v[104:107]\n\t"
    "buffer_load_dwordx4 v211, s[8:11], s39 offen lds\n\t"
    "v_cvt_pk_bf16_f32 v34, v68, v69\n\t"
    "v_mfma_f32_16x16x32_bf16 v[108:111], a[236:239], a[204:207], v[108:111]\n\t"
    "v_cvt_pk_bf16_f32 v35, v70, v71\n\t"
    "v_cvt_pk_bf16_f32 v36, v72, v73\n\t"
    "v_mfma_f32_16x16x32_bf16 v[112:115], a[240:243], a[204:207], v[112:115]\n\t"
    "s_add_u32 m0, s41, 0x18000\n\t"
    "v_cvt_pk_bf16_f32 v37, v74, v75\n\t"
    "v_cvt_pk_bf16_f32 v38, v76, v77\n\t"
    "v_mfma_f32_16x16x32_bf16 v[116:119], a[244:247], a[204:207], v[116:119]\n\t"
    "s_add_u32 s39, s57, 0x80000\n\t"
    "v_cvt_pk_bf16_f32 v39, v78, v79\n\t"
    "v_cvt_pk_bf16_f32 v40, v80, v81\n\t"
    "v_mfma_f32_16x16x32_bf16 v[120:123], a[248:251], a[204:207], v[120:123]\n\t"
    "buffer_load_dwordx4 v212, s[84:87], s39 offen lds\n\t"
    "v_cvt_pk_bf16_f32 v41, v82, v83\n\t"
    "v_mfma_f32_16x16x32_bf16 v[124:127], a[252:255], a[204:207], v[124:127]\n\t"
    "v_cvt_pk_bf16_f32 v42, v84, v85\n\t"
    "v_cvt_pk_bf16_f32 v43, v86, v87\n\t"
    "v_mfma_f32_16x16x32_bf16 a[0:3], a[224:227], a[208:211], a[0:3]\n\t"
    "s_add_u32 m0, m0, 0x400\n\t"
    "v_cvt_pk_bf16_f32 v44, v88, v89\n\t"
    "v_cvt_pk_bf16_f32 v45, v90, v91\n\t"
    "v_mfma_f32_16x16x32_bf16 a[4:7], a[228:231], a[208:211], a[4:7]\n\t"
    "s_add_u32 s39, s39, 0x8000\n\t"
    "v_cvt_pk_bf16_f32 v46, v92, v93\n\t"
    "v_cvt_pk_bf16_f32 v47, v94, v95\n\t"
    "v_mfma_f32_16x16x32_bf16 a[8:11], a[232:235], a[208:211], a[8:11]\n\t"
    "buffer_load_dwordx4 v212, s[84:87], s39 offen lds\n\t"
    "v_cvt_pk_bf16_f32 v48, v96, v97\n\t"
    "v_mfma_f32_16x16x32_bf16 a[12:15], a[236:239], a[208:211], a[12:15]\n\t"
    "v_cvt_pk_bf16_f32 v49, v98, v99\n\t"
    "v_cvt_pk_bf16_f32 v50, v100, v101\n\t"
    "v_mfma_f32_16x16x32_bf16 a[16:19], a[240:243], a[208:211], a[16:19]\n\t"
    "s_add_u32 m0, m0, 0x400\n\t"
    "v_cvt_pk_bf16_f32 v51, v102, v103\n\t"
    "v_cvt_pk_bf16_f32 v52, v104, v105\n\t"
    "v_mfma_f32_16x16x32_bf16 a[20:23], a[244:247], a[208:211], a[20:23]\n\t"
    "s_add_u32 s39, s39, 0x8000\n\t"
    "v_cvt_pk_bf16_f32 v53, v106, v107\n\t"
    "v_cvt_pk_bf16_f32 v54, v108, v109\n\t"
    "v_mfma_f32_16x16x32_bf16 a[24:27], a[248:251], a[208:211], a[24:27]\n\t"
    "buffer_load_dwordx4 v213, s[84:87], s39 offen lds\n\t"
    "v_cvt_pk_bf16_f32 v55, v110, v111\n\t"
    "v_mfma_f32_16x16x32_bf16 a[28:31], a[252:255], a[208:211], a[28:31]\n\t"
    "v_cvt_pk_bf16_f32 v56, v112, v113\n\t"
    "v_cvt_pk_bf16_f32 v57, v114, v115\n\t"
    "v_mfma_f32_16x16x32_bf16 a[32:35], a[224:227], a[212:215], a[32:35]\n\t"
    "s_add_u32 m0, m0, 0x400\n\t"
    "v_cvt_pk_bf16_f32 v58, v116, v117\n\t"
    "v_cvt_pk_bf16_f32 v59, v118, v119\n\t"
    "v_mfma_f32_16x16x32_bf16 a[36:39], a[228:231], a[212:215], a[36:39]\n\t"
    "s_add_u32 s39, s39, 0x8000\n\t"
    "v_cvt_pk_bf16_f32 v60, v120, v121\n\t"
    "v_cvt_pk_bf16_f32 v61, v122, v123\n\t"
    "v_mfma_f32_16x16x32_bf16 a[40:43], a[232:235], a[212:215], a[40:43]\n\t"
    "buffer_load_dwordx4 v213, s[84:87], s39 offen lds\n\t"
    "v_cvt_pk_bf16_f32 v62, v124, v125\n\t"
    "v_mfma_f32_16x16x32_bf16 a[44:47], a[236:239], a[212:215], a[44:47]\n\t"
    "v_cvt_pk_bf16_f32 v63, v126, v127\n\t"
    "v_accvgpr_read_b32 v218, a0\n\t"
    "v_mfma_f32_16x16x32_bf16 a[48:51], a[240:243], a[212:215], a[48:51]\n\t"
    "s_lshr_b32 s37, s41, 1\n\t"
    "s_add_u32 m0, s37, 0x1c000\n\t"
    "v_accvgpr_read_b32 v219, a1\n\t"
    "v_accvgpr_read_b32 v220, a2\n\t"
    "v_mfma_f32_16x16x32_bf16 a[52:55], a[244:247], a[212:215], a[52:55]\n\t"
    "s_add_u32 s39, s58, 0x30000\n\t"
    "v_accvgpr_read_b32 v221, a3\n\t"
    "v_accvgpr_read_b32 v222, a4\n\t"
    "v_mfma_f32_16x16x32_bf16 a[56:59], a[248:251], a[212:215], a[56:59]\n\t"
    "buffer_load_dwordx4 v214, s[88:91], s39 offen lds\n\t"
    "v_accvgpr_read_b32 v223, a5\n\t"
    "v_mfma_f32_16x16x32_bf16 a[60:63], a[252:255], a[212:215], a[60:63]\n\t"
    "v_accvgpr_read_b32 v224, a6\n\t"
    "v_accvgpr_read_b32 v225, a7\n\t"
    "v_mfma_f32_16x16x32_bf16 a[64:67], a[224:227], a[216:219], a[64:67]\n\t"
    "s_add_u32 m0, m0, 0x400\n\t"
    "v_cvt_pk_bf16_f32 v64, v218, v219\n\t"
    "v_cvt_pk_bf16_f32 v65, v220, v221\n\t"
    "v_mfma_f32_16x16x32_bf16 a[68:71], a[228:231], a[216:219], a[68:71]\n\t"
    "s_add_u32 s39, s39, 0x6000\n\t"
    "v_cvt_pk_bf16_f32 v66, v222, v223\n\t"
    "v_cvt_pk_bf16_f32 v67, v224, v225\n\t"
    "v_mfma_f32_16x16x32_bf16 a[72:75], a[232:235], a[216:219], a[72:75]\n\t"
    "buffer_load_dwordx4 v215, s[88:91], s39 offen lds\n\t"
    "v_accvgpr_read_b32 v226, a8\n\t"
    "v_mfma_f32_16x16x32_bf16 a[76:79], a[236:239], a[216:219], a[76:79]\n\t"
    "v_accvgpr_read_b32 v227, a9\n\t"
    "v_accvgpr_read_b32 v228, a10\n\t"
    "v_mfma_f32_16x16x32_bf16 a[80:83], a[240:243], a[216:219], a[80:83]\n\t"
    "s_add_u32 m0, s59, 0x1e000\n\t"
    "v_accvgpr_read_b32 v229, a11\n\t"
    "v_accvgpr_read_b32 v230, a12\n\t"
    "v_mfma_f32_16x16x32_bf16 a[84:87], a[244:247], a[216:219], a[84:87]\n\t"
    "s_add_u32 s39, s56, 0x80\n\t"
    "v_accvgpr_read_b32 v231, a13\n\t"
    "v_accvgpr_read_b32 v232, a14\n\t"
    "v_mfma_f32_16x16x32_bf16 a[88:91], a[248:251], a[216:219], a[88:91]\n\t"
    "buffer_load_dwordx4 v211, s[8:11], s39 offen lds\n\t"
    "v_accvgpr_read_b32 v233, a15\n\t"
    "v_mfma_f32_16x16x32_bf16 a[92:95], a[252:255], a[216:219], a[92:95]\n\t"
    "v_cvt_pk_bf16_f32 v68, v226, v227\n\t"
    "v_cvt_pk_bf16_f32 v69, v228, v229\n\t"
    "v_mfma_f32_16x16x32_bf16 a[96:99], a[224:227], a[220:223], a[96:99]\n\t"
    "v_cvt_pk_bf16_f32 v70, v230, v231\n\t"
    "v_cvt_pk_bf16_f32 v71, v232, v233\n\t"
    "v_mfma_f32_16x16x32_bf16 a[100:103], a[228:231], a[220:223], a[100:103]\n\t"
    "v_accvgpr_read_b32 v218, a16\n\t"
    "v_accvgpr_read_b32 v219, a17\n\t"
    "v_mfma_f32_16x16x32_bf16 a[104:107], a[232:235], a[220:223], a[104:107]\n\t"
    "v_accvgpr_read_b32 v220, a18\n\t"
    "v_accvgpr_read_b32 v221, a19\n\t"
    "v_mfma_f32_16x16x32_bf16 a[108:111], a[236:239], a[220:223], a[108:111]\n\t"
    "v_accvgpr_read_b32 v222, a20\n\t"
    "v_accvgpr_read_b32 v223, a21\n\t"
    "v_mfma_f32_16x16x32_bf16 a[112:115], a[240:243], a[220:223], a[112:115]\n\t"
    "v_accvgpr_read_b32 v224, a22\n\t"
    "v_accvgpr_read_b32 v225, a23\n\t"
    "v_mfma_f32_16x16x32_bf16 a[116:119], a[244:247], a[220:223], a[116:119]\n\t"
    "v_cvt_pk_bf16_f32 v72, v218, v219\n\t"
    "v_cvt_pk_bf16_f32 v73, v220, v221\n\t"
    "v_mfma_f32_16x16x32_bf16 a[120:123], a[248:251], a[220:223], a[120:123]\n\t"
    "v_cvt_pk_bf16_f32 v74, v222, v223\n\t"
    "v_cvt_pk_bf16_f32 v75, v224, v225\n\t"
    "v_mfma_f32_16x16x32_bf16 a[124:127], a[252:255], a[220:223], a[124:127]\n\t"
    "s_waitcnt vmcnt(7)\n\t"
    "s_barrier\n\t"
    "ds_read_b128 a[128:131], v234 offset:0\n\t"
    "ds_read_b128 a[132:135], v234 offset:8192\n\t"
    "ds_read_b128 a[136:139], v234 offset:16384\n\t"
    "ds_read_b128 a[140:143], v234 offset:24576\n\t"
    "ds_read_b128 a[144:147], v192 offset:0\n\t"
    "ds_read_b128 a[148:151], v192 offset:2048\n\t"
    "ds_read_b128 a[152:155], v192 offset:16384\n\t"
    "ds_read_b128 a[156:159], v192 offset:18432\n\t"
    "ds_read_b128 a[160:163], v194\n\t"
    "ds_read_b128 a[164:167], v196\n\t"
    "ds_read_b64_tr_b16 a[208:209], v198 offset:0\n\t"
    "ds_read_b64_tr_b16 a[210:211], v198 offset:1024\n\t"
    "ds_read_b64_tr_b16 a[212:213], v199 offset:0\n\t"
    "ds_read_b64_tr_b16 a[214:215], v199 offset:1024\n\t"
    "ds_read_b64_tr_b16 a[216:217], v200 offset:0\n\t"
    "ds_read_b64_tr_b16 a[218:219], v200 offset:1024\n\t"
    "ds_read_b64_tr_b16 a[220:221], v201 offset:0\n\t"
    "ds_read_b64_tr_b16 a[222:223], v201 offset:1024\n\t"
    "ds_read_b64_tr_b16 a[224:225], v202 offset:0\n\t"
    "ds_read_b64_tr_b16 a[226:227], v202 offset:1024\n\t"
    "ds_read_b64_tr_b16 a[228:229], v203 offset:0\n\t"
    "ds_read_b64_tr_b16 a[230:231], v203 offset:1024\n\t"
    "ds_read_b64_tr_b16 a[232:233], v204 offset:0\n\t"
    "ds_read_b64_tr_b16 a[234:235], v204 offset:1024\n\t"
    "ds_read_b64_tr_b16 a[236:237], v205 offset:0\n\t"
    "ds_read_b64_tr_b16 a[238:239], v205 offset:1024\n\t"
    "ds_read_b64_tr_b16 a[240:241], v206 offset:0\n\t"
    "ds_read_b64_tr_b16 a[242:243], v206 offset:512\n\t"
    "ds_read_b64_tr_b16 a[244:245], v207 offset:0\n\t"
    "ds_read_b64_tr_b16 a[246:247], v207 offset:512\n\t"
    "ds_read_b64_tr_b16 a[248:249], v208 offset:0\n\t"
    "ds_read_b64_tr_b16 a[250:251], v208 offset:512\n\t"
    "s_nop 3\n\t"
    "s_add_u32 s63, s53, 0x0\n\t"
    "buffer_store_dwordx4 v[0:3], v216, s[20:23], s63 offen offset:0 sc1\n\t"
    "v_accvgpr_read_b32 v226, a24\n\t"
    "v_accvgpr_read_b32 v227, a25\n\t"
    "v_accvgpr_read_b32 v228, a26\n\t"
    "v_accvgpr_read_b32 v229, a27\n\t"
    "v_accvgpr_read_b32 v230, a28\n\t"
    "v_accvgpr_read_b32 v231, a29\n\t"
    "v_accvgpr_read_b32 v232, a30\n\t"
    "v_accvgpr_read_b32 v233, a31\n\t"
    "v_cvt_pk_bf16_f32 v76, v226, v227\n\t"
    "v_cvt_pk_bf16_f32 v77, v228, v229\n\t"
    "v_cvt_pk_bf16_f32 v78, v230, v231\n\t"
    "v_cvt_pk_bf16_f32 v79, v232, v233\n\t"
    "v_accvgpr_read_b32 v218, a32\n\t"
    "v_accvgpr_read_b32 v219, a33\n\t"
    "v_accvgpr_read_b32 v220, a34\n\t"
    "v_accvgpr_read_b32 v221, a35\n\t"
    "v_accvgpr_read_b32 v222, a36\n\t"
    "v_accvgpr_read_b32 v223, a37\n\t"
    "v_accvgpr_read_b32 v224, a38\n\t"
    "s_add_u32 s63, s53, 0x0\n\t"
    "buffer_store_dwordx4 v[4:7], v216, s[20:23], s63 offen offset:1024 sc1\n\t"
    "v_accvgpr_read_b32 v225, a39\n\t"
    "v_cvt_pk_bf16_f32 v80, v218, v219\n\t"
    "v_cvt_pk_bf16_f32 v81, v220, v221\n\t"
    "v_cvt_pk_bf16_f32 v82, v222, v223\n\t"
    "v_cvt_pk_bf16_f32 v83, v224, v225\n\t"
    "v_accvgpr_read_b32 v226, a40\n\t"
    "v_accvgpr_read_b32 v227, a41\n\t"
    "v_accvgpr_read_b32 v228, a42\n\t"
    "v_accvgpr_read_b32 v229, a43\n\t"
    "v_accvgpr_read_b32 v230, a44\n\t"
    "v_accvgpr_read_b32 v231, a45\n\t"
    "v_accvgpr_read_b32 v232, a46\n\t"
    "v_accvgpr_read_b32 v233, a47\n\t"
    "v_cvt_pk_bf16_f32 v84, v226, v227\n\t"
    "v_cvt_pk_bf16_f32 v85, v228, v229\n\t"
    "v_cvt_pk_bf16_f32 v86, v230, v231\n\t"
    "v_cvt_pk_bf16_f32 v87, v232, v233\n\t"
    "v_accvgpr_read_b32 v218, a48\n\t"
    "v_accvgpr_read_b32 v219, a49\n\t"
    "v_accvgpr_read_b32 v220, a50\n\t"
    "s_add_u32 s63, s53, 0x0\n\t"
    "buffer_store_dwordx4 v[8:11], v216, s[20:23], s63 offen offset:2048 sc1\n\t"
    "v_accvgpr_read_b32 v221, a51\n\t"
    "v_accvgpr_read_b32 v222, a52\n\t"
    "v_accvgpr_read_b32 v223, a53\n\t"
    "v_accvgpr_read_b32 v224, a54\n\t"
    "v_accvgpr_read_b32 v225, a55\n\t"
    "v_cvt_pk_bf16_f32 v88, v218, v219\n\t"
    "v_cvt_pk_bf16_f32 v89, v220, v221\n\t"
    "v_cvt_pk_bf16_f32 v90, v222, v223\n\t"
    "v_cvt_pk_bf16_f32 v91, v224, v225\n\t"
    "v_accvgpr_read_b32 v226, a56\n\t"
    "v_accvgpr_read_b32 v227, a57\n\t"
    "v_accvgpr_read_b32 v228, a58\n\t"
    "v_accvgpr_read_b32 v229, a59\n\t"
    "v_accvgpr_read_b32 v230, a60\n\t"
    "v_accvgpr_read_b32 v231, a61\n\t"
    "v_accvgpr_read_b32 v232, a62\n\t"
    "v_accvgpr_read_b32 v233, a63\n\t"
    "v_cvt_pk_bf16_f32 v92, v226, v227\n\t"
    "v_cvt_pk_bf16_f32 v93, v228, v229\n\t"
    "s_add_u32 s63, s53, 0x0\n\t"
    "buffer_store_dwordx4 v[12:15], v216, s[20:23], s63 offen offset:3072 sc1\n\t"
    "v_cvt_pk_bf16_f32 v94, v230, v231\n\t"
    "v_cvt_pk_bf16_f32 v95, v232, v233\n\t"
    "v_accvgpr_read_b32 v218, a64\n\t"
    "v_accvgpr_read_b32 v219, a65\n\t"
    "v_accvgpr_read_b32 v220, a66\n\t"
    "v_accvgpr_read_b32 v221, a67\n\t"
    "v_accvgpr_read_b32 v222, a68\n\t"
    "v_accvgpr_read_b32 v223, a69\n\t"
    "v_accvgpr_read_b32 v224, a70\n\t"
    "v_accvgpr_read_b32 v225, a71\n\t"
    "v_cvt_pk_bf16_f32 v96, v218, v219\n\t"
    "v_cvt_pk_bf16_f32 v97, v220, v221\n\t"
    "v_cvt_pk_bf16_f32 v98, v222, v223\n\t"
    "v_cvt_pk_bf16_f32 v99, v224, v225\n\t"
    "v_accvgpr_read_b32 v226, a72\n\t"
    "v_accvgpr_read_b32 v227, a73\n\t"
    "v_accvgpr_read_b32 v228, a74\n\t"
    "v_accvgpr_read_b32 v229, a75\n\t"
    "v_accvgpr_read_b32 v230, a76\n\t"
    "v_accvgpr_read_b32 v231, a77\n\t"
    "s_add_u32 s63, s53, 0x1000\n\t"
    "buffer_store_dwordx4 v[16:19], v216, s[20:23], s63 offen offset:0 sc1\n\t"
    "v_accvgpr_read_b32 v232, a78\n\t"
    "v_accvgpr_read_b32 v233, a79\n\t"
    "v_cvt_pk_bf16_f32 v100, v226, v227\n\t"
    "v_cvt_pk_bf16_f32 v101, v228, v229\n\t"
    "v_cvt_pk_bf16_f32 v102, v230, v231\n\t"
    "v_cvt_pk_bf16_f32 v103, v232, v233\n\t"
    "v_accvgpr_read_b32 v218, a80\n\t"
    "v_accvgpr_read_b32 v219, a81\n\t"
    "v_accvgpr_read_b32 v220, a82\n\t"
    "v_accvgpr_read_b32 v221, a83\n\t"
    "v_accvgpr_read_b32 v222, a84\n\t"
    "v_accvgpr_read_b32 v223, a85\n\t"
    "v_accvgpr_read_b32 v224, a86\n\t"
    "v_accvgpr_read_b32 v225, a87\n\t"
    "v_cvt_pk_bf16_f32 v104, v218, v219\n\t"
    "v_cvt_pk_bf16_f32 v105, v220, v221\n\t"
    "v_cvt_pk_bf16_f32 v106, v222, v223\n\t"
    "v_cvt_pk_bf16_f32 v107, v224, v225\n\t"
    "v_accvgpr_read_b32 v226, a88\n\t"
    "s_add_u32 s63, s53, 0x1000\n\t"
    "buffer_store_dwordx4 v[20:23], v216, s[20:23], s63 offen offset:1024 sc1\n\t"
    "v_accvgpr_read_b32 v227, a89\n\t"
    "v_accvgpr_read_b32 v228, a90\n\t"
    "v_accvgpr_read_b32 v229, a91\n\t"
    "v_accvgpr_read_b32 v230, a92\n\t"
    "v_accvgpr_read_b32 v231, a93\n\t"
    "v_accvgpr_read_b32 v232, a94\n\t"
    "v_accvgpr_read_b32 v233, a95\n\t"
    "v_cvt_pk_bf16_f32 v108, v226, v227\n\t"
    "v_cvt_pk_bf16_f32 v109, v228, v229\n\t"
    "v_cvt_pk_bf16_f32 v110, v230, v231\n\t"
    "v_cvt_pk_bf16_f32 v111, v232, v233\n\t"
    "v_accvgpr_read_b32 v218, a96\n\t"
    "v_accvgpr_read_b32 v219, a97\n\t"
    "v_accvgpr_read_b32 v220, a98\n\t"
    "v_accvgpr_read_b32 v221, a99\n\t"
    "v_accvgpr_read_b32 v222, a100\n\t"
    "v_accvgpr_read_b32 v223, a101\n\t"
    "v_accvgpr_read_b32 v224, a102\n\t"
    "v_accvgpr_read_b32 v225, a103\n\t"
    "v_cvt_pk_bf16_f32 v112, v218, v219\n\t"
    "s_add_u32 s63, s53, 0x1000\n\t"
    "buffer_store_dwordx4 v[24:27], v216, s[20:23], s63 offen offset:2048 sc1\n\t"
    "v_cvt_pk_bf16_f32 v113, v220, v221\n\t"
    "v_cvt_pk_bf16_f32 v114, v222, v223\n\t"
    "v_cvt_pk_bf16_f32 v115, v224, v225\n\t"
    "v_accvgpr_read_b32 v226, a104\n\t"
    "v_accvgpr_read_b32 v227, a105\n\t"
    "v_accvgpr_read_b32 v228, a106\n\t"
    "v_accvgpr_read_b32 v229, a107\n\t"
    "v_accvgpr_read_b32 v230, a108\n\t"
    "v_accvgpr_read_b32 v231, a109\n\t"
    "v_accvgpr_read_b32 v232, a110\n\t"
    "v_accvgpr_read_b32 v233, a111\n\t"
    "v_cvt_pk_bf16_f32 v116, v226, v227\n\t"
    "v_cvt_pk_bf16_f32 v117, v228, v229\n\t"
    "v_cvt_pk_bf16_f32 v118, v230, v231\n\t"
    "v_cvt_pk_bf16_f32 v119, v232, v233\n\t"
    "v_accvgpr_read_b32 v218, a112\n\t"
    "v_accvgpr_read_b32 v219, a113\n\t"
    "v_accvgpr_read_b32 v220, a114\n\t"
    "v_accvgpr_read_b32 v221, a115\n\t"
    "s_add_u32 s63, s53, 0x1000\n\t"
    "buffer_store_dwordx4 v[28:31], v216, s[20:23], s63 offen offset:3072 sc1\n\t"
    "v_accvgpr_read_b32 v222, a116\n\t"
    "v_accvgpr_read_b32 v223, a117\n\t"
    "v_accvgpr_read_b32 v224, a118\n\t"
    "v_accvgpr_read_b32 v225, a119\n\t"
    "v_cvt_pk_bf16_f32 v120, v218, v219\n\t"
    "v_cvt_pk_bf16_f32 v121, v220, v221\n\t"
    "v_cvt_pk_bf16_f32 v122, v222, v223\n\t"
    "v_cvt_pk_bf16_f32 v123, v224, v225\n\t"
    "v_accvgpr_read_b32 v226, a120\n\t"
    "v_accvgpr_read_b32 v227, a121\n\t"
    "v_accvgpr_read_b32 v228, a122\n\t"
    "v_accvgpr_read_b32 v229, a123\n\t"
    "v_accvgpr_read_b32 v230, a124\n\t"
    "v_accvgpr_read_b32 v231, a125\n\t"
    "v_accvgpr_read_b32 v232, a126\n\t"
    "v_accvgpr_read_b32 v233, a127\n\t"
    "v_cvt_pk_bf16_f32 v124, v226, v227\n\t"
    "v_cvt_pk_bf16_f32 v125, v228, v229\n\t"
    "v_cvt_pk_bf16_f32 v126, v230, v231\n\t"
    "v_cvt_pk_bf16_f32 v127, v232, v233\n\t"
    "s_branch L_xq\n\t"
    "L_kv_v:\n\t"
    "s_waitcnt lgkmcnt(15)\n\t"
    "v_mfma_f32_16x16x32_bf16 v[0:3], a[128:131], a[160:163], 0\n\t"
    "s_waitcnt lgkmcnt(15)\n\t"
    "ds_read_b128 a[192:195], v129 offset:0\n\t"
    "v_mfma_f32_16x16x32_bf16 v[4:7], a[128:131], a[164:167], 0\n\t"
    "s_waitcnt lgkmcnt(15)\n\t"
    "s_add_u32 m0, s44, s48\n\t"
    "v_lshlrev_b32 v142, 16, v164\n\t"
    "v_and_b32 v143, 0xffff0000, v164\n\t"
    "v_mfma_f32_16x16x32_bf16 v[8:11], a[128:131], a[168:171], 0\n\t"
    "s_waitcnt lgkmcnt(15)\n\t"
    "s_add_u32 s39, s42, s45\n\t"
    "ds_read_b64_tr_b16 a[224:225], v130 offset:8192\n\t"
    "v_lshlrev_b32 v144, 16, v165\n\t"
    "v_mfma_f32_16x16x32_bf16 v[12:15], a[128:131], a[172:175], 0\n\t"
    "s_waitcnt lgkmcnt(15)\n\t"
    "buffer_load_dwordx4 v138, s[12:15], s39 offen lds\n\t"
    "v_and_b32 v145, 0xffff0000, v165\n\t"
    "v_mfma_f32_16x16x32_bf16 v[16:19], a[128:131], a[176:179], 0\n\t"
    "s_waitcnt lgkmcnt(13)\n\t"
    "ds_read_b64_tr_b16 a[226:227], v130 offset:9216\n\t"
    "v_lshlrev_b32 v146, 16, v166\n\t"
    "v_mfma_f32_16x16x32_bf16 v[20:23], a[128:131], a[180:183], 0\n\t"
    "s_waitcnt lgkmcnt(12)\n\t"
    "buffer_load_dwordx4 v[236:239], v216, s[12:15], s81 offen offset:0\n\t"
    "v_and_b32 v147, 0xffff0000, v166\n\t"
    "v_mfma_f32_16x16x32_bf16 v[24:27], a[128:131], a[184:187], 0\n\t"
    "s_waitcnt lgkmcnt(10)\n\t"
    "ds_read_b64_tr_b16 a[228:229], v131 offset:8192\n\t"
    "v_lshlrev_b32 v148, 16, v167\n\t"
    "v_mfma_f32_16x16x32_bf16 v[28:31], a[128:131], a[188:191], 0\n\t"
    "s_waitcnt lgkmcnt(10)\n\t"
    "s_add_u32 m0, m0, 0x400\n\t"
    "v_and_b32 v149, 0xffff0000, v167\n\t"
    "v_lshlrev_b32 v150, 16, v156\n\t"
    "v_mfma_f32_16x16x32_bf16 v[32:35], a[132:135], a[160:163], 0\n\t"
    "s_add_u32 s39, s39, 0x2000\n\t"
    "ds_read_b64_tr_b16 a[230:231], v131 offset:9216\n\t"
    "v_and_b32 v151, 0xffff0000, v156\n\t"
    "v_mfma_f32_16x16x32_bf16 v[36:39], a[132:135], a[164:167], 0\n\t"
    "buffer_load_dwordx4 v139, s[12:15], s39 offen lds\n\t"
    "buffer_load_dwordx4 v[236:239], v216, s[12:15], s81 offen offset:1024\n\t"
    "v_mfma_f32_16x16x32_bf16 v[40:43], a[132:135], a[168:171], 0\n\t"
    "ds_read_b64_tr_b16 a[232:233], v132 offset:8192\n\t"
    "v_mul_f32 v168, v150, v142\n\t"
    "v_mfma_f32_16x16x32_bf16 v[44:47], a[132:135], a[172:175], 0\n\t"
    "v_fmac_f32 v168, v151, v143\n\t"
    "v_lshlrev_b32 v150, 16, v157\n\t"
    "v_mfma_f32_16x16x32_bf16 v[48:51], a[132:135], a[176:179], 0\n\t"
    "s_add_u32 s83, s80, 0x0\n\t"
    "ds_read_b64_tr_b16 a[234:235], v132 offset:9216\n\t"
    "v_and_b32 v151, 0xffff0000, v157\n\t"
    "v_mfma_f32_16x16x32_bf16 v[52:55], a[132:135], a[180:183], 0\n\t"
    "s_add_u32 m0, m0, 0x400\n\t"
    "buffer_load_dwordx4 v[236:239], v216, s[16:19], s83 offen\n\t"
    "v_fmac_f32 v168, v150, v144\n\t"
    "v_mfma_f32_16x16x32_bf16 v[56:59], a[132:135], a[184:187], 0\n\t"
    "s_add_u32 s39, s39, 0x2000\n\t"
    "ds_read_b64_tr_b16 a[236:237], v133 offset:8192\n\t"
    "v_fmac_f32 v168, v151, v145\n\t"
    "v_mfma_f32_16x16x32_bf16 v[60:63], a[132:135], a[188:191], 0\n\t"
    "s_waitcnt lgkmcnt(13)\n\t"
    "buffer_load_dwordx4 v138, s[12:15], s39 offen lds\n\t"
    "v_lshlrev_b32 v150, 16, v158\n\t"
    "v_mfma_f32_16x16x32_bf16 v[64:67], a[136:139], a[160:163], 0\n\t"
    "s_add_u32 s83, s80, 0x2000\n\t"
    "ds_read_b64_tr_b16 a[238:239], v133 offset:9216\n\t"
    "v_and_b32 v151, 0xffff0000, v158\n\t"
    "v_mfma_f32_16x16x32_bf16 v[68:71], a[136:139], a[164:167], 0\n\t"
    "buffer_load_dwordx4 v[236:239], v216, s[16:19], s83 offen\n\t"
    "v_fmac_f32 v168, v150, v146\n\t"
    "v_mfma_f32_16x16x32_bf16 v[72:75], a[136:139], a[168:171], 0\n\t"
    "ds_read_b64_tr_b16 a[240:241], v134 offset:8192\n\t"
    "v_fmac_f32 v168, v151, v147\n\t"
    "v_mfma_f32_16x16x32_bf16 v[76:79], a[136:139], a[172:175], 0\n\t"
    "s_add_u32 m0, m0, 0x400\n\t"
    "v_lshlrev_b32 v150, 16, v159\n\t"
    "v_and_b32 v151, 0xffff0000, v159\n\t"
    "v_mfma_f32_16x16x32_bf16 v[80:83], a[136:139], a[176:179], 0\n\t"
    "s_add_u32 s39, s39, 0x2000\n\t"
    "s_add_u32 s83, s80, 0x4000\n\t"
    "ds_read_b64_tr_b16 a[242:243], v134 offset:9216\n\t"
    "v_fmac_f32 v168, v150, v148\n\t"
    "v_mfma_f32_16x16x32_bf16 v[84:87], a[136:139], a[180:183], 0\n\t"
    "buffer_load_dwordx4 v139, s[12:15], s39 offen lds\n\t"
    "buffer_load_dwordx4 v[236:239], v216, s[16:19], s83 offen\n\t"
    "v_mfma_f32_16x16x32_bf16 v[88:91], a[136:139], a[184:187], 0\n\t"
    "ds_read_b64_tr_b16 a[244:245], v135 offset:8192\n\t"
    "v_fmac_f32 v168, v151, v149\n\t"
    "v_mfma_f32_16x16x32_bf16 v[92:95], a[136:139], a[188:191], 0\n\t"
    "s_waitcnt lgkmcnt(15)\n\t"
    "v_lshlrev_b32 v150, 16, v160\n\t"
    "v_and_b32 v151, 0xffff0000, v160\n\t"
    "v_mfma_f32_16x16x32_bf16 v[96:99], a[140:143], a[160:163], 0\n\t"
    "s_add_u32 s83, s80, 0x6000\n\t"
    "ds_read_b64_tr_b16 a[246:247], v135 offset:9216\n\t"
    "v_mul_f32 v169, v150, v142\n\t"
    "v_mfma_f32_16x16x32_bf16 v[100:103], a[140:143], a[164:167], 0\n\t"
    "s_add_u32 m0, m0, 0x400\n\t"
    "buffer_load_dwordx4 v[236:239], v216, s[16:19], s83 offen\n\t"
    "v_fmac_f32 v169, v151, v143\n\t"
    "v_mfma_f32_16x16x32_bf16 v[104:107], a[140:143], a[168:171], 0\n\t"
    "s_add_u32 s39, s39, 0x2000\n\t"
    "ds_read_b64_tr_b16 a[248:249], v136 offset:8192\n\t"
    "v_lshlrev_b32 v150, 16, v161\n\t"
    "v_mfma_f32_16x16x32_bf16 v[108:111], a[140:143], a[172:175], 0\n\t"
    "buffer_load_dwordx4 v138, s[12:15], s39 offen lds\n\t"
    "v_and_b32 v151, 0xffff0000, v161\n\t"
    "v_mfma_f32_16x16x32_bf16 v[112:115], a[140:143], a[176:179], 0\n\t"
    "s_add_u32 s83, s80, 0x8000\n\t"
    "ds_read_b64_tr_b16 a[250:251], v136 offset:9216\n\t"
    "v_fmac_f32 v169, v150, v144\n\t"
    "v_mfma_f32_16x16x32_bf16 v[116:119], a[140:143], a[180:183], 0\n\t"
    "buffer_load_dwordx4 v[236:239], v216, s[16:19], s83 offen\n\t"
    "v_fmac_f32 v169, v151, v145\n\t"
    "v_mfma_f32_16x16x32_bf16 v[120:123], a[140:143], a[184:187], 0\n\t"
    "ds_read_b64_tr_b16 a[252:253], v137 offset:8192\n\t"
    "v_lshlrev_b32 v150, 16, v162\n\t"
    "v_mfma_f32_16x16x32_bf16 v[124:127], a[140:143], a[188:191], 0\n\t"
    "s_waitcnt lgkmcnt(15)\n\t"
    "s_add_u32 m0, m0, 0x400\n\t"
    "v_and_b32 v151, 0xffff0000, v162\n\t"
    "v_fmac_f32 v169, v150, v146\n\t"
    "v_mfma_f32_16x16x32_bf16 a[0:3], a[144:147], a[160:163], 0\n\t"
    "s_add_u32 s39, s39, 0x2000\n\t"
    "s_add_u32 s83, s80, 0xa000\n\t"
    "ds_read_b64_tr_b16 a[254:255], v137 offset:9216\n\t"
    "v_fmac_f32 v169, v151, v147\n\t"
    "v_mfma_f32_16x16x32_bf16 a[4:7], a[144:147], a[164:167], 0\n\t"
    "buffer_load_dwordx4 v139, s[12:15], s39 offen lds\n\t"
    "buffer_load_dwordx4 v[236:239], v216, s[16:19], s83 offen\n\t"
    "v_mfma_f32_16x16x32_bf16 a[8:11], a[144:147], a[168:171], 0\n\t"
    "ds_read_b128 a[196:199], v129 offset:2048\n\t"
    "v_lshlrev_b32 v150, 16, v163\n\t"
    "v_mfma_f32_16x16x32_bf16 a[12:15], a[144:147], a[172:175], 0\n\t"
    "v_and_b32 v151, 0xffff0000, v163\n\t"
    "v_fmac_f32 v169, v150, v148\n\t"
    "v_mfma_f32_16x16x32_bf16 a[16:19], a[144:147], a[176:179], 0\n\t"
    "s_add_u32 s83, s80, 0xc000\n\t"
    "ds_read_b128 a[200:203], v129 offset:4096\n\t"
    "v_fmac_f32 v169, v151, v149\n\t"
    "v_mfma_f32_16x16x32_bf16 a[20:23], a[144:147], a[180:183], 0\n\t"
    "s_add_u32 m0, m0, 0x400\n\t"
    "buffer_load_dwordx4 v[236:239], v216, s[16:19], s83 offen\n\t"
    "v_add_f32_dpp v168, v168, v168 quad_perm:[1,0,3,2] row_mask:0xf bank_mask:0xf\n\t"
    "v_mfma_f32_16x16x32_bf16 a[24:27], a[144:147], a[184:187], 0\n\t"
    "s_add_u32 s39, s39, 0x2000\n\t"
    "ds_read_b128 a[204:207], v129 offset:6144\n\t"
    "v_add_f32_dpp v169, v169, v169 quad_perm:[1,0,3,2] row_mask:0xf bank_mask:0xf\n\t"
    "v_mfma_f32_16x16x32_bf16 a[28:31], a[144:147], a[188:191], 0\n\t"
    "s_waitcnt lgkmcnt(15)\n\t"
    "buffer_load_dwordx4 v138, s[12:15], s39 offen lds\n\t"
    "v_add_f32_dpp v168, v168, v168 quad_perm:[2,3,0,1] row_mask:0xf bank_mask:0xf\n\t"
    "v_mfma_f32_16x16x32_bf16 a[32:35], a[148:151], a[160:163], 0\n\t"
    "s_add_u32 s83, s80, 0xe000\n\t"
    "ds_read_b128 a[208:211], v129 offset:8192\n\t"
    "v_add_f32_dpp v169, v169, v169 quad_perm:[2,3,0,1] row_mask:0xf bank_mask:0xf\n\t"
    "v_mfma_f32_16x16x32_bf16 a[36:39], a[148:151], a[164:167], 0\n\t"
    "buffer_load_dwordx4 v[236:239], v216, s[16:19], s83 offen\n\t"
    "v_add_f32_dpp v168, v168, v168 row_half_mirror row_mask:0xf bank_mask:0xf\n\t"
    "v_mfma_f32_16x16x32_bf16 a[40:43], a[148:151], a[168:171], 0\n\t"
    "ds_read_b128 a[212:215], v129 offset:10240\n\t"
    "v_add_f32_dpp v169, v169, v169 row_half_mirror row_mask:0xf bank_mask:0xf\n\t"
    "v_mfma_f32_16x16x32_bf16 a[44:47], a[148:151], a[172:175], 0\n\t"
    "s_add_u32 m0, m0, 0x400\n\t"
    "v_add_f32_dpp v168, v168, v168 row_mirror row_mask:0xf bank_mask:0xf\n\t"
    "v_mfma_f32_16x16x32_bf16 a[48:51], a[148:151], a[176:179], 0\n\t"
    "s_add_u32 s39, s39, 0x2000\n\t"
    "ds_read_b128 a[216:219], v129 offset:12288\n\t"
    "v_add_f32_dpp v169, v169, v169 row_mirror row_mask:0xf bank_mask:0xf\n\t"
    "v_mfma_f32_16x16x32_bf16 a[52:55], a[148:151], a[180:183], 0\n\t"
    "buffer_load_dwordx4 v139, s[12:15], s39 offen lds\n\t"
    "buffer_load_dwordx4 v[236:239], v216, s[8:11], s81 offen\n\t"
    "v_mfma_f32_16x16x32_bf16 a[56:59], a[148:151], a[184:187], 0\n\t"
    "s_add_u32 s45, s45, 0x80\n\t"
    "ds_read_b128 a[220:223], v129 offset:14336\n\t"
    "v_mov_b32 v152, v168\n\t"
    "v_mfma_f32_16x16x32_bf16 a[60:63], a[148:151], a[188:191], 0\n\t"
    "s_waitcnt lgkmcnt(15)\n\t"
    "s_add_u32 s48, s48, 0x8000\n\t"
    "v_mov_b32 v153, v169\n\t"
    "v_mfma_f32_16x16x32_bf16 a[64:67], a[152:155], a[160:163], 0\n\t"
    "s_cmp_eq_u32 s48, 0x28000\n\t"
    "s_cselect_b32 s48, 0x10000, s48\n\t"
    "v_permlane16_swap_b32 v168, v152\n\t"
    "v_mfma_f32_16x16x32_bf16 a[68:71], a[152:155], a[164:167], 0\n\t"
    "buffer_load_dwordx4 v[236:239], v216, s[8:11], s81 offen offset:1024\n\t"
    "v_permlane16_swap_b32 v169, v153\n\t"
    "v_mfma_f32_16x16x32_bf16 a[72:75], a[152:155], a[168:171], 0\n\t"
    "v_add_f32 v168, v168, v152\n\t"
    "v_add_f32 v169, v169, v153\n\t"
    "v_mfma_f32_16x16x32_bf16 a[76:79], a[152:155], a[172:175], 0\n\t"
    "v_mov_b32 v152, v168\n\t"
    "v_mov_b32 v153, v169\n\t"
    "v_mfma_f32_16x16x32_bf16 a[80:83], a[152:155], a[176:179], 0\n\t"
    "v_permlane32_swap_b32 v168, v152\n\t"
    "v_mfma_f32_16x16x32_bf16 a[84:87], a[152:155], a[180:183], 0\n\t"
    "buffer_load_dwordx4 v[236:239], v216, s[88:91], s81 offen\n\t"
    "v_permlane32_swap_b32 v169, v153\n\t"
    "v_mfma_f32_16x16x32_bf16 a[88:91], a[152:155], a[184:187], 0\n\t"
    "v_add_f32 v168, v168, v152\n\t"
    "v_add_f32 v169, v169, v153\n\t"
    "v_mfma_f32_16x16x32_bf16 a[92:95], a[152:155], a[188:191], 0\n\t"
    "s_waitcnt lgkmcnt(15)\n\t"
    "v_mul_f32 v168, 0xbfb8aa3b, v168\n\t"
    "v_mul_f32 v169, 0xbfb8aa3b, v169\n\t"
    "v_mfma_f32_16x16x32_bf16 a[96:99], a[156:159], a[160:163], 0\n\t"
    "v_exp_f32 v168, v168\n\t"
    "v_mfma_f32_16x16x32_bf16 a[100:103], a[156:159], a[164:167], 0\n\t"
    "buffer_load_dwordx4 v[236:239], v216, s[88:91], s81 offen offset:1024\n\t"
    "v_exp_f32 v169, v169\n\t"
    "v_mfma_f32_16x16x32_bf16 a[104:107], a[156:159], a[168:171], 0\n\t"
    "v_add_f32 v168, 1.0, v168\n\t"
    "v_add_f32 v169, 1.0, v169\n\t"
    "v_mfma_f32_16x16x32_bf16 a[108:111], a[156:159], a[172:175], 0\n\t"
    "v_rcp_f32 v168, v168\n\t"
    "v_mfma_f32_16x16x32_bf16 a[112:115], a[156:159], a[176:179], 0\n\t"
    "v_rcp_f32 v169, v169\n\t"
    "v_mfma_f32_16x16x32_bf16 a[116:119], a[156:159], a[180:183], 0\n\t"
    "v_mov_b32 v142, 0\n\t"
    "v_mfma_f32_16x16x32_bf16 a[120:123], a[156:159], a[184:187], 0\n\t"
    "v_mfma_f32_16x16x32_bf16 a[124:127], a[156:159], a[188:191], 0\n\t"
    "s_waitcnt vmcnt(22) lgkmcnt(0)\n\t"
    "s_barrier\n\t"
    "v_mfma_f32_16x16x32_bf16 v[0:3], a[192:195], a[224:227], v[0:3]\n\t"
    "s_cmp_eq_u32 s49, 0x20000\n\t"
    "s_cselect_b32 s50, s51, s52\n\t"
    "v_xor_b32 v130, 0x8000, v130\n\t"
    "v_xor_b32 v131, 0x8000, v131\n\t"
    "v_mfma_f32_16x16x32_bf16 v[4:7], a[192:195], a[228:231], v[4:7]\n\t"
    "s_add_u32 s49, s49, s50\n\t"
    "s_add_u32 m0, s44, s47\n\t"
    "buffer_store_dword v168, v142, s[28:31], s96 offen sc1\n\t"
    "v_xor_b32 v132, 0x8000, v132\n\t"
    "v_xor_b32 v133, 0x8000, v133\n\t"
    "v_mfma_f32_16x16x32_bf16 v[8:11], a[192:195], a[232:235], v[8:11]\n\t"
    "s_add_u32 s39, s43, s46\n\t"
    "buffer_store_dword v169, v142, s[28:31], s96 offen offset:4 sc1\n\t"
    "v_xor_b32 v134, 0x8000, v134\n\t"
    "v_xor_b32 v135, 0x8000, v135\n\t"
    "v_mfma_f32_16x16x32_bf16 v[12:15], a[192:195], a[236:239], v[12:15]\n\t"
    "buffer_load_dwordx4 v140, s[16:19], s39 offen lds\n\t"
    "v_xor_b32 v136, 0x8000, v136\n\t"
    "v_xor_b32 v137, 0x8000, v137\n\t"
    "v_mfma_f32_16x16x32_bf16 v[16:19], a[192:195], a[240:243], v[16:19]\n\t"
    "v_add_u32 v128, s50, v128\n\t"
    "v_add_u32 v129, s50, v129\n\t"
    "v_mfma_f32_16x16x32_bf16 v[20:23], a[192:195], a[244:247], v[20:23]\n\t"
    "v_mfma_f32_16x16x32_bf16 v[24:27], a[192:195], a[248:251], v[24:27]\n\t"
    "ds_read_b128 a[128:131], v128 offset:0\n\t"
    "v_mfma_f32_16x16x32_bf16 v[28:31], a[192:195], a[252:255], v[28:31]\n\t"
    "s_add_u32 m0, m0, 0x400\n\t"
    "v_mfma_f32_16x16x32_bf16 v[32:35], a[196:199], a[224:227], v[32:35]\n\t"
    "s_add_u32 s39, s39, 0x8000\n\t"
    "ds_read_b64_tr_b16 a[160:161], v130 offset:0\n\t"
    "v_mfma_f32_16x16x32_bf16 v[36:39], a[196:199], a[228:231], v[36:39]\n\t"
    "buffer_load_dwordx4 v140, s[16:19], s39 offen lds\n\t"
    "v_mfma_f32_16x16x32_bf16 v[40:43], a[196:199], a[232:235], v[40:43]\n\t"
    "ds_read_b64_tr_b16 a[162:163], v130 offset:1024\n\t"
    "v_mfma_f32_16x16x32_bf16 v[44:47], a[196:199], a[236:239], v[44:47]\n\t"
    "v_mfma_f32_16x16x32_bf16 v[48:51], a[196:199], a[240:243], v[48:51]\n\t"
    "ds_read_b64_tr_b16 a[164:165], v131 offset:0\n\t"
    "v_mfma_f32_16x16x32_bf16 v[52:55], a[196:199], a[244:247], v[52:55]\n\t"
    "s_add_u32 m0, m0, 0x400\n\t"
    "v_mfma_f32_16x16x32_bf16 v[56:59], a[196:199], a[248:251], v[56:59]\n\t"
    "s_add_u32 s39, s39, 0x8000\n\t"
    "ds_read_b64_tr_b16 a[166:167], v131 offset:1024\n\t"
    "v_mfma_f32_16x16x32_bf16 v[60:63], a[196:199], a[252:255], v[60:63]\n\t"
    "buffer_load_dwordx4 v141, s[16:19], s39 offen lds\n\t"
    "v_mfma_f32_16x16x32_bf16 v[64:67], a[200:203], a[224:227], v[64:67]\n\t"
    "ds_read_b64_tr_b16 a[168:169], v132 offset:0\n\t"
    "v_mfma_f32_16x16x32_bf16 v[68:71], a[200:203], a[228:231], v[68:71]\n\t"
    "v_mfma_f32_16x16x32_bf16 v[72:75], a[200:203], a[232:235], v[72:75]\n\t"
    "ds_read_b64_tr_b16 a[170:171], v132 offset:1024\n\t"
    "v_mfma_f32_16x16x32_bf16 v[76:79], a[200:203], a[236:239], v[76:79]\n\t"
    "s_add_u32 m0, m0, 0x400\n\t"
    "v_mfma_f32_16x16x32_bf16 v[80:83], a[200:203], a[240:243], v[80:83]\n\t"
    "s_add_u32 s39, s39, 0x8000\n\t"
    "ds_read_b64_tr_b16 a[172:173], v133 offset:0\n\t"
    "v_mfma_f32_16x16x32_bf16 v[84:87], a[200:203], a[244:247], v[84:87]\n\t"
    "buffer_load_dwordx4 v141, s[16:19], s39 offen lds\n\t"
    "v_mfma_f32_16x16x32_bf16 v[88:91], a[200:203], a[248:251], v[88:91]\n\t"
    "ds_read_b64_tr_b16 a[174:175], v133 offset:1024\n\t"
    "v_mfma_f32_16x16x32_bf16 v[92:95], a[200:203], a[252:255], v[92:95]\n\t"
    "v_mfma_f32_16x16x32_bf16 v[96:99], a[204:207], a[224:227], v[96:99]\n\t"
    "ds_read_b64_tr_b16 a[176:177], v134 offset:0\n\t"
    "v_mfma_f32_16x16x32_bf16 v[100:103], a[204:207], a[228:231], v[100:103]\n\t"
    "s_add_u32 m0, m0, 0x400\n\t"
    "v_mfma_f32_16x16x32_bf16 v[104:107], a[204:207], a[232:235], v[104:107]\n\t"
    "s_add_u32 s39, s39, 0x8000\n\t"
    "ds_read_b64_tr_b16 a[178:179], v134 offset:1024\n\t"
    "v_mfma_f32_16x16x32_bf16 v[108:111], a[204:207], a[236:239], v[108:111]\n\t"
    "buffer_load_dwordx4 v140, s[16:19], s39 offen lds\n\t"
    "v_mfma_f32_16x16x32_bf16 v[112:115], a[204:207], a[240:243], v[112:115]\n\t"
    "ds_read_b64_tr_b16 a[180:181], v135 offset:0\n\t"
    "v_mfma_f32_16x16x32_bf16 v[116:119], a[204:207], a[244:247], v[116:119]\n\t"
    "v_mfma_f32_16x16x32_bf16 v[120:123], a[204:207], a[248:251], v[120:123]\n\t"
    "ds_read_b64_tr_b16 a[182:183], v135 offset:1024\n\t"
    "v_mfma_f32_16x16x32_bf16 v[124:127], a[204:207], a[252:255], v[124:127]\n\t"
    "s_add_u32 m0, m0, 0x400\n\t"
    "v_mfma_f32_16x16x32_bf16 a[0:3], a[208:211], a[224:227], a[0:3]\n\t"
    "s_add_u32 s39, s39, 0x8000\n\t"
    "ds_read_b64_tr_b16 a[184:185], v136 offset:0\n\t"
    "v_mfma_f32_16x16x32_bf16 a[4:7], a[208:211], a[228:231], a[4:7]\n\t"
    "buffer_load_dwordx4 v140, s[16:19], s39 offen lds\n\t"
    "v_mfma_f32_16x16x32_bf16 a[8:11], a[208:211], a[232:235], a[8:11]\n\t"
    "ds_read_b64_tr_b16 a[186:187], v136 offset:1024\n\t"
    "v_mfma_f32_16x16x32_bf16 a[12:15], a[208:211], a[236:239], a[12:15]\n\t"
    "v_mfma_f32_16x16x32_bf16 a[16:19], a[208:211], a[240:243], a[16:19]\n\t"
    "ds_read_b64_tr_b16 a[188:189], v137 offset:0\n\t"
    "v_mfma_f32_16x16x32_bf16 a[20:23], a[208:211], a[244:247], a[20:23]\n\t"
    "s_add_u32 m0, m0, 0x400\n\t"
    "v_mfma_f32_16x16x32_bf16 a[24:27], a[208:211], a[248:251], a[24:27]\n\t"
    "s_add_u32 s39, s39, 0x8000\n\t"
    "ds_read_b64_tr_b16 a[190:191], v137 offset:1024\n\t"
    "v_mfma_f32_16x16x32_bf16 a[28:31], a[208:211], a[252:255], a[28:31]\n\t"
    "buffer_load_dwordx4 v141, s[16:19], s39 offen lds\n\t"
    "v_mfma_f32_16x16x32_bf16 a[32:35], a[212:215], a[224:227], a[32:35]\n\t"
    "ds_read_b128 a[132:135], v128 offset:2048\n\t"
    "v_mfma_f32_16x16x32_bf16 a[36:39], a[212:215], a[228:231], a[36:39]\n\t"
    "v_mfma_f32_16x16x32_bf16 a[40:43], a[212:215], a[232:235], a[40:43]\n\t"
    "ds_read_b128 a[136:139], v128 offset:4096\n\t"
    "v_mfma_f32_16x16x32_bf16 a[44:47], a[212:215], a[236:239], a[44:47]\n\t"
    "s_add_u32 m0, m0, 0x400\n\t"
    "v_mfma_f32_16x16x32_bf16 a[48:51], a[212:215], a[240:243], a[48:51]\n\t"
    "s_add_u32 s39, s39, 0x8000\n\t"
    "ds_read_b128 a[140:143], v128 offset:6144\n\t"
    "v_mfma_f32_16x16x32_bf16 a[52:55], a[212:215], a[244:247], a[52:55]\n\t"
    "buffer_load_dwordx4 v141, s[16:19], s39 offen lds\n\t"
    "v_mfma_f32_16x16x32_bf16 a[56:59], a[212:215], a[248:251], a[56:59]\n\t"
    "s_add_u32 s46, s46, 0x80000\n\t"
    "ds_read_b128 a[144:147], v128 offset:8192\n\t"
    "v_mfma_f32_16x16x32_bf16 a[60:63], a[212:215], a[252:255], a[60:63]\n\t"
    "s_xor_b32 s47, s47, 0x8000\n\t"
    "v_mfma_f32_16x16x32_bf16 a[64:67], a[216:219], a[224:227], a[64:67]\n\t"
    "ds_read_b128 a[148:151], v128 offset:10240\n\t"
    "v_mfma_f32_16x16x32_bf16 a[68:71], a[216:219], a[228:231], a[68:71]\n\t"
    "v_mfma_f32_16x16x32_bf16 a[72:75], a[216:219], a[232:235], a[72:75]\n\t"
    "ds_read_b128 a[152:155], v128 offset:12288\n\t"
    "v_mfma_f32_16x16x32_bf16 a[76:79], a[216:219], a[236:239], a[76:79]\n\t"
    "v_mfma_f32_16x16x32_bf16 a[80:83], a[216:219], a[240:243], a[80:83]\n\t"
    "ds_read_b128 a[156:159], v128 offset:14336\n\t"
    "v_mfma_f32_16x16x32_bf16 a[84:87], a[216:219], a[244:247], a[84:87]\n\t"
    "v_mfma_f32_16x16x32_bf16 a[88:91], a[216:219], a[248:251], a[88:91]\n\t"
    "v_mfma_f32_16x16x32_bf16 a[92:95], a[216:219], a[252:255], a[92:95]\n\t"
    "v_mfma_f32_16x16x32_bf16 a[96:99], a[220:223], a[224:227], a[96:99]\n\t"
    "v_mfma_f32_16x16x32_bf16 a[100:103], a[220:223], a[228:231], a[100:103]\n\t"
    "v_mfma_f32_16x16x32_bf16 a[104:107], a[220:223], a[232:235], a[104:107]\n\t"
    "v_mfma_f32_16x16x32_bf16 a[108:111], a[220:223], a[236:239], a[108:111]\n\t"
    "v_mfma_f32_16x16x32_bf16 a[112:115], a[220:223], a[240:243], a[112:115]\n\t"
    "v_mfma_f32_16x16x32_bf16 a[116:119], a[220:223], a[244:247], a[116:119]\n\t"
    "v_mfma_f32_16x16x32_bf16 a[120:123], a[220:223], a[248:251], a[120:123]\n\t"
    "v_mfma_f32_16x16x32_bf16 a[124:127], a[220:223], a[252:255], a[124:127]\n\t"
    "L_kv_vloop:\n\t"
    "s_waitcnt lgkmcnt(15)\n\t"
    "v_mfma_f32_16x16x32_bf16 v[0:3], a[128:131], a[160:163], v[0:3]\n\t"
    "s_waitcnt lgkmcnt(15)\n\t"
    "ds_read_b128 a[192:195], v129 offset:0\n\t"
    "v_mfma_f32_16x16x32_bf16 v[4:7], a[128:131], a[164:167], v[4:7]\n\t"
    "s_waitcnt lgkmcnt(15)\n\t"
    "s_add_u32 m0, s44, s48\n\t"
    "v_mfma_f32_16x16x32_bf16 v[8:11], a[128:131], a[168:171], v[8:11]\n\t"
    "s_waitcnt lgkmcnt(15)\n\t"
    "s_add_u32 s39, s42, s45\n\t"
    "ds_read_b64_tr_b16 a[224:225], v130 offset:8192\n\t"
    "v_mfma_f32_16x16x32_bf16 v[12:15], a[128:131], a[172:175], v[12:15]\n\t"
    "s_waitcnt lgkmcnt(15)\n\t"
    "buffer_load_dwordx4 v138, s[12:15], s39 offen lds\n\t"
    "v_mfma_f32_16x16x32_bf16 v[16:19], a[128:131], a[176:179], v[16:19]\n\t"
    "s_waitcnt lgkmcnt(13)\n\t"
    "ds_read_b64_tr_b16 a[226:227], v130 offset:9216\n\t"
    "v_mfma_f32_16x16x32_bf16 v[20:23], a[128:131], a[180:183], v[20:23]\n\t"
    "s_waitcnt lgkmcnt(12)\n\t"
    "v_mfma_f32_16x16x32_bf16 v[24:27], a[128:131], a[184:187], v[24:27]\n\t"
    "s_waitcnt lgkmcnt(10)\n\t"
    "ds_read_b64_tr_b16 a[228:229], v131 offset:8192\n\t"
    "v_mfma_f32_16x16x32_bf16 v[28:31], a[128:131], a[188:191], v[28:31]\n\t"
    "s_waitcnt lgkmcnt(10)\n\t"
    "s_add_u32 m0, m0, 0x400\n\t"
    "v_mfma_f32_16x16x32_bf16 v[32:35], a[132:135], a[160:163], v[32:35]\n\t"
    "s_add_u32 s39, s39, 0x2000\n\t"
    "ds_read_b64_tr_b16 a[230:231], v131 offset:9216\n\t"
    "v_mfma_f32_16x16x32_bf16 v[36:39], a[132:135], a[164:167], v[36:39]\n\t"
    "buffer_load_dwordx4 v139, s[12:15], s39 offen lds\n\t"
    "v_mfma_f32_16x16x32_bf16 v[40:43], a[132:135], a[168:171], v[40:43]\n\t"
    "ds_read_b64_tr_b16 a[232:233], v132 offset:8192\n\t"
    "v_mfma_f32_16x16x32_bf16 v[44:47], a[132:135], a[172:175], v[44:47]\n\t"
    "v_mfma_f32_16x16x32_bf16 v[48:51], a[132:135], a[176:179], v[48:51]\n\t"
    "ds_read_b64_tr_b16 a[234:235], v132 offset:9216\n\t"
    "v_mfma_f32_16x16x32_bf16 v[52:55], a[132:135], a[180:183], v[52:55]\n\t"
    "s_add_u32 m0, m0, 0x400\n\t"
    "v_mfma_f32_16x16x32_bf16 v[56:59], a[132:135], a[184:187], v[56:59]\n\t"
    "s_add_u32 s39, s39, 0x2000\n\t"
    "ds_read_b64_tr_b16 a[236:237], v133 offset:8192\n\t"
    "v_mfma_f32_16x16x32_bf16 v[60:63], a[132:135], a[188:191], v[60:63]\n\t"
    "s_waitcnt lgkmcnt(13)\n\t"
    "buffer_load_dwordx4 v138, s[12:15], s39 offen lds\n\t"
    "v_mfma_f32_16x16x32_bf16 v[64:67], a[136:139], a[160:163], v[64:67]\n\t"
    "ds_read_b64_tr_b16 a[238:239], v133 offset:9216\n\t"
    "v_mfma_f32_16x16x32_bf16 v[68:71], a[136:139], a[164:167], v[68:71]\n\t"
    "v_mfma_f32_16x16x32_bf16 v[72:75], a[136:139], a[168:171], v[72:75]\n\t"
    "ds_read_b64_tr_b16 a[240:241], v134 offset:8192\n\t"
    "v_mfma_f32_16x16x32_bf16 v[76:79], a[136:139], a[172:175], v[76:79]\n\t"
    "s_add_u32 m0, m0, 0x400\n\t"
    "v_mfma_f32_16x16x32_bf16 v[80:83], a[136:139], a[176:179], v[80:83]\n\t"
    "s_add_u32 s39, s39, 0x2000\n\t"
    "ds_read_b64_tr_b16 a[242:243], v134 offset:9216\n\t"
    "v_mfma_f32_16x16x32_bf16 v[84:87], a[136:139], a[180:183], v[84:87]\n\t"
    "buffer_load_dwordx4 v139, s[12:15], s39 offen lds\n\t"
    "v_mfma_f32_16x16x32_bf16 v[88:91], a[136:139], a[184:187], v[88:91]\n\t"
    "ds_read_b64_tr_b16 a[244:245], v135 offset:8192\n\t"
    "v_mfma_f32_16x16x32_bf16 v[92:95], a[136:139], a[188:191], v[92:95]\n\t"
    "s_waitcnt lgkmcnt(15)\n\t"
    "v_mfma_f32_16x16x32_bf16 v[96:99], a[140:143], a[160:163], v[96:99]\n\t"
    "ds_read_b64_tr_b16 a[246:247], v135 offset:9216\n\t"
    "v_mfma_f32_16x16x32_bf16 v[100:103], a[140:143], a[164:167], v[100:103]\n\t"
    "s_add_u32 m0, m0, 0x400\n\t"
    "v_mfma_f32_16x16x32_bf16 v[104:107], a[140:143], a[168:171], v[104:107]\n\t"
    "s_add_u32 s39, s39, 0x2000\n\t"
    "ds_read_b64_tr_b16 a[248:249], v136 offset:8192\n\t"
    "v_mfma_f32_16x16x32_bf16 v[108:111], a[140:143], a[172:175], v[108:111]\n\t"
    "buffer_load_dwordx4 v138, s[12:15], s39 offen lds\n\t"
    "v_mfma_f32_16x16x32_bf16 v[112:115], a[140:143], a[176:179], v[112:115]\n\t"
    "ds_read_b64_tr_b16 a[250:251], v136 offset:9216\n\t"
    "v_mfma_f32_16x16x32_bf16 v[116:119], a[140:143], a[180:183], v[116:119]\n\t"
    "v_mfma_f32_16x16x32_bf16 v[120:123], a[140:143], a[184:187], v[120:123]\n\t"
    "ds_read_b64_tr_b16 a[252:253], v137 offset:8192\n\t"
    "v_mfma_f32_16x16x32_bf16 v[124:127], a[140:143], a[188:191], v[124:127]\n\t"
    "s_waitcnt lgkmcnt(15)\n\t"
    "s_add_u32 m0, m0, 0x400\n\t"
    "v_mfma_f32_16x16x32_bf16 a[0:3], a[144:147], a[160:163], a[0:3]\n\t"
    "s_add_u32 s39, s39, 0x2000\n\t"
    "ds_read_b64_tr_b16 a[254:255], v137 offset:9216\n\t"
    "v_mfma_f32_16x16x32_bf16 a[4:7], a[144:147], a[164:167], a[4:7]\n\t"
    "buffer_load_dwordx4 v139, s[12:15], s39 offen lds\n\t"
    "v_mfma_f32_16x16x32_bf16 a[8:11], a[144:147], a[168:171], a[8:11]\n\t"
    "ds_read_b128 a[196:199], v129 offset:2048\n\t"
    "v_mfma_f32_16x16x32_bf16 a[12:15], a[144:147], a[172:175], a[12:15]\n\t"
    "v_mfma_f32_16x16x32_bf16 a[16:19], a[144:147], a[176:179], a[16:19]\n\t"
    "ds_read_b128 a[200:203], v129 offset:4096\n\t"
    "v_mfma_f32_16x16x32_bf16 a[20:23], a[144:147], a[180:183], a[20:23]\n\t"
    "s_add_u32 m0, m0, 0x400\n\t"
    "v_mfma_f32_16x16x32_bf16 a[24:27], a[144:147], a[184:187], a[24:27]\n\t"
    "s_add_u32 s39, s39, 0x2000\n\t"
    "ds_read_b128 a[204:207], v129 offset:6144\n\t"
    "v_mfma_f32_16x16x32_bf16 a[28:31], a[144:147], a[188:191], a[28:31]\n\t"
    "s_waitcnt lgkmcnt(15)\n\t"
    "buffer_load_dwordx4 v138, s[12:15], s39 offen lds\n\t"
    "v_mfma_f32_16x16x32_bf16 a[32:35], a[148:151], a[160:163], a[32:35]\n\t"
    "ds_read_b128 a[208:211], v129 offset:8192\n\t"
    "v_mfma_f32_16x16x32_bf16 a[36:39], a[148:151], a[164:167], a[36:39]\n\t"
    "v_mfma_f32_16x16x32_bf16 a[40:43], a[148:151], a[168:171], a[40:43]\n\t"
    "ds_read_b128 a[212:215], v129 offset:10240\n\t"
    "v_mfma_f32_16x16x32_bf16 a[44:47], a[148:151], a[172:175], a[44:47]\n\t"
    "s_add_u32 m0, m0, 0x400\n\t"
    "v_mfma_f32_16x16x32_bf16 a[48:51], a[148:151], a[176:179], a[48:51]\n\t"
    "s_add_u32 s39, s39, 0x2000\n\t"
    "ds_read_b128 a[216:219], v129 offset:12288\n\t"
    "v_mfma_f32_16x16x32_bf16 a[52:55], a[148:151], a[180:183], a[52:55]\n\t"
    "buffer_load_dwordx4 v139, s[12:15], s39 offen lds\n\t"
    "v_mfma_f32_16x16x32_bf16 a[56:59], a[148:151], a[184:187], a[56:59]\n\t"
    "s_add_u32 s45, s45, 0x80\n\t"
    "ds_read_b128 a[220:223], v129 offset:14336\n\t"
    "v_mfma_f32_16x16x32_bf16 a[60:63], a[148:151], a[188:191], a[60:63]\n\t"
    "s_waitcnt lgkmcnt(15)\n\t"
    "s_add_u32 s48, s48, 0x8000\n\t"
    "buffer_load_dwordx4 v[236:239], v216, s[84:87], s82 offen\n\t"
    "v_mfma_f32_16x16x32_bf16 a[64:67], a[152:155], a[160:163], a[64:67]\n\t"
    "s_cmp_eq_u32 s48, 0x28000\n\t"
    "s_cselect_b32 s48, 0x10000, s48\n\t"
    "v_mfma_f32_16x16x32_bf16 a[68:71], a[152:155], a[164:167], a[68:71]\n\t"
    "v_mfma_f32_16x16x32_bf16 a[72:75], a[152:155], a[168:171], a[72:75]\n\t"
    "s_add_u32 s82, s82, 0x2000\n\t"
    "v_mfma_f32_16x16x32_bf16 a[76:79], a[152:155], a[172:175], a[76:79]\n\t"
    "v_mfma_f32_16x16x32_bf16 a[80:83], a[152:155], a[176:179], a[80:83]\n\t"
    "v_mfma_f32_16x16x32_bf16 a[84:87], a[152:155], a[180:183], a[84:87]\n\t"
    "v_mfma_f32_16x16x32_bf16 a[88:91], a[152:155], a[184:187], a[88:91]\n\t"
    "v_mfma_f32_16x16x32_bf16 a[92:95], a[152:155], a[188:191], a[92:95]\n\t"
    "s_waitcnt lgkmcnt(15)\n\t"
    "v_mfma_f32_16x16x32_bf16 a[96:99], a[156:159], a[160:163], a[96:99]\n\t"
    "v_mfma_f32_16x16x32_bf16 a[100:103], a[156:159], a[164:167], a[100:103]\n\t"
    "v_mfma_f32_16x16x32_bf16 a[104:107], a[156:159], a[168:171], a[104:107]\n\t"
    "v_mfma_f32_16x16x32_bf16 a[108:111], a[156:159], a[172:175], a[108:111]\n\t"
    "v_mfma_f32_16x16x32_bf16 a[112:115], a[156:159], a[176:179], a[112:115]\n\t"
    "v_mfma_f32_16x16x32_bf16 a[116:119], a[156:159], a[180:183], a[116:119]\n\t"
    "v_mfma_f32_16x16x32_bf16 a[120:123], a[156:159], a[184:187], a[120:123]\n\t"
    "v_mfma_f32_16x16x32_bf16 a[124:127], a[156:159], a[188:191], a[124:127]\n\t"
    "s_waitcnt vmcnt(9) lgkmcnt(0)\n\t"
    "s_barrier\n\t"
    "v_mfma_f32_16x16x32_bf16 v[0:3], a[192:195], a[224:227], v[0:3]\n\t"
    "s_cmp_eq_u32 s49, 0x20000\n\t"
    "s_cselect_b32 s50, s51, s52\n\t"
    "v_xor_b32 v130, 0x8000, v130\n\t"
    "v_xor_b32 v131, 0x8000, v131\n\t"
    "v_mfma_f32_16x16x32_bf16 v[4:7], a[192:195], a[228:231], v[4:7]\n\t"
    "s_add_u32 s49, s49, s50\n\t"
    "s_add_u32 m0, s44, s47\n\t"
    "v_xor_b32 v132, 0x8000, v132\n\t"
    "v_xor_b32 v133, 0x8000, v133\n\t"
    "v_mfma_f32_16x16x32_bf16 v[8:11], a[192:195], a[232:235], v[8:11]\n\t"
    "s_add_u32 s39, s43, s46\n\t"
    "v_xor_b32 v134, 0x8000, v134\n\t"
    "v_xor_b32 v135, 0x8000, v135\n\t"
    "v_mfma_f32_16x16x32_bf16 v[12:15], a[192:195], a[236:239], v[12:15]\n\t"
    "buffer_load_dwordx4 v140, s[16:19], s39 offen lds\n\t"
    "v_xor_b32 v136, 0x8000, v136\n\t"
    "v_xor_b32 v137, 0x8000, v137\n\t"
    "v_mfma_f32_16x16x32_bf16 v[16:19], a[192:195], a[240:243], v[16:19]\n\t"
    "v_add_u32 v128, s50, v128\n\t"
    "v_add_u32 v129, s50, v129\n\t"
    "v_mfma_f32_16x16x32_bf16 v[20:23], a[192:195], a[244:247], v[20:23]\n\t"
    "v_mfma_f32_16x16x32_bf16 v[24:27], a[192:195], a[248:251], v[24:27]\n\t"
    "ds_read_b128 a[128:131], v128 offset:0\n\t"
    "v_mfma_f32_16x16x32_bf16 v[28:31], a[192:195], a[252:255], v[28:31]\n\t"
    "s_add_u32 m0, m0, 0x400\n\t"
    "v_mfma_f32_16x16x32_bf16 v[32:35], a[196:199], a[224:227], v[32:35]\n\t"
    "s_add_u32 s39, s39, 0x8000\n\t"
    "ds_read_b64_tr_b16 a[160:161], v130 offset:0\n\t"
    "v_mfma_f32_16x16x32_bf16 v[36:39], a[196:199], a[228:231], v[36:39]\n\t"
    "buffer_load_dwordx4 v140, s[16:19], s39 offen lds\n\t"
    "v_mfma_f32_16x16x32_bf16 v[40:43], a[196:199], a[232:235], v[40:43]\n\t"
    "ds_read_b64_tr_b16 a[162:163], v130 offset:1024\n\t"
    "v_mfma_f32_16x16x32_bf16 v[44:47], a[196:199], a[236:239], v[44:47]\n\t"
    "v_mfma_f32_16x16x32_bf16 v[48:51], a[196:199], a[240:243], v[48:51]\n\t"
    "ds_read_b64_tr_b16 a[164:165], v131 offset:0\n\t"
    "v_mfma_f32_16x16x32_bf16 v[52:55], a[196:199], a[244:247], v[52:55]\n\t"
    "s_add_u32 m0, m0, 0x400\n\t"
    "v_mfma_f32_16x16x32_bf16 v[56:59], a[196:199], a[248:251], v[56:59]\n\t"
    "s_add_u32 s39, s39, 0x8000\n\t"
    "ds_read_b64_tr_b16 a[166:167], v131 offset:1024\n\t"
    "v_mfma_f32_16x16x32_bf16 v[60:63], a[196:199], a[252:255], v[60:63]\n\t"
    "buffer_load_dwordx4 v141, s[16:19], s39 offen lds\n\t"
    "v_mfma_f32_16x16x32_bf16 v[64:67], a[200:203], a[224:227], v[64:67]\n\t"
    "ds_read_b64_tr_b16 a[168:169], v132 offset:0\n\t"
    "v_mfma_f32_16x16x32_bf16 v[68:71], a[200:203], a[228:231], v[68:71]\n\t"
    "v_mfma_f32_16x16x32_bf16 v[72:75], a[200:203], a[232:235], v[72:75]\n\t"
    "ds_read_b64_tr_b16 a[170:171], v132 offset:1024\n\t"
    "v_mfma_f32_16x16x32_bf16 v[76:79], a[200:203], a[236:239], v[76:79]\n\t"
    "s_add_u32 m0, m0, 0x400\n\t"
    "v_mfma_f32_16x16x32_bf16 v[80:83], a[200:203], a[240:243], v[80:83]\n\t"
    "s_add_u32 s39, s39, 0x8000\n\t"
    "ds_read_b64_tr_b16 a[172:173], v133 offset:0\n\t"
    "v_mfma_f32_16x16x32_bf16 v[84:87], a[200:203], a[244:247], v[84:87]\n\t"
    "buffer_load_dwordx4 v141, s[16:19], s39 offen lds\n\t"
    "v_mfma_f32_16x16x32_bf16 v[88:91], a[200:203], a[248:251], v[88:91]\n\t"
    "ds_read_b64_tr_b16 a[174:175], v133 offset:1024\n\t"
    "v_mfma_f32_16x16x32_bf16 v[92:95], a[200:203], a[252:255], v[92:95]\n\t"
    "v_mfma_f32_16x16x32_bf16 v[96:99], a[204:207], a[224:227], v[96:99]\n\t"
    "ds_read_b64_tr_b16 a[176:177], v134 offset:0\n\t"
    "v_mfma_f32_16x16x32_bf16 v[100:103], a[204:207], a[228:231], v[100:103]\n\t"
    "s_add_u32 m0, m0, 0x400\n\t"
    "v_mfma_f32_16x16x32_bf16 v[104:107], a[204:207], a[232:235], v[104:107]\n\t"
    "s_add_u32 s39, s39, 0x8000\n\t"
    "ds_read_b64_tr_b16 a[178:179], v134 offset:1024\n\t"
    "v_mfma_f32_16x16x32_bf16 v[108:111], a[204:207], a[236:239], v[108:111]\n\t"
    "buffer_load_dwordx4 v140, s[16:19], s39 offen lds\n\t"
    "v_mfma_f32_16x16x32_bf16 v[112:115], a[204:207], a[240:243], v[112:115]\n\t"
    "ds_read_b64_tr_b16 a[180:181], v135 offset:0\n\t"
    "v_mfma_f32_16x16x32_bf16 v[116:119], a[204:207], a[244:247], v[116:119]\n\t"
    "v_mfma_f32_16x16x32_bf16 v[120:123], a[204:207], a[248:251], v[120:123]\n\t"
    "ds_read_b64_tr_b16 a[182:183], v135 offset:1024\n\t"
    "v_mfma_f32_16x16x32_bf16 v[124:127], a[204:207], a[252:255], v[124:127]\n\t"
    "s_add_u32 m0, m0, 0x400\n\t"
    "v_mfma_f32_16x16x32_bf16 a[0:3], a[208:211], a[224:227], a[0:3]\n\t"
    "s_add_u32 s39, s39, 0x8000\n\t"
    "ds_read_b64_tr_b16 a[184:185], v136 offset:0\n\t"
    "v_mfma_f32_16x16x32_bf16 a[4:7], a[208:211], a[228:231], a[4:7]\n\t"
    "buffer_load_dwordx4 v140, s[16:19], s39 offen lds\n\t"
    "v_mfma_f32_16x16x32_bf16 a[8:11], a[208:211], a[232:235], a[8:11]\n\t"
    "ds_read_b64_tr_b16 a[186:187], v136 offset:1024\n\t"
    "v_mfma_f32_16x16x32_bf16 a[12:15], a[208:211], a[236:239], a[12:15]\n\t"
    "v_mfma_f32_16x16x32_bf16 a[16:19], a[208:211], a[240:243], a[16:19]\n\t"
    "ds_read_b64_tr_b16 a[188:189], v137 offset:0\n\t"
    "v_mfma_f32_16x16x32_bf16 a[20:23], a[208:211], a[244:247], a[20:23]\n\t"
    "s_add_u32 m0, m0, 0x400\n\t"
    "v_mfma_f32_16x16x32_bf16 a[24:27], a[208:211], a[248:251], a[24:27]\n\t"
    "s_add_u32 s39, s39, 0x8000\n\t"
    "ds_read_b64_tr_b16 a[190:191], v137 offset:1024\n\t"
    "v_mfma_f32_16x16x32_bf16 a[28:31], a[208:211], a[252:255], a[28:31]\n\t"
    "buffer_load_dwordx4 v141, s[16:19], s39 offen lds\n\t"
    "v_mfma_f32_16x16x32_bf16 a[32:35], a[212:215], a[224:227], a[32:35]\n\t"
    "ds_read_b128 a[132:135], v128 offset:2048\n\t"
    "v_mfma_f32_16x16x32_bf16 a[36:39], a[212:215], a[228:231], a[36:39]\n\t"
    "v_mfma_f32_16x16x32_bf16 a[40:43], a[212:215], a[232:235], a[40:43]\n\t"
    "ds_read_b128 a[136:139], v128 offset:4096\n\t"
    "v_mfma_f32_16x16x32_bf16 a[44:47], a[212:215], a[236:239], a[44:47]\n\t"
    "s_add_u32 m0, m0, 0x400\n\t"
    "v_mfma_f32_16x16x32_bf16 a[48:51], a[212:215], a[240:243], a[48:51]\n\t"
    "s_add_u32 s39, s39, 0x8000\n\t"
    "ds_read_b128 a[140:143], v128 offset:6144\n\t"
    "v_mfma_f32_16x16x32_bf16 a[52:55], a[212:215], a[244:247], a[52:55]\n\t"
    "buffer_load_dwordx4 v141, s[16:19], s39 offen lds\n\t"
    "v_mfma_f32_16x16x32_bf16 a[56:59], a[212:215], a[248:251], a[56:59]\n\t"
    "s_add_u32 s46, s46, 0x80000\n\t"
    "ds_read_b128 a[144:147], v128 offset:8192\n\t"
    "v_mfma_f32_16x16x32_bf16 a[60:63], a[212:215], a[252:255], a[60:63]\n\t"
    "s_xor_b32 s47, s47, 0x8000\n\t"
    "v_mfma_f32_16x16x32_bf16 a[64:67], a[216:219], a[224:227], a[64:67]\n\t"
    "ds_read_b128 a[148:151], v128 offset:10240\n\t"
    "v_mfma_f32_16x16x32_bf16 a[68:71], a[216:219], a[228:231], a[68:71]\n\t"
    "v_mfma_f32_16x16x32_bf16 a[72:75], a[216:219], a[232:235], a[72:75]\n\t"
    "ds_read_b128 a[152:155], v128 offset:12288\n\t"
    "v_mfma_f32_16x16x32_bf16 a[76:79], a[216:219], a[236:239], a[76:79]\n\t"
    "v_mfma_f32_16x16x32_bf16 a[80:83], a[216:219], a[240:243], a[80:83]\n\t"
    "ds_read_b128 a[156:159], v128 offset:14336\n\t"
    "v_mfma_f32_16x16x32_bf16 a[84:87], a[216:219], a[244:247], a[84:87]\n\t"
    "v_mfma_f32_16x16x32_bf16 a[88:91], a[216:219], a[248:251], a[88:91]\n\t"
    "v_mfma_f32_16x16x32_bf16 a[92:95], a[216:219], a[252:255], a[92:95]\n\t"
    "v_mfma_f32_16x16x32_bf16 a[96:99], a[220:223], a[224:227], a[96:99]\n\t"
    "v_mfma_f32_16x16x32_bf16 a[100:103], a[220:223], a[228:231], a[100:103]\n\t"
    "v_mfma_f32_16x16x32_bf16 a[104:107], a[220:223], a[232:235], a[104:107]\n\t"
    "v_mfma_f32_16x16x32_bf16 a[108:111], a[220:223], a[236:239], a[108:111]\n\t"
    "v_mfma_f32_16x16x32_bf16 a[112:115], a[220:223], a[240:243], a[112:115]\n\t"
    "v_mfma_f32_16x16x32_bf16 a[116:119], a[220:223], a[244:247], a[116:119]\n\t"
    "v_mfma_f32_16x16x32_bf16 a[120:123], a[220:223], a[248:251], a[120:123]\n\t"
    "v_mfma_f32_16x16x32_bf16 a[124:127], a[220:223], a[252:255], a[124:127]\n\t"
    "s_sub_u32 s40, s40, 1\n\t"
    "s_cmp_lg_u32 s40, 0\n\t"
    "s_cbranch_scc1 L_kv_vloop\n\t"
    "s_waitcnt lgkmcnt(15)\n\t"
    "v_mfma_f32_16x16x32_bf16 v[0:3], a[128:131], a[160:163], v[0:3]\n\t"
    "s_waitcnt lgkmcnt(15)\n\t"
    "ds_read_b128 a[192:195], v129 offset:0\n\t"
    "v_mfma_f32_16x16x32_bf16 v[4:7], a[128:131], a[164:167], v[4:7]\n\t"
    "s_waitcnt lgkmcnt(15)\n\t"
    "s_add_u32 m0, s41, 0x20000\n\t"
    "v_mfma_f32_16x16x32_bf16 v[8:11], a[128:131], a[168:171], v[8:11]\n\t"
    "s_waitcnt lgkmcnt(15)\n\t"
    "s_add_u32 s39, s54, 0x0\n\t"
    "ds_read_b64_tr_b16 a[224:225], v130 offset:8192\n\t"
    "v_mfma_f32_16x16x32_bf16 v[12:15], a[128:131], a[172:175], v[12:15]\n\t"
    "s_waitcnt lgkmcnt(15)\n\t"
    "buffer_load_dwordx4 v209, s[8:11], s39 offen lds\n\t"
    "v_mfma_f32_16x16x32_bf16 v[16:19], a[128:131], a[176:179], v[16:19]\n\t"
    "s_waitcnt lgkmcnt(13)\n\t"
    "ds_read_b64_tr_b16 a[226:227], v130 offset:9216\n\t"
    "v_mfma_f32_16x16x32_bf16 v[20:23], a[128:131], a[180:183], v[20:23]\n\t"
    "s_waitcnt lgkmcnt(12)\n\t"
    "v_mfma_f32_16x16x32_bf16 v[24:27], a[128:131], a[184:187], v[24:27]\n\t"
    "s_waitcnt lgkmcnt(10)\n\t"
    "ds_read_b64_tr_b16 a[228:229], v131 offset:8192\n\t"
    "v_mfma_f32_16x16x32_bf16 v[28:31], a[128:131], a[188:191], v[28:31]\n\t"
    "s_waitcnt lgkmcnt(10)\n\t"
    "s_add_u32 m0, m0, 0x400\n\t"
    "v_mfma_f32_16x16x32_bf16 v[32:35], a[132:135], a[160:163], v[32:35]\n\t"
    "s_add_u32 s39, s39, 0x2000\n\t"
    "ds_read_b64_tr_b16 a[230:231], v131 offset:9216\n\t"
    "v_mfma_f32_16x16x32_bf16 v[36:39], a[132:135], a[164:167], v[36:39]\n\t"
    "buffer_load_dwordx4 v210, s[8:11], s39 offen lds\n\t"
    "v_mfma_f32_16x16x32_bf16 v[40:43], a[132:135], a[168:171], v[40:43]\n\t"
    "ds_read_b64_tr_b16 a[232:233], v132 offset:8192\n\t"
    "v_mfma_f32_16x16x32_bf16 v[44:47], a[132:135], a[172:175], v[44:47]\n\t"
    "v_mfma_f32_16x16x32_bf16 v[48:51], a[132:135], a[176:179], v[48:51]\n\t"
    "ds_read_b64_tr_b16 a[234:235], v132 offset:9216\n\t"
    "v_mfma_f32_16x16x32_bf16 v[52:55], a[132:135], a[180:183], v[52:55]\n\t"
    "s_add_u32 m0, m0, 0x400\n\t"
    "v_mfma_f32_16x16x32_bf16 v[56:59], a[132:135], a[184:187], v[56:59]\n\t"
    "s_add_u32 s39, s39, 0x2000\n\t"
    "ds_read_b64_tr_b16 a[236:237], v133 offset:8192\n\t"
    "v_mfma_f32_16x16x32_bf16 v[60:63], a[132:135], a[188:191], v[60:63]\n\t"
    "s_waitcnt lgkmcnt(13)\n\t"
    "buffer_load_dwordx4 v209, s[8:11], s39 offen lds\n\t"
    "v_mfma_f32_16x16x32_bf16 v[64:67], a[136:139], a[160:163], v[64:67]\n\t"
    "ds_read_b64_tr_b16 a[238:239], v133 offset:9216\n\t"
    "v_mfma_f32_16x16x32_bf16 v[68:71], a[136:139], a[164:167], v[68:71]\n\t"
    "v_mfma_f32_16x16x32_bf16 v[72:75], a[136:139], a[168:171], v[72:75]\n\t"
    "ds_read_b64_tr_b16 a[240:241], v134 offset:8192\n\t"
    "v_mfma_f32_16x16x32_bf16 v[76:79], a[136:139], a[172:175], v[76:79]\n\t"
    "s_add_u32 m0, m0, 0x400\n\t"
    "v_mfma_f32_16x16x32_bf16 v[80:83], a[136:139], a[176:179], v[80:83]\n\t"
    "s_add_u32 s39, s39, 0x2000\n\t"
    "ds_read_b64_tr_b16 a[242:243], v134 offset:9216\n\t"
    "v_mfma_f32_16x16x32_bf16 v[84:87], a[136:139], a[180:183], v[84:87]\n\t"
    "buffer_load_dwordx4 v210, s[8:11], s39 offen lds\n\t"
    "v_mfma_f32_16x16x32_bf16 v[88:91], a[136:139], a[184:187], v[88:91]\n\t"
    "ds_read_b64_tr_b16 a[244:245], v135 offset:8192\n\t"
    "v_mfma_f32_16x16x32_bf16 v[92:95], a[136:139], a[188:191], v[92:95]\n\t"
    "s_waitcnt lgkmcnt(15)\n\t"
    "v_mfma_f32_16x16x32_bf16 v[96:99], a[140:143], a[160:163], v[96:99]\n\t"
    "ds_read_b64_tr_b16 a[246:247], v135 offset:9216\n\t"
    "v_mfma_f32_16x16x32_bf16 v[100:103], a[140:143], a[164:167], v[100:103]\n\t"
    "s_add_u32 m0, s41, 0x24000\n\t"
    "v_mfma_f32_16x16x32_bf16 v[104:107], a[140:143], a[168:171], v[104:107]\n\t"
    "s_add_u32 s39, s55, 0x0\n\t"
    "ds_read_b64_tr_b16 a[248:249], v136 offset:8192\n\t"
    "v_mfma_f32_16x16x32_bf16 v[108:111], a[140:143], a[172:175], v[108:111]\n\t"
    "buffer_load_dwordx4 v209, s[8:11], s39 offen lds\n\t"
    "v_mfma_f32_16x16x32_bf16 v[112:115], a[140:143], a[176:179], v[112:115]\n\t"
    "ds_read_b64_tr_b16 a[250:251], v136 offset:9216\n\t"
    "v_mfma_f32_16x16x32_bf16 v[116:119], a[140:143], a[180:183], v[116:119]\n\t"
    "v_mfma_f32_16x16x32_bf16 v[120:123], a[140:143], a[184:187], v[120:123]\n\t"
    "ds_read_b64_tr_b16 a[252:253], v137 offset:8192\n\t"
    "v_mfma_f32_16x16x32_bf16 v[124:127], a[140:143], a[188:191], v[124:127]\n\t"
    "s_waitcnt lgkmcnt(15)\n\t"
    "s_add_u32 m0, m0, 0x400\n\t"
    "v_mfma_f32_16x16x32_bf16 a[0:3], a[144:147], a[160:163], a[0:3]\n\t"
    "s_add_u32 s39, s39, 0x2000\n\t"
    "ds_read_b64_tr_b16 a[254:255], v137 offset:9216\n\t"
    "v_mfma_f32_16x16x32_bf16 a[4:7], a[144:147], a[164:167], a[4:7]\n\t"
    "buffer_load_dwordx4 v210, s[8:11], s39 offen lds\n\t"
    "v_mfma_f32_16x16x32_bf16 a[8:11], a[144:147], a[168:171], a[8:11]\n\t"
    "ds_read_b128 a[196:199], v129 offset:2048\n\t"
    "v_mfma_f32_16x16x32_bf16 a[12:15], a[144:147], a[172:175], a[12:15]\n\t"
    "v_mfma_f32_16x16x32_bf16 a[16:19], a[144:147], a[176:179], a[16:19]\n\t"
    "ds_read_b128 a[200:203], v129 offset:4096\n\t"
    "v_mfma_f32_16x16x32_bf16 a[20:23], a[144:147], a[180:183], a[20:23]\n\t"
    "s_add_u32 m0, m0, 0x400\n\t"
    "v_mfma_f32_16x16x32_bf16 a[24:27], a[144:147], a[184:187], a[24:27]\n\t"
    "s_add_u32 s39, s39, 0x2000\n\t"
    "ds_read_b128 a[204:207], v129 offset:6144\n\t"
    "v_mfma_f32_16x16x32_bf16 a[28:31], a[144:147], a[188:191], a[28:31]\n\t"
    "s_waitcnt lgkmcnt(15)\n\t"
    "buffer_load_dwordx4 v209, s[8:11], s39 offen lds\n\t"
    "v_mfma_f32_16x16x32_bf16 a[32:35], a[148:151], a[160:163], a[32:35]\n\t"
    "ds_read_b128 a[208:211], v129 offset:8192\n\t"
    "v_mfma_f32_16x16x32_bf16 a[36:39], a[148:151], a[164:167], a[36:39]\n\t"
    "v_mfma_f32_16x16x32_bf16 a[40:43], a[148:151], a[168:171], a[40:43]\n\t"
    "ds_read_b128 a[212:215], v129 offset:10240\n\t"
    "v_mfma_f32_16x16x32_bf16 a[44:47], a[148:151], a[172:175], a[44:47]\n\t"
    "s_add_u32 m0, m0, 0x400\n\t"
    "v_mfma_f32_16x16x32_bf16 a[48:51], a[148:151], a[176:179], a[48:51]\n\t"
    "s_add_u32 s39, s39, 0x2000\n\t"
    "ds_read_b128 a[216:219], v129 offset:12288\n\t"
    "v_mfma_f32_16x16x32_bf16 a[52:55], a[148:151], a[180:183], a[52:55]\n\t"
    "buffer_load_dwordx4 v210, s[8:11], s39 offen lds\n\t"
    "v_mfma_f32_16x16x32_bf16 a[56:59], a[148:151], a[184:187], a[56:59]\n\t"
    "ds_read_b128 a[220:223], v129 offset:14336\n\t"
    "v_mfma_f32_16x16x32_bf16 a[60:63], a[148:151], a[188:191], a[60:63]\n\t"
    "s_waitcnt lgkmcnt(15)\n\t"
    "v_mfma_f32_16x16x32_bf16 a[64:67], a[152:155], a[160:163], a[64:67]\n\t"
    "v_mfma_f32_16x16x32_bf16 a[68:71], a[152:155], a[164:167], a[68:71]\n\t"
    "v_mfma_f32_16x16x32_bf16 a[72:75], a[152:155], a[168:171], a[72:75]\n\t"
    "v_mfma_f32_16x16x32_bf16 a[76:79], a[152:155], a[172:175], a[76:79]\n\t"
    "v_mfma_f32_16x16x32_bf16 a[80:83], a[152:155], a[176:179], a[80:83]\n\t"
    "v_mfma_f32_16x16x32_bf16 a[84:87], a[152:155], a[180:183], a[84:87]\n\t"
    "v_mfma_f32_16x16x32_bf16 a[88:91], a[152:155], a[184:187], a[88:91]\n\t"
    "v_mfma_f32_16x16x32_bf16 a[92:95], a[152:155], a[188:191], a[92:95]\n\t"
    "s_waitcnt lgkmcnt(15)\n\t"
    "v_mfma_f32_16x16x32_bf16 a[96:99], a[156:159], a[160:163], a[96:99]\n\t"
    "v_mfma_f32_16x16x32_bf16 a[100:103], a[156:159], a[164:167], a[100:103]\n\t"
    "v_mfma_f32_16x16x32_bf16 a[104:107], a[156:159], a[168:171], a[104:107]\n\t"
    "v_mfma_f32_16x16x32_bf16 a[108:111], a[156:159], a[172:175], a[108:111]\n\t"
    "v_mfma_f32_16x16x32_bf16 a[112:115], a[156:159], a[176:179], a[112:115]\n\t"
    "v_mfma_f32_16x16x32_bf16 a[116:119], a[156:159], a[180:183], a[116:119]\n\t"
    "v_mfma_f32_16x16x32_bf16 a[120:123], a[156:159], a[184:187], a[120:123]\n\t"
    "v_mfma_f32_16x16x32_bf16 a[124:127], a[156:159], a[188:191], a[124:127]\n\t"
    "s_waitcnt vmcnt(8) lgkmcnt(0)\n\t"
    "s_barrier\n\t"
    "v_mfma_f32_16x16x32_bf16 v[0:3], a[192:195], a[224:227], v[0:3]\n\t"
    "s_cmp_eq_u32 s49, 0x20000\n\t"
    "s_cselect_b32 s50, s51, s52\n\t"
    "v_xor_b32 v130, 0x8000, v130\n\t"
    "v_xor_b32 v131, 0x8000, v131\n\t"
    "v_mfma_f32_16x16x32_bf16 v[4:7], a[192:195], a[228:231], v[4:7]\n\t"
    "s_add_u32 s49, s49, s50\n\t"
    "v_xor_b32 v132, 0x8000, v132\n\t"
    "v_xor_b32 v133, 0x8000, v133\n\t"
    "v_mfma_f32_16x16x32_bf16 v[8:11], a[192:195], a[232:235], v[8:11]\n\t"
    "v_xor_b32 v134, 0x8000, v134\n\t"
    "v_xor_b32 v135, 0x8000, v135\n\t"
    "v_mfma_f32_16x16x32_bf16 v[12:15], a[192:195], a[236:239], v[12:15]\n\t"
    "v_xor_b32 v136, 0x8000, v136\n\t"
    "v_xor_b32 v137, 0x8000, v137\n\t"
    "v_mfma_f32_16x16x32_bf16 v[16:19], a[192:195], a[240:243], v[16:19]\n\t"
    "v_add_u32 v128, s50, v128\n\t"
    "v_add_u32 v129, s50, v129\n\t"
    "v_mfma_f32_16x16x32_bf16 v[20:23], a[192:195], a[244:247], v[20:23]\n\t"
    "v_mfma_f32_16x16x32_bf16 v[24:27], a[192:195], a[248:251], v[24:27]\n\t"
    "ds_read_b128 a[128:131], v128 offset:0\n\t"
    "v_mfma_f32_16x16x32_bf16 v[28:31], a[192:195], a[252:255], v[28:31]\n\t"
    "v_mfma_f32_16x16x32_bf16 v[32:35], a[196:199], a[224:227], v[32:35]\n\t"
    "ds_read_b64_tr_b16 a[160:161], v130 offset:0\n\t"
    "v_mfma_f32_16x16x32_bf16 v[36:39], a[196:199], a[228:231], v[36:39]\n\t"
    "v_mfma_f32_16x16x32_bf16 v[40:43], a[196:199], a[232:235], v[40:43]\n\t"
    "ds_read_b64_tr_b16 a[162:163], v130 offset:1024\n\t"
    "v_mfma_f32_16x16x32_bf16 v[44:47], a[196:199], a[236:239], v[44:47]\n\t"
    "v_mfma_f32_16x16x32_bf16 v[48:51], a[196:199], a[240:243], v[48:51]\n\t"
    "ds_read_b64_tr_b16 a[164:165], v131 offset:0\n\t"
    "v_mfma_f32_16x16x32_bf16 v[52:55], a[196:199], a[244:247], v[52:55]\n\t"
    "v_mfma_f32_16x16x32_bf16 v[56:59], a[196:199], a[248:251], v[56:59]\n\t"
    "ds_read_b64_tr_b16 a[166:167], v131 offset:1024\n\t"
    "v_mfma_f32_16x16x32_bf16 v[60:63], a[196:199], a[252:255], v[60:63]\n\t"
    "v_mfma_f32_16x16x32_bf16 v[64:67], a[200:203], a[224:227], v[64:67]\n\t"
    "ds_read_b64_tr_b16 a[168:169], v132 offset:0\n\t"
    "v_mfma_f32_16x16x32_bf16 v[68:71], a[200:203], a[228:231], v[68:71]\n\t"
    "v_mfma_f32_16x16x32_bf16 v[72:75], a[200:203], a[232:235], v[72:75]\n\t"
    "ds_read_b64_tr_b16 a[170:171], v132 offset:1024\n\t"
    "v_mfma_f32_16x16x32_bf16 v[76:79], a[200:203], a[236:239], v[76:79]\n\t"
    "v_mfma_f32_16x16x32_bf16 v[80:83], a[200:203], a[240:243], v[80:83]\n\t"
    "ds_read_b64_tr_b16 a[172:173], v133 offset:0\n\t"
    "v_mfma_f32_16x16x32_bf16 v[84:87], a[200:203], a[244:247], v[84:87]\n\t"
    "v_mfma_f32_16x16x32_bf16 v[88:91], a[200:203], a[248:251], v[88:91]\n\t"
    "ds_read_b64_tr_b16 a[174:175], v133 offset:1024\n\t"
    "v_mfma_f32_16x16x32_bf16 v[92:95], a[200:203], a[252:255], v[92:95]\n\t"
    "v_mfma_f32_16x16x32_bf16 v[96:99], a[204:207], a[224:227], v[96:99]\n\t"
    "ds_read_b64_tr_b16 a[176:177], v134 offset:0\n\t"
    "v_mfma_f32_16x16x32_bf16 v[100:103], a[204:207], a[228:231], v[100:103]\n\t"
    "v_mfma_f32_16x16x32_bf16 v[104:107], a[204:207], a[232:235], v[104:107]\n\t"
    "ds_read_b64_tr_b16 a[178:179], v134 offset:1024\n\t"
    "v_mfma_f32_16x16x32_bf16 v[108:111], a[204:207], a[236:239], v[108:111]\n\t"
    "v_mfma_f32_16x16x32_bf16 v[112:115], a[204:207], a[240:243], v[112:115]\n\t"
    "ds_read_b64_tr_b16 a[180:181], v135 offset:0\n\t"
    "v_mfma_f32_16x16x32_bf16 v[116:119], a[204:207], a[244:247], v[116:119]\n\t"
    "v_mfma_f32_16x16x32_bf16 v[120:123], a[204:207], a[248:251], v[120:123]\n\t"
    "ds_read_b64_tr_b16 a[182:183], v135 offset:1024\n\t"
    "v_mfma_f32_16x16x32_bf16 v[124:127], a[204:207], a[252:255], v[124:127]\n\t"
    "v_mfma_f32_16x16x32_bf16 a[0:3], a[208:211], a[224:227], a[0:3]\n\t"
    "ds_read_b64_tr_b16 a[184:185], v136 offset:0\n\t"
    "v_mfma_f32_16x16x32_bf16 a[4:7], a[208:211], a[228:231], a[4:7]\n\t"
    "v_mfma_f32_16x16x32_bf16 a[8:11], a[208:211], a[232:235], a[8:11]\n\t"
    "ds_read_b64_tr_b16 a[186:187], v136 offset:1024\n\t"
    "v_mfma_f32_16x16x32_bf16 a[12:15], a[208:211], a[236:239], a[12:15]\n\t"
    "v_mfma_f32_16x16x32_bf16 a[16:19], a[208:211], a[240:243], a[16:19]\n\t"
    "ds_read_b64_tr_b16 a[188:189], v137 offset:0\n\t"
    "v_mfma_f32_16x16x32_bf16 a[20:23], a[208:211], a[244:247], a[20:23]\n\t"
    "v_mfma_f32_16x16x32_bf16 a[24:27], a[208:211], a[248:251], a[24:27]\n\t"
    "ds_read_b64_tr_b16 a[190:191], v137 offset:1024\n\t"
    "v_mfma_f32_16x16x32_bf16 a[28:31], a[208:211], a[252:255], a[28:31]\n\t"
    "v_mfma_f32_16x16x32_bf16 a[32:35], a[212:215], a[224:227], a[32:35]\n\t"
    "ds_read_b128 a[132:135], v128 offset:2048\n\t"
    "v_mfma_f32_16x16x32_bf16 a[36:39], a[212:215], a[228:231], a[36:39]\n\t"
    "v_mfma_f32_16x16x32_bf16 a[40:43], a[212:215], a[232:235], a[40:43]\n\t"
    "ds_read_b128 a[136:139], v128 offset:4096\n\t"
    "v_mfma_f32_16x16x32_bf16 a[44:47], a[212:215], a[236:239], a[44:47]\n\t"
    "v_mfma_f32_16x16x32_bf16 a[48:51], a[212:215], a[240:243], a[48:51]\n\t"
    "ds_read_b128 a[140:143], v128 offset:6144\n\t"
    "v_mfma_f32_16x16x32_bf16 a[52:55], a[212:215], a[244:247], a[52:55]\n\t"
    "v_mfma_f32_16x16x32_bf16 a[56:59], a[212:215], a[248:251], a[56:59]\n\t"
    "ds_read_b128 a[144:147], v128 offset:8192\n\t"
    "v_mfma_f32_16x16x32_bf16 a[60:63], a[212:215], a[252:255], a[60:63]\n\t"
    "v_mfma_f32_16x16x32_bf16 a[64:67], a[216:219], a[224:227], a[64:67]\n\t"
    "ds_read_b128 a[148:151], v128 offset:10240\n\t"
    "v_mfma_f32_16x16x32_bf16 a[68:71], a[216:219], a[228:231], a[68:71]\n\t"
    "v_mfma_f32_16x16x32_bf16 a[72:75], a[216:219], a[232:235], a[72:75]\n\t"
    "ds_read_b128 a[152:155], v128 offset:12288\n\t"
    "v_mfma_f32_16x16x32_bf16 a[76:79], a[216:219], a[236:239], a[76:79]\n\t"
    "v_mfma_f32_16x16x32_bf16 a[80:83], a[216:219], a[240:243], a[80:83]\n\t"
    "ds_read_b128 a[156:159], v128 offset:14336\n\t"
    "v_mfma_f32_16x16x32_bf16 a[84:87], a[216:219], a[244:247], a[84:87]\n\t"
    "v_mfma_f32_16x16x32_bf16 a[88:91], a[216:219], a[248:251], a[88:91]\n\t"
    "v_mfma_f32_16x16x32_bf16 a[92:95], a[216:219], a[252:255], a[92:95]\n\t"
    "v_mfma_f32_16x16x32_bf16 a[96:99], a[220:223], a[224:227], a[96:99]\n\t"
    "v_mfma_f32_16x16x32_bf16 a[100:103], a[220:223], a[228:231], a[100:103]\n\t"
    "v_mfma_f32_16x16x32_bf16 a[104:107], a[220:223], a[232:235], a[104:107]\n\t"
    "v_mfma_f32_16x16x32_bf16 a[108:111], a[220:223], a[236:239], a[108:111]\n\t"
    "v_mfma_f32_16x16x32_bf16 a[112:115], a[220:223], a[240:243], a[112:115]\n\t"
    "v_mfma_f32_16x16x32_bf16 a[116:119], a[220:223], a[244:247], a[116:119]\n\t"
    "v_mfma_f32_16x16x32_bf16 a[120:123], a[220:223], a[248:251], a[120:123]\n\t"
    "v_mfma_f32_16x16x32_bf16 a[124:127], a[220:223], a[252:255], a[124:127]\n\t"
    "s_waitcnt lgkmcnt(15)\n\t"
    "v_mfma_f32_16x16x32_bf16 v[0:3], a[128:131], a[160:163], v[0:3]\n\t"
    "s_waitcnt lgkmcnt(15)\n\t"
    "ds_read_b128 a[192:195], v129 offset:0\n\t"
    "v_mfma_f32_16x16x32_bf16 v[4:7], a[128:131], a[164:167], v[4:7]\n\t"
    "s_waitcnt lgkmcnt(15)\n\t"
    "s_add_u32 m0, s41, 0x10000\n\t"
    "v_mfma_f32_16x16x32_bf16 v[8:11], a[128:131], a[168:171], v[8:11]\n\t"
    "s_waitcnt lgkmcnt(15)\n\t"
    "s_add_u32 s39, s54, 0x80\n\t"
    "ds_read_b64_tr_b16 a[224:225], v130 offset:8192\n\t"
    "v_mfma_f32_16x16x32_bf16 v[12:15], a[128:131], a[172:175], v[12:15]\n\t"
    "s_waitcnt lgkmcnt(15)\n\t"
    "buffer_load_dwordx4 v209, s[8:11], s39 offen lds\n\t"
    "v_mfma_f32_16x16x32_bf16 v[16:19], a[128:131], a[176:179], v[16:19]\n\t"
    "s_waitcnt lgkmcnt(13)\n\t"
    "ds_read_b64_tr_b16 a[226:227], v130 offset:9216\n\t"
    "v_mfma_f32_16x16x32_bf16 v[20:23], a[128:131], a[180:183], v[20:23]\n\t"
    "s_waitcnt lgkmcnt(12)\n\t"
    "v_mfma_f32_16x16x32_bf16 v[24:27], a[128:131], a[184:187], v[24:27]\n\t"
    "s_waitcnt lgkmcnt(10)\n\t"
    "ds_read_b64_tr_b16 a[228:229], v131 offset:8192\n\t"
    "v_mfma_f32_16x16x32_bf16 v[28:31], a[128:131], a[188:191], v[28:31]\n\t"
    "s_waitcnt lgkmcnt(10)\n\t"
    "s_add_u32 m0, m0, 0x400\n\t"
    "v_mfma_f32_16x16x32_bf16 v[32:35], a[132:135], a[160:163], v[32:35]\n\t"
    "s_add_u32 s39, s39, 0x2000\n\t"
    "ds_read_b64_tr_b16 a[230:231], v131 offset:9216\n\t"
    "v_mfma_f32_16x16x32_bf16 v[36:39], a[132:135], a[164:167], v[36:39]\n\t"
    "buffer_load_dwordx4 v210, s[8:11], s39 offen lds\n\t"
    "v_mfma_f32_16x16x32_bf16 v[40:43], a[132:135], a[168:171], v[40:43]\n\t"
    "ds_read_b64_tr_b16 a[232:233], v132 offset:8192\n\t"
    "v_mfma_f32_16x16x32_bf16 v[44:47], a[132:135], a[172:175], v[44:47]\n\t"
    "v_mfma_f32_16x16x32_bf16 v[48:51], a[132:135], a[176:179], v[48:51]\n\t"
    "ds_read_b64_tr_b16 a[234:235], v132 offset:9216\n\t"
    "v_mfma_f32_16x16x32_bf16 v[52:55], a[132:135], a[180:183], v[52:55]\n\t"
    "s_add_u32 m0, m0, 0x400\n\t"
    "v_mfma_f32_16x16x32_bf16 v[56:59], a[132:135], a[184:187], v[56:59]\n\t"
    "s_add_u32 s39, s39, 0x2000\n\t"
    "ds_read_b64_tr_b16 a[236:237], v133 offset:8192\n\t"
    "v_mfma_f32_16x16x32_bf16 v[60:63], a[132:135], a[188:191], v[60:63]\n\t"
    "s_waitcnt lgkmcnt(13)\n\t"
    "buffer_load_dwordx4 v209, s[8:11], s39 offen lds\n\t"
    "v_mfma_f32_16x16x32_bf16 v[64:67], a[136:139], a[160:163], v[64:67]\n\t"
    "ds_read_b64_tr_b16 a[238:239], v133 offset:9216\n\t"
    "v_mfma_f32_16x16x32_bf16 v[68:71], a[136:139], a[164:167], v[68:71]\n\t"
    "v_mfma_f32_16x16x32_bf16 v[72:75], a[136:139], a[168:171], v[72:75]\n\t"
    "ds_read_b64_tr_b16 a[240:241], v134 offset:8192\n\t"
    "v_mfma_f32_16x16x32_bf16 v[76:79], a[136:139], a[172:175], v[76:79]\n\t"
    "s_add_u32 m0, m0, 0x400\n\t"
    "v_mfma_f32_16x16x32_bf16 v[80:83], a[136:139], a[176:179], v[80:83]\n\t"
    "s_add_u32 s39, s39, 0x2000\n\t"
    "ds_read_b64_tr_b16 a[242:243], v134 offset:9216\n\t"
    "v_mfma_f32_16x16x32_bf16 v[84:87], a[136:139], a[180:183], v[84:87]\n\t"
    "buffer_load_dwordx4 v210, s[8:11], s39 offen lds\n\t"
    "v_mfma_f32_16x16x32_bf16 v[88:91], a[136:139], a[184:187], v[88:91]\n\t"
    "ds_read_b64_tr_b16 a[244:245], v135 offset:8192\n\t"
    "v_mfma_f32_16x16x32_bf16 v[92:95], a[136:139], a[188:191], v[92:95]\n\t"
    "s_waitcnt lgkmcnt(15)\n\t"
    "v_mfma_f32_16x16x32_bf16 v[96:99], a[140:143], a[160:163], v[96:99]\n\t"
    "ds_read_b64_tr_b16 a[246:247], v135 offset:9216\n\t"
    "v_mfma_f32_16x16x32_bf16 v[100:103], a[140:143], a[164:167], v[100:103]\n\t"
    "s_add_u32 m0, s41, 0x14000\n\t"
    "v_mfma_f32_16x16x32_bf16 v[104:107], a[140:143], a[168:171], v[104:107]\n\t"
    "s_add_u32 s39, s55, 0x80\n\t"
    "ds_read_b64_tr_b16 a[248:249], v136 offset:8192\n\t"
    "v_mfma_f32_16x16x32_bf16 v[108:111], a[140:143], a[172:175], v[108:111]\n\t"
    "buffer_load_dwordx4 v209, s[8:11], s39 offen lds\n\t"
    "v_mfma_f32_16x16x32_bf16 v[112:115], a[140:143], a[176:179], v[112:115]\n\t"
    "ds_read_b64_tr_b16 a[250:251], v136 offset:9216\n\t"
    "v_mfma_f32_16x16x32_bf16 v[116:119], a[140:143], a[180:183], v[116:119]\n\t"
    "v_mfma_f32_16x16x32_bf16 v[120:123], a[140:143], a[184:187], v[120:123]\n\t"
    "ds_read_b64_tr_b16 a[252:253], v137 offset:8192\n\t"
    "v_mfma_f32_16x16x32_bf16 v[124:127], a[140:143], a[188:191], v[124:127]\n\t"
    "s_waitcnt lgkmcnt(15)\n\t"
    "s_add_u32 m0, m0, 0x400\n\t"
    "v_mfma_f32_16x16x32_bf16 a[0:3], a[144:147], a[160:163], a[0:3]\n\t"
    "s_add_u32 s39, s39, 0x2000\n\t"
    "ds_read_b64_tr_b16 a[254:255], v137 offset:9216\n\t"
    "v_mfma_f32_16x16x32_bf16 a[4:7], a[144:147], a[164:167], a[4:7]\n\t"
    "buffer_load_dwordx4 v210, s[8:11], s39 offen lds\n\t"
    "v_mfma_f32_16x16x32_bf16 a[8:11], a[144:147], a[168:171], a[8:11]\n\t"
    "ds_read_b128 a[196:199], v129 offset:2048\n\t"
    "v_mfma_f32_16x16x32_bf16 a[12:15], a[144:147], a[172:175], a[12:15]\n\t"
    "v_mfma_f32_16x16x32_bf16 a[16:19], a[144:147], a[176:179], a[16:19]\n\t"
    "ds_read_b128 a[200:203], v129 offset:4096\n\t"
    "v_mfma_f32_16x16x32_bf16 a[20:23], a[144:147], a[180:183], a[20:23]\n\t"
    "s_add_u32 m0, m0, 0x400\n\t"
    "v_mfma_f32_16x16x32_bf16 a[24:27], a[144:147], a[184:187], a[24:27]\n\t"
    "s_add_u32 s39, s39, 0x2000\n\t"
    "ds_read_b128 a[204:207], v129 offset:6144\n\t"
    "v_mfma_f32_16x16x32_bf16 a[28:31], a[144:147], a[188:191], a[28:31]\n\t"
    "s_waitcnt lgkmcnt(15)\n\t"
    "buffer_load_dwordx4 v209, s[8:11], s39 offen lds\n\t"
    "v_mfma_f32_16x16x32_bf16 a[32:35], a[148:151], a[160:163], a[32:35]\n\t"
    "ds_read_b128 a[208:211], v129 offset:8192\n\t"
    "v_mfma_f32_16x16x32_bf16 a[36:39], a[148:151], a[164:167], a[36:39]\n\t"
    "v_mfma_f32_16x16x32_bf16 a[40:43], a[148:151], a[168:171], a[40:43]\n\t"
    "ds_read_b128 a[212:215], v129 offset:10240\n\t"
    "v_mfma_f32_16x16x32_bf16 a[44:47], a[148:151], a[172:175], a[44:47]\n\t"
    "s_add_u32 m0, m0, 0x400\n\t"
    "v_mfma_f32_16x16x32_bf16 a[48:51], a[148:151], a[176:179], a[48:51]\n\t"
    "s_add_u32 s39, s39, 0x2000\n\t"
    "ds_read_b128 a[216:219], v129 offset:12288\n\t"
    "v_mfma_f32_16x16x32_bf16 a[52:55], a[148:151], a[180:183], a[52:55]\n\t"
    "buffer_load_dwordx4 v210, s[8:11], s39 offen lds\n\t"
    "v_mfma_f32_16x16x32_bf16 a[56:59], a[148:151], a[184:187], a[56:59]\n\t"
    "ds_read_b128 a[220:223], v129 offset:14336\n\t"
    "v_mfma_f32_16x16x32_bf16 a[60:63], a[148:151], a[188:191], a[60:63]\n\t"
    "s_waitcnt lgkmcnt(15)\n\t"
    "v_mfma_f32_16x16x32_bf16 a[64:67], a[152:155], a[160:163], a[64:67]\n\t"
    "v_mfma_f32_16x16x32_bf16 a[68:71], a[152:155], a[164:167], a[68:71]\n\t"
    "v_mfma_f32_16x16x32_bf16 a[72:75], a[152:155], a[168:171], a[72:75]\n\t"
    "v_mfma_f32_16x16x32_bf16 a[76:79], a[152:155], a[172:175], a[76:79]\n\t"
    "v_mfma_f32_16x16x32_bf16 a[80:83], a[152:155], a[176:179], a[80:83]\n\t"
    "v_mfma_f32_16x16x32_bf16 a[84:87], a[152:155], a[180:183], a[84:87]\n\t"
    "v_mfma_f32_16x16x32_bf16 a[88:91], a[152:155], a[184:187], a[88:91]\n\t"
    "v_mfma_f32_16x16x32_bf16 a[92:95], a[152:155], a[188:191], a[92:95]\n\t"
    "s_waitcnt lgkmcnt(15)\n\t"
    "v_mfma_f32_16x16x32_bf16 a[96:99], a[156:159], a[160:163], a[96:99]\n\t"
    "v_mfma_f32_16x16x32_bf16 a[100:103], a[156:159], a[164:167], a[100:103]\n\t"
    "v_mfma_f32_16x16x32_bf16 a[104:107], a[156:159], a[168:171], a[104:107]\n\t"
    "v_mfma_f32_16x16x32_bf16 a[108:111], a[156:159], a[172:175], a[108:111]\n\t"
    "v_mfma_f32_16x16x32_bf16 a[112:115], a[156:159], a[176:179], a[112:115]\n\t"
    "v_mfma_f32_16x16x32_bf16 a[116:119], a[156:159], a[180:183], a[116:119]\n\t"
    "v_mfma_f32_16x16x32_bf16 a[120:123], a[156:159], a[184:187], a[120:123]\n\t"
    "v_mfma_f32_16x16x32_bf16 a[124:127], a[156:159], a[188:191], a[124:127]\n\t"
    "s_waitcnt lgkmcnt(0)\n\t"
    "s_barrier\n\t"
    "v_mfma_f32_16x16x32_bf16 v[0:3], a[192:195], a[224:227], v[0:3]\n\t"
    "s_add_u32 m0, s41, 0x8000\n\t"
    "v_mfma_f32_16x16x32_bf16 v[4:7], a[192:195], a[228:231], v[4:7]\n\t"
    "s_add_u32 s39, s57, 0x0\n\t"
    "v_mfma_f32_16x16x32_bf16 v[8:11], a[192:195], a[232:235], v[8:11]\n\t"
    "buffer_load_dwordx4 v212, s[84:87], s39 offen lds\n\t"
    "v_mfma_f32_16x16x32_bf16 v[12:15], a[192:195], a[236:239], v[12:15]\n\t"
    "v_mfma_f32_16x16x32_bf16 v[16:19], a[192:195], a[240:243], v[16:19]\n\t"
    "s_add_u32 m0, m0, 0x400\n\t"
    "v_mfma_f32_16x16x32_bf16 v[20:23], a[192:195], a[244:247], v[20:23]\n\t"
    "s_add_u32 s39, s39, 0x8000\n\t"
    "v_mfma_f32_16x16x32_bf16 v[24:27], a[192:195], a[248:251], v[24:27]\n\t"
    "buffer_load_dwordx4 v212, s[84:87], s39 offen lds\n\t"
    "v_mfma_f32_16x16x32_bf16 v[28:31], a[192:195], a[252:255], v[28:31]\n\t"
    "v_mfma_f32_16x16x32_bf16 v[32:35], a[196:199], a[224:227], v[32:35]\n\t"
    "s_add_u32 m0, m0, 0x400\n\t"
    "v_mfma_f32_16x16x32_bf16 v[36:39], a[196:199], a[228:231], v[36:39]\n\t"
    "s_add_u32 s39, s39, 0x8000\n\t"
    "v_mfma_f32_16x16x32_bf16 v[40:43], a[196:199], a[232:235], v[40:43]\n\t"
    "buffer_load_dwordx4 v213, s[84:87], s39 offen lds\n\t"
    "v_mfma_f32_16x16x32_bf16 v[44:47], a[196:199], a[236:239], v[44:47]\n\t"
    "v_mfma_f32_16x16x32_bf16 v[48:51], a[196:199], a[240:243], v[48:51]\n\t"
    "s_add_u32 m0, m0, 0x400\n\t"
    "v_mfma_f32_16x16x32_bf16 v[52:55], a[196:199], a[244:247], v[52:55]\n\t"
    "s_add_u32 s39, s39, 0x8000\n\t"
    "v_mfma_f32_16x16x32_bf16 v[56:59], a[196:199], a[248:251], v[56:59]\n\t"
    "buffer_load_dwordx4 v213, s[84:87], s39 offen lds\n\t"
    "v_cvt_pk_bf16_f32 v0, v0, v1\n\t"
    "v_mfma_f32_16x16x32_bf16 v[60:63], a[196:199], a[252:255], v[60:63]\n\t"
    "v_cvt_pk_bf16_f32 v1, v2, v3\n\t"
    "v_cvt_pk_bf16_f32 v2, v32, v33\n\t"
    "v_mfma_f32_16x16x32_bf16 v[64:67], a[200:203], a[224:227], v[64:67]\n\t"
    "s_lshr_b32 s37, s41, 1\n\t"
    "s_add_u32 m0, s37, 0xc000\n\t"
    "v_cvt_pk_bf16_f32 v3, v34, v35\n\t"
    "v_cvt_pk_bf16_f32 v4, v4, v5\n\t"
    "v_mfma_f32_16x16x32_bf16 v[68:71], a[200:203], a[228:231], v[68:71]\n\t"
    "s_add_u32 s39, s58, 0x0\n\t"
    "v_cvt_pk_bf16_f32 v5, v6, v7\n\t"
    "v_cvt_pk_bf16_f32 v6, v36, v37\n\t"
    "v_mfma_f32_16x16x32_bf16 v[72:75], a[200:203], a[232:235], v[72:75]\n\t"
    "buffer_load_dwordx4 v214, s[88:91], s39 offen lds\n\t"
    "v_cvt_pk_bf16_f32 v7, v38, v39\n\t"
    "v_mfma_f32_16x16x32_bf16 v[76:79], a[200:203], a[236:239], v[76:79]\n\t"
    "v_cvt_pk_bf16_f32 v8, v8, v9\n\t"
    "v_cvt_pk_bf16_f32 v9, v10, v11\n\t"
    "v_mfma_f32_16x16x32_bf16 v[80:83], a[200:203], a[240:243], v[80:83]\n\t"
    "s_add_u32 m0, m0, 0x400\n\t"
    "v_cvt_pk_bf16_f32 v10, v40, v41\n\t"
    "v_cvt_pk_bf16_f32 v11, v42, v43\n\t"
    "v_mfma_f32_16x16x32_bf16 v[84:87], a[200:203], a[244:247], v[84:87]\n\t"
    "s_add_u32 s39, s39, 0x6000\n\t"
    "v_cvt_pk_bf16_f32 v12, v12, v13\n\t"
    "v_cvt_pk_bf16_f32 v13, v14, v15\n\t"
    "v_mfma_f32_16x16x32_bf16 v[88:91], a[200:203], a[248:251], v[88:91]\n\t"
    "buffer_load_dwordx4 v215, s[88:91], s39 offen lds\n\t"
    "v_cvt_pk_bf16_f32 v14, v44, v45\n\t"
    "v_mfma_f32_16x16x32_bf16 v[92:95], a[200:203], a[252:255], v[92:95]\n\t"
    "v_cvt_pk_bf16_f32 v15, v46, v47\n\t"
    "v_cvt_pk_bf16_f32 v16, v16, v17\n\t"
    "v_mfma_f32_16x16x32_bf16 v[96:99], a[204:207], a[224:227], v[96:99]\n\t"
    "s_add_u32 m0, s59, 0xe000\n\t"
    "v_cvt_pk_bf16_f32 v17, v18, v19\n\t"
    "v_cvt_pk_bf16_f32 v18, v48, v49\n\t"
    "v_mfma_f32_16x16x32_bf16 v[100:103], a[204:207], a[228:231], v[100:103]\n\t"
    "s_add_u32 s39, s56, 0x0\n\t"
    "v_cvt_pk_bf16_f32 v19, v50, v51\n\t"
    "v_cvt_pk_bf16_f32 v20, v20, v21\n\t"
    "v_mfma_f32_16x16x32_bf16 v[104:107], a[204:207], a[232:235], v[104:107]\n\t"
    "buffer_load_dwordx4 v211, s[8:11], s39 offen lds\n\t"
    "v_cvt_pk_bf16_f32 v21, v22, v23\n\t"
    "v_mfma_f32_16x16x32_bf16 v[108:111], a[204:207], a[236:239], v[108:111]\n\t"
    "v_cvt_pk_bf16_f32 v22, v52, v53\n\t"
    "v_cvt_pk_bf16_f32 v23, v54, v55\n\t"
    "v_mfma_f32_16x16x32_bf16 v[112:115], a[204:207], a[240:243], v[112:115]\n\t"
    "s_add_u32 m0, s41, 0x18000\n\t"
    "v_cvt_pk_bf16_f32 v24, v24, v25\n\t"
    "v_cvt_pk_bf16_f32 v25, v26, v27\n\t"
    "v_mfma_f32_16x16x32_bf16 v[116:119], a[204:207], a[244:247], v[116:119]\n\t"
    "s_add_u32 s39, s57, 0x80000\n\t"
    "v_cvt_pk_bf16_f32 v26, v56, v57\n\t"
    "v_cvt_pk_bf16_f32 v27, v58, v59\n\t"
    "v_mfma_f32_16x16x32_bf16 v[120:123], a[204:207], a[248:251], v[120:123]\n\t"
    "buffer_load_dwordx4 v212, s[84:87], s39 offen lds\n\t"
    "v_cvt_pk_bf16_f32 v28, v28, v29\n\t"
    "v_mfma_f32_16x16x32_bf16 v[124:127], a[204:207], a[252:255], v[124:127]\n\t"
    "v_cvt_pk_bf16_f32 v29, v30, v31\n\t"
    "v_cvt_pk_bf16_f32 v30, v60, v61\n\t"
    "v_mfma_f32_16x16x32_bf16 a[0:3], a[208:211], a[224:227], a[0:3]\n\t"
    "s_add_u32 m0, m0, 0x400\n\t"
    "v_cvt_pk_bf16_f32 v31, v62, v63\n\t"
    "v_cvt_pk_bf16_f32 v32, v64, v65\n\t"
    "v_mfma_f32_16x16x32_bf16 a[4:7], a[208:211], a[228:231], a[4:7]\n\t"
    "s_add_u32 s39, s39, 0x8000\n\t"
    "v_cvt_pk_bf16_f32 v33, v66, v67\n\t"
    "v_cvt_pk_bf16_f32 v34, v96, v97\n\t"
    "v_mfma_f32_16x16x32_bf16 a[8:11], a[208:211], a[232:235], a[8:11]\n\t"
    "buffer_load_dwordx4 v212, s[84:87], s39 offen lds\n\t"
    "v_cvt_pk_bf16_f32 v35, v98, v99\n\t"
    "v_mfma_f32_16x16x32_bf16 a[12:15], a[208:211], a[236:239], a[12:15]\n\t"
    "v_cvt_pk_bf16_f32 v36, v68, v69\n\t"
    "v_cvt_pk_bf16_f32 v37, v70, v71\n\t"
    "v_mfma_f32_16x16x32_bf16 a[16:19], a[208:211], a[240:243], a[16:19]\n\t"
    "s_add_u32 m0, m0, 0x400\n\t"
    "v_cvt_pk_bf16_f32 v38, v100, v101\n\t"
    "v_cvt_pk_bf16_f32 v39, v102, v103\n\t"
    "v_mfma_f32_16x16x32_bf16 a[20:23], a[208:211], a[244:247], a[20:23]\n\t"
    "s_add_u32 s39, s39, 0x8000\n\t"
    "v_cvt_pk_bf16_f32 v40, v72, v73\n\t"
    "v_cvt_pk_bf16_f32 v41, v74, v75\n\t"
    "v_mfma_f32_16x16x32_bf16 a[24:27], a[208:211], a[248:251], a[24:27]\n\t"
    "buffer_load_dwordx4 v213, s[84:87], s39 offen lds\n\t"
    "v_cvt_pk_bf16_f32 v42, v104, v105\n\t"
    "v_mfma_f32_16x16x32_bf16 a[28:31], a[208:211], a[252:255], a[28:31]\n\t"
    "v_cvt_pk_bf16_f32 v43, v106, v107\n\t"
    "v_cvt_pk_bf16_f32 v44, v76, v77\n\t"
    "v_mfma_f32_16x16x32_bf16 a[32:35], a[212:215], a[224:227], a[32:35]\n\t"
    "s_add_u32 m0, m0, 0x400\n\t"
    "v_cvt_pk_bf16_f32 v45, v78, v79\n\t"
    "v_cvt_pk_bf16_f32 v46, v108, v109\n\t"
    "v_mfma_f32_16x16x32_bf16 a[36:39], a[212:215], a[228:231], a[36:39]\n\t"
    "s_add_u32 s39, s39, 0x8000\n\t"
    "v_cvt_pk_bf16_f32 v47, v110, v111\n\t"
    "v_cvt_pk_bf16_f32 v48, v80, v81\n\t"
    "v_mfma_f32_16x16x32_bf16 a[40:43], a[212:215], a[232:235], a[40:43]\n\t"
    "buffer_load_dwordx4 v213, s[84:87], s39 offen lds\n\t"
    "v_cvt_pk_bf16_f32 v49, v82, v83\n\t"
    "v_mfma_f32_16x16x32_bf16 a[44:47], a[212:215], a[236:239], a[44:47]\n\t"
    "v_cvt_pk_bf16_f32 v50, v112, v113\n\t"
    "v_cvt_pk_bf16_f32 v51, v114, v115\n\t"
    "v_mfma_f32_16x16x32_bf16 a[48:51], a[212:215], a[240:243], a[48:51]\n\t"
    "s_lshr_b32 s37, s41, 1\n\t"
    "s_add_u32 m0, s37, 0x1c000\n\t"
    "v_cvt_pk_bf16_f32 v52, v84, v85\n\t"
    "v_cvt_pk_bf16_f32 v53, v86, v87\n\t"
    "v_mfma_f32_16x16x32_bf16 a[52:55], a[212:215], a[244:247], a[52:55]\n\t"
    "s_add_u32 s39, s58, 0x30000\n\t"
    "v_cvt_pk_bf16_f32 v54, v116, v117\n\t"
    "v_cvt_pk_bf16_f32 v55, v118, v119\n\t"
    "v_mfma_f32_16x16x32_bf16 a[56:59], a[212:215], a[248:251], a[56:59]\n\t"
    "buffer_load_dwordx4 v214, s[88:91], s39 offen lds\n\t"
    "v_cvt_pk_bf16_f32 v56, v88, v89\n\t"
    "v_mfma_f32_16x16x32_bf16 a[60:63], a[212:215], a[252:255], a[60:63]\n\t"
    "v_cvt_pk_bf16_f32 v57, v90, v91\n\t"
    "v_cvt_pk_bf16_f32 v58, v120, v121\n\t"
    "v_mfma_f32_16x16x32_bf16 a[64:67], a[216:219], a[224:227], a[64:67]\n\t"
    "s_add_u32 m0, m0, 0x400\n\t"
    "v_cvt_pk_bf16_f32 v59, v122, v123\n\t"
    "v_cvt_pk_bf16_f32 v60, v92, v93\n\t"
    "v_mfma_f32_16x16x32_bf16 a[68:71], a[216:219], a[228:231], a[68:71]\n\t"
    "s_add_u32 s39, s39, 0x6000\n\t"
    "v_cvt_pk_bf16_f32 v61, v94, v95\n\t"
    "v_cvt_pk_bf16_f32 v62, v124, v125\n\t"
    "v_mfma_f32_16x16x32_bf16 a[72:75], a[216:219], a[232:235], a[72:75]\n\t"
    "buffer_load_dwordx4 v215, s[88:91], s39 offen lds\n\t"
    "v_cvt_pk_bf16_f32 v63, v126, v127\n\t"
    "v_mfma_f32_16x16x32_bf16 a[76:79], a[216:219], a[236:239], a[76:79]\n\t"
    "v_accvgpr_read_b32 v218, a0\n\t"
    "v_accvgpr_read_b32 v219, a1\n\t"
    "v_mfma_f32_16x16x32_bf16 a[80:83], a[216:219], a[240:243], a[80:83]\n\t"
    "s_add_u32 m0, s59, 0x1e000\n\t"
    "v_accvgpr_read_b32 v220, a2\n\t"
    "v_accvgpr_read_b32 v221, a3\n\t"
    "v_mfma_f32_16x16x32_bf16 a[84:87], a[216:219], a[244:247], a[84:87]\n\t"
    "s_add_u32 s39, s56, 0x80\n\t"
    "v_accvgpr_read_b32 v222, a32\n\t"
    "v_accvgpr_read_b32 v223, a33\n\t"
    "v_mfma_f32_16x16x32_bf16 a[88:91], a[216:219], a[248:251], a[88:91]\n\t"
    "buffer_load_dwordx4 v211, s[8:11], s39 offen lds\n\t"
    "v_accvgpr_read_b32 v224, a34\n\t"
    "v_mfma_f32_16x16x32_bf16 a[92:95], a[216:219], a[252:255], a[92:95]\n\t"
    "v_accvgpr_read_b32 v225, a35\n\t"
    "v_cvt_pk_bf16_f32 v64, v218, v219\n\t"
    "v_mfma_f32_16x16x32_bf16 a[96:99], a[220:223], a[224:227], a[96:99]\n\t"
    "v_cvt_pk_bf16_f32 v65, v220, v221\n\t"
    "v_cvt_pk_bf16_f32 v66, v222, v223\n\t"
    "v_mfma_f32_16x16x32_bf16 a[100:103], a[220:223], a[228:231], a[100:103]\n\t"
    "v_cvt_pk_bf16_f32 v67, v224, v225\n\t"
    "v_accvgpr_read_b32 v226, a4\n\t"
    "v_mfma_f32_16x16x32_bf16 a[104:107], a[220:223], a[232:235], a[104:107]\n\t"
    "v_accvgpr_read_b32 v227, a5\n\t"
    "v_accvgpr_read_b32 v228, a6\n\t"
    "v_mfma_f32_16x16x32_bf16 a[108:111], a[220:223], a[236:239], a[108:111]\n\t"
    "v_accvgpr_read_b32 v229, a7\n\t"
    "v_accvgpr_read_b32 v230, a36\n\t"
    "v_mfma_f32_16x16x32_bf16 a[112:115], a[220:223], a[240:243], a[112:115]\n\t"
    "v_accvgpr_read_b32 v231, a37\n\t"
    "v_accvgpr_read_b32 v232, a38\n\t"
    "v_mfma_f32_16x16x32_bf16 a[116:119], a[220:223], a[244:247], a[116:119]\n\t"
    "v_accvgpr_read_b32 v233, a39\n\t"
    "v_cvt_pk_bf16_f32 v68, v226, v227\n\t"
    "v_mfma_f32_16x16x32_bf16 a[120:123], a[220:223], a[248:251], a[120:123]\n\t"
    "v_cvt_pk_bf16_f32 v69, v228, v229\n\t"
    "v_cvt_pk_bf16_f32 v70, v230, v231\n\t"
    "v_mfma_f32_16x16x32_bf16 a[124:127], a[220:223], a[252:255], a[124:127]\n\t"
    "v_cvt_pk_bf16_f32 v71, v232, v233\n\t"
    "s_waitcnt vmcnt(7)\n\t"
    "s_barrier\n\t"
    "ds_read_b128 a[128:131], v234 offset:0\n\t"
    "ds_read_b128 a[132:135], v234 offset:8192\n\t"
    "ds_read_b128 a[136:139], v234 offset:16384\n\t"
    "ds_read_b128 a[140:143], v234 offset:24576\n\t"
    "ds_read_b128 a[144:147], v192 offset:0\n\t"
    "ds_read_b128 a[148:151], v192 offset:2048\n\t"
    "ds_read_b128 a[152:155], v192 offset:16384\n\t"
    "ds_read_b128 a[156:159], v192 offset:18432\n\t"
    "ds_read_b128 a[160:163], v194\n\t"
    "ds_read_b128 a[164:167], v196\n\t"
    "ds_read_b64_tr_b16 a[208:209], v198 offset:0\n\t"
    "ds_read_b64_tr_b16 a[210:211], v198 offset:1024\n\t"
    "ds_read_b64_tr_b16 a[212:213], v199 offset:0\n\t"
    "ds_read_b64_tr_b16 a[214:215], v199 offset:1024\n\t"
    "ds_read_b64_tr_b16 a[216:217], v200 offset:0\n\t"
    "ds_read_b64_tr_b16 a[218:219], v200 offset:1024\n\t"
    "ds_read_b64_tr_b16 a[220:221], v201 offset:0\n\t"
    "ds_read_b64_tr_b16 a[222:223], v201 offset:1024\n\t"
    "ds_read_b64_tr_b16 a[224:225], v202 offset:0\n\t"
    "ds_read_b64_tr_b16 a[226:227], v202 offset:1024\n\t"
    "ds_read_b64_tr_b16 a[228:229], v203 offset:0\n\t"
    "ds_read_b64_tr_b16 a[230:231], v203 offset:1024\n\t"
    "ds_read_b64_tr_b16 a[232:233], v204 offset:0\n\t"
    "ds_read_b64_tr_b16 a[234:235], v204 offset:1024\n\t"
    "ds_read_b64_tr_b16 a[236:237], v205 offset:0\n\t"
    "ds_read_b64_tr_b16 a[238:239], v205 offset:1024\n\t"
    "ds_read_b64_tr_b16 a[240:241], v206 offset:0\n\t"
    "ds_read_b64_tr_b16 a[242:243], v206 offset:512\n\t"
    "ds_read_b64_tr_b16 a[244:245], v207 offset:0\n\t"
    "ds_read_b64_tr_b16 a[246:247], v207 offset:512\n\t"
    "ds_read_b64_tr_b16 a[248:249], v208 offset:0\n\t"
    "ds_read_b64_tr_b16 a[250:251], v208 offset:512\n\t"
    "s_nop 3\n\t"
    "s_add_u32 s63, s53, 0x0\n\t"
    "buffer_store_dwordx4 v[0:3], v216, s[20:23], s63 offen offset:0 sc1\n\t"
    "v_accvgpr_read_b32 v218, a8\n\t"
    "v_accvgpr_read_b32 v219, a9\n\t"
    "v_accvgpr_read_b32 v220, a10\n\t"
    "v_accvgpr_read_b32 v221, a11\n\t"
    "v_accvgpr_read_b32 v222, a40\n\t"
    "v_accvgpr_read_b32 v223, a41\n\t"
    "v_accvgpr_read_b32 v224, a42\n\t"
    "v_accvgpr_read_b32 v225, a43\n\t"
    "v_cvt_pk_bf16_f32 v72, v218, v219\n\t"
    "v_cvt_pk_bf16_f32 v73, v220, v221\n\t"
    "v_cvt_pk_bf16_f32 v74, v222, v223\n\t"
    "v_cvt_pk_bf16_f32 v75, v224, v225\n\t"
    "v_accvgpr_read_b32 v226, a12\n\t"
    "v_accvgpr_read_b32 v227, a13\n\t"
    "v_accvgpr_read_b32 v228, a14\n\t"
    "v_accvgpr_read_b32 v229, a15\n\t"
    "v_accvgpr_read_b32 v230, a44\n\t"
    "v_accvgpr_read_b32 v231, a45\n\t"
    "v_accvgpr_read_b32 v232, a46\n\t"
    "v_accvgpr_read_b32 v233, a47\n\t"
    "v_cvt_pk_bf16_f32 v76, v226, v227\n\t"
    "s_add_u32 s63, s53, 0x0\n\t"
    "buffer_store_dwordx4 v[4:7], v216, s[20:23], s63 offen offset:1024 sc1\n\t"
    "v_cvt_pk_bf16_f32 v77, v228, v229\n\t"
    "v_cvt_pk_bf16_f32 v78, v230, v231\n\t"
    "v_cvt_pk_bf16_f32 v79, v232, v233\n\t"
    "v_accvgpr_read_b32 v218, a16\n\t"
    "v_accvgpr_read_b32 v219, a17\n\t"
    "v_accvgpr_read_b32 v220, a18\n\t"
    "v_accvgpr_read_b32 v221, a19\n\t"
    "v_accvgpr_read_b32 v222, a48\n\t"
    "v_accvgpr_read_b32 v223, a49\n\t"
    "v_accvgpr_read_b32 v224, a50\n\t"
    "v_accvgpr_read_b32 v225, a51\n\t"
    "v_cvt_pk_bf16_f32 v80, v218, v219\n\t"
    "v_cvt_pk_bf16_f32 v81, v220, v221\n\t"
    "v_cvt_pk_bf16_f32 v82, v222, v223\n\t"
    "v_cvt_pk_bf16_f32 v83, v224, v225\n\t"
    "v_accvgpr_read_b32 v226, a20\n\t"
    "v_accvgpr_read_b32 v227, a21\n\t"
    "v_accvgpr_read_b32 v228, a22\n\t"
    "v_accvgpr_read_b32 v229, a23\n\t"
    "v_accvgpr_read_b32 v230, a52\n\t"
    "v_accvgpr_read_b32 v231, a53\n\t"
    "s_add_u32 s63, s53, 0x0\n\t"
    "buffer_store_dwordx4 v[8:11], v216, s[20:23], s63 offen offset:2048 sc1\n\t"
    "v_accvgpr_read_b32 v232, a54\n\t"
    "v_accvgpr_read_b32 v233, a55\n\t"
    "v_cvt_pk_bf16_f32 v84, v226, v227\n\t"
    "v_cvt_pk_bf16_f32 v85, v228, v229\n\t"
    "v_cvt_pk_bf16_f32 v86, v230, v231\n\t"
    "v_cvt_pk_bf16_f32 v87, v232, v233\n\t"
    "v_accvgpr_read_b32 v218, a24\n\t"
    "v_accvgpr_read_b32 v219, a25\n\t"
    "v_accvgpr_read_b32 v220, a26\n\t"
    "v_accvgpr_read_b32 v221, a27\n\t"
    "v_accvgpr_read_b32 v222, a56\n\t"
    "v_accvgpr_read_b32 v223, a57\n\t"
    "v_accvgpr_read_b32 v224, a58\n\t"
    "v_accvgpr_read_b32 v225, a59\n\t"
    "v_cvt_pk_bf16_f32 v88, v218, v219\n\t"
    "v_cvt_pk_bf16_f32 v89, v220, v221\n\t"
    "v_cvt_pk_bf16_f32 v90, v222, v223\n\t"
    "v_cvt_pk_bf16_f32 v91, v224, v225\n\t"
    "v_accvgpr_read_b32 v226, a28\n\t"
    "v_accvgpr_read_b32 v227, a29\n\t"
    "v_accvgpr_read_b32 v228, a30\n\t"
    "s_add_u32 s63, s53, 0x0\n\t"
    "buffer_store_dwordx4 v[12:15], v216, s[20:23], s63 offen offset:3072 sc1\n\t"
    "v_accvgpr_read_b32 v229, a31\n\t"
    "v_accvgpr_read_b32 v230, a60\n\t"
    "v_accvgpr_read_b32 v231, a61\n\t"
    "v_accvgpr_read_b32 v232, a62\n\t"
    "v_accvgpr_read_b32 v233, a63\n\t"
    "v_cvt_pk_bf16_f32 v92, v226, v227\n\t"
    "v_cvt_pk_bf16_f32 v93, v228, v229\n\t"
    "v_cvt_pk_bf16_f32 v94, v230, v231\n\t"
    "v_cvt_pk_bf16_f32 v95, v232, v233\n\t"
    "v_accvgpr_read_b32 v218, a64\n\t"
    "v_accvgpr_read_b32 v219, a65\n\t"
    "v_accvgpr_read_b32 v220, a66\n\t"
    "v_accvgpr_read_b32 v221, a67\n\t"
    "v_accvgpr_read_b32 v222, a96\n\t"
    "v_accvgpr_read_b32 v223, a97\n\t"
    "v_accvgpr_read_b32 v224, a98\n\t"
    "v_accvgpr_read_b32 v225, a99\n\t"
    "v_cvt_pk_bf16_f32 v96, v218, v219\n\t"
    "v_cvt_pk_bf16_f32 v97, v220, v221\n\t"
    "v_cvt_pk_bf16_f32 v98, v222, v223\n\t"
    "v_cvt_pk_bf16_f32 v99, v224, v225\n\t"
    "s_add_u32 s63, s53, 0x1000\n\t"
    "buffer_store_dwordx4 v[16:19], v216, s[20:23], s63 offen offset:0 sc1\n\t"
    "v_accvgpr_read_b32 v226, a68\n\t"
    "v_accvgpr_read_b32 v227, a69\n\t"
    "v_accvgpr_read_b32 v228, a70\n\t"
    "v_accvgpr_read_b32 v229, a71\n\t"
    "v_accvgpr_read_b32 v230, a100\n\t"
    "v_accvgpr_read_b32 v231, a101\n\t"
    "v_accvgpr_read_b32 v232, a102\n\t"
    "v_accvgpr_read_b32 v233, a103\n\t"
    "v_cvt_pk_bf16_f32 v100, v226, v227\n\t"
    "v_cvt_pk_bf16_f32 v101, v228, v229\n\t"
    "v_cvt_pk_bf16_f32 v102, v230, v231\n\t"
    "v_cvt_pk_bf16_f32 v103, v232, v233\n\t"
    "v_accvgpr_read_b32 v218, a72\n\t"
    "v_accvgpr_read_b32 v219, a73\n\t"
    "v_accvgpr_read_b32 v220, a74\n\t"
    "v_accvgpr_read_b32 v221, a75\n\t"
    "v_accvgpr_read_b32 v222, a104\n\t"
    "v_accvgpr_read_b32 v223, a105\n\t"
    "v_accvgpr_read_b32 v224, a106\n\t"
    "v_accvgpr_read_b32 v225, a107\n\t"
    "v_cvt_pk_bf16_f32 v104, v218, v219\n\t"
    "s_add_u32 s63, s53, 0x1000\n\t"
    "buffer_store_dwordx4 v[20:23], v216, s[20:23], s63 offen offset:1024 sc1\n\t"
    "v_cvt_pk_bf16_f32 v105, v220, v221\n\t"
    "v_cvt_pk_bf16_f32 v106, v222, v223\n\t"
    "v_cvt_pk_bf16_f32 v107, v224, v225\n\t"
    "v_accvgpr_read_b32 v226, a76\n\t"
    "v_accvgpr_read_b32 v227, a77\n\t"
    "v_accvgpr_read_b32 v228, a78\n\t"
    "v_accvgpr_read_b32 v229, a79\n\t"
    "v_accvgpr_read_b32 v230, a108\n\t"
    "v_accvgpr_read_b32 v231, a109\n\t"
    "v_accvgpr_read_b32 v232, a110\n\t"
    "v_accvgpr_read_b32 v233, a111\n\t"
    "v_cvt_pk_bf16_f32 v108, v226, v227\n\t"
    "v_cvt_pk_bf16_f32 v109, v228, v229\n\t"
    "v_cvt_pk_bf16_f32 v110, v230, v231\n\t"
    "v_cvt_pk_bf16_f32 v111, v232, v233\n\t"
    "v_accvgpr_read_b32 v218, a80\n\t"
    "v_accvgpr_read_b32 v219, a81\n\t"
    "v_accvgpr_read_b32 v220, a82\n\t"
    "v_accvgpr_read_b32 v221, a83\n\t"
    "v_accvgpr_read_b32 v222, a112\n\t"
    "v_accvgpr_read_b32 v223, a113\n\t"
    "s_add_u32 s63, s53, 0x1000\n\t"
    "buffer_store_dwordx4 v[24:27], v216, s[20:23], s63 offen offset:2048 sc1\n\t"
    "v_accvgpr_read_b32 v224, a114\n\t"
    "v_accvgpr_read_b32 v225, a115\n\t"
    "v_cvt_pk_bf16_f32 v112, v218, v219\n\t"
    "v_cvt_pk_bf16_f32 v113, v220, v221\n\t"
    "v_cvt_pk_bf16_f32 v114, v222, v223\n\t"
    "v_cvt_pk_bf16_f32 v115, v224, v225\n\t"
    "v_accvgpr_read_b32 v226, a84\n\t"
    "v_accvgpr_read_b32 v227, a85\n\t"
    "v_accvgpr_read_b32 v228, a86\n\t"
    "v_accvgpr_read_b32 v229, a87\n\t"
    "v_accvgpr_read_b32 v230, a116\n\t"
    "v_accvgpr_read_b32 v231, a117\n\t"
    "v_accvgpr_read_b32 v232, a118\n\t"
    "v_accvgpr_read_b32 v233, a119\n\t"
    "v_cvt_pk_bf16_f32 v116, v226, v227\n\t"
    "v_cvt_pk_bf16_f32 v117, v228, v229\n\t"
    "v_cvt_pk_bf16_f32 v118, v230, v231\n\t"
    "v_cvt_pk_bf16_f32 v119, v232, v233\n\t"
    "v_accvgpr_read_b32 v218, a88\n\t"
    "v_accvgpr_read_b32 v219, a89\n\t"
    "v_accvgpr_read_b32 v220, a90\n\t"
    "s_add_u32 s63, s53, 0x1000\n\t"
    "buffer_store_dwordx4 v[28:31], v216, s[20:23], s63 offen offset:3072 sc1\n\t"
    "v_accvgpr_read_b32 v221, a91\n\t"
    "v_accvgpr_read_b32 v222, a120\n\t"
    "v_accvgpr_read_b32 v223, a121\n\t"
    "v_accvgpr_read_b32 v224, a122\n\t"
    "v_accvgpr_read_b32 v225, a123\n\t"
    "v_cvt_pk_bf16_f32 v120, v218, v219\n\t"
    "v_cvt_pk_bf16_f32 v121, v220, v221\n\t"
    "v_cvt_pk_bf16_f32 v122, v222, v223\n\t"
    "v_cvt_pk_bf16_f32 v123, v224, v225\n\t"
    "v_accvgpr_read_b32 v226, a92\n\t"
    "v_accvgpr_read_b32 v227, a93\n\t"
    "v_accvgpr_read_b32 v228, a94\n\t"
    "v_accvgpr_read_b32 v229, a95\n\t"
    "v_accvgpr_read_b32 v230, a124\n\t"
    "v_accvgpr_read_b32 v231, a125\n\t"
    "v_accvgpr_read_b32 v232, a126\n\t"
    "v_accvgpr_read_b32 v233, a127\n\t"
    "v_cvt_pk_bf16_f32 v124, v226, v227\n\t"
    "v_cvt_pk_bf16_f32 v125, v228, v229\n\t"
    "v_cvt_pk_bf16_f32 v126, v230, v231\n\t"
    "v_cvt_pk_bf16_f32 v127, v232, v233\n\t"
    "L_xq:\n\t"
    "s_waitcnt lgkmcnt(15)\n\t"
    "v_mfma_f32_16x16x32_bf16 a[0:3], a[208:211], a[128:131], 0\n\t"
    "v_mfma_f32_16x16x32_bf16 a[32:35], a[208:211], a[132:135], 0\n\t"
    "ds_read_b128 a[168:171], v235 offset:0\n\t"
    "v_mfma_f32_16x16x32_bf16 a[64:67], a[208:211], a[136:139], 0\n\t"
    "v_mfma_f32_16x16x32_bf16 a[96:99], a[208:211], a[140:143], 0\n\t"
    "s_waitcnt lgkmcnt(15)\n\t"
    "s_add_u32 m0, s41, 0x0\n\t"
    "ds_read_b64_tr_b16 a[208:209], v198 offset:8192\n\t"
    "v_mfma_f32_16x16x32_bf16 a[4:7], a[212:215], a[128:131], 0\n\t"
    "s_add_u32 s39, s54, 0x100\n\t"
    "ds_read_b64_tr_b16 a[210:211], v198 offset:9216\n\t"
    "v_mfma_f32_16x16x32_bf16 a[36:39], a[212:215], a[132:135], 0\n\t"
    "s_add_u32 s63, s53, 0x2000\n\t"
    "buffer_load_dwordx4 v209, s[8:11], s39 offen lds\n\t"
    "v_mfma_f32_16x16x32_bf16 a[68:71], a[212:215], a[136:139], 0\n\t"
    "ds_read_b128 a[172:175], v235 offset:8192\n\t"
    "buffer_store_dwordx4 v[32:35], v216, s[20:23], s63 offen offset:0 sc1\n\t"
    "v_mfma_f32_16x16x32_bf16 a[100:103], a[212:215], a[140:143], 0\n\t"
    "s_waitcnt lgkmcnt(15)\n\t"
    "ds_read_b64_tr_b16 a[212:213], v199 offset:8192\n\t"
    "v_mfma_f32_16x16x32_bf16 a[8:11], a[216:219], a[128:131], 0\n\t"
    "s_add_u32 m0, m0, 0x400\n\t"
    "ds_read_b64_tr_b16 a[214:215], v199 offset:9216\n\t"
    "v_mfma_f32_16x16x32_bf16 a[40:43], a[216:219], a[132:135], 0\n\t"
    "s_add_u32 s39, s39, 0x2000\n\t"
    "v_mfma_f32_16x16x32_bf16 a[72:75], a[216:219], a[136:139], 0\n\t"
    "ds_read_b128 a[176:179], v235 offset:16384\n\t"
    "buffer_load_dwordx4 v210, s[8:11], s39 offen lds\n\t"
    "v_mfma_f32_16x16x32_bf16 a[104:107], a[216:219], a[140:143], 0\n\t"
    "s_waitcnt lgkmcnt(15)\n\t"
    "ds_read_b64_tr_b16 a[216:217], v200 offset:8192\n\t"
    "v_mfma_f32_16x16x32_bf16 a[12:15], a[220:223], a[128:131], 0\n\t"
    "ds_read_b64_tr_b16 a[218:219], v200 offset:9216\n\t"
    "v_mfma_f32_16x16x32_bf16 a[44:47], a[220:223], a[132:135], 0\n\t"
    "s_add_u32 m0, m0, 0x400\n\t"
    "v_mfma_f32_16x16x32_bf16 a[76:79], a[220:223], a[136:139], 0\n\t"
    "s_add_u32 s39, s39, 0x2000\n\t"
    "ds_read_b128 a[180:183], v235 offset:24576\n\t"
    "v_mfma_f32_16x16x32_bf16 a[108:111], a[220:223], a[140:143], 0\n\t"
    "s_waitcnt lgkmcnt(15)\n\t"
    "ds_read_b64_tr_b16 a[220:221], v201 offset:8192\n\t"
    "buffer_load_dwordx4 v209, s[8:11], s39 offen lds\n\t"
    "v_mfma_f32_16x16x32_bf16 a[16:19], a[224:227], a[128:131], 0\n\t"
    "ds_read_b64_tr_b16 a[222:223], v201 offset:9216\n\t"
    "v_mfma_f32_16x16x32_bf16 a[48:51], a[224:227], a[132:135], 0\n\t"
    "v_mfma_f32_16x16x32_bf16 a[80:83], a[224:227], a[136:139], 0\n\t"
    "s_add_u32 m0, m0, 0x400\n\t"
    "ds_read_b128 a[184:187], v193 offset:0\n\t"
    "v_mfma_f32_16x16x32_bf16 a[112:115], a[224:227], a[140:143], 0\n\t"
    "s_waitcnt lgkmcnt(15)\n\t"
    "s_add_u32 s39, s39, 0x2000\n\t"
    "ds_read_b64_tr_b16 a[224:225], v202 offset:8192\n\t"
    "v_mfma_f32_16x16x32_bf16 a[20:23], a[228:231], a[128:131], 0\n\t"
    "ds_read_b64_tr_b16 a[226:227], v202 offset:9216\n\t"
    "buffer_load_dwordx4 v210, s[8:11], s39 offen lds\n\t"
    "v_mfma_f32_16x16x32_bf16 a[52:55], a[228:231], a[132:135], 0\n\t"
    "v_mfma_f32_16x16x32_bf16 a[84:87], a[228:231], a[136:139], 0\n\t"
    "ds_read_b128 a[188:191], v193 offset:2048\n\t"
    "v_mfma_f32_16x16x32_bf16 a[116:119], a[228:231], a[140:143], 0\n\t"
    "s_waitcnt lgkmcnt(15)\n\t"
    "s_add_u32 m0, s41, 0x4000\n\t"
    "ds_read_b64_tr_b16 a[228:229], v203 offset:8192\n\t"
    "v_mfma_f32_16x16x32_bf16 a[24:27], a[232:235], a[128:131], 0\n\t"
    "s_add_u32 s39, s55, 0x100\n\t"
    "ds_read_b64_tr_b16 a[230:231], v203 offset:9216\n\t"
    "v_mfma_f32_16x16x32_bf16 a[56:59], a[232:235], a[132:135], 0\n\t"
    "buffer_load_dwordx4 v209, s[8:11], s39 offen lds\n\t"
    "v_mfma_f32_16x16x32_bf16 a[88:91], a[232:235], a[136:139], 0\n\t"
    "s_add_u32 s63, s53, 0x2000\n\t"
    "ds_read_b128 a[192:195], v193 offset:16384\n\t"
    "v_mfma_f32_16x16x32_bf16 a[120:123], a[232:235], a[140:143], 0\n\t"
    "s_waitcnt lgkmcnt(15)\n\t"
    "ds_read_b64_tr_b16 a[232:233], v204 offset:8192\n\t"
    "buffer_store_dwordx4 v[36:39], v216, s[20:23], s63 offen offset:1024 sc1\n\t"
    "v_mfma_f32_16x16x32_bf16 a[28:31], a[236:239], a[128:131], 0\n\t"
    "s_add_u32 m0, m0, 0x400\n\t"
    "ds_read_b64_tr_b16 a[234:235], v204 offset:9216\n\t"
    "v_mfma_f32_16x16x32_bf16 a[60:63], a[236:239], a[132:135], 0\n\t"
    "s_add_u32 s39, s39, 0x2000\n\t"
    "v_mfma_f32_16x16x32_bf16 a[92:95], a[236:239], a[136:139], 0\n\t"
    "ds_read_b128 a[196:199], v193 offset:18432\n\t"
    "buffer_load_dwordx4 v210, s[8:11], s39 offen lds\n\t"
    "v_mfma_f32_16x16x32_bf16 a[124:127], a[236:239], a[140:143], 0\n\t"
    "s_waitcnt lgkmcnt(15)\n\t"
    "ds_read_b64_tr_b16 a[236:237], v205 offset:8192\n\t"
    "v_mfma_f32_16x16x32_bf16 v[128:131], a[240:243], a[144:147], 0\n\t"
    "ds_read_b64_tr_b16 a[238:239], v205 offset:9216\n\t"
    "v_mfma_f32_16x16x32_bf16 v[132:135], a[240:243], a[148:151], 0\n\t"
    "s_add_u32 m0, m0, 0x400\n\t"
    "v_mfma_f32_16x16x32_bf16 v[136:139], a[240:243], a[152:155], 0\n\t"
    "s_add_u32 s39, s39, 0x2000\n\t"
    "ds_read_b128 a[200:203], v195\n\t"
    "v_mfma_f32_16x16x32_bf16 v[140:143], a[240:243], a[156:159], 0\n\t"
    "s_waitcnt lgkmcnt(15)\n\t"
    "ds_read_b64_tr_b16 a[240:241], v206 offset:4096\n\t"
    "buffer_load_dwordx4 v209, s[8:11], s39 offen lds\n\t"
    "v_mfma_f32_16x16x32_bf16 v[144:147], a[244:247], a[144:147], 0\n\t"
    "ds_read_b64_tr_b16 a[242:243], v206 offset:4608\n\t"
    "v_mfma_f32_16x16x32_bf16 v[148:151], a[244:247], a[148:151], 0\n\t"
    "v_mfma_f32_16x16x32_bf16 v[152:155], a[244:247], a[152:155], 0\n\t"
    "s_add_u32 m0, m0, 0x400\n\t"
    "ds_read_b128 a[204:207], v197\n\t"
    "v_mfma_f32_16x16x32_bf16 v[156:159], a[244:247], a[156:159], 0\n\t"
    "s_add_u32 s39, s39, 0x2000\n\t"
    "v_mfma_f32_16x16x32_bf16 v[160:163], a[244:247], a[160:163], 0\n\t"
    "buffer_load_dwordx4 v210, s[8:11], s39 offen lds\n\t"
    "v_mfma_f32_16x16x32_bf16 v[164:167], a[244:247], a[164:167], 0\n\t"
    "s_waitcnt lgkmcnt(15)\n\t"
    "ds_read_b64_tr_b16 a[244:245], v207 offset:4096\n\t"
    "v_mfma_f32_16x16x32_bf16 v[168:171], a[144:147], a[248:251], 0\n\t"
    "ds_read_b64_tr_b16 a[246:247], v207 offset:4608\n\t"
    "v_mfma_f32_16x16x32_bf16 v[172:175], a[148:151], a[248:251], 0\n\t"
    "v_mfma_f32_16x16x32_bf16 v[176:179], a[152:155], a[248:251], 0\n\t"
    "v_mfma_f32_16x16x32_bf16 v[180:183], a[156:159], a[248:251], 0\n\t"
    "v_mfma_f32_16x16x32_bf16 v[184:187], a[160:163], a[248:251], 0\n\t"
    "v_mfma_f32_16x16x32_bf16 v[188:191], a[164:167], a[248:251], 0\n\t"
    "s_waitcnt lgkmcnt(15)\n\t"
    "ds_read_b64_tr_b16 a[248:249], v208 offset:4096\n\t"
    "v_mfma_f32_16x16x32_bf16 a[0:3], a[208:211], a[168:171], a[0:3]\n\t"
    "s_waitcnt lgkmcnt(15)\n\t"
    "s_cmp_eq_u32 s33, 0\n\t"
    "s_cselect_b32 s62, s64, s65\n\t"
    "ds_read_b64_tr_b16 a[250:251], v208 offset:4608\n\t"
    "v_mfma_f32_16x16x32_bf16 a[32:35], a[208:211], a[172:175], a[32:35]\n\t"
    "s_waitcnt lgkmcnt(15)\n\t"
    "v_mfma_f32_16x16x32_bf16 a[64:67], a[208:211], a[176:179], a[64:67]\n\t"
    "s_waitcnt lgkmcnt(15)\n\t"
    "v_mfma_f32_16x16x32_bf16 a[96:99], a[208:211], a[180:183], a[96:99]\n\t"
    "v_add_u32 v198, 0x10000, v198\n\t"
    "v_add_u32 v199, 0x10000, v199\n\t"
    "v_mfma_f32_16x16x32_bf16 a[4:7], a[212:215], a[168:171], a[4:7]\n\t"
    "v_add_u32 v200, 0x10000, v200\n\t"
    "v_add_u32 v201, 0x10000, v201\n\t"
    "v_mfma_f32_16x16x32_bf16 a[36:39], a[212:215], a[172:175], a[36:39]\n\t"
    "s_waitcnt vmcnt(18) lgkmcnt(0)\n\t"
    "s_barrier\n\t"
    "v_add_u32 v202, 0x10000, v202\n\t"
    "v_add_u32 v203, 0x10000, v203\n\t"
    "v_mfma_f32_16x16x32_bf16 a[68:71], a[212:215], a[176:179], a[68:71]\n\t"
    "v_add_u32 v204, 0x10000, v204\n\t"
    "v_add_u32 v205, 0x10000, v205\n\t"
    "v_mfma_f32_16x16x32_bf16 a[100:103], a[212:215], a[180:183], a[100:103]\n\t"
    "s_add_u32 m0, s41, 0x8000\n\t"
    "v_add_u32 v206, 0x10000, v206\n\t"
    "v_add_u32 v207, 0x10000, v207\n\t"
    "v_mfma_f32_16x16x32_bf16 a[8:11], a[216:219], a[168:171], a[8:11]\n\t"
    "s_add_u32 s39, s57, 0x100000\n\t"
    "v_add_u32 v208, 0x10000, v208\n\t"
    "v_add_u32 v192, 0xffff0000, v192\n\t"
    "v_mfma_f32_16x16x32_bf16 a[40:43], a[216:219], a[172:175], a[40:43]\n\t"
    "buffer_load_dwordx4 v212, s[84:87], s39 offen lds\n\t"
    "v_add_u32 v193, 0xffff0000, v193\n\t"
    "v_add_u32 v234, 0xffff0000, v234\n\t"
    "v_mfma_f32_16x16x32_bf16 a[72:75], a[216:219], a[176:179], a[72:75]\n\t"
    "v_add_u32 v235, 0xffff0000, v235\n\t"
    "v_add_u32 v194, s62, v194\n\t"
    "v_mfma_f32_16x16x32_bf16 a[104:107], a[216:219], a[180:183], a[104:107]\n\t"
    "v_add_u32 v195, s62, v195\n\t"
    "v_add_u32 v196, s62, v196\n\t"
    "v_mfma_f32_16x16x32_bf16 a[12:15], a[220:223], a[168:171], a[12:15]\n\t"
    "s_add_u32 m0, m0, 0x400\n\t"
    "v_add_u32 v197, s62, v197\n\t"
    "v_mfma_f32_16x16x32_bf16 a[44:47], a[220:223], a[172:175], a[44:47]\n\t"
    "s_add_u32 s39, s39, 0x8000\n\t"
    "ds_read_b64_tr_b16 a[208:209], v198 offset:0\n\t"
    "v_mfma_f32_16x16x32_bf16 a[76:79], a[220:223], a[176:179], a[76:79]\n\t"
    "ds_read_b64_tr_b16 a[210:211], v198 offset:1024\n\t"
    "buffer_load_dwordx4 v212, s[84:87], s39 offen lds\n\t"
    "v_mfma_f32_16x16x32_bf16 a[108:111], a[220:223], a[180:183], a[108:111]\n\t"
    "ds_read_b64_tr_b16 a[212:213], v199 offset:0\n\t"
    "v_mfma_f32_16x16x32_bf16 a[16:19], a[224:227], a[168:171], a[16:19]\n\t"
    "ds_read_b64_tr_b16 a[214:215], v199 offset:1024\n\t"
    "v_mfma_f32_16x16x32_bf16 a[48:51], a[224:227], a[172:175], a[48:51]\n\t"
    "s_add_u32 m0, m0, 0x400\n\t"
    "ds_read_b64_tr_b16 a[216:217], v200 offset:0\n\t"
    "v_mfma_f32_16x16x32_bf16 a[80:83], a[224:227], a[176:179], a[80:83]\n\t"
    "s_add_u32 s39, s39, 0x8000\n\t"
    "ds_read_b64_tr_b16 a[218:219], v200 offset:1024\n\t"
    "v_mfma_f32_16x16x32_bf16 a[112:115], a[224:227], a[180:183], a[112:115]\n\t"
    "ds_read_b64_tr_b16 a[220:221], v201 offset:0\n\t"
    "buffer_load_dwordx4 v213, s[84:87], s39 offen lds\n\t"
    "v_mfma_f32_16x16x32_bf16 a[20:23], a[228:231], a[168:171], a[20:23]\n\t"
    "ds_read_b64_tr_b16 a[222:223], v201 offset:1024\n\t"
    "v_mfma_f32_16x16x32_bf16 a[52:55], a[228:231], a[172:175], a[52:55]\n\t"
    "ds_read_b64_tr_b16 a[224:225], v202 offset:0\n\t"
    "v_mfma_f32_16x16x32_bf16 a[84:87], a[228:231], a[176:179], a[84:87]\n\t"
    "s_add_u32 m0, m0, 0x400\n\t"
    "ds_read_b64_tr_b16 a[226:227], v202 offset:1024\n\t"
    "v_mfma_f32_16x16x32_bf16 a[116:119], a[228:231], a[180:183], a[116:119]\n\t"
    "s_add_u32 s39, s39, 0x8000\n\t"
    "ds_read_b64_tr_b16 a[228:229], v203 offset:0\n\t"
    "v_mfma_f32_16x16x32_bf16 a[24:27], a[232:235], a[168:171], a[24:27]\n\t"
    "ds_read_b64_tr_b16 a[230:231], v203 offset:1024\n\t"
    "buffer_load_dwordx4 v213, s[84:87], s39 offen lds\n\t"
    "v_mfma_f32_16x16x32_bf16 a[56:59], a[232:235], a[172:175], a[56:59]\n\t"
    "ds_read_b128 a[128:131], v234 offset:0\n\t"
    "v_mfma_f32_16x16x32_bf16 a[88:91], a[232:235], a[176:179], a[88:91]\n\t"
    "ds_read_b128 a[132:135], v234 offset:8192\n\t"
    "v_mfma_f32_16x16x32_bf16 a[120:123], a[232:235], a[180:183], a[120:123]\n\t"
    "s_lshr_b32 s37, s41, 1\n\t"
    "s_add_u32 m0, s37, 0xc000\n\t"
    "ds_read_b64_tr_b16 a[232:233], v204 offset:0\n\t"
    "v_mfma_f32_16x16x32_bf16 a[28:31], a[236:239], a[168:171], a[28:31]\n\t"
    "s_add_u32 s39, s58, 0x60000\n\t"
    "ds_read_b64_tr_b16 a[234:235], v204 offset:1024\n\t"
    "v_mfma_f32_16x16x32_bf16 a[60:63], a[236:239], a[172:175], a[60:63]\n\t"
    "ds_read_b128 a[136:139], v234 offset:16384\n\t"
    "buffer_load_dwordx4 v214, s[88:91], s39 offen lds\n\t"
    "v_mfma_f32_16x16x32_bf16 a[92:95], a[236:239], a[176:179], a[92:95]\n\t"
    "ds_read_b128 a[140:143], v234 offset:24576\n\t"
    "v_mfma_f32_16x16x32_bf16 a[124:127], a[236:239], a[180:183], a[124:127]\n\t"
    "ds_read_b64_tr_b16 a[236:237], v205 offset:0\n\t"
    "v_mfma_f32_16x16x32_bf16 v[128:131], a[240:243], a[184:187], v[128:131]\n\t"
    "s_add_u32 m0, m0, 0x400\n\t"
    "ds_read_b64_tr_b16 a[238:239], v205 offset:1024\n\t"
    "v_mfma_f32_16x16x32_bf16 v[132:135], a[240:243], a[188:191], v[132:135]\n\t"
    "s_add_u32 s39, s39, 0x6000\n\t"
    "ds_read_b128 a[144:147], v192 offset:0\n\t"
    "v_mfma_f32_16x16x32_bf16 v[136:139], a[240:243], a[192:195], v[136:139]\n\t"
    "ds_read_b128 a[148:151], v192 offset:2048\n\t"
    "buffer_load_dwordx4 v215, s[88:91], s39 offen lds\n\t"
    "v_mfma_f32_16x16x32_bf16 v[140:143], a[240:243], a[196:199], v[140:143]\n\t"
    "ds_read_b64_tr_b16 a[240:241], v206 offset:0\n\t"
    "v_mfma_f32_16x16x32_bf16 v[144:147], a[244:247], a[184:187], v[144:147]\n\t"
    "ds_read_b64_tr_b16 a[242:243], v206 offset:512\n\t"
    "v_mfma_f32_16x16x32_bf16 v[148:151], a[244:247], a[188:191], v[148:151]\n\t"
    "s_add_u32 m0, s59, 0xe000\n\t"
    "ds_read_b128 a[152:155], v192 offset:16384\n\t"
    "v_mfma_f32_16x16x32_bf16 v[152:155], a[244:247], a[192:195], v[152:155]\n\t"
    "s_add_u32 s39, s56, 0x100\n\t"
    "ds_read_b128 a[156:159], v192 offset:18432\n\t"
    "v_mfma_f32_16x16x32_bf16 v[156:159], a[244:247], a[196:199], v[156:159]\n\t"
    "ds_read_b128 a[160:163], v194\n\t"
    "buffer_load_dwordx4 v211, s[8:11], s39 offen lds\n\t"
    "v_mfma_f32_16x16x32_bf16 v[160:163], a[244:247], a[200:203], v[160:163]\n\t"
    "ds_read_b128 a[164:167], v196\n\t"
    "v_mfma_f32_16x16x32_bf16 v[164:167], a[244:247], a[204:207], v[164:167]\n\t"
    "ds_read_b64_tr_b16 a[244:245], v207 offset:0\n\t"
    "v_mfma_f32_16x16x32_bf16 v[168:171], a[184:187], a[248:251], v[168:171]\n\t"
    "ds_read_b64_tr_b16 a[246:247], v207 offset:512\n\t"
    "v_mfma_f32_16x16x32_bf16 v[172:175], a[188:191], a[248:251], v[172:175]\n\t"
    "v_mfma_f32_16x16x32_bf16 v[176:179], a[192:195], a[248:251], v[176:179]\n\t"
    "v_mfma_f32_16x16x32_bf16 v[180:183], a[196:199], a[248:251], v[180:183]\n\t"
    "v_mfma_f32_16x16x32_bf16 v[184:187], a[200:203], a[248:251], v[184:187]\n\t"
    "v_mfma_f32_16x16x32_bf16 v[188:191], a[204:207], a[248:251], v[188:191]\n\t"
    "ds_read_b64_tr_b16 a[248:249], v208 offset:0\n\t"
    "ds_read_b64_tr_b16 a[250:251], v208 offset:512\n\t"
    "s_waitcnt lgkmcnt(15)\n\t"
    "v_mfma_f32_16x16x32_bf16 a[0:3], a[208:211], a[128:131], a[0:3]\n\t"
    "s_waitcnt lgkmcnt(15)\n\t"
    "v_mfma_f32_16x16x32_bf16 a[32:35], a[208:211], a[132:135], a[32:35]\n\t"
    "s_waitcnt lgkmcnt(15)\n\t"
    "ds_read_b128 a[168:171], v235 offset:0\n\t"
    "v_mfma_f32_16x16x32_bf16 a[64:67], a[208:211], a[136:139], a[64:67]\n\t"
    "s_waitcnt lgkmcnt(15)\n\t"
    "v_mfma_f32_16x16x32_bf16 a[96:99], a[208:211], a[140:143], a[96:99]\n\t"
    "s_add_u32 m0, s41, 0x20000\n\t"
    "ds_read_b64_tr_b16 a[208:209], v198 offset:8192\n\t"
    "v_mfma_f32_16x16x32_bf16 a[4:7], a[212:215], a[128:131], a[4:7]\n\t"
    "s_add_u32 s39, s54, 0x180\n\t"
    "ds_read_b64_tr_b16 a[210:211], v198 offset:9216\n\t"
    "v_mfma_f32_16x16x32_bf16 a[36:39], a[212:215], a[132:135], a[36:39]\n\t"
    "s_add_u32 s63, s53, 0x2000\n\t"
    "buffer_load_dwordx4 v209, s[8:11], s39 offen lds\n\t"
    "v_mfma_f32_16x16x32_bf16 a[68:71], a[212:215], a[136:139], a[68:71]\n\t"
    "ds_read_b128 a[172:175], v235 offset:8192\n\t"
    "buffer_store_dwordx4 v[40:43], v216, s[20:23], s63 offen offset:2048 sc1\n\t"
    "v_mfma_f32_16x16x32_bf16 a[100:103], a[212:215], a[140:143], a[100:103]\n\t"
    "ds_read_b64_tr_b16 a[212:213], v199 offset:8192\n\t"
    "v_mfma_f32_16x16x32_bf16 a[8:11], a[216:219], a[128:131], a[8:11]\n\t"
    "s_add_u32 m0, m0, 0x400\n\t"
    "ds_read_b64_tr_b16 a[214:215], v199 offset:9216\n\t"
    "v_mfma_f32_16x16x32_bf16 a[40:43], a[216:219], a[132:135], a[40:43]\n\t"
    "s_add_u32 s39, s39, 0x2000\n\t"
    "v_mfma_f32_16x16x32_bf16 a[72:75], a[216:219], a[136:139], a[72:75]\n\t"
    "ds_read_b128 a[176:179], v235 offset:16384\n\t"
    "buffer_load_dwordx4 v210, s[8:11], s39 offen lds\n\t"
    "v_mfma_f32_16x16x32_bf16 a[104:107], a[216:219], a[140:143], a[104:107]\n\t"
    "ds_read_b64_tr_b16 a[216:217], v200 offset:8192\n\t"
    "v_mfma_f32_16x16x32_bf16 a[12:15], a[220:223], a[128:131], a[12:15]\n\t"
    "ds_read_b64_tr_b16 a[218:219], v200 offset:9216\n\t"
    "v_mfma_f32_16x16x32_bf16 a[44:47], a[220:223], a[132:135], a[44:47]\n\t"
    "s_add_u32 m0, m0, 0x400\n\t"
    "v_mfma_f32_16x16x32_bf16 a[76:79], a[220:223], a[136:139], a[76:79]\n\t"
    "s_add_u32 s39, s39, 0x2000\n\t"
    "ds_read_b128 a[180:183], v235 offset:24576\n\t"
    "v_mfma_f32_16x16x32_bf16 a[108:111], a[220:223], a[140:143], a[108:111]\n\t"
    "ds_read_b64_tr_b16 a[220:221], v201 offset:8192\n\t"
    "buffer_load_dwordx4 v209, s[8:11], s39 offen lds\n\t"
    "v_mfma_f32_16x16x32_bf16 a[16:19], a[224:227], a[128:131], a[16:19]\n\t"
    "ds_read_b64_tr_b16 a[222:223], v201 offset:9216\n\t"
    "v_mfma_f32_16x16x32_bf16 a[48:51], a[224:227], a[132:135], a[48:51]\n\t"
    "v_mfma_f32_16x16x32_bf16 a[80:83], a[224:227], a[136:139], a[80:83]\n\t"
    "s_add_u32 m0, m0, 0x400\n\t"
    "ds_read_b128 a[184:187], v193 offset:0\n\t"
    "v_mfma_f32_16x16x32_bf16 a[112:115], a[224:227], a[140:143], a[112:115]\n\t"
    "s_add_u32 s39, s39, 0x2000\n\t"
    "ds_read_b64_tr_b16 a[224:225], v202 offset:8192\n\t"
    "v_mfma_f32_16x16x32_bf16 a[20:23], a[228:231], a[128:131], a[20:23]\n\t"
    "ds_read_b64_tr_b16 a[226:227], v202 offset:9216\n\t"
    "buffer_load_dwordx4 v210, s[8:11], s39 offen lds\n\t"
    "v_mfma_f32_16x16x32_bf16 a[52:55], a[228:231], a[132:135], a[52:55]\n\t"
    "v_mfma_f32_16x16x32_bf16 a[84:87], a[228:231], a[136:139], a[84:87]\n\t"
    "ds_read_b128 a[188:191], v193 offset:2048\n\t"
    "v_mfma_f32_16x16x32_bf16 a[116:119], a[228:231], a[140:143], a[116:119]\n\t"
    "s_add_u32 m0, s41, 0x24000\n\t"
    "ds_read_b64_tr_b16 a[228:229], v203 offset:8192\n\t"
    "v_mfma_f32_16x16x32_bf16 a[24:27], a[232:235], a[128:131], a[24:27]\n\t"
    "s_add_u32 s39, s55, 0x180\n\t"
    "ds_read_b64_tr_b16 a[230:231], v203 offset:9216\n\t"
    "v_mfma_f32_16x16x32_bf16 a[56:59], a[232:235], a[132:135], a[56:59]\n\t"
    "buffer_load_dwordx4 v209, s[8:11], s39 offen lds\n\t"
    "v_mfma_f32_16x16x32_bf16 a[88:91], a[232:235], a[136:139], a[88:91]\n\t"
    "s_add_u32 s63, s53, 0x2000\n\t"
    "ds_read_b128 a[192:195], v193 offset:16384\n\t"
    "v_mfma_f32_16x16x32_bf16 a[120:123], a[232:235], a[140:143], a[120:123]\n\t"
    "s_waitcnt lgkmcnt(15)\n\t"
    "ds_read_b64_tr_b16 a[232:233], v204 offset:8192\n\t"
    "buffer_store_dwordx4 v[44:47], v216, s[20:23], s63 offen offset:3072 sc1\n\t"
    "v_mfma_f32_16x16x32_bf16 a[28:31], a[236:239], a[128:131], a[28:31]\n\t"
    "s_add_u32 m0, m0, 0x400\n\t"
    "ds_read_b64_tr_b16 a[234:235], v204 offset:9216\n\t"
    "v_mfma_f32_16x16x32_bf16 a[60:63], a[236:239], a[132:135], a[60:63]\n\t"
    "s_add_u32 s39, s39, 0x2000\n\t"
    "v_mfma_f32_16x16x32_bf16 a[92:95], a[236:239], a[136:139], a[92:95]\n\t"
    "ds_read_b128 a[196:199], v193 offset:18432\n\t"
    "buffer_load_dwordx4 v210, s[8:11], s39 offen lds\n\t"
    "v_mfma_f32_16x16x32_bf16 a[124:127], a[236:239], a[140:143], a[124:127]\n\t"
    "s_waitcnt lgkmcnt(15)\n\t"
    "ds_read_b64_tr_b16 a[236:237], v205 offset:8192\n\t"
    "v_mfma_f32_16x16x32_bf16 v[128:131], a[240:243], a[144:147], v[128:131]\n\t"
    "ds_read_b64_tr_b16 a[238:239], v205 offset:9216\n\t"
    "v_mfma_f32_16x16x32_bf16 v[132:135], a[240:243], a[148:151], v[132:135]\n\t"
    "s_waitcnt lgkmcnt(15)\n\t"
    "s_add_u32 m0, m0, 0x400\n\t"
    "v_mfma_f32_16x16x32_bf16 v[136:139], a[240:243], a[152:155], v[136:139]\n\t"
    "s_waitcnt lgkmcnt(15)\n\t"
    "s_add_u32 s39, s39, 0x2000\n\t"
    "ds_read_b128 a[200:203], v195\n\t"
    "v_mfma_f32_16x16x32_bf16 v[140:143], a[240:243], a[156:159], v[140:143]\n\t"
    "s_waitcnt lgkmcnt(15)\n\t"
    "ds_read_b64_tr_b16 a[240:241], v206 offset:4096\n\t"
    "buffer_load_dwordx4 v209, s[8:11], s39 offen lds\n\t"
    "v_mfma_f32_16x16x32_bf16 v[144:147], a[244:247], a[144:147], v[144:147]\n\t"
    "ds_read_b64_tr_b16 a[242:243], v206 offset:4608\n\t"
    "v_mfma_f32_16x16x32_bf16 v[148:151], a[244:247], a[148:151], v[148:151]\n\t"
    "v_mfma_f32_16x16x32_bf16 v[152:155], a[244:247], a[152:155], v[152:155]\n\t"
    "s_add_u32 m0, m0, 0x400\n\t"
    "ds_read_b128 a[204:207], v197\n\t"
    "v_mfma_f32_16x16x32_bf16 v[156:159], a[244:247], a[156:159], v[156:159]\n\t"
    "s_add_u32 s39, s39, 0x2000\n\t"
    "v_mfma_f32_16x16x32_bf16 v[160:163], a[244:247], a[160:163], v[160:163]\n\t"
    "buffer_load_dwordx4 v210, s[8:11], s39 offen lds\n\t"
    "v_mfma_f32_16x16x32_bf16 v[164:167], a[244:247], a[164:167], v[164:167]\n\t"
    "s_waitcnt lgkmcnt(15)\n\t"
    "ds_read_b64_tr_b16 a[244:245], v207 offset:4096\n\t"
    "v_mfma_f32_16x16x32_bf16 v[168:171], a[144:147], a[248:251], v[168:171]\n\t"
    "ds_read_b64_tr_b16 a[246:247], v207 offset:4608\n\t"
    "v_mfma_f32_16x16x32_bf16 v[172:175], a[148:151], a[248:251], v[172:175]\n\t"
    "v_mfma_f32_16x16x32_bf16 v[176:179], a[152:155], a[248:251], v[176:179]\n\t"
    "v_mfma_f32_16x16x32_bf16 v[180:183], a[156:159], a[248:251], v[180:183]\n\t"
    "v_mfma_f32_16x16x32_bf16 v[184:187], a[160:163], a[248:251], v[184:187]\n\t"
    "v_mfma_f32_16x16x32_bf16 v[188:191], a[164:167], a[248:251], v[188:191]\n\t"
    "s_waitcnt lgkmcnt(15)\n\t"
    "ds_read_b64_tr_b16 a[248:249], v208 offset:4096\n\t"
    "v_mfma_f32_16x16x32_bf16 a[0:3], a[208:211], a[168:171], a[0:3]\n\t"
    "s_waitcnt lgkmcnt(15)\n\t"
    "s_cmp_eq_u32 s33, 0\n\t"
    "s_cselect_b32 s62, s65, s65\n\t"
    "ds_read_b64_tr_b16 a[250:251], v208 offset:4608\n\t"
    "v_mfma_f32_16x16x32_bf16 a[32:35], a[208:211], a[172:175], a[32:35]\n\t"
    "s_waitcnt lgkmcnt(15)\n\t"
    "v_mfma_f32_16x16x32_bf16 a[64:67], a[208:211], a[176:179], a[64:67]\n\t"
    "s_waitcnt lgkmcnt(15)\n\t"
    "v_mfma_f32_16x16x32_bf16 a[96:99], a[208:211], a[180:183], a[96:99]\n\t"
    "v_add_u32 v198, 0xffff0000, v198\n\t"
    "v_add_u32 v199, 0xffff0000, v199\n\t"
    "v_mfma_f32_16x16x32_bf16 a[4:7], a[212:215], a[168:171], a[4:7]\n\t"
    "v_add_u32 v200, 0xffff0000, v200\n\t"
    "v_add_u32 v201, 0xffff0000, v201\n\t"
    "v_mfma_f32_16x16x32_bf16 a[36:39], a[212:215], a[172:175], a[36:39]\n\t"
    "s_waitcnt vmcnt(10) lgkmcnt(0)\n\t"
    "s_barrier\n\t"
    "v_add_u32 v202, 0xffff0000, v202\n\t"
    "v_add_u32 v203, 0xffff0000, v203\n\t"
    "v_mfma_f32_16x16x32_bf16 a[68:71], a[212:215], a[176:179], a[68:71]\n\t"
    "v_add_u32 v204, 0xffff0000, v204\n\t"
    "v_add_u32 v205, 0xffff0000, v205\n\t"
    "v_mfma_f32_16x16x32_bf16 a[100:103], a[212:215], a[180:183], a[100:103]\n\t"
    "s_add_u32 m0, s41, 0x18000\n\t"
    "v_add_u32 v206, 0xffff0000, v206\n\t"
    "v_add_u32 v207, 0xffff0000, v207\n\t"
    "v_mfma_f32_16x16x32_bf16 a[8:11], a[216:219], a[168:171], a[8:11]\n\t"
    "s_add_u32 s39, s57, 0x180000\n\t"
    "v_add_u32 v208, 0xffff0000, v208\n\t"
    "v_add_u32 v192, 0xffff0000, v192\n\t"
    "v_mfma_f32_16x16x32_bf16 a[40:43], a[216:219], a[172:175], a[40:43]\n\t"
    "buffer_load_dwordx4 v212, s[84:87], s39 offen lds\n\t"
    "v_add_u32 v193, 0xffff0000, v193\n\t"
    "v_add_u32 v234, 0xffff0000, v234\n\t"
    "v_mfma_f32_16x16x32_bf16 a[72:75], a[216:219], a[176:179], a[72:75]\n\t"
    "v_add_u32 v235, 0xffff0000, v235\n\t"
    "v_add_u32 v194, s62, v194\n\t"
    "v_mfma_f32_16x16x32_bf16 a[104:107], a[216:219], a[180:183], a[104:107]\n\t"
    "v_add_u32 v195, s62, v195\n\t"
    "v_add_u32 v196, s62, v196\n\t"
    "v_mfma_f32_16x16x32_bf16 a[12:15], a[220:223], a[168:171], a[12:15]\n\t"
    "s_add_u32 m0, m0, 0x400\n\t"
    "v_add_u32 v197, s62, v197\n\t"
    "v_mfma_f32_16x16x32_bf16 a[44:47], a[220:223], a[172:175], a[44:47]\n\t"
    "s_add_u32 s39, s39, 0x8000\n\t"
    "ds_read_b64_tr_b16 a[208:209], v198 offset:0\n\t"
    "v_mfma_f32_16x16x32_bf16 a[76:79], a[220:223], a[176:179], a[76:79]\n\t"
    "ds_read_b64_tr_b16 a[210:211], v198 offset:1024\n\t"
    "buffer_load_dwordx4 v212, s[84:87], s39 offen lds\n\t"
    "v_mfma_f32_16x16x32_bf16 a[108:111], a[220:223], a[180:183], a[108:111]\n\t"
    "ds_read_b64_tr_b16 a[212:213], v199 offset:0\n\t"
    "v_mfma_f32_16x16x32_bf16 a[16:19], a[224:227], a[168:171], a[16:19]\n\t"
    "ds_read_b64_tr_b16 a[214:215], v199 offset:1024\n\t"
    "v_mfma_f32_16x16x32_bf16 a[48:51], a[224:227], a[172:175], a[48:51]\n\t"
    "s_add_u32 m0, m0, 0x400\n\t"
    "ds_read_b64_tr_b16 a[216:217], v200 offset:0\n\t"
    "v_mfma_f32_16x16x32_bf16 a[80:83], a[224:227], a[176:179], a[80:83]\n\t"
    "s_add_u32 s39, s39, 0x8000\n\t"
    "ds_read_b64_tr_b16 a[218:219], v200 offset:1024\n\t"
    "v_mfma_f32_16x16x32_bf16 a[112:115], a[224:227], a[180:183], a[112:115]\n\t"
    "ds_read_b64_tr_b16 a[220:221], v201 offset:0\n\t"
    "buffer_load_dwordx4 v213, s[84:87], s39 offen lds\n\t"
    "v_mfma_f32_16x16x32_bf16 a[20:23], a[228:231], a[168:171], a[20:23]\n\t"
    "ds_read_b64_tr_b16 a[222:223], v201 offset:1024\n\t"
    "v_mfma_f32_16x16x32_bf16 a[52:55], a[228:231], a[172:175], a[52:55]\n\t"
    "ds_read_b64_tr_b16 a[224:225], v202 offset:0\n\t"
    "v_mfma_f32_16x16x32_bf16 a[84:87], a[228:231], a[176:179], a[84:87]\n\t"
    "s_add_u32 m0, m0, 0x400\n\t"
    "ds_read_b64_tr_b16 a[226:227], v202 offset:1024\n\t"
    "v_mfma_f32_16x16x32_bf16 a[116:119], a[228:231], a[180:183], a[116:119]\n\t"
    "s_add_u32 s39, s39, 0x8000\n\t"
    "ds_read_b64_tr_b16 a[228:229], v203 offset:0\n\t"
    "v_mfma_f32_16x16x32_bf16 a[24:27], a[232:235], a[168:171], a[24:27]\n\t"
    "ds_read_b64_tr_b16 a[230:231], v203 offset:1024\n\t"
    "buffer_load_dwordx4 v213, s[84:87], s39 offen lds\n\t"
    "v_mfma_f32_16x16x32_bf16 a[56:59], a[232:235], a[172:175], a[56:59]\n\t"
    "ds_read_b128 a[128:131], v234 offset:0\n\t"
    "v_mfma_f32_16x16x32_bf16 a[88:91], a[232:235], a[176:179], a[88:91]\n\t"
    "ds_read_b128 a[132:135], v234 offset:8192\n\t"
    "v_mfma_f32_16x16x32_bf16 a[120:123], a[232:235], a[180:183], a[120:123]\n\t"
    "s_lshr_b32 s37, s41, 1\n\t"
    "s_add_u32 m0, s37, 0x1c000\n\t"
    "ds_read_b64_tr_b16 a[232:233], v204 offset:0\n\t"
    "v_mfma_f32_16x16x32_bf16 a[28:31], a[236:239], a[168:171], a[28:31]\n\t"
    "s_add_u32 s39, s58, 0x90000\n\t"
    "ds_read_b64_tr_b16 a[234:235], v204 offset:1024\n\t"
    "v_mfma_f32_16x16x32_bf16 a[60:63], a[236:239], a[172:175], a[60:63]\n\t"
    "ds_read_b128 a[136:139], v234 offset:16384\n\t"
    "buffer_load_dwordx4 v214, s[88:91], s39 offen lds\n\t"
    "v_mfma_f32_16x16x32_bf16 a[92:95], a[236:239], a[176:179], a[92:95]\n\t"
    "ds_read_b128 a[140:143], v234 offset:24576\n\t"
    "v_mfma_f32_16x16x32_bf16 a[124:127], a[236:239], a[180:183], a[124:127]\n\t"
    "ds_read_b64_tr_b16 a[236:237], v205 offset:0\n\t"
    "v_mfma_f32_16x16x32_bf16 v[128:131], a[240:243], a[184:187], v[128:131]\n\t"
    "s_add_u32 m0, m0, 0x400\n\t"
    "ds_read_b64_tr_b16 a[238:239], v205 offset:1024\n\t"
    "v_mfma_f32_16x16x32_bf16 v[132:135], a[240:243], a[188:191], v[132:135]\n\t"
    "s_add_u32 s39, s39, 0x6000\n\t"
    "ds_read_b128 a[144:147], v192 offset:0\n\t"
    "v_mfma_f32_16x16x32_bf16 v[136:139], a[240:243], a[192:195], v[136:139]\n\t"
    "ds_read_b128 a[148:151], v192 offset:2048\n\t"
    "buffer_load_dwordx4 v215, s[88:91], s39 offen lds\n\t"
    "v_mfma_f32_16x16x32_bf16 v[140:143], a[240:243], a[196:199], v[140:143]\n\t"
    "ds_read_b64_tr_b16 a[240:241], v206 offset:0\n\t"
    "v_mfma_f32_16x16x32_bf16 v[144:147], a[244:247], a[184:187], v[144:147]\n\t"
    "ds_read_b64_tr_b16 a[242:243], v206 offset:512\n\t"
    "v_mfma_f32_16x16x32_bf16 v[148:151], a[244:247], a[188:191], v[148:151]\n\t"
    "s_add_u32 m0, s59, 0x1e000\n\t"
    "ds_read_b128 a[152:155], v192 offset:16384\n\t"
    "v_mfma_f32_16x16x32_bf16 v[152:155], a[244:247], a[192:195], v[152:155]\n\t"
    "s_add_u32 s39, s56, 0x180\n\t"
    "ds_read_b128 a[156:159], v192 offset:18432\n\t"
    "v_mfma_f32_16x16x32_bf16 v[156:159], a[244:247], a[196:199], v[156:159]\n\t"
    "ds_read_b128 a[160:163], v194\n\t"
    "buffer_load_dwordx4 v211, s[8:11], s39 offen lds\n\t"
    "v_mfma_f32_16x16x32_bf16 v[160:163], a[244:247], a[200:203], v[160:163]\n\t"
    "ds_read_b128 a[164:167], v196\n\t"
    "v_mfma_f32_16x16x32_bf16 v[164:167], a[244:247], a[204:207], v[164:167]\n\t"
    "ds_read_b64_tr_b16 a[244:245], v207 offset:0\n\t"
    "v_mfma_f32_16x16x32_bf16 v[168:171], a[184:187], a[248:251], v[168:171]\n\t"
    "ds_read_b64_tr_b16 a[246:247], v207 offset:512\n\t"
    "v_mfma_f32_16x16x32_bf16 v[172:175], a[188:191], a[248:251], v[172:175]\n\t"
    "v_mfma_f32_16x16x32_bf16 v[176:179], a[192:195], a[248:251], v[176:179]\n\t"
    "v_mfma_f32_16x16x32_bf16 v[180:183], a[196:199], a[248:251], v[180:183]\n\t"
    "v_mfma_f32_16x16x32_bf16 v[184:187], a[200:203], a[248:251], v[184:187]\n\t"
    "v_mfma_f32_16x16x32_bf16 v[188:191], a[204:207], a[248:251], v[188:191]\n\t"
    "ds_read_b64_tr_b16 a[248:249], v208 offset:0\n\t"
    "ds_read_b64_tr_b16 a[250:251], v208 offset:512\n\t"
    "s_waitcnt lgkmcnt(15)\n\t"
    "v_mfma_f32_16x16x32_bf16 a[0:3], a[208:211], a[128:131], a[0:3]\n\t"
    "s_waitcnt lgkmcnt(15)\n\t"
    "v_mfma_f32_16x16x32_bf16 a[32:35], a[208:211], a[132:135], a[32:35]\n\t"
    "s_waitcnt lgkmcnt(15)\n\t"
    "ds_read_b128 a[168:171], v235 offset:0\n\t"
    "v_mfma_f32_16x16x32_bf16 a[64:67], a[208:211], a[136:139], a[64:67]\n\t"
    "s_waitcnt lgkmcnt(15)\n\t"
    "v_mfma_f32_16x16x32_bf16 a[96:99], a[208:211], a[140:143], a[96:99]\n\t"
    "s_add_u32 m0, s41, 0x10000\n\t"
    "ds_read_b64_tr_b16 a[208:209], v198 offset:8192\n\t"
    "v_mfma_f32_16x16x32_bf16 a[4:7], a[212:215], a[128:131], a[4:7]\n\t"
    "s_add_u32 s39, s54, 0x200\n\t"
    "ds_read_b64_tr_b16 a[210:211], v198 offset:9216\n\t"
    "v_mfma_f32_16x16x32_bf16 a[36:39], a[212:215], a[132:135], a[36:39]\n\t"
    "s_add_u32 s63, s53, 0x3000\n\t"
    "buffer_load_dwordx4 v209, s[8:11], s39 offen lds\n\t"
    "v_mfma_f32_16x16x32_bf16 a[68:71], a[212:215], a[136:139], a[68:71]\n\t"
    "ds_read_b128 a[172:175], v235 offset:8192\n\t"
    "buffer_store_dwordx4 v[48:51], v216, s[20:23], s63 offen offset:0 sc1\n\t"
    "v_mfma_f32_16x16x32_bf16 a[100:103], a[212:215], a[140:143], a[100:103]\n\t"
    "ds_read_b64_tr_b16 a[212:213], v199 offset:8192\n\t"
    "v_mfma_f32_16x16x32_bf16 a[8:11], a[216:219], a[128:131], a[8:11]\n\t"
    "s_add_u32 m0, m0, 0x400\n\t"
    "ds_read_b64_tr_b16 a[214:215], v199 offset:9216\n\t"
    "v_mfma_f32_16x16x32_bf16 a[40:43], a[216:219], a[132:135], a[40:43]\n\t"
    "s_add_u32 s39, s39, 0x2000\n\t"
    "v_mfma_f32_16x16x32_bf16 a[72:75], a[216:219], a[136:139], a[72:75]\n\t"
    "ds_read_b128 a[176:179], v235 offset:16384\n\t"
    "buffer_load_dwordx4 v210, s[8:11], s39 offen lds\n\t"
    "v_mfma_f32_16x16x32_bf16 a[104:107], a[216:219], a[140:143], a[104:107]\n\t"
    "ds_read_b64_tr_b16 a[216:217], v200 offset:8192\n\t"
    "v_mfma_f32_16x16x32_bf16 a[12:15], a[220:223], a[128:131], a[12:15]\n\t"
    "ds_read_b64_tr_b16 a[218:219], v200 offset:9216\n\t"
    "v_mfma_f32_16x16x32_bf16 a[44:47], a[220:223], a[132:135], a[44:47]\n\t"
    "s_add_u32 m0, m0, 0x400\n\t"
    "v_mfma_f32_16x16x32_bf16 a[76:79], a[220:223], a[136:139], a[76:79]\n\t"
    "s_add_u32 s39, s39, 0x2000\n\t"
    "ds_read_b128 a[180:183], v235 offset:24576\n\t"
    "v_mfma_f32_16x16x32_bf16 a[108:111], a[220:223], a[140:143], a[108:111]\n\t"
    "ds_read_b64_tr_b16 a[220:221], v201 offset:8192\n\t"
    "buffer_load_dwordx4 v209, s[8:11], s39 offen lds\n\t"
    "v_mfma_f32_16x16x32_bf16 a[16:19], a[224:227], a[128:131], a[16:19]\n\t"
    "ds_read_b64_tr_b16 a[222:223], v201 offset:9216\n\t"
    "v_mfma_f32_16x16x32_bf16 a[48:51], a[224:227], a[132:135], a[48:51]\n\t"
    "v_mfma_f32_16x16x32_bf16 a[80:83], a[224:227], a[136:139], a[80:83]\n\t"
    "s_add_u32 m0, m0, 0x400\n\t"
    "ds_read_b128 a[184:187], v193 offset:0\n\t"
    "v_mfma_f32_16x16x32_bf16 a[112:115], a[224:227], a[140:143], a[112:115]\n\t"
    "s_add_u32 s39, s39, 0x2000\n\t"
    "ds_read_b64_tr_b16 a[224:225], v202 offset:8192\n\t"
    "v_mfma_f32_16x16x32_bf16 a[20:23], a[228:231], a[128:131], a[20:23]\n\t"
    "ds_read_b64_tr_b16 a[226:227], v202 offset:9216\n\t"
    "buffer_load_dwordx4 v210, s[8:11], s39 offen lds\n\t"
    "v_mfma_f32_16x16x32_bf16 a[52:55], a[228:231], a[132:135], a[52:55]\n\t"
    "v_mfma_f32_16x16x32_bf16 a[84:87], a[228:231], a[136:139], a[84:87]\n\t"
    "ds_read_b128 a[188:191], v193 offset:2048\n\t"
    "v_mfma_f32_16x16x32_bf16 a[116:119], a[228:231], a[140:143], a[116:119]\n\t"
    "s_add_u32 m0, s41, 0x14000\n\t"
    "ds_read_b64_tr_b16 a[228:229], v203 offset:8192\n\t"
    "v_mfma_f32_16x16x32_bf16 a[24:27], a[232:235], a[128:131], a[24:27]\n\t"
    "s_add_u32 s39, s55, 0x200\n\t"
    "ds_read_b64_tr_b16 a[230:231], v203 offset:9216\n\t"
    "v_mfma_f32_16x16x32_bf16 a[56:59], a[232:235], a[132:135], a[56:59]\n\t"
    "buffer_load_dwordx4 v209, s[8:11], s39 offen lds\n\t"
    "v_mfma_f32_16x16x32_bf16 a[88:91], a[232:235], a[136:139], a[88:91]\n\t"
    "s_add_u32 s63, s53, 0x3000\n\t"
    "ds_read_b128 a[192:195], v193 offset:16384\n\t"
    "v_mfma_f32_16x16x32_bf16 a[120:123], a[232:235], a[140:143], a[120:123]\n\t"
    "s_waitcnt lgkmcnt(15)\n\t"
    "ds_read_b64_tr_b16 a[232:233], v204 offset:8192\n\t"
    "buffer_store_dwordx4 v[52:55], v216, s[20:23], s63 offen offset:1024 sc1\n\t"
    "v_mfma_f32_16x16x32_bf16 a[28:31], a[236:239], a[128:131], a[28:31]\n\t"
    "s_add_u32 m0, m0, 0x400\n\t"
    "ds_read_b64_tr_b16 a[234:235], v204 offset:9216\n\t"
    "v_mfma_f32_16x16x32_bf16 a[60:63], a[236:239], a[132:135], a[60:63]\n\t"
    "s_add_u32 s39, s39, 0x2000\n\t"
    "v_mfma_f32_16x16x32_bf16 a[92:95], a[236:239], a[136:139], a[92:95]\n\t"
    "ds_read_b128 a[196:199], v193 offset:18432\n\t"
    "buffer_load_dwordx4 v210, s[8:11], s39 offen lds\n\t"
    "v_mfma_f32_16x16x32_bf16 a[124:127], a[236:239], a[140:143], a[124:127]\n\t"
    "s_waitcnt lgkmcnt(15)\n\t"
    "ds_read_b64_tr_b16 a[236:237], v205 offset:8192\n\t"
    "v_mfma_f32_16x16x32_bf16 v[128:131], a[240:243], a[144:147], v[128:131]\n\t"
    "ds_read_b64_tr_b16 a[238:239], v205 offset:9216\n\t"
    "v_mfma_f32_16x16x32_bf16 v[132:135], a[240:243], a[148:151], v[132:135]\n\t"
    "s_waitcnt lgkmcnt(15)\n\t"
    "s_add_u32 m0, m0, 0x400\n\t"
    "v_mfma_f32_16x16x32_bf16 v[136:139], a[240:243], a[152:155], v[136:139]\n\t"
    "s_waitcnt lgkmcnt(15)\n\t"
    "s_add_u32 s39, s39, 0x2000\n\t"
    "ds_read_b128 a[200:203], v195\n\t"
    "v_mfma_f32_16x16x32_bf16 v[140:143], a[240:243], a[156:159], v[140:143]\n\t"
    "s_waitcnt lgkmcnt(15)\n\t"
    "ds_read_b64_tr_b16 a[240:241], v206 offset:4096\n\t"
    "buffer_load_dwordx4 v209, s[8:11], s39 offen lds\n\t"
    "v_mfma_f32_16x16x32_bf16 v[144:147], a[244:247], a[144:147], v[144:147]\n\t"
    "ds_read_b64_tr_b16 a[242:243], v206 offset:4608\n\t"
    "v_mfma_f32_16x16x32_bf16 v[148:151], a[244:247], a[148:151], v[148:151]\n\t"
    "v_mfma_f32_16x16x32_bf16 v[152:155], a[244:247], a[152:155], v[152:155]\n\t"
    "s_add_u32 m0, m0, 0x400\n\t"
    "ds_read_b128 a[204:207], v197\n\t"
    "v_mfma_f32_16x16x32_bf16 v[156:159], a[244:247], a[156:159], v[156:159]\n\t"
    "s_add_u32 s39, s39, 0x2000\n\t"
    "v_mfma_f32_16x16x32_bf16 v[160:163], a[244:247], a[160:163], v[160:163]\n\t"
    "buffer_load_dwordx4 v210, s[8:11], s39 offen lds\n\t"
    "v_mfma_f32_16x16x32_bf16 v[164:167], a[244:247], a[164:167], v[164:167]\n\t"
    "s_waitcnt lgkmcnt(15)\n\t"
    "ds_read_b64_tr_b16 a[244:245], v207 offset:4096\n\t"
    "v_mfma_f32_16x16x32_bf16 v[168:171], a[144:147], a[248:251], v[168:171]\n\t"
    "ds_read_b64_tr_b16 a[246:247], v207 offset:4608\n\t"
    "v_mfma_f32_16x16x32_bf16 v[172:175], a[148:151], a[248:251], v[172:175]\n\t"
    "v_mfma_f32_16x16x32_bf16 v[176:179], a[152:155], a[248:251], v[176:179]\n\t"
    "v_mfma_f32_16x16x32_bf16 v[180:183], a[156:159], a[248:251], v[180:183]\n\t"
    "v_mfma_f32_16x16x32_bf16 v[184:187], a[160:163], a[248:251], v[184:187]\n\t"
    "v_mfma_f32_16x16x32_bf16 v[188:191], a[164:167], a[248:251], v[188:191]\n\t"
    "s_waitcnt lgkmcnt(15)\n\t"
    "ds_read_b64_tr_b16 a[248:249], v208 offset:4096\n\t"
    "v_mfma_f32_16x16x32_bf16 a[0:3], a[208:211], a[168:171], a[0:3]\n\t"
    "s_waitcnt lgkmcnt(15)\n\t"
    "s_cmp_eq_u32 s33, 0\n\t"
    "s_cselect_b32 s62, s64, 0x20000\n\t"
    "ds_read_b64_tr_b16 a[250:251], v208 offset:4608\n\t"
    "v_mfma_f32_16x16x32_bf16 a[32:35], a[208:211], a[172:175], a[32:35]\n\t"
    "s_waitcnt lgkmcnt(15)\n\t"
    "v_mfma_f32_16x16x32_bf16 a[64:67], a[208:211], a[176:179], a[64:67]\n\t"
    "s_waitcnt lgkmcnt(15)\n\t"
    "v_mfma_f32_16x16x32_bf16 a[96:99], a[208:211], a[180:183], a[96:99]\n\t"
    "v_add_u32 v198, 0x10000, v198\n\t"
    "v_add_u32 v199, 0x10000, v199\n\t"
    "v_mfma_f32_16x16x32_bf16 a[4:7], a[212:215], a[168:171], a[4:7]\n\t"
    "v_add_u32 v200, 0x10000, v200\n\t"
    "v_add_u32 v201, 0x10000, v201\n\t"
    "v_mfma_f32_16x16x32_bf16 a[36:39], a[212:215], a[172:175], a[36:39]\n\t"
    "s_waitcnt vmcnt(10) lgkmcnt(0)\n\t"
    "s_barrier\n\t"
    "v_add_u32 v202, 0x10000, v202\n\t"
    "v_add_u32 v203, 0x10000, v203\n\t"
    "v_mfma_f32_16x16x32_bf16 a[68:71], a[212:215], a[176:179], a[68:71]\n\t"
    "v_add_u32 v204, 0x10000, v204\n\t"
    "v_add_u32 v205, 0x10000, v205\n\t"
    "v_mfma_f32_16x16x32_bf16 a[100:103], a[212:215], a[180:183], a[100:103]\n\t"
    "s_add_u32 m0, s41, 0x8000\n\t"
    "v_add_u32 v206, 0x10000, v206\n\t"
    "v_add_u32 v207, 0x10000, v207\n\t"
    "v_mfma_f32_16x16x32_bf16 a[8:11], a[216:219], a[168:171], a[8:11]\n\t"
    "s_add_u32 s39, s57, 0x200000\n\t"
    "v_add_u32 v208, 0x10000, v208\n\t"
    "v_add_u32 v192, 0x20000, v192\n\t"
    "v_mfma_f32_16x16x32_bf16 a[40:43], a[216:219], a[172:175], a[40:43]\n\t"
    "buffer_load_dwordx4 v212, s[84:87], s39 offen lds\n\t"
    "v_add_u32 v193, 0x20000, v193\n\t"
    "v_add_u32 v234, 0x20000, v234\n\t"
    "v_mfma_f32_16x16x32_bf16 a[72:75], a[216:219], a[176:179], a[72:75]\n\t"
    "v_add_u32 v235, 0x20000, v235\n\t"
    "v_add_u32 v194, s62, v194\n\t"
    "v_mfma_f32_16x16x32_bf16 a[104:107], a[216:219], a[180:183], a[104:107]\n\t"
    "v_add_u32 v195, s62, v195\n\t"
    "v_add_u32 v196, s62, v196\n\t"
    "v_mfma_f32_16x16x32_bf16 a[12:15], a[220:223], a[168:171], a[12:15]\n\t"
    "s_add_u32 m0, m0, 0x400\n\t"
    "v_add_u32 v197, s62, v197\n\t"
    "v_mfma_f32_16x16x32_bf16 a[44:47], a[220:223], a[172:175], a[44:47]\n\t"
    "s_add_u32 s39, s39, 0x8000\n\t"
    "ds_read_b64_tr_b16 a[208:209], v198 offset:0\n\t"
    "v_mfma_f32_16x16x32_bf16 a[76:79], a[220:223], a[176:179], a[76:79]\n\t"
    "ds_read_b64_tr_b16 a[210:211], v198 offset:1024\n\t"
    "buffer_load_dwordx4 v212, s[84:87], s39 offen lds\n\t"
    "v_mfma_f32_16x16x32_bf16 a[108:111], a[220:223], a[180:183], a[108:111]\n\t"
    "ds_read_b64_tr_b16 a[212:213], v199 offset:0\n\t"
    "v_mfma_f32_16x16x32_bf16 a[16:19], a[224:227], a[168:171], a[16:19]\n\t"
    "ds_read_b64_tr_b16 a[214:215], v199 offset:1024\n\t"
    "v_mfma_f32_16x16x32_bf16 a[48:51], a[224:227], a[172:175], a[48:51]\n\t"
    "s_add_u32 m0, m0, 0x400\n\t"
    "ds_read_b64_tr_b16 a[216:217], v200 offset:0\n\t"
    "v_mfma_f32_16x16x32_bf16 a[80:83], a[224:227], a[176:179], a[80:83]\n\t"
    "s_add_u32 s39, s39, 0x8000\n\t"
    "ds_read_b64_tr_b16 a[218:219], v200 offset:1024\n\t"
    "v_mfma_f32_16x16x32_bf16 a[112:115], a[224:227], a[180:183], a[112:115]\n\t"
    "ds_read_b64_tr_b16 a[220:221], v201 offset:0\n\t"
    "buffer_load_dwordx4 v213, s[84:87], s39 offen lds\n\t"
    "v_mfma_f32_16x16x32_bf16 a[20:23], a[228:231], a[168:171], a[20:23]\n\t"
    "ds_read_b64_tr_b16 a[222:223], v201 offset:1024\n\t"
    "v_mfma_f32_16x16x32_bf16 a[52:55], a[228:231], a[172:175], a[52:55]\n\t"
    "ds_read_b64_tr_b16 a[224:225], v202 offset:0\n\t"
    "v_mfma_f32_16x16x32_bf16 a[84:87], a[228:231], a[176:179], a[84:87]\n\t"
    "s_add_u32 m0, m0, 0x400\n\t"
    "ds_read_b64_tr_b16 a[226:227], v202 offset:1024\n\t"
    "v_mfma_f32_16x16x32_bf16 a[116:119], a[228:231], a[180:183], a[116:119]\n\t"
    "s_add_u32 s39, s39, 0x8000\n\t"
    "ds_read_b64_tr_b16 a[228:229], v203 offset:0\n\t"
    "v_mfma_f32_16x16x32_bf16 a[24:27], a[232:235], a[168:171], a[24:27]\n\t"
    "ds_read_b64_tr_b16 a[230:231], v203 offset:1024\n\t"
    "buffer_load_dwordx4 v213, s[84:87], s39 offen lds\n\t"
    "v_mfma_f32_16x16x32_bf16 a[56:59], a[232:235], a[172:175], a[56:59]\n\t"
    "ds_read_b128 a[128:131], v234 offset:0\n\t"
    "v_mfma_f32_16x16x32_bf16 a[88:91], a[232:235], a[176:179], a[88:91]\n\t"
    "ds_read_b128 a[132:135], v234 offset:8192\n\t"
    "v_mfma_f32_16x16x32_bf16 a[120:123], a[232:235], a[180:183], a[120:123]\n\t"
    "s_lshr_b32 s37, s41, 1\n\t"
    "s_add_u32 m0, s37, 0xc000\n\t"
    "ds_read_b64_tr_b16 a[232:233], v204 offset:0\n\t"
    "v_mfma_f32_16x16x32_bf16 a[28:31], a[236:239], a[168:171], a[28:31]\n\t"
    "s_add_u32 s39, s58, 0xc0000\n\t"
    "ds_read_b64_tr_b16 a[234:235], v204 offset:1024\n\t"
    "v_mfma_f32_16x16x32_bf16 a[60:63], a[236:239], a[172:175], a[60:63]\n\t"
    "ds_read_b128 a[136:139], v234 offset:16384\n\t"
    "buffer_load_dwordx4 v214, s[88:91], s39 offen lds\n\t"
    "v_mfma_f32_16x16x32_bf16 a[92:95], a[236:239], a[176:179], a[92:95]\n\t"
    "ds_read_b128 a[140:143], v234 offset:24576\n\t"
    "v_mfma_f32_16x16x32_bf16 a[124:127], a[236:239], a[180:183], a[124:127]\n\t"
    "ds_read_b64_tr_b16 a[236:237], v205 offset:0\n\t"
    "v_mfma_f32_16x16x32_bf16 v[128:131], a[240:243], a[184:187], v[128:131]\n\t"
    "s_add_u32 m0, m0, 0x400\n\t"
    "ds_read_b64_tr_b16 a[238:239], v205 offset:1024\n\t"
    "v_mfma_f32_16x16x32_bf16 v[132:135], a[240:243], a[188:191], v[132:135]\n\t"
    "s_add_u32 s39, s39, 0x6000\n\t"
    "ds_read_b128 a[144:147], v192 offset:0\n\t"
    "v_mfma_f32_16x16x32_bf16 v[136:139], a[240:243], a[192:195], v[136:139]\n\t"
    "ds_read_b128 a[148:151], v192 offset:2048\n\t"
    "buffer_load_dwordx4 v215, s[88:91], s39 offen lds\n\t"
    "v_mfma_f32_16x16x32_bf16 v[140:143], a[240:243], a[196:199], v[140:143]\n\t"
    "ds_read_b64_tr_b16 a[240:241], v206 offset:0\n\t"
    "v_mfma_f32_16x16x32_bf16 v[144:147], a[244:247], a[184:187], v[144:147]\n\t"
    "ds_read_b64_tr_b16 a[242:243], v206 offset:512\n\t"
    "v_mfma_f32_16x16x32_bf16 v[148:151], a[244:247], a[188:191], v[148:151]\n\t"
    "s_add_u32 m0, s59, 0xe000\n\t"
    "ds_read_b128 a[152:155], v192 offset:16384\n\t"
    "v_mfma_f32_16x16x32_bf16 v[152:155], a[244:247], a[192:195], v[152:155]\n\t"
    "s_add_u32 s39, s56, 0x200\n\t"
    "ds_read_b128 a[156:159], v192 offset:18432\n\t"
    "v_mfma_f32_16x16x32_bf16 v[156:159], a[244:247], a[196:199], v[156:159]\n\t"
    "ds_read_b128 a[160:163], v194\n\t"
    "buffer_load_dwordx4 v211, s[8:11], s39 offen lds\n\t"
    "v_mfma_f32_16x16x32_bf16 v[160:163], a[244:247], a[200:203], v[160:163]\n\t"
    "ds_read_b128 a[164:167], v196\n\t"
    "v_mfma_f32_16x16x32_bf16 v[164:167], a[244:247], a[204:207], v[164:167]\n\t"
    "ds_read_b64_tr_b16 a[244:245], v207 offset:0\n\t"
    "v_mfma_f32_16x16x32_bf16 v[168:171], a[184:187], a[248:251], v[168:171]\n\t"
    "ds_read_b64_tr_b16 a[246:247], v207 offset:512\n\t"
    "v_mfma_f32_16x16x32_bf16 v[172:175], a[188:191], a[248:251], v[172:175]\n\t"
    "v_mfma_f32_16x16x32_bf16 v[176:179], a[192:195], a[248:251], v[176:179]\n\t"
    "v_mfma_f32_16x16x32_bf16 v[180:183], a[196:199], a[248:251], v[180:183]\n\t"
    "v_mfma_f32_16x16x32_bf16 v[184:187], a[200:203], a[248:251], v[184:187]\n\t"
    "v_mfma_f32_16x16x32_bf16 v[188:191], a[204:207], a[248:251], v[188:191]\n\t"
    "ds_read_b64_tr_b16 a[248:249], v208 offset:0\n\t"
    "ds_read_b64_tr_b16 a[250:251], v208 offset:512\n\t"
    "s_waitcnt lgkmcnt(15)\n\t"
    "v_mfma_f32_16x16x32_bf16 a[0:3], a[208:211], a[128:131], a[0:3]\n\t"
    "s_waitcnt lgkmcnt(15)\n\t"
    "v_mfma_f32_16x16x32_bf16 a[32:35], a[208:211], a[132:135], a[32:35]\n\t"
    "s_waitcnt lgkmcnt(15)\n\t"
    "ds_read_b128 a[168:171], v235 offset:0\n\t"
    "v_mfma_f32_16x16x32_bf16 a[64:67], a[208:211], a[136:139], a[64:67]\n\t"
    "s_waitcnt lgkmcnt(15)\n\t"
    "v_mfma_f32_16x16x32_bf16 a[96:99], a[208:211], a[140:143], a[96:99]\n\t"
    "s_add_u32 m0, s41, 0x0\n\t"
    "ds_read_b64_tr_b16 a[208:209], v198 offset:8192\n\t"
    "v_mfma_f32_16x16x32_bf16 a[4:7], a[212:215], a[128:131], a[4:7]\n\t"
    "s_add_u32 s39, s54, 0x280\n\t"
    "ds_read_b64_tr_b16 a[210:211], v198 offset:9216\n\t"
    "v_mfma_f32_16x16x32_bf16 a[36:39], a[212:215], a[132:135], a[36:39]\n\t"
    "s_add_u32 s63, s53, 0x3000\n\t"
    "buffer_load_dwordx4 v209, s[8:11], s39 offen lds\n\t"
    "v_mfma_f32_16x16x32_bf16 a[68:71], a[212:215], a[136:139], a[68:71]\n\t"
    "ds_read_b128 a[172:175], v235 offset:8192\n\t"
    "buffer_store_dwordx4 v[56:59], v216, s[20:23], s63 offen offset:2048 sc1\n\t"
    "v_mfma_f32_16x16x32_bf16 a[100:103], a[212:215], a[140:143], a[100:103]\n\t"
    "ds_read_b64_tr_b16 a[212:213], v199 offset:8192\n\t"
    "v_mfma_f32_16x16x32_bf16 a[8:11], a[216:219], a[128:131], a[8:11]\n\t"
    "s_add_u32 m0, m0, 0x400\n\t"
    "ds_read_b64_tr_b16 a[214:215], v199 offset:9216\n\t"
    "v_mfma_f32_16x16x32_bf16 a[40:43], a[216:219], a[132:135], a[40:43]\n\t"
    "s_add_u32 s39, s39, 0x2000\n\t"
    "v_mfma_f32_16x16x32_bf16 a[72:75], a[216:219], a[136:139], a[72:75]\n\t"
    "ds_read_b128 a[176:179], v235 offset:16384\n\t"
    "buffer_load_dwordx4 v210, s[8:11], s39 offen lds\n\t"
    "v_mfma_f32_16x16x32_bf16 a[104:107], a[216:219], a[140:143], a[104:107]\n\t"
    "ds_read_b64_tr_b16 a[216:217], v200 offset:8192\n\t"
    "v_mfma_f32_16x16x32_bf16 a[12:15], a[220:223], a[128:131], a[12:15]\n\t"
    "ds_read_b64_tr_b16 a[218:219], v200 offset:9216\n\t"
    "v_mfma_f32_16x16x32_bf16 a[44:47], a[220:223], a[132:135], a[44:47]\n\t"
    "s_add_u32 m0, m0, 0x400\n\t"
    "v_mfma_f32_16x16x32_bf16 a[76:79], a[220:223], a[136:139], a[76:79]\n\t"
    "s_add_u32 s39, s39, 0x2000\n\t"
    "ds_read_b128 a[180:183], v235 offset:24576\n\t"
    "v_mfma_f32_16x16x32_bf16 a[108:111], a[220:223], a[140:143], a[108:111]\n\t"
    "ds_read_b64_tr_b16 a[220:221], v201 offset:8192\n\t"
    "buffer_load_dwordx4 v209, s[8:11], s39 offen lds\n\t"
    "v_mfma_f32_16x16x32_bf16 a[16:19], a[224:227], a[128:131], a[16:19]\n\t"
    "ds_read_b64_tr_b16 a[222:223], v201 offset:9216\n\t"
    "v_mfma_f32_16x16x32_bf16 a[48:51], a[224:227], a[132:135], a[48:51]\n\t"
    "v_mfma_f32_16x16x32_bf16 a[80:83], a[224:227], a[136:139], a[80:83]\n\t"
    "s_add_u32 m0, m0, 0x400\n\t"
    "ds_read_b128 a[184:187], v193 offset:0\n\t"
    "v_mfma_f32_16x16x32_bf16 a[112:115], a[224:227], a[140:143], a[112:115]\n\t"
    "s_add_u32 s39, s39, 0x2000\n\t"
    "ds_read_b64_tr_b16 a[224:225], v202 offset:8192\n\t"
    "v_mfma_f32_16x16x32_bf16 a[20:23], a[228:231], a[128:131], a[20:23]\n\t"
    "ds_read_b64_tr_b16 a[226:227], v202 offset:9216\n\t"
    "buffer_load_dwordx4 v210, s[8:11], s39 offen lds\n\t"
    "v_mfma_f32_16x16x32_bf16 a[52:55], a[228:231], a[132:135], a[52:55]\n\t"
    "v_mfma_f32_16x16x32_bf16 a[84:87], a[228:231], a[136:139], a[84:87]\n\t"
    "ds_read_b128 a[188:191], v193 offset:2048\n\t"
    "v_mfma_f32_16x16x32_bf16 a[116:119], a[228:231], a[140:143], a[116:119]\n\t"
    "s_add_u32 m0, s41, 0x4000\n\t"
    "ds_read_b64_tr_b16 a[228:229], v203 offset:8192\n\t"
    "v_mfma_f32_16x16x32_bf16 a[24:27], a[232:235], a[128:131], a[24:27]\n\t"
    "s_add_u32 s39, s55, 0x280\n\t"
    "ds_read_b64_tr_b16 a[230:231], v203 offset:9216\n\t"
    "v_mfma_f32_16x16x32_bf16 a[56:59], a[232:235], a[132:135], a[56:59]\n\t"
    "buffer_load_dwordx4 v209, s[8:11], s39 offen lds\n\t"
    "v_mfma_f32_16x16x32_bf16 a[88:91], a[232:235], a[136:139], a[88:91]\n\t"
    "s_add_u32 s63, s53, 0x3000\n\t"
    "ds_read_b128 a[192:195], v193 offset:16384\n\t"
    "v_mfma_f32_16x16x32_bf16 a[120:123], a[232:235], a[140:143], a[120:123]\n\t"
    "s_waitcnt lgkmcnt(15)\n\t"
    "ds_read_b64_tr_b16 a[232:233], v204 offset:8192\n\t"
    "buffer_store_dwordx4 v[60:63], v216, s[20:23], s63 offen offset:3072 sc1\n\t"
    "v_mfma_f32_16x16x32_bf16 a[28:31], a[236:239], a[128:131], a[28:31]\n\t"
    "s_add_u32 m0, m0, 0x400\n\t"
    "ds_read_b64_tr_b16 a[234:235], v204 offset:9216\n\t"
    "v_mfma_f32_16x16x32_bf16 a[60:63], a[236:239], a[132:135], a[60:63]\n\t"
    "s_add_u32 s39, s39, 0x2000\n\t"
    "v_mfma_f32_16x16x32_bf16 a[92:95], a[236:239], a[136:139], a[92:95]\n\t"
    "ds_read_b128 a[196:199], v193 offset:18432\n\t"
    "buffer_load_dwordx4 v210, s[8:11], s39 offen lds\n\t"
    "v_mfma_f32_16x16x32_bf16 a[124:127], a[236:239], a[140:143], a[124:127]\n\t"
    "s_waitcnt lgkmcnt(15)\n\t"
    "ds_read_b64_tr_b16 a[236:237], v205 offset:8192\n\t"
    "v_mfma_f32_16x16x32_bf16 v[128:131], a[240:243], a[144:147], v[128:131]\n\t"
    "ds_read_b64_tr_b16 a[238:239], v205 offset:9216\n\t"
    "v_mfma_f32_16x16x32_bf16 v[132:135], a[240:243], a[148:151], v[132:135]\n\t"
    "s_waitcnt lgkmcnt(15)\n\t"
    "s_add_u32 m0, m0, 0x400\n\t"
    "v_mfma_f32_16x16x32_bf16 v[136:139], a[240:243], a[152:155], v[136:139]\n\t"
    "s_waitcnt lgkmcnt(15)\n\t"
    "s_add_u32 s39, s39, 0x2000\n\t"
    "ds_read_b128 a[200:203], v195\n\t"
    "v_mfma_f32_16x16x32_bf16 v[140:143], a[240:243], a[156:159], v[140:143]\n\t"
    "s_waitcnt lgkmcnt(15)\n\t"
    "ds_read_b64_tr_b16 a[240:241], v206 offset:4096\n\t"
    "buffer_load_dwordx4 v209, s[8:11], s39 offen lds\n\t"
    "v_mfma_f32_16x16x32_bf16 v[144:147], a[244:247], a[144:147], v[144:147]\n\t"
    "ds_read_b64_tr_b16 a[242:243], v206 offset:4608\n\t"
    "v_mfma_f32_16x16x32_bf16 v[148:151], a[244:247], a[148:151], v[148:151]\n\t"
    "v_mfma_f32_16x16x32_bf16 v[152:155], a[244:247], a[152:155], v[152:155]\n\t"
    "s_add_u32 m0, m0, 0x400\n\t"
    "ds_read_b128 a[204:207], v197\n\t"
    "v_mfma_f32_16x16x32_bf16 v[156:159], a[244:247], a[156:159], v[156:159]\n\t"
    "s_add_u32 s39, s39, 0x2000\n\t"
    "v_mfma_f32_16x16x32_bf16 v[160:163], a[244:247], a[160:163], v[160:163]\n\t"
    "buffer_load_dwordx4 v210, s[8:11], s39 offen lds\n\t"
    "v_mfma_f32_16x16x32_bf16 v[164:167], a[244:247], a[164:167], v[164:167]\n\t"
    "s_waitcnt lgkmcnt(15)\n\t"
    "ds_read_b64_tr_b16 a[244:245], v207 offset:4096\n\t"
    "v_mfma_f32_16x16x32_bf16 v[168:171], a[144:147], a[248:251], v[168:171]\n\t"
    "ds_read_b64_tr_b16 a[246:247], v207 offset:4608\n\t"
    "v_mfma_f32_16x16x32_bf16 v[172:175], a[148:151], a[248:251], v[172:175]\n\t"
    "v_mfma_f32_16x16x32_bf16 v[176:179], a[152:155], a[248:251], v[176:179]\n\t"
    "v_mfma_f32_16x16x32_bf16 v[180:183], a[156:159], a[248:251], v[180:183]\n\t"
    "v_mfma_f32_16x16x32_bf16 v[184:187], a[160:163], a[248:251], v[184:187]\n\t"
    "v_mfma_f32_16x16x32_bf16 v[188:191], a[164:167], a[248:251], v[188:191]\n\t"
    "s_waitcnt lgkmcnt(15)\n\t"
    "ds_read_b64_tr_b16 a[248:249], v208 offset:4096\n\t"
    "v_mfma_f32_16x16x32_bf16 a[0:3], a[208:211], a[168:171], a[0:3]\n\t"
    "s_waitcnt lgkmcnt(15)\n\t"
    "s_cmp_eq_u32 s33, 0\n\t"
    "s_cselect_b32 s62, s65, s65\n\t"
    "ds_read_b64_tr_b16 a[250:251], v208 offset:4608\n\t"
    "v_mfma_f32_16x16x32_bf16 a[32:35], a[208:211], a[172:175], a[32:35]\n\t"
    "s_waitcnt lgkmcnt(15)\n\t"
    "v_mfma_f32_16x16x32_bf16 a[64:67], a[208:211], a[176:179], a[64:67]\n\t"
    "s_waitcnt lgkmcnt(15)\n\t"
    "v_mfma_f32_16x16x32_bf16 a[96:99], a[208:211], a[180:183], a[96:99]\n\t"
    "v_add_u32 v198, 0xffff0000, v198\n\t"
    "v_add_u32 v199, 0xffff0000, v199\n\t"
    "v_mfma_f32_16x16x32_bf16 a[4:7], a[212:215], a[168:171], a[4:7]\n\t"
    "v_add_u32 v200, 0xffff0000, v200\n\t"
    "v_add_u32 v201, 0xffff0000, v201\n\t"
    "v_mfma_f32_16x16x32_bf16 a[36:39], a[212:215], a[172:175], a[36:39]\n\t"
    "s_waitcnt vmcnt(10) lgkmcnt(0)\n\t"
    "s_barrier\n\t"
    "v_add_u32 v202, 0xffff0000, v202\n\t"
    "v_add_u32 v203, 0xffff0000, v203\n\t"
    "v_mfma_f32_16x16x32_bf16 a[68:71], a[212:215], a[176:179], a[68:71]\n\t"
    "v_add_u32 v204, 0xffff0000, v204\n\t"
    "v_add_u32 v205, 0xffff0000, v205\n\t"
    "v_mfma_f32_16x16x32_bf16 a[100:103], a[212:215], a[180:183], a[100:103]\n\t"
    "s_add_u32 m0, s41, 0x18000\n\t"
    "v_add_u32 v206, 0xffff0000, v206\n\t"
    "v_add_u32 v207, 0xffff0000, v207\n\t"
    "v_mfma_f32_16x16x32_bf16 a[8:11], a[216:219], a[168:171], a[8:11]\n\t"
    "s_add_u32 s39, s57, 0x280000\n\t"
    "v_add_u32 v208, 0xffff0000, v208\n\t"
    "v_add_u32 v192, 0xffff0000, v192\n\t"
    "v_mfma_f32_16x16x32_bf16 a[40:43], a[216:219], a[172:175], a[40:43]\n\t"
    "buffer_load_dwordx4 v212, s[84:87], s39 offen lds\n\t"
    "v_add_u32 v193, 0xffff0000, v193\n\t"
    "v_add_u32 v234, 0xffff0000, v234\n\t"
    "v_mfma_f32_16x16x32_bf16 a[72:75], a[216:219], a[176:179], a[72:75]\n\t"
    "v_add_u32 v235, 0xffff0000, v235\n\t"
    "v_add_u32 v194, s62, v194\n\t"
    "v_mfma_f32_16x16x32_bf16 a[104:107], a[216:219], a[180:183], a[104:107]\n\t"
    "v_add_u32 v195, s62, v195\n\t"
    "v_add_u32 v196, s62, v196\n\t"
    "v_mfma_f32_16x16x32_bf16 a[12:15], a[220:223], a[168:171], a[12:15]\n\t"
    "s_add_u32 m0, m0, 0x400\n\t"
    "v_add_u32 v197, s62, v197\n\t"
    "v_mfma_f32_16x16x32_bf16 a[44:47], a[220:223], a[172:175], a[44:47]\n\t"
    "s_add_u32 s39, s39, 0x8000\n\t"
    "ds_read_b64_tr_b16 a[208:209], v198 offset:0\n\t"
    "v_mfma_f32_16x16x32_bf16 a[76:79], a[220:223], a[176:179], a[76:79]\n\t"
    "ds_read_b64_tr_b16 a[210:211], v198 offset:1024\n\t"
    "buffer_load_dwordx4 v212, s[84:87], s39 offen lds\n\t"
    "v_mfma_f32_16x16x32_bf16 a[108:111], a[220:223], a[180:183], a[108:111]\n\t"
    "ds_read_b64_tr_b16 a[212:213], v199 offset:0\n\t"
    "v_mfma_f32_16x16x32_bf16 a[16:19], a[224:227], a[168:171], a[16:19]\n\t"
    "ds_read_b64_tr_b16 a[214:215], v199 offset:1024\n\t"
    "v_mfma_f32_16x16x32_bf16 a[48:51], a[224:227], a[172:175], a[48:51]\n\t"
    "s_add_u32 m0, m0, 0x400\n\t"
    "ds_read_b64_tr_b16 a[216:217], v200 offset:0\n\t"
    "v_mfma_f32_16x16x32_bf16 a[80:83], a[224:227], a[176:179], a[80:83]\n\t"
    "s_add_u32 s39, s39, 0x8000\n\t"
    "ds_read_b64_tr_b16 a[218:219], v200 offset:1024\n\t"
    "v_mfma_f32_16x16x32_bf16 a[112:115], a[224:227], a[180:183], a[112:115]\n\t"
    "ds_read_b64_tr_b16 a[220:221], v201 offset:0\n\t"
    "buffer_load_dwordx4 v213, s[84:87], s39 offen lds\n\t"
    "v_mfma_f32_16x16x32_bf16 a[20:23], a[228:231], a[168:171], a[20:23]\n\t"
    "ds_read_b64_tr_b16 a[222:223], v201 offset:1024\n\t"
    "v_mfma_f32_16x16x32_bf16 a[52:55], a[228:231], a[172:175], a[52:55]\n\t"
    "ds_read_b64_tr_b16 a[224:225], v202 offset:0\n\t"
    "v_mfma_f32_16x16x32_bf16 a[84:87], a[228:231], a[176:179], a[84:87]\n\t"
    "s_add_u32 m0, m0, 0x400\n\t"
    "ds_read_b64_tr_b16 a[226:227], v202 offset:1024\n\t"
    "v_mfma_f32_16x16x32_bf16 a[116:119], a[228:231], a[180:183], a[116:119]\n\t"
    "s_add_u32 s39, s39, 0x8000\n\t"
    "ds_read_b64_tr_b16 a[228:229], v203 offset:0\n\t"
    "v_mfma_f32_16x16x32_bf16 a[24:27], a[232:235], a[168:171], a[24:27]\n\t"
    "ds_read_b64_tr_b16 a[230:231], v203 offset:1024\n\t"
    "buffer_load_dwordx4 v213, s[84:87], s39 offen lds\n\t"
    "v_mfma_f32_16x16x32_bf16 a[56:59], a[232:235], a[172:175], a[56:59]\n\t"
    "ds_read_b128 a[128:131], v234 offset:0\n\t"
    "v_mfma_f32_16x16x32_bf16 a[88:91], a[232:235], a[176:179], a[88:91]\n\t"
    "ds_read_b128 a[132:135], v234 offset:8192\n\t"
    "v_mfma_f32_16x16x32_bf16 a[120:123], a[232:235], a[180:183], a[120:123]\n\t"
    "s_lshr_b32 s37, s41, 1\n\t"
    "s_add_u32 m0, s37, 0x1c000\n\t"
    "ds_read_b64_tr_b16 a[232:233], v204 offset:0\n\t"
    "v_mfma_f32_16x16x32_bf16 a[28:31], a[236:239], a[168:171], a[28:31]\n\t"
    "s_add_u32 s39, s58, 0xf0000\n\t"
    "ds_read_b64_tr_b16 a[234:235], v204 offset:1024\n\t"
    "v_mfma_f32_16x16x32_bf16 a[60:63], a[236:239], a[172:175], a[60:63]\n\t"
    "ds_read_b128 a[136:139], v234 offset:16384\n\t"
    "buffer_load_dwordx4 v214, s[88:91], s39 offen lds\n\t"
    "v_mfma_f32_16x16x32_bf16 a[92:95], a[236:239], a[176:179], a[92:95]\n\t"
    "ds_read_b128 a[140:143], v234 offset:24576\n\t"
    "v_mfma_f32_16x16x32_bf16 a[124:127], a[236:239], a[180:183], a[124:127]\n\t"
    "ds_read_b64_tr_b16 a[236:237], v205 offset:0\n\t"
    "v_mfma_f32_16x16x32_bf16 v[128:131], a[240:243], a[184:187], v[128:131]\n\t"
    "s_add_u32 m0, m0, 0x400\n\t"
    "ds_read_b64_tr_b16 a[238:239], v205 offset:1024\n\t"
    "v_mfma_f32_16x16x32_bf16 v[132:135], a[240:243], a[188:191], v[132:135]\n\t"
    "s_add_u32 s39, s39, 0x6000\n\t"
    "ds_read_b128 a[144:147], v192 offset:0\n\t"
    "v_mfma_f32_16x16x32_bf16 v[136:139], a[240:243], a[192:195], v[136:139]\n\t"
    "ds_read_b128 a[148:151], v192 offset:2048\n\t"
    "buffer_load_dwordx4 v215, s[88:91], s39 offen lds\n\t"
    "v_mfma_f32_16x16x32_bf16 v[140:143], a[240:243], a[196:199], v[140:143]\n\t"
    "ds_read_b64_tr_b16 a[240:241], v206 offset:0\n\t"
    "v_mfma_f32_16x16x32_bf16 v[144:147], a[244:247], a[184:187], v[144:147]\n\t"
    "ds_read_b64_tr_b16 a[242:243], v206 offset:512\n\t"
    "v_mfma_f32_16x16x32_bf16 v[148:151], a[244:247], a[188:191], v[148:151]\n\t"
    "s_add_u32 m0, s59, 0x1e000\n\t"
    "ds_read_b128 a[152:155], v192 offset:16384\n\t"
    "v_mfma_f32_16x16x32_bf16 v[152:155], a[244:247], a[192:195], v[152:155]\n\t"
    "s_add_u32 s39, s56, 0x280\n\t"
    "ds_read_b128 a[156:159], v192 offset:18432\n\t"
    "v_mfma_f32_16x16x32_bf16 v[156:159], a[244:247], a[196:199], v[156:159]\n\t"
    "ds_read_b128 a[160:163], v194\n\t"
    "buffer_load_dwordx4 v211, s[8:11], s39 offen lds\n\t"
    "v_mfma_f32_16x16x32_bf16 v[160:163], a[244:247], a[200:203], v[160:163]\n\t"
    "ds_read_b128 a[164:167], v196\n\t"
    "v_mfma_f32_16x16x32_bf16 v[164:167], a[244:247], a[204:207], v[164:167]\n\t"
    "ds_read_b64_tr_b16 a[244:245], v207 offset:0\n\t"
    "v_mfma_f32_16x16x32_bf16 v[168:171], a[184:187], a[248:251], v[168:171]\n\t"
    "ds_read_b64_tr_b16 a[246:247], v207 offset:512\n\t"
    "v_mfma_f32_16x16x32_bf16 v[172:175], a[188:191], a[248:251], v[172:175]\n\t"
    "v_mfma_f32_16x16x32_bf16 v[176:179], a[192:195], a[248:251], v[176:179]\n\t"
    "v_mfma_f32_16x16x32_bf16 v[180:183], a[196:199], a[248:251], v[180:183]\n\t"
    "v_mfma_f32_16x16x32_bf16 v[184:187], a[200:203], a[248:251], v[184:187]\n\t"
    "v_mfma_f32_16x16x32_bf16 v[188:191], a[204:207], a[248:251], v[188:191]\n\t"
    "ds_read_b64_tr_b16 a[248:249], v208 offset:0\n\t"
    "ds_read_b64_tr_b16 a[250:251], v208 offset:512\n\t"
    "s_waitcnt lgkmcnt(15)\n\t"
    "v_mfma_f32_16x16x32_bf16 a[0:3], a[208:211], a[128:131], a[0:3]\n\t"
    "s_waitcnt lgkmcnt(15)\n\t"
    "v_mfma_f32_16x16x32_bf16 a[32:35], a[208:211], a[132:135], a[32:35]\n\t"
    "s_waitcnt lgkmcnt(15)\n\t"
    "ds_read_b128 a[168:171], v235 offset:0\n\t"
    "v_mfma_f32_16x16x32_bf16 a[64:67], a[208:211], a[136:139], a[64:67]\n\t"
    "s_waitcnt lgkmcnt(15)\n\t"
    "v_mfma_f32_16x16x32_bf16 a[96:99], a[208:211], a[140:143], a[96:99]\n\t"
    "s_add_u32 m0, s41, 0x20000\n\t"
    "ds_read_b64_tr_b16 a[208:209], v198 offset:8192\n\t"
    "v_mfma_f32_16x16x32_bf16 a[4:7], a[212:215], a[128:131], a[4:7]\n\t"
    "s_add_u32 s39, s54, 0x300\n\t"
    "ds_read_b64_tr_b16 a[210:211], v198 offset:9216\n\t"
    "v_mfma_f32_16x16x32_bf16 a[36:39], a[212:215], a[132:135], a[36:39]\n\t"
    "s_add_u32 s63, s53, 0x8000\n\t"
    "buffer_load_dwordx4 v209, s[8:11], s39 offen lds\n\t"
    "v_mfma_f32_16x16x32_bf16 a[68:71], a[212:215], a[136:139], a[68:71]\n\t"
    "ds_read_b128 a[172:175], v235 offset:8192\n\t"
    "buffer_store_dwordx4 v[64:67], v216, s[20:23], s63 offen offset:0 sc1\n\t"
    "v_mfma_f32_16x16x32_bf16 a[100:103], a[212:215], a[140:143], a[100:103]\n\t"
    "ds_read_b64_tr_b16 a[212:213], v199 offset:8192\n\t"
    "v_mfma_f32_16x16x32_bf16 a[8:11], a[216:219], a[128:131], a[8:11]\n\t"
    "s_add_u32 m0, m0, 0x400\n\t"
    "ds_read_b64_tr_b16 a[214:215], v199 offset:9216\n\t"
    "v_mfma_f32_16x16x32_bf16 a[40:43], a[216:219], a[132:135], a[40:43]\n\t"
    "s_add_u32 s39, s39, 0x2000\n\t"
    "v_mfma_f32_16x16x32_bf16 a[72:75], a[216:219], a[136:139], a[72:75]\n\t"
    "ds_read_b128 a[176:179], v235 offset:16384\n\t"
    "buffer_load_dwordx4 v210, s[8:11], s39 offen lds\n\t"
    "v_mfma_f32_16x16x32_bf16 a[104:107], a[216:219], a[140:143], a[104:107]\n\t"
    "ds_read_b64_tr_b16 a[216:217], v200 offset:8192\n\t"
    "v_mfma_f32_16x16x32_bf16 a[12:15], a[220:223], a[128:131], a[12:15]\n\t"
    "ds_read_b64_tr_b16 a[218:219], v200 offset:9216\n\t"
    "v_mfma_f32_16x16x32_bf16 a[44:47], a[220:223], a[132:135], a[44:47]\n\t"
    "s_add_u32 m0, m0, 0x400\n\t"
    "v_mfma_f32_16x16x32_bf16 a[76:79], a[220:223], a[136:139], a[76:79]\n\t"
    "s_add_u32 s39, s39, 0x2000\n\t"
    "ds_read_b128 a[180:183], v235 offset:24576\n\t"
    "v_mfma_f32_16x16x32_bf16 a[108:111], a[220:223], a[140:143], a[108:111]\n\t"
    "ds_read_b64_tr_b16 a[220:221], v201 offset:8192\n\t"
    "buffer_load_dwordx4 v209, s[8:11], s39 offen lds\n\t"
    "v_mfma_f32_16x16x32_bf16 a[16:19], a[224:227], a[128:131], a[16:19]\n\t"
    "ds_read_b64_tr_b16 a[222:223], v201 offset:9216\n\t"
    "v_mfma_f32_16x16x32_bf16 a[48:51], a[224:227], a[132:135], a[48:51]\n\t"
    "v_mfma_f32_16x16x32_bf16 a[80:83], a[224:227], a[136:139], a[80:83]\n\t"
    "s_add_u32 m0, m0, 0x400\n\t"
    "ds_read_b128 a[184:187], v193 offset:0\n\t"
    "v_mfma_f32_16x16x32_bf16 a[112:115], a[224:227], a[140:143], a[112:115]\n\t"
    "s_add_u32 s39, s39, 0x2000\n\t"
    "ds_read_b64_tr_b16 a[224:225], v202 offset:8192\n\t"
    "v_mfma_f32_16x16x32_bf16 a[20:23], a[228:231], a[128:131], a[20:23]\n\t"
    "ds_read_b64_tr_b16 a[226:227], v202 offset:9216\n\t"
    "buffer_load_dwordx4 v210, s[8:11], s39 offen lds\n\t"
    "v_mfma_f32_16x16x32_bf16 a[52:55], a[228:231], a[132:135], a[52:55]\n\t"
    "v_mfma_f32_16x16x32_bf16 a[84:87], a[228:231], a[136:139], a[84:87]\n\t"
    "ds_read_b128 a[188:191], v193 offset:2048\n\t"
    "v_mfma_f32_16x16x32_bf16 a[116:119], a[228:231], a[140:143], a[116:119]\n\t"
    "s_add_u32 m0, s41, 0x24000\n\t"
    "ds_read_b64_tr_b16 a[228:229], v203 offset:8192\n\t"
    "v_mfma_f32_16x16x32_bf16 a[24:27], a[232:235], a[128:131], a[24:27]\n\t"
    "s_add_u32 s39, s55, 0x300\n\t"
    "ds_read_b64_tr_b16 a[230:231], v203 offset:9216\n\t"
    "v_mfma_f32_16x16x32_bf16 a[56:59], a[232:235], a[132:135], a[56:59]\n\t"
    "buffer_load_dwordx4 v209, s[8:11], s39 offen lds\n\t"
    "v_mfma_f32_16x16x32_bf16 a[88:91], a[232:235], a[136:139], a[88:91]\n\t"
    "s_add_u32 s63, s53, 0x8000\n\t"
    "ds_read_b128 a[192:195], v193 offset:16384\n\t"
    "v_mfma_f32_16x16x32_bf16 a[120:123], a[232:235], a[140:143], a[120:123]\n\t"
    "s_waitcnt lgkmcnt(15)\n\t"
    "ds_read_b64_tr_b16 a[232:233], v204 offset:8192\n\t"
    "buffer_store_dwordx4 v[68:71], v216, s[20:23], s63 offen offset:1024 sc1\n\t"
    "v_mfma_f32_16x16x32_bf16 a[28:31], a[236:239], a[128:131], a[28:31]\n\t"
    "s_add_u32 m0, m0, 0x400\n\t"
    "ds_read_b64_tr_b16 a[234:235], v204 offset:9216\n\t"
    "v_mfma_f32_16x16x32_bf16 a[60:63], a[236:239], a[132:135], a[60:63]\n\t"
    "s_add_u32 s39, s39, 0x2000\n\t"
    "v_mfma_f32_16x16x32_bf16 a[92:95], a[236:239], a[136:139], a[92:95]\n\t"
    "ds_read_b128 a[196:199], v193 offset:18432\n\t"
    "buffer_load_dwordx4 v210, s[8:11], s39 offen lds\n\t"
    "v_mfma_f32_16x16x32_bf16 a[124:127], a[236:239], a[140:143], a[124:127]\n\t"
    "s_waitcnt lgkmcnt(15)\n\t"
    "ds_read_b64_tr_b16 a[236:237], v205 offset:8192\n\t"
    "v_mfma_f32_16x16x32_bf16 v[128:131], a[240:243], a[144:147], v[128:131]\n\t"
    "ds_read_b64_tr_b16 a[238:239], v205 offset:9216\n\t"
    "v_mfma_f32_16x16x32_bf16 v[132:135], a[240:243], a[148:151], v[132:135]\n\t"
    "s_waitcnt lgkmcnt(15)\n\t"
    "s_add_u32 m0, m0, 0x400\n\t"
    "v_mfma_f32_16x16x32_bf16 v[136:139], a[240:243], a[152:155], v[136:139]\n\t"
    "s_waitcnt lgkmcnt(15)\n\t"
    "s_add_u32 s39, s39, 0x2000\n\t"
    "ds_read_b128 a[200:203], v195\n\t"
    "v_mfma_f32_16x16x32_bf16 v[140:143], a[240:243], a[156:159], v[140:143]\n\t"
    "s_waitcnt lgkmcnt(15)\n\t"
    "ds_read_b64_tr_b16 a[240:241], v206 offset:4096\n\t"
    "buffer_load_dwordx4 v209, s[8:11], s39 offen lds\n\t"
    "v_mfma_f32_16x16x32_bf16 v[144:147], a[244:247], a[144:147], v[144:147]\n\t"
    "ds_read_b64_tr_b16 a[242:243], v206 offset:4608\n\t"
    "v_mfma_f32_16x16x32_bf16 v[148:151], a[244:247], a[148:151], v[148:151]\n\t"
    "v_mfma_f32_16x16x32_bf16 v[152:155], a[244:247], a[152:155], v[152:155]\n\t"
    "s_add_u32 m0, m0, 0x400\n\t"
    "ds_read_b128 a[204:207], v197\n\t"
    "v_mfma_f32_16x16x32_bf16 v[156:159], a[244:247], a[156:159], v[156:159]\n\t"
    "s_add_u32 s39, s39, 0x2000\n\t"
    "v_mfma_f32_16x16x32_bf16 v[160:163], a[244:247], a[160:163], v[160:163]\n\t"
    "buffer_load_dwordx4 v210, s[8:11], s39 offen lds\n\t"
    "v_mfma_f32_16x16x32_bf16 v[164:167], a[244:247], a[164:167], v[164:167]\n\t"
    "s_waitcnt lgkmcnt(15)\n\t"
    "ds_read_b64_tr_b16 a[244:245], v207 offset:4096\n\t"
    "v_mfma_f32_16x16x32_bf16 v[168:171], a[144:147], a[248:251], v[168:171]\n\t"
    "ds_read_b64_tr_b16 a[246:247], v207 offset:4608\n\t"
    "v_mfma_f32_16x16x32_bf16 v[172:175], a[148:151], a[248:251], v[172:175]\n\t"
    "v_mfma_f32_16x16x32_bf16 v[176:179], a[152:155], a[248:251], v[176:179]\n\t"
    "v_mfma_f32_16x16x32_bf16 v[180:183], a[156:159], a[248:251], v[180:183]\n\t"
    "v_mfma_f32_16x16x32_bf16 v[184:187], a[160:163], a[248:251], v[184:187]\n\t"
    "v_mfma_f32_16x16x32_bf16 v[188:191], a[164:167], a[248:251], v[188:191]\n\t"
    "s_waitcnt lgkmcnt(15)\n\t"
    "ds_read_b64_tr_b16 a[248:249], v208 offset:4096\n\t"
    "v_mfma_f32_16x16x32_bf16 a[0:3], a[208:211], a[168:171], a[0:3]\n\t"
    "s_waitcnt lgkmcnt(15)\n\t"
    "s_cmp_eq_u32 s33, 0\n\t"
    "s_cselect_b32 s62, s64, s65\n\t"
    "ds_read_b64_tr_b16 a[250:251], v208 offset:4608\n\t"
    "v_mfma_f32_16x16x32_bf16 a[32:35], a[208:211], a[172:175], a[32:35]\n\t"
    "s_waitcnt lgkmcnt(15)\n\t"
    "v_mfma_f32_16x16x32_bf16 a[64:67], a[208:211], a[176:179], a[64:67]\n\t"
    "s_waitcnt lgkmcnt(15)\n\t"
    "v_mfma_f32_16x16x32_bf16 a[96:99], a[208:211], a[180:183], a[96:99]\n\t"
    "v_add_u32 v198, 0x10000, v198\n\t"
    "v_add_u32 v199, 0x10000, v199\n\t"
    "v_mfma_f32_16x16x32_bf16 a[4:7], a[212:215], a[168:171], a[4:7]\n\t"
    "v_add_u32 v200, 0x10000, v200\n\t"
    "v_add_u32 v201, 0x10000, v201\n\t"
    "v_mfma_f32_16x16x32_bf16 a[36:39], a[212:215], a[172:175], a[36:39]\n\t"
    "s_waitcnt vmcnt(10) lgkmcnt(0)\n\t"
    "s_barrier\n\t"
    "v_add_u32 v202, 0x10000, v202\n\t"
    "v_add_u32 v203, 0x10000, v203\n\t"
    "v_mfma_f32_16x16x32_bf16 a[68:71], a[212:215], a[176:179], a[68:71]\n\t"
    "v_add_u32 v204, 0x10000, v204\n\t"
    "v_add_u32 v205, 0x10000, v205\n\t"
    "v_mfma_f32_16x16x32_bf16 a[100:103], a[212:215], a[180:183], a[100:103]\n\t"
    "s_add_u32 m0, s41, 0x8000\n\t"
    "v_add_u32 v206, 0x10000, v206\n\t"
    "v_add_u32 v207, 0x10000, v207\n\t"
    "v_mfma_f32_16x16x32_bf16 a[8:11], a[216:219], a[168:171], a[8:11]\n\t"
    "s_add_u32 s39, s57, 0x300000\n\t"
    "v_add_u32 v208, 0x10000, v208\n\t"
    "v_add_u32 v192, 0xffff0000, v192\n\t"
    "v_mfma_f32_16x16x32_bf16 a[40:43], a[216:219], a[172:175], a[40:43]\n\t"
    "buffer_load_dwordx4 v212, s[84:87], s39 offen lds\n\t"
    "v_add_u32 v193, 0xffff0000, v193\n\t"
    "v_add_u32 v234, 0xffff0000, v234\n\t"
    "v_mfma_f32_16x16x32_bf16 a[72:75], a[216:219], a[176:179], a[72:75]\n\t"
    "v_add_u32 v235, 0xffff0000, v235\n\t"
    "v_add_u32 v194, s62, v194\n\t"
    "v_mfma_f32_16x16x32_bf16 a[104:107], a[216:219], a[180:183], a[104:107]\n\t"
    "v_add_u32 v195, s62, v195\n\t"
    "v_add_u32 v196, s62, v196\n\t"
    "v_mfma_f32_16x16x32_bf16 a[12:15], a[220:223], a[168:171], a[12:15]\n\t"
    "s_add_u32 m0, m0, 0x400\n\t"
    "v_add_u32 v197, s62, v197\n\t"
    "v_mfma_f32_16x16x32_bf16 a[44:47], a[220:223], a[172:175], a[44:47]\n\t"
    "s_add_u32 s39, s39, 0x8000\n\t"
    "ds_read_b64_tr_b16 a[208:209], v198 offset:0\n\t"
    "v_mfma_f32_16x16x32_bf16 a[76:79], a[220:223], a[176:179], a[76:79]\n\t"
    "ds_read_b64_tr_b16 a[210:211], v198 offset:1024\n\t"
    "buffer_load_dwordx4 v212, s[84:87], s39 offen lds\n\t"
    "v_mfma_f32_16x16x32_bf16 a[108:111], a[220:223], a[180:183], a[108:111]\n\t"
    "ds_read_b64_tr_b16 a[212:213], v199 offset:0\n\t"
    "v_mfma_f32_16x16x32_bf16 a[16:19], a[224:227], a[168:171], a[16:19]\n\t"
    "ds_read_b64_tr_b16 a[214:215], v199 offset:1024\n\t"
    "v_mfma_f32_16x16x32_bf16 a[48:51], a[224:227], a[172:175], a[48:51]\n\t"
    "s_add_u32 m0, m0, 0x400\n\t"
    "ds_read_b64_tr_b16 a[216:217], v200 offset:0\n\t"
    "v_mfma_f32_16x16x32_bf16 a[80:83], a[224:227], a[176:179], a[80:83]\n\t"
    "s_add_u32 s39, s39, 0x8000\n\t"
    "ds_read_b64_tr_b16 a[218:219], v200 offset:1024\n\t"
    "v_mfma_f32_16x16x32_bf16 a[112:115], a[224:227], a[180:183], a[112:115]\n\t"
    "ds_read_b64_tr_b16 a[220:221], v201 offset:0\n\t"
    "buffer_load_dwordx4 v213, s[84:87], s39 offen lds\n\t"
    "v_mfma_f32_16x16x32_bf16 a[20:23], a[228:231], a[168:171], a[20:23]\n\t"
    "ds_read_b64_tr_b16 a[222:223], v201 offset:1024\n\t"
    "v_mfma_f32_16x16x32_bf16 a[52:55], a[228:231], a[172:175], a[52:55]\n\t"
    "ds_read_b64_tr_b16 a[224:225], v202 offset:0\n\t"
    "v_mfma_f32_16x16x32_bf16 a[84:87], a[228:231], a[176:179], a[84:87]\n\t"
    "s_add_u32 m0, m0, 0x400\n\t"
    "ds_read_b64_tr_b16 a[226:227], v202 offset:1024\n\t"
    "v_mfma_f32_16x16x32_bf16 a[116:119], a[228:231], a[180:183], a[116:119]\n\t"
    "s_add_u32 s39, s39, 0x8000\n\t"
    "ds_read_b64_tr_b16 a[228:229], v203 offset:0\n\t"
    "v_mfma_f32_16x16x32_bf16 a[24:27], a[232:235], a[168:171], a[24:27]\n\t"
    "ds_read_b64_tr_b16 a[230:231], v203 offset:1024\n\t"
    "buffer_load_dwordx4 v213, s[84:87], s39 offen lds\n\t"
    "v_mfma_f32_16x16x32_bf16 a[56:59], a[232:235], a[172:175], a[56:59]\n\t"
    "ds_read_b128 a[128:131], v234 offset:0\n\t"
    "v_mfma_f32_16x16x32_bf16 a[88:91], a[232:235], a[176:179], a[88:91]\n\t"
    "ds_read_b128 a[132:135], v234 offset:8192\n\t"
    "v_mfma_f32_16x16x32_bf16 a[120:123], a[232:235], a[180:183], a[120:123]\n\t"
    "s_lshr_b32 s37, s41, 1\n\t"
    "s_add_u32 m0, s37, 0xc000\n\t"
    "ds_read_b64_tr_b16 a[232:233], v204 offset:0\n\t"
    "v_mfma_f32_16x16x32_bf16 a[28:31], a[236:239], a[168:171], a[28:31]\n\t"
    "s_add_u32 s39, s58, 0x120000\n\t"
    "ds_read_b64_tr_b16 a[234:235], v204 offset:1024\n\t"
    "v_mfma_f32_16x16x32_bf16 a[60:63], a[236:239], a[172:175], a[60:63]\n\t"
    "ds_read_b128 a[136:139], v234 offset:16384\n\t"
    "buffer_load_dwordx4 v214, s[88:91], s39 offen lds\n\t"
    "v_mfma_f32_16x16x32_bf16 a[92:95], a[236:239], a[176:179], a[92:95]\n\t"
    "ds_read_b128 a[140:143], v234 offset:24576\n\t"
    "v_mfma_f32_16x16x32_bf16 a[124:127], a[236:239], a[180:183], a[124:127]\n\t"
    "ds_read_b64_tr_b16 a[236:237], v205 offset:0\n\t"
    "v_mfma_f32_16x16x32_bf16 v[128:131], a[240:243], a[184:187], v[128:131]\n\t"
    "s_add_u32 m0, m0, 0x400\n\t"
    "ds_read_b64_tr_b16 a[238:239], v205 offset:1024\n\t"
    "v_mfma_f32_16x16x32_bf16 v[132:135], a[240:243], a[188:191], v[132:135]\n\t"
    "s_add_u32 s39, s39, 0x6000\n\t"
    "ds_read_b128 a[144:147], v192 offset:0\n\t"
    "v_mfma_f32_16x16x32_bf16 v[136:139], a[240:243], a[192:195], v[136:139]\n\t"
    "ds_read_b128 a[148:151], v192 offset:2048\n\t"
    "buffer_load_dwordx4 v215, s[88:91], s39 offen lds\n\t"
    "v_mfma_f32_16x16x32_bf16 v[140:143], a[240:243], a[196:199], v[140:143]\n\t"
    "ds_read_b64_tr_b16 a[240:241], v206 offset:0\n\t"
    "v_mfma_f32_16x16x32_bf16 v[144:147], a[244:247], a[184:187], v[144:147]\n\t"
    "ds_read_b64_tr_b16 a[242:243], v206 offset:512\n\t"
    "v_mfma_f32_16x16x32_bf16 v[148:151], a[244:247], a[188:191], v[148:151]\n\t"
    "s_add_u32 m0, s59, 0xe000\n\t"
    "ds_read_b128 a[152:155], v192 offset:16384\n\t"
    "v_mfma_f32_16x16x32_bf16 v[152:155], a[244:247], a[192:195], v[152:155]\n\t"
    "s_add_u32 s39, s56, 0x300\n\t"
    "ds_read_b128 a[156:159], v192 offset:18432\n\t"
    "v_mfma_f32_16x16x32_bf16 v[156:159], a[244:247], a[196:199], v[156:159]\n\t"
    "ds_read_b128 a[160:163], v194\n\t"
    "buffer_load_dwordx4 v211, s[8:11], s39 offen lds\n\t"
    "v_mfma_f32_16x16x32_bf16 v[160:163], a[244:247], a[200:203], v[160:163]\n\t"
    "ds_read_b128 a[164:167], v196\n\t"
    "v_mfma_f32_16x16x32_bf16 v[164:167], a[244:247], a[204:207], v[164:167]\n\t"
    "ds_read_b64_tr_b16 a[244:245], v207 offset:0\n\t"
    "v_mfma_f32_16x16x32_bf16 v[168:171], a[184:187], a[248:251], v[168:171]\n\t"
    "ds_read_b64_tr_b16 a[246:247], v207 offset:512\n\t"
    "v_mfma_f32_16x16x32_bf16 v[172:175], a[188:191], a[248:251], v[172:175]\n\t"
    "v_mfma_f32_16x16x32_bf16 v[176:179], a[192:195], a[248:251], v[176:179]\n\t"
    "v_mfma_f32_16x16x32_bf16 v[180:183], a[196:199], a[248:251], v[180:183]\n\t"
    "v_mfma_f32_16x16x32_bf16 v[184:187], a[200:203], a[248:251], v[184:187]\n\t"
    "v_mfma_f32_16x16x32_bf16 v[188:191], a[204:207], a[248:251], v[188:191]\n\t"
    "ds_read_b64_tr_b16 a[248:249], v208 offset:0\n\t"
    "ds_read_b64_tr_b16 a[250:251], v208 offset:512\n\t"
    "s_waitcnt lgkmcnt(15)\n\t"
    "v_mfma_f32_16x16x32_bf16 a[0:3], a[208:211], a[128:131], a[0:3]\n\t"
    "s_waitcnt lgkmcnt(15)\n\t"
    "v_mfma_f32_16x16x32_bf16 a[32:35], a[208:211], a[132:135], a[32:35]\n\t"
    "s_waitcnt lgkmcnt(15)\n\t"
    "ds_read_b128 a[168:171], v235 offset:0\n\t"
    "v_mfma_f32_16x16x32_bf16 a[64:67], a[208:211], a[136:139], a[64:67]\n\t"
    "s_waitcnt lgkmcnt(15)\n\t"
    "v_mfma_f32_16x16x32_bf16 a[96:99], a[208:211], a[140:143], a[96:99]\n\t"
    "s_add_u32 m0, s41, 0x10000\n\t"
    "ds_read_b64_tr_b16 a[208:209], v198 offset:8192\n\t"
    "v_mfma_f32_16x16x32_bf16 a[4:7], a[212:215], a[128:131], a[4:7]\n\t"
    "s_add_u32 s39, s54, 0x380\n\t"
    "ds_read_b64_tr_b16 a[210:211], v198 offset:9216\n\t"
    "v_mfma_f32_16x16x32_bf16 a[36:39], a[212:215], a[132:135], a[36:39]\n\t"
    "s_add_u32 s63, s53, 0x8000\n\t"
    "buffer_load_dwordx4 v209, s[8:11], s39 offen lds\n\t"
    "v_mfma_f32_16x16x32_bf16 a[68:71], a[212:215], a[136:139], a[68:71]\n\t"
    "ds_read_b128 a[172:175], v235 offset:8192\n\t"
    "buffer_store_dwordx4 v[72:75], v216, s[20:23], s63 offen offset:2048 sc1\n\t"
    "v_mfma_f32_16x16x32_bf16 a[100:103], a[212:215], a[140:143], a[100:103]\n\t"
    "ds_read_b64_tr_b16 a[212:213], v199 offset:8192\n\t"
    "v_mfma_f32_16x16x32_bf16 a[8:11], a[216:219], a[128:131], a[8:11]\n\t"
    "s_add_u32 m0, m0, 0x400\n\t"
    "ds_read_b64_tr_b16 a[214:215], v199 offset:9216\n\t"
    "v_mfma_f32_16x16x32_bf16 a[40:43], a[216:219], a[132:135], a[40:43]\n\t"
    "s_add_u32 s39, s39, 0x2000\n\t"
    "v_mfma_f32_16x16x32_bf16 a[72:75], a[216:219], a[136:139], a[72:75]\n\t"
    "ds_read_b128 a[176:179], v235 offset:16384\n\t"
    "buffer_load_dwordx4 v210, s[8:11], s39 offen lds\n\t"
    "v_mfma_f32_16x16x32_bf16 a[104:107], a[216:219], a[140:143], a[104:107]\n\t"
    "ds_read_b64_tr_b16 a[216:217], v200 offset:8192\n\t"
    "v_mfma_f32_16x16x32_bf16 a[12:15], a[220:223], a[128:131], a[12:15]\n\t"
    "ds_read_b64_tr_b16 a[218:219], v200 offset:9216\n\t"
    "v_mfma_f32_16x16x32_bf16 a[44:47], a[220:223], a[132:135], a[44:47]\n\t"
    "s_add_u32 m0, m0, 0x400\n\t"
    "v_mfma_f32_16x16x32_bf16 a[76:79], a[220:223], a[136:139], a[76:79]\n\t"
    "s_add_u32 s39, s39, 0x2000\n\t"
    "ds_read_b128 a[180:183], v235 offset:24576\n\t"
    "v_mfma_f32_16x16x32_bf16 a[108:111], a[220:223], a[140:143], a[108:111]\n\t"
    "ds_read_b64_tr_b16 a[220:221], v201 offset:8192\n\t"
    "buffer_load_dwordx4 v209, s[8:11], s39 offen lds\n\t"
    "v_mfma_f32_16x16x32_bf16 a[16:19], a[224:227], a[128:131], a[16:19]\n\t"
    "ds_read_b64_tr_b16 a[222:223], v201 offset:9216\n\t"
    "v_mfma_f32_16x16x32_bf16 a[48:51], a[224:227], a[132:135], a[48:51]\n\t"
    "v_mfma_f32_16x16x32_bf16 a[80:83], a[224:227], a[136:139], a[80:83]\n\t"
    "s_add_u32 m0, m0, 0x400\n\t"
    "ds_read_b128 a[184:187], v193 offset:0\n\t"
    "v_mfma_f32_16x16x32_bf16 a[112:115], a[224:227], a[140:143], a[112:115]\n\t"
    "s_add_u32 s39, s39, 0x2000\n\t"
    "ds_read_b64_tr_b16 a[224:225], v202 offset:8192\n\t"
    "v_mfma_f32_16x16x32_bf16 a[20:23], a[228:231], a[128:131], a[20:23]\n\t"
    "ds_read_b64_tr_b16 a[226:227], v202 offset:9216\n\t"
    "buffer_load_dwordx4 v210, s[8:11], s39 offen lds\n\t"
    "v_mfma_f32_16x16x32_bf16 a[52:55], a[228:231], a[132:135], a[52:55]\n\t"
    "v_mfma_f32_16x16x32_bf16 a[84:87], a[228:231], a[136:139], a[84:87]\n\t"
    "ds_read_b128 a[188:191], v193 offset:2048\n\t"
    "v_mfma_f32_16x16x32_bf16 a[116:119], a[228:231], a[140:143], a[116:119]\n\t"
    "s_add_u32 m0, s41, 0x14000\n\t"
    "ds_read_b64_tr_b16 a[228:229], v203 offset:8192\n\t"
    "v_mfma_f32_16x16x32_bf16 a[24:27], a[232:235], a[128:131], a[24:27]\n\t"
    "s_add_u32 s39, s55, 0x380\n\t"
    "ds_read_b64_tr_b16 a[230:231], v203 offset:9216\n\t"
    "v_mfma_f32_16x16x32_bf16 a[56:59], a[232:235], a[132:135], a[56:59]\n\t"
    "buffer_load_dwordx4 v209, s[8:11], s39 offen lds\n\t"
    "v_mfma_f32_16x16x32_bf16 a[88:91], a[232:235], a[136:139], a[88:91]\n\t"
    "s_add_u32 s63, s53, 0x8000\n\t"
    "ds_read_b128 a[192:195], v193 offset:16384\n\t"
    "v_mfma_f32_16x16x32_bf16 a[120:123], a[232:235], a[140:143], a[120:123]\n\t"
    "s_waitcnt lgkmcnt(15)\n\t"
    "ds_read_b64_tr_b16 a[232:233], v204 offset:8192\n\t"
    "buffer_store_dwordx4 v[76:79], v216, s[20:23], s63 offen offset:3072 sc1\n\t"
    "v_mfma_f32_16x16x32_bf16 a[28:31], a[236:239], a[128:131], a[28:31]\n\t"
    "s_add_u32 m0, m0, 0x400\n\t"
    "ds_read_b64_tr_b16 a[234:235], v204 offset:9216\n\t"
    "v_mfma_f32_16x16x32_bf16 a[60:63], a[236:239], a[132:135], a[60:63]\n\t"
    "s_add_u32 s39, s39, 0x2000\n\t"
    "v_mfma_f32_16x16x32_bf16 a[92:95], a[236:239], a[136:139], a[92:95]\n\t"
    "ds_read_b128 a[196:199], v193 offset:18432\n\t"
    "buffer_load_dwordx4 v210, s[8:11], s39 offen lds\n\t"
    "v_mfma_f32_16x16x32_bf16 a[124:127], a[236:239], a[140:143], a[124:127]\n\t"
    "s_waitcnt lgkmcnt(15)\n\t"
    "ds_read_b64_tr_b16 a[236:237], v205 offset:8192\n\t"
    "v_mfma_f32_16x16x32_bf16 v[128:131], a[240:243], a[144:147], v[128:131]\n\t"
    "ds_read_b64_tr_b16 a[238:239], v205 offset:9216\n\t"
    "v_mfma_f32_16x16x32_bf16 v[132:135], a[240:243], a[148:151], v[132:135]\n\t"
    "s_waitcnt lgkmcnt(15)\n\t"
    "s_add_u32 m0, m0, 0x400\n\t"
    "v_mfma_f32_16x16x32_bf16 v[136:139], a[240:243], a[152:155], v[136:139]\n\t"
    "s_waitcnt lgkmcnt(15)\n\t"
    "s_add_u32 s39, s39, 0x2000\n\t"
    "ds_read_b128 a[200:203], v195\n\t"
    "v_mfma_f32_16x16x32_bf16 v[140:143], a[240:243], a[156:159], v[140:143]\n\t"
    "s_waitcnt lgkmcnt(15)\n\t"
    "ds_read_b64_tr_b16 a[240:241], v206 offset:4096\n\t"
    "buffer_load_dwordx4 v209, s[8:11], s39 offen lds\n\t"
    "v_mfma_f32_16x16x32_bf16 v[144:147], a[244:247], a[144:147], v[144:147]\n\t"
    "ds_read_b64_tr_b16 a[242:243], v206 offset:4608\n\t"
    "v_mfma_f32_16x16x32_bf16 v[148:151], a[244:247], a[148:151], v[148:151]\n\t"
    "v_mfma_f32_16x16x32_bf16 v[152:155], a[244:247], a[152:155], v[152:155]\n\t"
    "s_add_u32 m0, m0, 0x400\n\t"
    "ds_read_b128 a[204:207], v197\n\t"
    "v_mfma_f32_16x16x32_bf16 v[156:159], a[244:247], a[156:159], v[156:159]\n\t"
    "s_add_u32 s39, s39, 0x2000\n\t"
    "v_mfma_f32_16x16x32_bf16 v[160:163], a[244:247], a[160:163], v[160:163]\n\t"
    "buffer_load_dwordx4 v210, s[8:11], s39 offen lds\n\t"
    "v_mfma_f32_16x16x32_bf16 v[164:167], a[244:247], a[164:167], v[164:167]\n\t"
    "s_waitcnt lgkmcnt(15)\n\t"
    "ds_read_b64_tr_b16 a[244:245], v207 offset:4096\n\t"
    "v_mfma_f32_16x16x32_bf16 v[168:171], a[144:147], a[248:251], v[168:171]\n\t"
    "ds_read_b64_tr_b16 a[246:247], v207 offset:4608\n\t"
    "v_mfma_f32_16x16x32_bf16 v[172:175], a[148:151], a[248:251], v[172:175]\n\t"
    "v_mfma_f32_16x16x32_bf16 v[176:179], a[152:155], a[248:251], v[176:179]\n\t"
    "v_mfma_f32_16x16x32_bf16 v[180:183], a[156:159], a[248:251], v[180:183]\n\t"
    "v_mfma_f32_16x16x32_bf16 v[184:187], a[160:163], a[248:251], v[184:187]\n\t"
    "v_mfma_f32_16x16x32_bf16 v[188:191], a[164:167], a[248:251], v[188:191]\n\t"
    "s_waitcnt lgkmcnt(15)\n\t"
    "ds_read_b64_tr_b16 a[248:249], v208 offset:4096\n\t"
    "v_mfma_f32_16x16x32_bf16 a[0:3], a[208:211], a[168:171], a[0:3]\n\t"
    "s_waitcnt lgkmcnt(15)\n\t"
    "s_cmp_eq_u32 s33, 0\n\t"
    "s_cselect_b32 s62, s65, 0x20000\n\t"
    "ds_read_b64_tr_b16 a[250:251], v208 offset:4608\n\t"
    "v_mfma_f32_16x16x32_bf16 a[32:35], a[208:211], a[172:175], a[32:35]\n\t"
    "s_waitcnt lgkmcnt(15)\n\t"
    "v_mfma_f32_16x16x32_bf16 a[64:67], a[208:211], a[176:179], a[64:67]\n\t"
    "s_waitcnt lgkmcnt(15)\n\t"
    "v_mfma_f32_16x16x32_bf16 a[96:99], a[208:211], a[180:183], a[96:99]\n\t"
    "v_add_u32 v198, 0xffff0000, v198\n\t"
    "v_add_u32 v199, 0xffff0000, v199\n\t"
    "v_mfma_f32_16x16x32_bf16 a[4:7], a[212:215], a[168:171], a[4:7]\n\t"
    "v_add_u32 v200, 0xffff0000, v200\n\t"
    "v_add_u32 v201, 0xffff0000, v201\n\t"
    "v_mfma_f32_16x16x32_bf16 a[36:39], a[212:215], a[172:175], a[36:39]\n\t"
    "s_waitcnt vmcnt(10) lgkmcnt(0)\n\t"
    "s_barrier\n\t"
    "v_add_u32 v202, 0xffff0000, v202\n\t"
    "v_add_u32 v203, 0xffff0000, v203\n\t"
    "v_mfma_f32_16x16x32_bf16 a[68:71], a[212:215], a[176:179], a[68:71]\n\t"
    "v_add_u32 v204, 0xffff0000, v204\n\t"
    "v_add_u32 v205, 0xffff0000, v205\n\t"
    "v_mfma_f32_16x16x32_bf16 a[100:103], a[212:215], a[180:183], a[100:103]\n\t"
    "s_add_u32 m0, s41, 0x18000\n\t"
    "v_add_u32 v206, 0xffff0000, v206\n\t"
    "v_add_u32 v207, 0xffff0000, v207\n\t"
    "v_mfma_f32_16x16x32_bf16 a[8:11], a[216:219], a[168:171], a[8:11]\n\t"
    "s_add_u32 s39, s57, 0x380000\n\t"
    "v_add_u32 v208, 0xffff0000, v208\n\t"
    "v_add_u32 v192, 0x20000, v192\n\t"
    "v_mfma_f32_16x16x32_bf16 a[40:43], a[216:219], a[172:175], a[40:43]\n\t"
    "buffer_load_dwordx4 v212, s[84:87], s39 offen lds\n\t"
    "v_add_u32 v193, 0x20000, v193\n\t"
    "v_add_u32 v234, 0x20000, v234\n\t"
    "v_mfma_f32_16x16x32_bf16 a[72:75], a[216:219], a[176:179], a[72:75]\n\t"
    "v_add_u32 v235, 0x20000, v235\n\t"
    "v_add_u32 v194, s62, v194\n\t"
    "v_mfma_f32_16x16x32_bf16 a[104:107], a[216:219], a[180:183], a[104:107]\n\t"
    "v_add_u32 v195, s62, v195\n\t"
    "v_add_u32 v196, s62, v196\n\t"
    "v_mfma_f32_16x16x32_bf16 a[12:15], a[220:223], a[168:171], a[12:15]\n\t"
    "s_add_u32 m0, m0, 0x400\n\t"
    "v_add_u32 v197, s62, v197\n\t"
    "v_mfma_f32_16x16x32_bf16 a[44:47], a[220:223], a[172:175], a[44:47]\n\t"
    "s_add_u32 s39, s39, 0x8000\n\t"
    "ds_read_b64_tr_b16 a[208:209], v198 offset:0\n\t"
    "v_mfma_f32_16x16x32_bf16 a[76:79], a[220:223], a[176:179], a[76:79]\n\t"
    "ds_read_b64_tr_b16 a[210:211], v198 offset:1024\n\t"
    "buffer_load_dwordx4 v212, s[84:87], s39 offen lds\n\t"
    "v_mfma_f32_16x16x32_bf16 a[108:111], a[220:223], a[180:183], a[108:111]\n\t"
    "ds_read_b64_tr_b16 a[212:213], v199 offset:0\n\t"
    "v_mfma_f32_16x16x32_bf16 a[16:19], a[224:227], a[168:171], a[16:19]\n\t"
    "ds_read_b64_tr_b16 a[214:215], v199 offset:1024\n\t"
    "v_mfma_f32_16x16x32_bf16 a[48:51], a[224:227], a[172:175], a[48:51]\n\t"
    "s_add_u32 m0, m0, 0x400\n\t"
    "ds_read_b64_tr_b16 a[216:217], v200 offset:0\n\t"
    "v_mfma_f32_16x16x32_bf16 a[80:83], a[224:227], a[176:179], a[80:83]\n\t"
    "s_add_u32 s39, s39, 0x8000\n\t"
    "ds_read_b64_tr_b16 a[218:219], v200 offset:1024\n\t"
    "v_mfma_f32_16x16x32_bf16 a[112:115], a[224:227], a[180:183], a[112:115]\n\t"
    "ds_read_b64_tr_b16 a[220:221], v201 offset:0\n\t"
    "buffer_load_dwordx4 v213, s[84:87], s39 offen lds\n\t"
    "v_mfma_f32_16x16x32_bf16 a[20:23], a[228:231], a[168:171], a[20:23]\n\t"
    "ds_read_b64_tr_b16 a[222:223], v201 offset:1024\n\t"
    "v_mfma_f32_16x16x32_bf16 a[52:55], a[228:231], a[172:175], a[52:55]\n\t"
    "ds_read_b64_tr_b16 a[224:225], v202 offset:0\n\t"
    "v_mfma_f32_16x16x32_bf16 a[84:87], a[228:231], a[176:179], a[84:87]\n\t"
    "s_add_u32 m0, m0, 0x400\n\t"
    "ds_read_b64_tr_b16 a[226:227], v202 offset:1024\n\t"
    "v_mfma_f32_16x16x32_bf16 a[116:119], a[228:231], a[180:183], a[116:119]\n\t"
    "s_add_u32 s39, s39, 0x8000\n\t"
    "ds_read_b64_tr_b16 a[228:229], v203 offset:0\n\t"
    "v_mfma_f32_16x16x32_bf16 a[24:27], a[232:235], a[168:171], a[24:27]\n\t"
    "ds_read_b64_tr_b16 a[230:231], v203 offset:1024\n\t"
    "buffer_load_dwordx4 v213, s[84:87], s39 offen lds\n\t"
    "v_mfma_f32_16x16x32_bf16 a[56:59], a[232:235], a[172:175], a[56:59]\n\t"
    "ds_read_b128 a[128:131], v234 offset:0\n\t"
    "v_mfma_f32_16x16x32_bf16 a[88:91], a[232:235], a[176:179], a[88:91]\n\t"
    "ds_read_b128 a[132:135], v234 offset:8192\n\t"
    "v_mfma_f32_16x16x32_bf16 a[120:123], a[232:235], a[180:183], a[120:123]\n\t"
    "s_lshr_b32 s37, s41, 1\n\t"
    "s_add_u32 m0, s37, 0x1c000\n\t"
    "ds_read_b64_tr_b16 a[232:233], v204 offset:0\n\t"
    "v_mfma_f32_16x16x32_bf16 a[28:31], a[236:239], a[168:171], a[28:31]\n\t"
    "s_add_u32 s39, s58, 0x150000\n\t"
    "ds_read_b64_tr_b16 a[234:235], v204 offset:1024\n\t"
    "v_mfma_f32_16x16x32_bf16 a[60:63], a[236:239], a[172:175], a[60:63]\n\t"
    "ds_read_b128 a[136:139], v234 offset:16384\n\t"
    "buffer_load_dwordx4 v214, s[88:91], s39 offen lds\n\t"
    "v_mfma_f32_16x16x32_bf16 a[92:95], a[236:239], a[176:179], a[92:95]\n\t"
    "ds_read_b128 a[140:143], v234 offset:24576\n\t"
    "v_mfma_f32_16x16x32_bf16 a[124:127], a[236:239], a[180:183], a[124:127]\n\t"
    "ds_read_b64_tr_b16 a[236:237], v205 offset:0\n\t"
    "v_mfma_f32_16x16x32_bf16 v[128:131], a[240:243], a[184:187], v[128:131]\n\t"
    "s_add_u32 m0, m0, 0x400\n\t"
    "ds_read_b64_tr_b16 a[238:239], v205 offset:1024\n\t"
    "v_mfma_f32_16x16x32_bf16 v[132:135], a[240:243], a[188:191], v[132:135]\n\t"
    "s_add_u32 s39, s39, 0x6000\n\t"
    "ds_read_b128 a[144:147], v192 offset:0\n\t"
    "v_mfma_f32_16x16x32_bf16 v[136:139], a[240:243], a[192:195], v[136:139]\n\t"
    "ds_read_b128 a[148:151], v192 offset:2048\n\t"
    "buffer_load_dwordx4 v215, s[88:91], s39 offen lds\n\t"
    "v_mfma_f32_16x16x32_bf16 v[140:143], a[240:243], a[196:199], v[140:143]\n\t"
    "ds_read_b64_tr_b16 a[240:241], v206 offset:0\n\t"
    "v_mfma_f32_16x16x32_bf16 v[144:147], a[244:247], a[184:187], v[144:147]\n\t"
    "ds_read_b64_tr_b16 a[242:243], v206 offset:512\n\t"
    "v_mfma_f32_16x16x32_bf16 v[148:151], a[244:247], a[188:191], v[148:151]\n\t"
    "s_add_u32 m0, s59, 0x1e000\n\t"
    "ds_read_b128 a[152:155], v192 offset:16384\n\t"
    "v_mfma_f32_16x16x32_bf16 v[152:155], a[244:247], a[192:195], v[152:155]\n\t"
    "s_add_u32 s39, s56, 0x380\n\t"
    "ds_read_b128 a[156:159], v192 offset:18432\n\t"
    "v_mfma_f32_16x16x32_bf16 v[156:159], a[244:247], a[196:199], v[156:159]\n\t"
    "ds_read_b128 a[160:163], v194\n\t"
    "buffer_load_dwordx4 v211, s[8:11], s39 offen lds\n\t"
    "v_mfma_f32_16x16x32_bf16 v[160:163], a[244:247], a[200:203], v[160:163]\n\t"
    "ds_read_b128 a[164:167], v196\n\t"
    "v_mfma_f32_16x16x32_bf16 v[164:167], a[244:247], a[204:207], v[164:167]\n\t"
    "ds_read_b64_tr_b16 a[244:245], v207 offset:0\n\t"
    "v_mfma_f32_16x16x32_bf16 v[168:171], a[184:187], a[248:251], v[168:171]\n\t"
    "ds_read_b64_tr_b16 a[246:247], v207 offset:512\n\t"
    "v_mfma_f32_16x16x32_bf16 v[172:175], a[188:191], a[248:251], v[172:175]\n\t"
    "v_mfma_f32_16x16x32_bf16 v[176:179], a[192:195], a[248:251], v[176:179]\n\t"
    "v_mfma_f32_16x16x32_bf16 v[180:183], a[196:199], a[248:251], v[180:183]\n\t"
    "v_mfma_f32_16x16x32_bf16 v[184:187], a[200:203], a[248:251], v[184:187]\n\t"
    "v_mfma_f32_16x16x32_bf16 v[188:191], a[204:207], a[248:251], v[188:191]\n\t"
    "ds_read_b64_tr_b16 a[248:249], v208 offset:0\n\t"
    "ds_read_b64_tr_b16 a[250:251], v208 offset:512\n\t"
    "s_waitcnt lgkmcnt(15)\n\t"
    "v_mfma_f32_16x16x32_bf16 a[0:3], a[208:211], a[128:131], a[0:3]\n\t"
    "s_waitcnt lgkmcnt(15)\n\t"
    "v_mfma_f32_16x16x32_bf16 a[32:35], a[208:211], a[132:135], a[32:35]\n\t"
    "s_waitcnt lgkmcnt(15)\n\t"
    "ds_read_b128 a[168:171], v235 offset:0\n\t"
    "v_mfma_f32_16x16x32_bf16 a[64:67], a[208:211], a[136:139], a[64:67]\n\t"
    "s_waitcnt lgkmcnt(15)\n\t"
    "v_mfma_f32_16x16x32_bf16 a[96:99], a[208:211], a[140:143], a[96:99]\n\t"
    "ds_read_b64_tr_b16 a[208:209], v198 offset:8192\n\t"
    "v_mfma_f32_16x16x32_bf16 a[4:7], a[212:215], a[128:131], a[4:7]\n\t"
    "s_add_u32 s63, s53, 0x9000\n\t"
    "ds_read_b64_tr_b16 a[210:211], v198 offset:9216\n\t"
    "v_mfma_f32_16x16x32_bf16 a[36:39], a[212:215], a[132:135], a[36:39]\n\t"
    "buffer_store_dwordx4 v[80:83], v216, s[20:23], s63 offen offset:0 sc1\n\t"
    "v_mfma_f32_16x16x32_bf16 a[68:71], a[212:215], a[136:139], a[68:71]\n\t"
    "ds_read_b128 a[172:175], v235 offset:8192\n\t"
    "v_mfma_f32_16x16x32_bf16 a[100:103], a[212:215], a[140:143], a[100:103]\n\t"
    "ds_read_b64_tr_b16 a[212:213], v199 offset:8192\n\t"
    "v_mfma_f32_16x16x32_bf16 a[8:11], a[216:219], a[128:131], a[8:11]\n\t"
    "ds_read_b64_tr_b16 a[214:215], v199 offset:9216\n\t"
    "v_mfma_f32_16x16x32_bf16 a[40:43], a[216:219], a[132:135], a[40:43]\n\t"
    "v_mfma_f32_16x16x32_bf16 a[72:75], a[216:219], a[136:139], a[72:75]\n\t"
    "ds_read_b128 a[176:179], v235 offset:16384\n\t"
    "v_mfma_f32_16x16x32_bf16 a[104:107], a[216:219], a[140:143], a[104:107]\n\t"
    "s_add_u32 s63, s53, 0x9000\n\t"
    "ds_read_b64_tr_b16 a[216:217], v200 offset:8192\n\t"
    "v_mfma_f32_16x16x32_bf16 a[12:15], a[220:223], a[128:131], a[12:15]\n\t"
    "ds_read_b64_tr_b16 a[218:219], v200 offset:9216\n\t"
    "buffer_store_dwordx4 v[84:87], v216, s[20:23], s63 offen offset:1024 sc1\n\t"
    "v_mfma_f32_16x16x32_bf16 a[44:47], a[220:223], a[132:135], a[44:47]\n\t"
    "v_mfma_f32_16x16x32_bf16 a[76:79], a[220:223], a[136:139], a[76:79]\n\t"
    "ds_read_b128 a[180:183], v235 offset:24576\n\t"
    "v_mfma_f32_16x16x32_bf16 a[108:111], a[220:223], a[140:143], a[108:111]\n\t"
    "ds_read_b64_tr_b16 a[220:221], v201 offset:8192\n\t"
    "v_mfma_f32_16x16x32_bf16 a[16:19], a[224:227], a[128:131], a[16:19]\n\t"
    "ds_read_b64_tr_b16 a[222:223], v201 offset:9216\n\t"
    "v_mfma_f32_16x16x32_bf16 a[48:51], a[224:227], a[132:135], a[48:51]\n\t"
    "v_mfma_f32_16x16x32_bf16 a[80:83], a[224:227], a[136:139], a[80:83]\n\t"
    "s_add_u32 s63, s53, 0x9000\n\t"
    "ds_read_b128 a[184:187], v193 offset:0\n\t"
    "v_mfma_f32_16x16x32_bf16 a[112:115], a[224:227], a[140:143], a[112:115]\n\t"
    "ds_read_b64_tr_b16 a[224:225], v202 offset:8192\n\t"
    "buffer_store_dwordx4 v[88:91], v216, s[20:23], s63 offen offset:2048 sc1\n\t"
    "v_mfma_f32_16x16x32_bf16 a[20:23], a[228:231], a[128:131], a[20:23]\n\t"
    "ds_read_b64_tr_b16 a[226:227], v202 offset:9216\n\t"
    "v_mfma_f32_16x16x32_bf16 a[52:55], a[228:231], a[132:135], a[52:55]\n\t"
    "v_mfma_f32_16x16x32_bf16 a[84:87], a[228:231], a[136:139], a[84:87]\n\t"
    "ds_read_b128 a[188:191], v193 offset:2048\n\t"
    "v_mfma_f32_16x16x32_bf16 a[116:119], a[228:231], a[140:143], a[116:119]\n\t"
    "ds_read_b64_tr_b16 a[228:229], v203 offset:8192\n\t"
    "v_mfma_f32_16x16x32_bf16 a[24:27], a[232:235], a[128:131], a[24:27]\n\t"
    "ds_read_b64_tr_b16 a[230:231], v203 offset:9216\n\t"
    "v_mfma_f32_16x16x32_bf16 a[56:59], a[232:235], a[132:135], a[56:59]\n\t"
    "s_add_u32 s63, s53, 0x9000\n\t"
    "v_mfma_f32_16x16x32_bf16 a[88:91], a[232:235], a[136:139], a[88:91]\n\t"
    "ds_read_b128 a[192:195], v193 offset:16384\n\t"
    "buffer_store_dwordx4 v[92:95], v216, s[20:23], s63 offen offset:3072 sc1\n\t"
    "v_mfma_f32_16x16x32_bf16 a[120:123], a[232:235], a[140:143], a[120:123]\n\t"
    "s_waitcnt lgkmcnt(15)\n\t"
    "ds_read_b64_tr_b16 a[232:233], v204 offset:8192\n\t"
    "v_mfma_f32_16x16x32_bf16 a[28:31], a[236:239], a[128:131], a[28:31]\n\t"
    "ds_read_b64_tr_b16 a[234:235], v204 offset:9216\n\t"
    "v_mfma_f32_16x16x32_bf16 a[60:63], a[236:239], a[132:135], a[60:63]\n\t"
    "v_mfma_f32_16x16x32_bf16 a[92:95], a[236:239], a[136:139], a[92:95]\n\t"
    "ds_read_b128 a[196:199], v193 offset:18432\n\t"
    "v_mfma_f32_16x16x32_bf16 a[124:127], a[236:239], a[140:143], a[124:127]\n\t"
    "s_waitcnt lgkmcnt(15)\n\t"
    "ds_read_b64_tr_b16 a[236:237], v205 offset:8192\n\t"
    "v_mfma_f32_16x16x32_bf16 v[128:131], a[240:243], a[144:147], v[128:131]\n\t"
    "s_add_u32 s63, s53, 0xa000\n\t"
    "ds_read_b64_tr_b16 a[238:239], v205 offset:9216\n\t"
    "v_mfma_f32_16x16x32_bf16 v[132:135], a[240:243], a[148:151], v[132:135]\n\t"
    "s_waitcnt lgkmcnt(15)\n\t"
    "buffer_store_dwordx4 v[96:99], v216, s[20:23], s63 offen offset:0 sc1\n\t"
    "v_mfma_f32_16x16x32_bf16 v[136:139], a[240:243], a[152:155], v[136:139]\n\t"
    "s_waitcnt lgkmcnt(15)\n\t"
    "ds_read_b128 a[200:203], v195\n\t"
    "v_mfma_f32_16x16x32_bf16 v[140:143], a[240:243], a[156:159], v[140:143]\n\t"
    "s_waitcnt lgkmcnt(15)\n\t"
    "ds_read_b64_tr_b16 a[240:241], v206 offset:4096\n\t"
    "v_mfma_f32_16x16x32_bf16 v[144:147], a[244:247], a[144:147], v[144:147]\n\t"
    "ds_read_b64_tr_b16 a[242:243], v206 offset:4608\n\t"
    "v_mfma_f32_16x16x32_bf16 v[148:151], a[244:247], a[148:151], v[148:151]\n\t"
    "v_mfma_f32_16x16x32_bf16 v[152:155], a[244:247], a[152:155], v[152:155]\n\t"
    "ds_read_b128 a[204:207], v197\n\t"
    "v_mfma_f32_16x16x32_bf16 v[156:159], a[244:247], a[156:159], v[156:159]\n\t"
    "s_add_u32 s63, s53, 0xa000\n\t"
    "v_mfma_f32_16x16x32_bf16 v[160:163], a[244:247], a[160:163], v[160:163]\n\t"
    "buffer_store_dwordx4 v[100:103], v216, s[20:23], s63 offen offset:1024 sc1\n\t"
    "v_mfma_f32_16x16x32_bf16 v[164:167], a[244:247], a[164:167], v[164:167]\n\t"
    "s_waitcnt lgkmcnt(15)\n\t"
    "ds_read_b64_tr_b16 a[244:245], v207 offset:4096\n\t"
    "v_mfma_f32_16x16x32_bf16 v[168:171], a[144:147], a[248:251], v[168:171]\n\t"
    "ds_read_b64_tr_b16 a[246:247], v207 offset:4608\n\t"
    "v_mfma_f32_16x16x32_bf16 v[172:175], a[148:151], a[248:251], v[172:175]\n\t"
    "v_mfma_f32_16x16x32_bf16 v[176:179], a[152:155], a[248:251], v[176:179]\n\t"
    "v_mfma_f32_16x16x32_bf16 v[180:183], a[156:159], a[248:251], v[180:183]\n\t"
    "v_mfma_f32_16x16x32_bf16 v[184:187], a[160:163], a[248:251], v[184:187]\n\t"
    "v_mfma_f32_16x16x32_bf16 v[188:191], a[164:167], a[248:251], v[188:191]\n\t"
    "s_waitcnt lgkmcnt(15)\n\t"
    "ds_read_b64_tr_b16 a[248:249], v208 offset:4096\n\t"
    "v_mfma_f32_16x16x32_bf16 a[0:3], a[208:211], a[168:171], a[0:3]\n\t"
    "s_waitcnt lgkmcnt(15)\n\t"
    "s_cmp_eq_u32 s33, 0\n\t"
    "s_cselect_b32 s62, s64, s65\n\t"
    "ds_read_b64_tr_b16 a[250:251], v208 offset:4608\n\t"
    "v_mfma_f32_16x16x32_bf16 a[32:35], a[208:211], a[172:175], a[32:35]\n\t"
    "s_waitcnt lgkmcnt(15)\n\t"
    "v_mfma_f32_16x16x32_bf16 a[64:67], a[208:211], a[176:179], a[64:67]\n\t"
    "s_waitcnt lgkmcnt(15)\n\t"
    "v_mfma_f32_16x16x32_bf16 a[96:99], a[208:211], a[180:183], a[96:99]\n\t"
    "v_add_u32 v198, 0x10000, v198\n\t"
    "v_add_u32 v199, 0x10000, v199\n\t"
    "v_mfma_f32_16x16x32_bf16 a[4:7], a[212:215], a[168:171], a[4:7]\n\t"
    "v_add_u32 v200, 0x10000, v200\n\t"
    "v_add_u32 v201, 0x10000, v201\n\t"
    "v_mfma_f32_16x16x32_bf16 a[36:39], a[212:215], a[172:175], a[36:39]\n\t"
    "s_waitcnt vmcnt(6) lgkmcnt(0)\n\t"
    "s_barrier\n\t"
    "v_add_u32 v202, 0x10000, v202\n\t"
    "v_add_u32 v203, 0x10000, v203\n\t"
    "v_mfma_f32_16x16x32_bf16 a[68:71], a[212:215], a[176:179], a[68:71]\n\t"
    "v_add_u32 v204, 0x10000, v204\n\t"
    "v_add_u32 v205, 0x10000, v205\n\t"
    "v_mfma_f32_16x16x32_bf16 a[100:103], a[212:215], a[180:183], a[100:103]\n\t"
    "v_add_u32 v206, 0x10000, v206\n\t"
    "v_add_u32 v207, 0x10000, v207\n\t"
    "v_mfma_f32_16x16x32_bf16 a[8:11], a[216:219], a[168:171], a[8:11]\n\t"
    "v_add_u32 v208, 0x10000, v208\n\t"
    "v_add_u32 v192, 0xffff0000, v192\n\t"
    "v_mfma_f32_16x16x32_bf16 a[40:43], a[216:219], a[172:175], a[40:43]\n\t"
    "v_add_u32 v193, 0xffff0000, v193\n\t"
    "v_add_u32 v234, 0xffff0000, v234\n\t"
    "v_mfma_f32_16x16x32_bf16 a[72:75], a[216:219], a[176:179], a[72:75]\n\t"
    "v_add_u32 v235, 0xffff0000, v235\n\t"
    "v_add_u32 v194, s62, v194\n\t"
    "v_mfma_f32_16x16x32_bf16 a[104:107], a[216:219], a[180:183], a[104:107]\n\t"
    "v_add_u32 v195, s62, v195\n\t"
    "v_add_u32 v196, s62, v196\n\t"
    "v_mfma_f32_16x16x32_bf16 a[12:15], a[220:223], a[168:171], a[12:15]\n\t"
    "v_add_u32 v197, s62, v197\n\t"
    "v_mfma_f32_16x16x32_bf16 a[44:47], a[220:223], a[172:175], a[44:47]\n\t"
    "ds_read_b64_tr_b16 a[208:209], v198 offset:0\n\t"
    "v_mfma_f32_16x16x32_bf16 a[76:79], a[220:223], a[176:179], a[76:79]\n\t"
    "ds_read_b64_tr_b16 a[210:211], v198 offset:1024\n\t"
    "v_mfma_f32_16x16x32_bf16 a[108:111], a[220:223], a[180:183], a[108:111]\n\t"
    "ds_read_b64_tr_b16 a[212:213], v199 offset:0\n\t"
    "v_mfma_f32_16x16x32_bf16 a[16:19], a[224:227], a[168:171], a[16:19]\n\t"
    "ds_read_b64_tr_b16 a[214:215], v199 offset:1024\n\t"
    "v_mfma_f32_16x16x32_bf16 a[48:51], a[224:227], a[172:175], a[48:51]\n\t"
    "ds_read_b64_tr_b16 a[216:217], v200 offset:0\n\t"
    "v_mfma_f32_16x16x32_bf16 a[80:83], a[224:227], a[176:179], a[80:83]\n\t"
    "ds_read_b64_tr_b16 a[218:219], v200 offset:1024\n\t"
    "v_mfma_f32_16x16x32_bf16 a[112:115], a[224:227], a[180:183], a[112:115]\n\t"
    "ds_read_b64_tr_b16 a[220:221], v201 offset:0\n\t"
    "v_mfma_f32_16x16x32_bf16 a[20:23], a[228:231], a[168:171], a[20:23]\n\t"
    "ds_read_b64_tr_b16 a[222:223], v201 offset:1024\n\t"
    "v_mfma_f32_16x16x32_bf16 a[52:55], a[228:231], a[172:175], a[52:55]\n\t"
    "ds_read_b64_tr_b16 a[224:225], v202 offset:0\n\t"
    "v_mfma_f32_16x16x32_bf16 a[84:87], a[228:231], a[176:179], a[84:87]\n\t"
    "ds_read_b64_tr_b16 a[226:227], v202 offset:1024\n\t"
    "v_mfma_f32_16x16x32_bf16 a[116:119], a[228:231], a[180:183], a[116:119]\n\t"
    "ds_read_b64_tr_b16 a[228:229], v203 offset:0\n\t"
    "v_mfma_f32_16x16x32_bf16 a[24:27], a[232:235], a[168:171], a[24:27]\n\t"
    "ds_read_b64_tr_b16 a[230:231], v203 offset:1024\n\t"
    "v_mfma_f32_16x16x32_bf16 a[56:59], a[232:235], a[172:175], a[56:59]\n\t"
    "ds_read_b128 a[128:131], v234 offset:0\n\t"
    "v_mfma_f32_16x16x32_bf16 a[88:91], a[232:235], a[176:179], a[88:91]\n\t"
    "ds_read_b128 a[132:135], v234 offset:8192\n\t"
    "v_mfma_f32_16x16x32_bf16 a[120:123], a[232:235], a[180:183], a[120:123]\n\t"
    "ds_read_b64_tr_b16 a[232:233], v204 offset:0\n\t"
    "v_mfma_f32_16x16x32_bf16 a[28:31], a[236:239], a[168:171], a[28:31]\n\t"
    "ds_read_b64_tr_b16 a[234:235], v204 offset:1024\n\t"
    "v_mfma_f32_16x16x32_bf16 a[60:63], a[236:239], a[172:175], a[60:63]\n\t"
    "ds_read_b128 a[136:139], v234 offset:16384\n\t"
    "v_mfma_f32_16x16x32_bf16 a[92:95], a[236:239], a[176:179], a[92:95]\n\t"
    "ds_read_b128 a[140:143], v234 offset:24576\n\t"
    "v_mfma_f32_16x16x32_bf16 a[124:127], a[236:239], a[180:183], a[124:127]\n\t"
    "ds_read_b64_tr_b16 a[236:237], v205 offset:0\n\t"
    "v_mfma_f32_16x16x32_bf16 v[128:131], a[240:243], a[184:187], v[128:131]\n\t"
    "ds_read_b64_tr_b16 a[238:239], v205 offset:1024\n\t"
    "v_mfma_f32_16x16x32_bf16 v[132:135], a[240:243], a[188:191], v[132:135]\n\t"
    "ds_read_b128 a[144:147], v192 offset:0\n\t"
    "v_mfma_f32_16x16x32_bf16 v[136:139], a[240:243], a[192:195], v[136:139]\n\t"
    "ds_read_b128 a[148:151], v192 offset:2048\n\t"
    "v_mfma_f32_16x16x32_bf16 v[140:143], a[240:243], a[196:199], v[140:143]\n\t"
    "ds_read_b64_tr_b16 a[240:241], v206 offset:0\n\t"
    "v_mfma_f32_16x16x32_bf16 v[144:147], a[244:247], a[184:187], v[144:147]\n\t"
    "ds_read_b64_tr_b16 a[242:243], v206 offset:512\n\t"
    "v_mfma_f32_16x16x32_bf16 v[148:151], a[244:247], a[188:191], v[148:151]\n\t"
    "ds_read_b128 a[152:155], v192 offset:16384\n\t"
    "v_mfma_f32_16x16x32_bf16 v[152:155], a[244:247], a[192:195], v[152:155]\n\t"
    "ds_read_b128 a[156:159], v192 offset:18432\n\t"
    "v_mfma_f32_16x16x32_bf16 v[156:159], a[244:247], a[196:199], v[156:159]\n\t"
    "ds_read_b128 a[160:163], v194\n\t"
    "v_mfma_f32_16x16x32_bf16 v[160:163], a[244:247], a[200:203], v[160:163]\n\t"
    "ds_read_b128 a[164:167], v196\n\t"
    "v_mfma_f32_16x16x32_bf16 v[164:167], a[244:247], a[204:207], v[164:167]\n\t"
    "ds_read_b64_tr_b16 a[244:245], v207 offset:0\n\t"
    "v_mfma_f32_16x16x32_bf16 v[168:171], a[184:187], a[248:251], v[168:171]\n\t"
    "ds_read_b64_tr_b16 a[246:247], v207 offset:512\n\t"
    "v_mfma_f32_16x16x32_bf16 v[172:175], a[188:191], a[248:251], v[172:175]\n\t"
    "v_mfma_f32_16x16x32_bf16 v[176:179], a[192:195], a[248:251], v[176:179]\n\t"
    "v_mfma_f32_16x16x32_bf16 v[180:183], a[196:199], a[248:251], v[180:183]\n\t"
    "v_mfma_f32_16x16x32_bf16 v[184:187], a[200:203], a[248:251], v[184:187]\n\t"
    "v_mfma_f32_16x16x32_bf16 v[188:191], a[204:207], a[248:251], v[188:191]\n\t"
    "ds_read_b64_tr_b16 a[248:249], v208 offset:0\n\t"
    "ds_read_b64_tr_b16 a[250:251], v208 offset:512\n\t"
    "s_cmp_lg_u32 s33, 0\n\t"
    "s_cbranch_scc1 L_pk1\n\t"
    "buffer_load_dwordx2 v[250:251], v252, s[28:31], 0 offen sc1\n\t"
    "L_pk1:\n\t"
    "s_waitcnt lgkmcnt(15)\n\t"
    "v_mfma_f32_16x16x32_bf16 a[0:3], a[208:211], a[128:131], a[0:3]\n\t"
    "s_waitcnt lgkmcnt(15)\n\t"
    "v_mfma_f32_16x16x32_bf16 a[32:35], a[208:211], a[132:135], a[32:35]\n\t"
    "s_waitcnt lgkmcnt(15)\n\t"
    "ds_read_b128 a[168:171], v235 offset:0\n\t"
    "v_mfma_f32_16x16x32_bf16 a[64:67], a[208:211], a[136:139], a[64:67]\n\t"
    "s_waitcnt lgkmcnt(15)\n\t"
    "v_mfma_f32_16x16x32_bf16 a[96:99], a[208:211], a[140:143], a[96:99]\n\t"
    "ds_read_b64_tr_b16 a[208:209], v198 offset:8192\n\t"
    "v_mfma_f32_16x16x32_bf16 a[4:7], a[212:215], a[128:131], a[4:7]\n\t"
    "s_add_u32 s63, s53, 0xa000\n\t"
    "ds_read_b64_tr_b16 a[210:211], v198 offset:9216\n\t"
    "v_mfma_f32_16x16x32_bf16 a[36:39], a[212:215], a[132:135], a[36:39]\n\t"
    "buffer_store_dwordx4 v[104:107], v216, s[20:23], s63 offen offset:2048 sc1\n\t"
    "v_mfma_f32_16x16x32_bf16 a[68:71], a[212:215], a[136:139], a[68:71]\n\t"
    "ds_read_b128 a[172:175], v235 offset:8192\n\t"
    "v_mfma_f32_16x16x32_bf16 a[100:103], a[212:215], a[140:143], a[100:103]\n\t"
    "ds_read_b64_tr_b16 a[212:213], v199 offset:8192\n\t"
    "v_mfma_f32_16x16x32_bf16 a[8:11], a[216:219], a[128:131], a[8:11]\n\t"
    "ds_read_b64_tr_b16 a[214:215], v199 offset:9216\n\t"
    "v_mfma_f32_16x16x32_bf16 a[40:43], a[216:219], a[132:135], a[40:43]\n\t"
    "v_mfma_f32_16x16x32_bf16 a[72:75], a[216:219], a[136:139], a[72:75]\n\t"
    "ds_read_b128 a[176:179], v235 offset:16384\n\t"
    "v_mfma_f32_16x16x32_bf16 a[104:107], a[216:219], a[140:143], a[104:107]\n\t"
    "s_add_u32 s63, s53, 0xa000\n\t"
    "ds_read_b64_tr_b16 a[216:217], v200 offset:8192\n\t"
    "v_mfma_f32_16x16x32_bf16 a[12:15], a[220:223], a[128:131], a[12:15]\n\t"
    "ds_read_b64_tr_b16 a[218:219], v200 offset:9216\n\t"
    "buffer_store_dwordx4 v[108:111], v216, s[20:23], s63 offen offset:3072 sc1\n\t"
    "v_mfma_f32_16x16x32_bf16 a[44:47], a[220:223], a[132:135], a[44:47]\n\t"
    "v_mfma_f32_16x16x32_bf16 a[76:79], a[220:223], a[136:139], a[76:79]\n\t"
    "ds_read_b128 a[180:183], v235 offset:24576\n\t"
    "v_mfma_f32_16x16x32_bf16 a[108:111], a[220:223], a[140:143], a[108:111]\n\t"
    "ds_read_b64_tr_b16 a[220:221], v201 offset:8192\n\t"
    "v_mfma_f32_16x16x32_bf16 a[16:19], a[224:227], a[128:131], a[16:19]\n\t"
    "ds_read_b64_tr_b16 a[222:223], v201 offset:9216\n\t"
    "v_mfma_f32_16x16x32_bf16 a[48:51], a[224:227], a[132:135], a[48:51]\n\t"
    "v_mfma_f32_16x16x32_bf16 a[80:83], a[224:227], a[136:139], a[80:83]\n\t"
    "s_add_u32 s63, s53, 0xb000\n\t"
    "ds_read_b128 a[184:187], v193 offset:0\n\t"
    "v_mfma_f32_16x16x32_bf16 a[112:115], a[224:227], a[140:143], a[112:115]\n\t"
    "ds_read_b64_tr_b16 a[224:225], v202 offset:8192\n\t"
    "buffer_store_dwordx4 v[112:115], v216, s[20:23], s63 offen offset:0 sc1\n\t"
    "v_mfma_f32_16x16x32_bf16 a[20:23], a[228:231], a[128:131], a[20:23]\n\t"
    "ds_read_b64_tr_b16 a[226:227], v202 offset:9216\n\t"
    "v_mfma_f32_16x16x32_bf16 a[52:55], a[228:231], a[132:135], a[52:55]\n\t"
    "v_mfma_f32_16x16x32_bf16 a[84:87], a[228:231], a[136:139], a[84:87]\n\t"
    "ds_read_b128 a[188:191], v193 offset:2048\n\t"
    "v_mfma_f32_16x16x32_bf16 a[116:119], a[228:231], a[140:143], a[116:119]\n\t"
    "ds_read_b64_tr_b16 a[228:229], v203 offset:8192\n\t"
    "v_mfma_f32_16x16x32_bf16 a[24:27], a[232:235], a[128:131], a[24:27]\n\t"
    "ds_read_b64_tr_b16 a[230:231], v203 offset:9216\n\t"
    "v_mfma_f32_16x16x32_bf16 a[56:59], a[232:235], a[132:135], a[56:59]\n\t"
    "s_add_u32 s63, s53, 0xb000\n\t"
    "v_mfma_f32_16x16x32_bf16 a[88:91], a[232:235], a[136:139], a[88:91]\n\t"
    "ds_read_b128 a[192:195], v193 offset:16384\n\t"
    "buffer_store_dwordx4 v[116:119], v216, s[20:23], s63 offen offset:1024 sc1\n\t"
    "v_mfma_f32_16x16x32_bf16 a[120:123], a[232:235], a[140:143], a[120:123]\n\t"
    "s_waitcnt lgkmcnt(15)\n\t"
    "ds_read_b64_tr_b16 a[232:233], v204 offset:8192\n\t"
    "v_mfma_f32_16x16x32_bf16 a[28:31], a[236:239], a[128:131], a[28:31]\n\t"
    "ds_read_b64_tr_b16 a[234:235], v204 offset:9216\n\t"
    "v_mfma_f32_16x16x32_bf16 a[60:63], a[236:239], a[132:135], a[60:63]\n\t"
    "v_mfma_f32_16x16x32_bf16 a[92:95], a[236:239], a[136:139], a[92:95]\n\t"
    "ds_read_b128 a[196:199], v193 offset:18432\n\t"
    "v_mfma_f32_16x16x32_bf16 a[124:127], a[236:239], a[140:143], a[124:127]\n\t"
    "s_waitcnt lgkmcnt(15)\n\t"
    "ds_read_b64_tr_b16 a[236:237], v205 offset:8192\n\t"
    "v_mfma_f32_16x16x32_bf16 v[128:131], a[240:243], a[144:147], v[128:131]\n\t"
    "s_add_u32 s63, s53, 0xb000\n\t"
    "ds_read_b64_tr_b16 a[238:239], v205 offset:9216\n\t"
    "v_mfma_f32_16x16x32_bf16 v[132:135], a[240:243], a[148:151], v[132:135]\n\t"
    "s_waitcnt lgkmcnt(15)\n\t"
    "buffer_store_dwordx4 v[120:123], v216, s[20:23], s63 offen offset:2048 sc1\n\t"
    "v_mfma_f32_16x16x32_bf16 v[136:139], a[240:243], a[152:155], v[136:139]\n\t"
    "s_waitcnt lgkmcnt(15)\n\t"
    "ds_read_b128 a[200:203], v195\n\t"
    "v_mfma_f32_16x16x32_bf16 v[140:143], a[240:243], a[156:159], v[140:143]\n\t"
    "s_waitcnt lgkmcnt(15)\n\t"
    "ds_read_b64_tr_b16 a[240:241], v206 offset:4096\n\t"
    "v_mfma_f32_16x16x32_bf16 v[144:147], a[244:247], a[144:147], v[144:147]\n\t"
    "ds_read_b64_tr_b16 a[242:243], v206 offset:4608\n\t"
    "v_mfma_f32_16x16x32_bf16 v[148:151], a[244:247], a[148:151], v[148:151]\n\t"
    "v_mfma_f32_16x16x32_bf16 v[152:155], a[244:247], a[152:155], v[152:155]\n\t"
    "ds_read_b128 a[204:207], v197\n\t"
    "v_mfma_f32_16x16x32_bf16 v[156:159], a[244:247], a[156:159], v[156:159]\n\t"
    "s_add_u32 s63, s53, 0xb000\n\t"
    "v_mfma_f32_16x16x32_bf16 v[160:163], a[244:247], a[160:163], v[160:163]\n\t"
    "buffer_store_dwordx4 v[124:127], v216, s[20:23], s63 offen offset:3072 sc1\n\t"
    "v_mfma_f32_16x16x32_bf16 v[164:167], a[244:247], a[164:167], v[164:167]\n\t"
    "s_waitcnt lgkmcnt(15)\n\t"
    "ds_read_b64_tr_b16 a[244:245], v207 offset:4096\n\t"
    "v_mfma_f32_16x16x32_bf16 v[168:171], a[144:147], a[248:251], v[168:171]\n\t"
    "ds_read_b64_tr_b16 a[246:247], v207 offset:4608\n\t"
    "v_mfma_f32_16x16x32_bf16 v[172:175], a[148:151], a[248:251], v[172:175]\n\t"
    "v_mfma_f32_16x16x32_bf16 v[176:179], a[152:155], a[248:251], v[176:179]\n\t"
    "v_mfma_f32_16x16x32_bf16 v[180:183], a[156:159], a[248:251], v[180:183]\n\t"
    "v_mfma_f32_16x16x32_bf16 v[184:187], a[160:163], a[248:251], v[184:187]\n\t"
    "v_mfma_f32_16x16x32_bf16 v[188:191], a[164:167], a[248:251], v[188:191]\n\t"
    "s_waitcnt lgkmcnt(15)\n\t"
    "ds_read_b64_tr_b16 a[248:249], v208 offset:4096\n\t"
    "v_mfma_f32_16x16x32_bf16 a[0:3], a[208:211], a[168:171], a[0:3]\n\t"
    "s_waitcnt lgkmcnt(15)\n\t"
    "ds_read_b64_tr_b16 a[250:251], v208 offset:4608\n\t"
    "v_mfma_f32_16x16x32_bf16 a[32:35], a[208:211], a[172:175], a[32:35]\n\t"
    "s_waitcnt lgkmcnt(15)\n\t"
    "v_mfma_f32_16x16x32_bf16 a[64:67], a[208:211], a[176:179], a[64:67]\n\t"
    "s_waitcnt lgkmcnt(15)\n\t"
    "v_mfma_f32_16x16x32_bf16 a[96:99], a[208:211], a[180:183], a[96:99]\n\t"
    "v_mfma_f32_16x16x32_bf16 a[4:7], a[212:215], a[168:171], a[4:7]\n\t"
    "v_mfma_f32_16x16x32_bf16 a[36:39], a[212:215], a[172:175], a[36:39]\n\t"
    "v_mfma_f32_16x16x32_bf16 a[68:71], a[212:215], a[176:179], a[68:71]\n\t"
    "v_mfma_f32_16x16x32_bf16 a[100:103], a[212:215], a[180:183], a[100:103]\n\t"
    "v_mfma_f32_16x16x32_bf16 a[8:11], a[216:219], a[168:171], a[8:11]\n\t"
    "v_mfma_f32_16x16x32_bf16 a[40:43], a[216:219], a[172:175], a[40:43]\n\t"
    "v_mfma_f32_16x16x32_bf16 a[72:75], a[216:219], a[176:179], a[72:75]\n\t"
    "v_mfma_f32_16x16x32_bf16 a[104:107], a[216:219], a[180:183], a[104:107]\n\t"
    "s_waitcnt lgkmcnt(15)\n\t"
    "v_mfma_f32_16x16x32_bf16 a[12:15], a[220:223], a[168:171], a[12:15]\n\t"
    "v_mfma_f32_16x16x32_bf16 a[44:47], a[220:223], a[172:175], a[44:47]\n\t"
    "v_mfma_f32_16x16x32_bf16 a[76:79], a[220:223], a[176:179], a[76:79]\n\t"
    "v_mfma_f32_16x16x32_bf16 a[108:111], a[220:223], a[180:183], a[108:111]\n\t"
    "s_waitcnt lgkmcnt(15)\n\t"
    "v_mfma_f32_16x16x32_bf16 a[16:19], a[224:227], a[168:171], a[16:19]\n\t"
    "v_mfma_f32_16x16x32_bf16 a[48:51], a[224:227], a[172:175], a[48:51]\n\t"
    "v_mfma_f32_16x16x32_bf16 a[80:83], a[224:227], a[176:179], a[80:83]\n\t"
    "v_mfma_f32_16x16x32_bf16 a[112:115], a[224:227], a[180:183], a[112:115]\n\t"
    "s_waitcnt lgkmcnt(14)\n\t"
    "v_mfma_f32_16x16x32_bf16 a[20:23], a[228:231], a[168:171], a[20:23]\n\t"
    "v_mfma_f32_16x16x32_bf16 a[52:55], a[228:231], a[172:175], a[52:55]\n\t"
    "v_mfma_f32_16x16x32_bf16 a[84:87], a[228:231], a[176:179], a[84:87]\n\t"
    "v_mfma_f32_16x16x32_bf16 a[116:119], a[228:231], a[180:183], a[116:119]\n\t"
    "s_waitcnt lgkmcnt(11)\n\t"
    "v_mfma_f32_16x16x32_bf16 a[24:27], a[232:235], a[168:171], a[24:27]\n\t"
    "v_mfma_f32_16x16x32_bf16 a[56:59], a[232:235], a[172:175], a[56:59]\n\t"
    "v_mfma_f32_16x16x32_bf16 a[88:91], a[232:235], a[176:179], a[88:91]\n\t"
    "v_mfma_f32_16x16x32_bf16 a[120:123], a[232:235], a[180:183], a[120:123]\n\t"
    "s_waitcnt lgkmcnt(8)\n\t"
    "v_mfma_f32_16x16x32_bf16 a[28:31], a[236:239], a[168:171], a[28:31]\n\t"
    "v_mfma_f32_16x16x32_bf16 a[60:63], a[236:239], a[172:175], a[60:63]\n\t"
    "v_mfma_f32_16x16x32_bf16 a[92:95], a[236:239], a[176:179], a[92:95]\n\t"
    "v_mfma_f32_16x16x32_bf16 a[124:127], a[236:239], a[180:183], a[124:127]\n\t"
    "s_waitcnt lgkmcnt(5)\n\t"
    "v_mfma_f32_16x16x32_bf16 v[128:131], a[240:243], a[184:187], v[128:131]\n\t"
    "v_mfma_f32_16x16x32_bf16 v[132:135], a[240:243], a[188:191], v[132:135]\n\t"
    "v_mfma_f32_16x16x32_bf16 v[136:139], a[240:243], a[192:195], v[136:139]\n\t"
    "v_mfma_f32_16x16x32_bf16 v[140:143], a[240:243], a[196:199], v[140:143]\n\t"
    "s_waitcnt lgkmcnt(2)\n\t"
    "v_mfma_f32_16x16x32_bf16 v[144:147], a[244:247], a[184:187], v[144:147]\n\t"
    "v_mfma_f32_16x16x32_bf16 v[148:151], a[244:247], a[188:191], v[148:151]\n\t"
    "v_mfma_f32_16x16x32_bf16 v[152:155], a[244:247], a[192:195], v[152:155]\n\t"
    "v_mfma_f32_16x16x32_bf16 v[156:159], a[244:247], a[196:199], v[156:159]\n\t"
    "v_mfma_f32_16x16x32_bf16 v[160:163], a[244:247], a[200:203], v[160:163]\n\t"
    "v_mfma_f32_16x16x32_bf16 v[164:167], a[244:247], a[204:207], v[164:167]\n\t"
    "s_waitcnt lgkmcnt(0)\n\t"
    "v_mfma_f32_16x16x32_bf16 v[168:171], a[184:187], a[248:251], v[168:171]\n\t"
    "v_mfma_f32_16x16x32_bf16 v[172:175], a[188:191], a[248:251], v[172:175]\n\t"
    "v_mfma_f32_16x16x32_bf16 v[176:179], a[192:195], a[248:251], v[176:179]\n\t"
    "v_mfma_f32_16x16x32_bf16 v[180:183], a[196:199], a[248:251], v[180:183]\n\t"
    "v_mfma_f32_16x16x32_bf16 v[184:187], a[200:203], a[248:251], v[184:187]\n\t"
    "v_mfma_f32_16x16x32_bf16 v[188:191], a[204:207], a[248:251], v[188:191]\n\t"
    "s_waitcnt vmcnt(0)\n\t"
    "s_barrier\n\t"
    "s_cmp_lg_u32 s33, 0\n\t"
    "s_cbranch_scc1 L_pub1\n\t"
    "v_mov_b32 v226, v250\n\t"
    "v_mov_b32 v227, v251\n\t"
    "v_mov_b32 v228, s76\n\t"
    "v_mov_b32 v229, s77\n\t"
    "buffer_store_dwordx4 v[226:229], v254, s[28:31], 0 offen sc1\n\t"
    "L_pub1:\n\t"
    "s_cmp_eq_u32 s33, 0\n\t"
    "s_cbranch_scc1 L_pfo\n\t"
    "s_load_dwordx4 s[92:95], %[ka], 56\n\t"
    "s_mul_i32 s37, s36, 3\n\t"
    "s_add_u32 s37, s37, s33\n\t"
    "s_sub_u32 s37, s37, 1\n\t"
    "s_waitcnt lgkmcnt(0)\n\t"
    "s_mov_b64 s[80:81], s[92:93]\n\t"
    "s_and_b32 s81, s81, 0xffff\n\t"
    "s_mov_b32 s82, 0x400000\n\t"
    "s_mov_b32 s83, 0x20000\n\t"
    "s_mov_b64 s[84:85], s[94:95]\n\t"
    "s_and_b32 s85, s85, 0xffff\n\t"
    "s_mov_b32 s86, 0x80000\n\t"
    "s_mov_b32 s87, 0x20000\n\t"
    "s_mul_i32 s39, s37, 6144\n\t"
    "s_lshl_b32 s37, s37, 10\n\t"
    "buffer_load_dwordx4 v[124:127], v216, s[80:83], s39 offen offset:0\n\t"
    "buffer_load_dwordx4 v[124:127], v216, s[80:83], s39 offen offset:1024\n\t"
    "buffer_load_dwordx4 v[124:127], v216, s[80:83], s39 offen offset:2048\n\t"
    "buffer_load_dwordx4 v[124:127], v216, s[80:83], s39 offen offset:3072\n\t"
    "s_add_u32 s39, s39, 0x1000\n\t"
    "buffer_load_dwordx4 v[124:127], v216, s[80:83], s39 offen offset:0\n\t"
    "buffer_load_dwordx4 v[124:127], v216, s[80:83], s39 offen offset:1024\n\t"
    "buffer_load_dwordx4 v[124:127], v216, s[84:87], s37 offen\n\t"
    "L_pfo:\n\t"
    "s_load_dwordx2 s[12:13], %[ka], 56\n\t"
    "s_load_dwordx2 s[16:17], %[ka], 64\n\t"
    "s_load_dwordx2 s[72:73], %[ka], 80\n\t"
    "s_nop 7\n\t"
    "v_and_b32 v84, 15, v217\n\t"
    "v_lshrrev_b32 v88, 4, v217\n\t"
    "v_and_b32 v85, 1, v217\n\t"
    "v_lshlrev_b32 v88, 2, v88\n\t"
    "v_mov_b32 v86, 0xf149f2ca\n\t"
    "v_sub_u32 v84, v84, v88\n\t"
    "v_lshlrev_b32 v87, 3, v217\n\t"
    "v_cmp_eq_u32 s[92:93], 0, v85\n\t"
    "v_cmp_eq_u32 s[94:95], 1, v85\n\t"
    "v_cmp_le_i32 vcc, 0, v84\n\t"
    "v_cmp_ge_i32 s[78:79], -2, v84\n\t"
    "v_cvt_pk_bf16_f32 v128, v128, v129\n\t"
    "v_cvt_pk_bf16_f32 v129, v130, v131\n\t"
    "s_and_b64 vcc, vcc, s[92:93]\n\t"
    "s_and_b64 s[78:79], s[78:79], s[92:93]\n\t"
    "v_cvt_pk_bf16_f32 v132, v132, v133\n\t"
    "v_cvt_pk_bf16_f32 v133, v134, v135\n\t"
    "v_cndmask_b32_e64 v0, v86, 0, vcc\n\t"
    "v_cndmask_b32_e64 v4, v86, 0, s[78:79]\n\t"
    "v_cmp_le_i32 vcc, 1, v84\n\t"
    "v_cmp_ge_i32 s[78:79], -1, v84\n\t"
    "v_cvt_pk_bf16_f32 v136, v136, v137\n\t"
    "v_cvt_pk_bf16_f32 v137, v138, v139\n\t"
    "s_and_b64 vcc, vcc, s[94:95]\n\t"
    "s_and_b64 s[78:79], s[78:79], s[94:95]\n\t"
    "v_cvt_pk_bf16_f32 v140, v140, v141\n\t"
    "v_cvt_pk_bf16_f32 v141, v142, v143\n\t"
    "v_cndmask_b32_e64 v1, v86, 0, vcc\n\t"
    "v_cndmask_b32_e64 v5, v86, 0, s[78:79]\n\t"
    "v_cmp_le_i32 vcc, 2, v84\n\t"
    "v_cmp_ge_i32 s[78:79], 0, v84\n\t"
    "v_cvt_pk_bf16_f32 v144, v144, v145\n\t"
    "v_cvt_pk_bf16_f32 v145, v146, v147\n\t"
    "s_and_b64 vcc, vcc, s[92:93]\n\t"
    "s_and_b64 s[78:79], s[78:79], s[92:93]\n\t"
    "v_cvt_pk_bf16_f32 v148, v148, v149\n\t"
    "v_cvt_pk_bf16_f32 v149, v150, v151\n\t"
    "v_cndmask_b32_e64 v2, v86, 0, vcc\n\t"
    "v_cndmask_b32_e64 v6, v86, 0, s[78:79]\n\t"
    "v_cmp_le_i32 vcc, 3, v84\n\t"
    "v_cmp_ge_i32 s[78:79], 1, v84\n\t"
    "v_cvt_pk_bf16_f32 v152, v152, v153\n\t"
    "v_cvt_pk_bf16_f32 v153, v154, v155\n\t"
    "s_and_b64 vcc, vcc, s[94:95]\n\t"
    "s_and_b64 s[78:79], s[78:79], s[94:95]\n\t"
    "v_cvt_pk_bf16_f32 v156, v156, v157\n\t"
    "v_cvt_pk_bf16_f32 v157, v158, v159\n\t"
    "v_cndmask_b32_e64 v3, v86, 0, vcc\n\t"
    "v_cndmask_b32_e64 v7, v86, 0, s[78:79]\n\t"
    "v_cvt_pk_bf16_f32 v160, v160, v161\n\t"
    "v_cvt_pk_bf16_f32 v161, v162, v163\n\t"
    "v_cvt_pk_bf16_f32 v164, v164, v165\n\t"
    "v_cvt_pk_bf16_f32 v165, v166, v167\n\t"
    "v_cvt_pk_bf16_f32 v168, v168, v169\n\t"
    "v_cvt_pk_bf16_f32 v169, v170, v171\n\t"
    "v_cvt_pk_bf16_f32 v172, v172, v173\n\t"
    "v_cvt_pk_bf16_f32 v173, v174, v175\n\t"
    "v_cvt_pk_bf16_f32 v176, v176, v177\n\t"
    "v_cvt_pk_bf16_f32 v177, v178, v179\n\t"
    "v_cvt_pk_bf16_f32 v180, v180, v181\n\t"
    "v_cvt_pk_bf16_f32 v181, v182, v183\n\t"
    "v_cvt_pk_bf16_f32 v184, v184, v185\n\t"
    "v_cvt_pk_bf16_f32 v185, v186, v187\n\t"
    "v_cvt_pk_bf16_f32 v188, v188, v189\n\t"
    "v_cvt_pk_bf16_f32 v189, v190, v191\n\t"
    "s_or_b32 s37, s35, s33\n\t"
    "s_cmp_eq_u32 s37, 0\n\t"
    "s_cselect_b32 s37, 0xf149f2ca, 0\n\t"
    "s_mov_b32 s38, 0x3eb8aa3b\n\t"
    "v_add_f32 v8, s37, v4\n\t"
    "v_add_f32 v9, s37, v5\n\t"
    "v_add_f32 v10, s37, v6\n\t"
    "v_add_f32 v11, s37, v7\n\t"
    "s_nop 0\n\t"
    "v_mfma_f32_16x16x16_bf16 v[16:19], v[144:145], v[128:129], v[0:3]\n\t"
    "v_mfma_f32_16x16x16_bf16 v[20:23], v[160:161], v[128:129], v[8:11]\n\t"
    "v_mfma_f32_16x16x16_bf16 v[24:27], v[148:149], v[132:133], v[0:3]\n\t"
    "v_mfma_f32_16x16x16_bf16 v[28:31], v[144:145], v[132:133], v[4:7]\n\t"
    "v_mfma_f32_16x16x16_bf16 v[32:35], v[152:153], v[136:137], v[0:3]\n\t"
    "v_mfma_f32_16x16x16_bf16 v[36:39], v[164:165], v[136:137], v[4:7]\n\t"
    "v_mfma_f32_16x16x16_bf16 v[40:43], v[156:157], v[140:141], v[0:3]\n\t"
    "v_mfma_f32_16x16x16_bf16 v[44:47], v[152:153], v[140:141], v[4:7]\n\t"
    "s_lshl_b32 s39, s34, 9\n\t"
    "s_lshl_b32 s92, s60, 14\n\t"
    "s_add_u32 s92, s92, s39\n\t"
    "s_add_u32 s93, s92, 0x4000\n\t"
    "s_lshl_b32 s94, s61, 14\n\t"
    "s_add_u32 s94, s94, s39\n\t"
    "s_add_u32 s95, s94, 0x4000\n\t"
    "s_nop 1\n\t"
    "v_max3_f32 v64, v16, v17, v18\n\t"
    "v_max3_f32 v65, v24, v25, v26\n\t"
    "v_max3_f32 v66, v32, v33, v34\n\t"
    "v_max3_f32 v67, v40, v41, v42\n\t"
    "v_max3_f32 v72, v19, v20, v21\n\t"
    "v_max3_f32 v73, v27, v28, v29\n\t"
    "v_max3_f32 v74, v35, v36, v37\n\t"
    "v_max3_f32 v75, v43, v44, v45\n\t"
    "v_max3_f32 v64, v64, v22, v23\n\t"
    "v_max3_f32 v65, v65, v30, v31\n\t"
    "v_max3_f32 v66, v66, v38, v39\n\t"
    "v_max3_f32 v67, v67, v46, v47\n\t"
    "v_max_f32 v64, v64, v72\n\t"
    "v_max_f32 v65, v65, v73\n\t"
    "v_max_f32 v66, v66, v74\n\t"
    "v_max_f32 v67, v67, v75\n\t"
    "v_mov_b32 v72, v64\n\t"
    "v_mov_b32 v73, v65\n\t"
    "v_mov_b32 v74, v66\n\t"
    "v_mov_b32 v75, v67\n\t"
    "v_permlane16_swap_b32 v64, v72\n\t"
    "v_permlane16_swap_b32 v65, v73\n\t"
    "v_permlane16_swap_b32 v66, v74\n\t"
    "v_permlane16_swap_b32 v67, v75\n\t"
    "v_max_f32 v64, v64, v72\n\t"
    "v_max_f32 v65, v65, v73\n\t"
    "v_max_f32 v66, v66, v74\n\t"
    "v_max_f32 v67, v67, v75\n\t"
    "v_mov_b32 v72, v64\n\t"
    "v_mov_b32 v73, v65\n\t"
    "v_mov_b32 v74, v66\n\t"
    "v_mov_b32 v75, v67\n\t"
    "v_permlane32_swap_b32 v64, v72\n\t"
    "v_permlane32_swap_b32 v65, v73\n\t"
    "v_permlane32_swap_b32 v66, v74\n\t"
    "v_permlane32_swap_b32 v67, v75\n\t"
    "v_max_f32 v64, v64, v72\n\t"
    "v_max_f32 v65, v65, v73\n\t"
    "v_max_f32 v66, v66, v74\n\t"
    "v_max_f32 v67, v67, v75\n\t"
    "v_mul_f32 v64, s38, v64\n\t"
    "v_mul_f32 v65, s38, v65\n\t"
    "v_mul_f32 v66, s38, v66\n\t"
    "v_mul_f32 v67, s38, v67\n\t"
    "v_fma_f32 v16, v16, s38, -v64\n\t"
    "v_fma_f32 v24, v24, s38, -v65\n\t"
    "v_fma_f32 v32, v32, s38, -v66\n\t"
    "v_fma_f32 v40, v40, s38, -v67\n\t"
    "v_fma_f32 v17, v17, s38, -v64\n\t"
    "v_fma_f32 v25, v25, s38, -v65\n\t"
    "v_fma_f32 v33, v33, s38, -v66\n\t"
    "v_fma_f32 v41, v41, s38, -v67\n\t"
    "v_fma_f32 v18, v18, s38, -v64\n\t"
    "v_fma_f32 v26, v26, s38, -v65\n\t"
    "v_fma_f32 v34, v34, s38, -v66\n\t"
    "v_fma_f32 v42, v42, s38, -v67\n\t"
    "v_fma_f32 v19, v19, s38, -v64\n\t"
    "v_fma_f32 v27, v27, s38, -v65\n\t"
    "v_fma_f32 v35, v35, s38, -v66\n\t"
    "v_fma_f32 v43, v43, s38, -v67\n\t"
    "v_fma_f32 v20, v20, s38, -v64\n\t"
    "v_fma_f32 v28, v28, s38, -v65\n\t"
    "v_fma_f32 v36, v36, s38, -v66\n\t"
    "v_fma_f32 v44, v44, s38, -v67\n\t"
    "v_fma_f32 v21, v21, s38, -v64\n\t"
    "v_fma_f32 v29, v29, s38, -v65\n\t"
    "v_fma_f32 v37, v37, s38, -v66\n\t"
    "v_fma_f32 v45, v45, s38, -v67\n\t"
    "v_fma_f32 v22, v22, s38, -v64\n\t"
    "v_fma_f32 v30, v30, s38, -v65\n\t"
    "v_fma_f32 v38, v38, s38, -v66\n\t"
    "v_fma_f32 v46, v46, s38, -v67\n\t"
    "v_fma_f32 v23, v23, s38, -v64\n\t"
    "v_fma_f32 v31, v31, s38, -v65\n\t"
    "v_fma_f32 v39, v39, s38, -v66\n\t"
    "v_fma_f32 v47, v47, s38, -v67\n\t"
    "v_exp_f32 v16, v16\n\t"
    "v_exp_f32 v24, v24\n\t"
    "v_exp_f32 v32, v32\n\t"
    "v_exp_f32 v40, v40\n\t"
    "v_exp_f32 v17, v17\n\t"
    "v_exp_f32 v25, v25\n\t"
    "v_exp_f32 v33, v33\n\t"
    "v_exp_f32 v41, v41\n\t"
    "v_exp_f32 v18, v18\n\t"
    "v_exp_f32 v26, v26\n\t"
    "v_exp_f32 v34, v34\n\t"
    "v_exp_f32 v42, v42\n\t"
    "v_exp_f32 v19, v19\n\t"
    "v_exp_f32 v27, v27\n\t"
    "v_exp_f32 v35, v35\n\t"
    "v_exp_f32 v43, v43\n\t"
    "v_exp_f32 v20, v20\n\t"
    "v_exp_f32 v28, v28\n\t"
    "v_exp_f32 v36, v36\n\t"
    "v_exp_f32 v44, v44\n\t"
    "v_exp_f32 v21, v21\n\t"
    "v_exp_f32 v29, v29\n\t"
    "v_exp_f32 v37, v37\n\t"
    "v_exp_f32 v45, v45\n\t"
    "v_exp_f32 v22, v22\n\t"
    "v_exp_f32 v30, v30\n\t"
    "v_exp_f32 v38, v38\n\t"
    "v_exp_f32 v46, v46\n\t"
    "v_exp_f32 v23, v23\n\t"
    "v_exp_f32 v31, v31\n\t"
    "v_exp_f32 v39, v39\n\t"
    "v_exp_f32 v47, v47\n\t"
    "v_add_f32 v68, v16, v17\n\t"
    "v_add_f32 v69, v24, v25\n\t"
    "v_add_f32 v70, v32, v33\n\t"
    "v_add_f32 v71, v40, v41\n\t"
    "v_add_f32 v68, v68, v18\n\t"
    "v_add_f32 v69, v69, v26\n\t"
    "v_add_f32 v70, v70, v34\n\t"
    "v_add_f32 v71, v71, v42\n\t"
    "v_add_f32 v68, v68, v19\n\t"
    "v_add_f32 v69, v69, v27\n\t"
    "v_add_f32 v70, v70, v35\n\t"
    "v_add_f32 v71, v71, v43\n\t"
    "v_add_f32 v68, v68, v20\n\t"
    "v_add_f32 v69, v69, v28\n\t"
    "v_add_f32 v70, v70, v36\n\t"
    "v_add_f32 v71, v71, v44\n\t"
    "v_add_f32 v68, v68, v21\n\t"
    "v_add_f32 v69, v69, v29\n\t"
    "v_add_f32 v70, v70, v37\n\t"
    "v_add_f32 v71, v71, v45\n\t"
    "v_add_f32 v68, v68, v22\n\t"
    "v_add_f32 v69, v69, v30\n\t"
    "v_add_f32 v70, v70, v38\n\t"
    "v_add_f32 v71, v71, v46\n\t"
    "v_add_f32 v68, v68, v23\n\t"
    "v_add_f32 v69, v69, v31\n\t"
    "v_add_f32 v70, v70, v39\n\t"
    "v_add_f32 v71, v71, v47\n\t"
    "v_cvt_pk_bf16_f32 v16, v16, v17\n\t"
    "v_cvt_pk_bf16_f32 v24, v24, v25\n\t"
    "v_cvt_pk_bf16_f32 v32, v32, v33\n\t"
    "v_cvt_pk_bf16_f32 v40, v40, v41\n\t"
    "v_cvt_pk_bf16_f32 v17, v18, v19\n\t"
    "v_cvt_pk_bf16_f32 v25, v26, v27\n\t"
    "v_cvt_pk_bf16_f32 v33, v34, v35\n\t"
    "v_cvt_pk_bf16_f32 v41, v42, v43\n\t"
    "v_cvt_pk_bf16_f32 v20, v20, v21\n\t"
    "v_cvt_pk_bf16_f32 v28, v28, v29\n\t"
    "v_cvt_pk_bf16_f32 v36, v36, v37\n\t"
    "v_cvt_pk_bf16_f32 v44, v44, v45\n\t"
    "v_cvt_pk_bf16_f32 v21, v22, v23\n\t"
    "v_cvt_pk_bf16_f32 v29, v30, v31\n\t"
    "v_cvt_pk_bf16_f32 v37, v38, v39\n\t"
    "v_cvt_pk_bf16_f32 v45, v46, v47\n\t"
    "s_nop 0\n\t"
    "v_mfma_f32_16x16x16_bf16 v[48:51], v[168:169], v[16:17], 0\n\t"
    "v_mfma_f32_16x16x16_bf16 v[52:55], v[172:173], v[24:25], 0\n\t"
    "v_mfma_f32_16x16x16_bf16 v[56:59], v[176:177], v[32:33], 0\n\t"
    "v_mfma_f32_16x16x16_bf16 v[60:63], v[180:181], v[40:41], 0\n\t"
    "v_mfma_f32_16x16x16_bf16 v[48:51], v[184:185], v[20:21], v[48:51]\n\t"
    "v_mfma_f32_16x16x16_bf16 v[52:55], v[168:169], v[28:29], v[52:55]\n\t"
    "v_mfma_f32_16x16x16_bf16 v[56:59], v[188:189], v[36:37], v[56:59]\n\t"
    "v_mfma_f32_16x16x16_bf16 v[60:63], v[176:177], v[44:45], v[60:63]\n\t"
    "v_mov_b32 v72, v68\n\t"
    "v_mov_b32 v73, v69\n\t"
    "v_mov_b32 v74, v70\n\t"
    "v_mov_b32 v75, v71\n\t"
    "v_permlane16_swap_b32 v68, v72\n\t"
    "v_permlane16_swap_b32 v69, v73\n\t"
    "v_permlane16_swap_b32 v70, v74\n\t"
    "v_permlane16_swap_b32 v71, v75\n\t"
    "v_add_f32 v68, v68, v72\n\t"
    "v_add_f32 v69, v69, v73\n\t"
    "v_add_f32 v70, v70, v74\n\t"
    "v_add_f32 v71, v71, v75\n\t"
    "v_mov_b32 v72, v68\n\t"
    "v_mov_b32 v73, v69\n\t"
    "v_mov_b32 v74, v70\n\t"
    "v_mov_b32 v75, v71\n\t"
    "v_permlane32_swap_b32 v68, v72\n\t"
    "v_permlane32_swap_b32 v69, v73\n\t"
    "v_permlane32_swap_b32 v70, v74\n\t"
    "v_permlane32_swap_b32 v71, v75\n\t"
    "v_add_f32 v68, v68, v72\n\t"
    "v_add_f32 v69, v69, v73\n\t"
    "v_add_f32 v70, v70, v74\n\t"
    "v_add_f32 v71, v71, v75\n\t"
    "v_rcp_f32 v68, v68\n\t"
    "v_rcp_f32 v69, v69\n\t"
    "v_rcp_f32 v70, v70\n\t"
    "v_rcp_f32 v71, v71\n\t"
    "v_mul_f32 v48, v48, v68\n\t"
    "v_mul_f32 v52, v52, v69\n\t"
    "v_mul_f32 v56, v56, v70\n\t"
    "v_mul_f32 v60, v60, v71\n\t"
    "v_mul_f32 v49, v49, v68\n\t"
    "v_mul_f32 v53, v53, v69\n\t"
    "v_mul_f32 v57, v57, v70\n\t"
    "v_mul_f32 v61, v61, v71\n\t"
    "v_mul_f32 v50, v50, v68\n\t"
    "v_mul_f32 v54, v54, v69\n\t"
    "v_mul_f32 v58, v58, v70\n\t"
    "v_mul_f32 v62, v62, v71\n\t"
    "v_mul_f32 v51, v51, v68\n\t"
    "v_mul_f32 v55, v55, v69\n\t"
    "v_mul_f32 v59, v59, v70\n\t"
    "v_mul_f32 v63, v63, v71\n\t"
    "v_cvt_pk_bf16_f32 v76, v48, v49\n\t"
    "v_cvt_pk_bf16_f32 v78, v52, v53\n\t"
    "v_cvt_pk_bf16_f32 v80, v56, v57\n\t"
    "v_cvt_pk_bf16_f32 v82, v60, v61\n\t"
    "v_cvt_pk_bf16_f32 v77, v50, v51\n\t"
    "v_cvt_pk_bf16_f32 v79, v54, v55\n\t"
    "v_cvt_pk_bf16_f32 v81, v58, v59\n\t"
    "v_cvt_pk_bf16_f32 v83, v62, v63\n\t"
    "buffer_store_dwordx2 v[76:77], v87, s[28:31], s92 offen sc1\n\t"
    "buffer_store_dwordx2 v[78:79], v87, s[28:31], s93 offen sc1\n\t"
    "buffer_store_dwordx2 v[80:81], v87, s[28:31], s94 offen sc1\n\t"
    "buffer_store_dwordx2 v[82:83], v87, s[28:31], s95 offen sc1\n\t"
    "s_waitcnt lgkmcnt(0)\n\t"
    "s_and_b32 s13, s13, 0xffff\n\t"
    "s_mov_b32 s14, 0x400000\n\t"
    "s_mov_b32 s15, 0x20000\n\t"
    "s_and_b32 s17, s17, 0xffff\n\t"
    "s_mov_b32 s18, 0x80000\n\t"
    "s_mov_b32 s19, 0x20000\n\t"
    "s_and_b32 s73, s73, 0xffff\n\t"
    "s_mov_b32 s74, 0x200000\n\t"
    "s_mov_b32 s75, 0x20000\n\t"
    "s_bfe_u32 s37, s34, 0x10002\n\t"
    "s_sub_u32 s38, 15, s35\n\t"
    "s_cmp_eq_u32 s37, 0\n\t"
    "s_cselect_b32 s38, s35, s38\n\t"
    "s_lshr_b32 s39, s34, 3\n\t"
    "s_lshl_b32 s80, s38, 20\n\t"
    "s_lshl_b32 s81, s33, 18\n\t"
    "s_add_u32 s80, s80, s81\n\t"
    "s_lshl_b32 s81, s39, 15\n\t"
    "s_add_u32 s80, s80, s81\n\t"
    "s_add_u32 s81, s80, 130048\n\t"
    "s_lshl_b32 s83, s38, 17\n\t"
    "s_lshl_b32 s84, s33, 15\n\t"
    "s_add_u32 s83, s83, s84\n\t"
    "s_lshl_b32 s84, s39, 12\n\t"
    "s_add_u32 s83, s83, s84\n\t"
    "s_add_u32 s84, s83, 15360\n\t"
    "s_and_b32 s88, s34, 3\n\t"
    "s_lshl_b32 s88, s88, 8\n\t"
    "s_lshl_b32 s87, s33, 13\n\t"
    "s_add_u32 s88, s88, s87\n\t"
    "s_lshl_b32 s82, s39, 20\n\t"
    "s_add_u32 s82, s82, s88\n\t"
    "s_lshl_b32 s85, s39, 17\n\t"
    "s_add_u32 s85, s85, s88\n\t"
    "s_lshl_b32 s96, s38, 9\n\t"
    "s_lshr_b32 s88, s33, 1\n\t"
    "s_lshl_b32 s88, s88, 8\n\t"
    "s_add_u32 s96, s96, s88\n\t"
    "s_add_u32 s96, s96, 0x200000\n\t"
    "s_lshl_b32 s91, s38, 17\n\t"
    "s_lshl_b32 s88, s39, 15\n\t"
    "s_add_u32 s91, s91, s88\n\t"
    "s_lshr_b32 s88, s33, 1\n\t"
    "s_lshl_b32 s88, s88, 14\n\t"
    "s_add_u32 s91, s91, s88\n\t"
    "s_and_b32 s88, s34, 3\n\t"
    "s_lshl_b32 s88, s88, 8\n\t"
    "s_add_u32 s91, s91, s88\n\t"
    "s_and_b32 s88, s33, 1\n\t"
    "s_lshl_b32 s88, s88, 7\n\t"
    "s_add_u32 s91, s91, s88\n\t"
    "s_lshl_b32 s89, s35, 3\n\t"
    "s_and_b32 s88, s34, 7\n\t"
    "s_add_u32 s89, s89, s88\n\t"
    "s_lshl_b32 s89, s89, 18\n\t"
    "s_lshl_b32 s88, s39, 16\n\t"
    "s_add_u32 s90, s89, s88\n\t"
    "s_lshl_b32 s88, s33, 12\n\t"
    "s_add_u32 s90, s90, s88\n\t"
    "s_lshr_b32 s88, s33, 1\n\t"
    "s_lshl_b32 s88, s88, 17\n\t"
    "s_add_u32 s89, s89, s88\n\t"
    "s_lshl_b32 s88, s39, 14\n\t"
    "s_add_u32 s89, s89, s88\n\t"
    "s_and_b32 s88, s33, 1\n\t"
    "s_lshl_b32 s88, s88, 12\n\t"
    "s_add_u32 s89, s89, s88\n\t"
    "s_lshl_b32 s97, s33, 11\n\t"
    "s_add_u32 s86, s97, 0x20000\n\t"
    "s_mov_b32 s87, 0\n\t"
    "s_add_u32 s92, s28, 0x1000000\n\t"
    "s_addc_u32 s93, s29, 0\n\t"
    "s_mov_b32 s94, 0x1000000\n\t"
    "s_mov_b32 s95, 0x20000\n\t"
    "v_lshrrev_b32 v219, 4, v217\n\t"
    "v_lshlrev_b32 v218, 10, v219\n\t"
    "v_and_b32 v220, 1, v217\n\t"
    "v_lshl_add_u32 v218, v220, 4, v218\n\t"
    "v_bfe_u32 v220, v217, 1, 3\n\t"
    "v_xor_b32 v221, v220, v219\n\t"
    "v_lshl_add_u32 v203, v221, 5, v218\n\t"
    "v_xor_b32 v221, 4, v221\n\t"
    "v_lshl_add_u32 v204, v221, 5, v218\n\t"
    "v_add_u32 v204, 3072, v204\n\t"
    "s_lshr_b32 s37, s33, 1\n\t"
    "s_lshl_b32 s37, s37, 12\n\t"
    "v_add_u32 v205, s37, v216\n\t"
    "v_lshlrev_b32 v206, 3, v217\n\t"
    "v_add_u32 v206, s37, v206\n\t"
    "v_bfe_u32 v220, v217, 2, 2\n\t"
    "v_lshl_add_u32 v221, v219, 2, v220\n\t"
    "v_lshlrev_b32 v222, 8, v221\n\t"
    "v_and_b32 v223, 3, v217\n\t"
    "v_lshl_add_u32 v222, v223, 3, v222\n\t"
    "v_add_u32 v222, 8192, v222\n\t"
    "v_and_b32 v221, 7, v221\n\t"
    "s_and_b32 s37, s33, 1\n\t"
    "s_lshl_b32 s37, s37, 2\n\t"
    "v_xor_b32 v221, s37, v221\n\t"
    "v_xor_b32 v219, 0, v221\n\t"
    "v_lshl_add_u32 v207, v219, 5, v222\n\t"
    "v_xor_b32 v219, 1, v221\n\t"
    "v_lshl_add_u32 v208, v219, 5, v222\n\t"
    "v_xor_b32 v219, 2, v221\n\t"
    "v_lshl_add_u32 v209, v219, 5, v222\n\t"
    "v_xor_b32 v219, 3, v221\n\t"
    "v_lshl_add_u32 v210, v219, 5, v222\n\t"
    "v_and_b32 v212, 15, v217\n\t"
    "v_lshlrev_b32 v212, 2, v212\n\t"
    "s_cmp_lg_u32 s33, 0\n\t"
    "s_cbranch_scc1 L_s1\n\t"
    "v_mov_b32 v228, s76\n\t"
    "v_mov_b32 v229, s77\n\t"
    "v_mov_b32 v234, v250\n\t"
    "v_mov_b32 v235, v251\n\t"
    "s_mov_b32 s40, 0x40000\n\t"
    "L_poll1:\n\t"
    "buffer_load_dwordx4 v[230:233], v255, s[28:31], 0 offen sc1\n\t"
    "s_waitcnt vmcnt(0)\n\t"
    "v_cmp_eq_u64 vcc, s[76:77], v[230:231]\n\t"
    "s_nop 1\n\t"
    "s_cmp_eq_u64 vcc, -1\n\t"
    "s_cbranch_scc1 L_poll1_done\n\t"
    "s_sub_u32 s40, s40, 1\n\t"
    "s_cmp_eq_u32 s40, 0\n\t"
    "s_cbranch_scc1 L_poll1_done\n\t"
    "s_and_b32 s37, s40, 15\n\t"
    "s_cmp_lg_u32 s37, 0\n\t"
    "s_cbranch_scc1 L_poll1\n\t"
    "buffer_load_dwordx2 v[234:235], v252, s[28:31], 0 offen sc1\n\t"
    "s_waitcnt vmcnt(0)\n\t"
    "v_cndmask_b32 v226, v234, v232, vcc\n\t"
    "v_cndmask_b32 v227, v235, v233, vcc\n\t"
    "buffer_store_dwordx4 v[226:229], v254, s[28:31], 0 offen sc1\n\t"
    "s_branch L_poll1\n\t"
    "L_poll1_done:\n\t"
    "s_nop 0\n\t"
    "v_cndmask_b32 v226, v234, v232, vcc\n\t"
    "v_cndmask_b32 v227, v235, v233, vcc\n\t"
    "buffer_store_dwordx4 v[226:229], v254, s[28:31], 0 offen sc1\n\t"
    "L_s1:\n\t"
    "s_barrier\n\t"
    "s_and_b32 m0, s68, 0x1ffff\n\t"
    "s_add_u32 s68, s68, 0x8000\n\t"
    "s_nop 0\n\t"
    "buffer_load_dwordx4 v216, s[20:23], s70 offen offset:0 sc1 lds\n\t"
    "s_mov_b32 s37, 0x3e0293ee\n\t"
    "v_accvgpr_read_b32 v218, a0\n\t"
    "v_accvgpr_read_b32 v219, a1\n\t"
    "v_accvgpr_read_b32 v220, a2\n\t"
    "v_accvgpr_read_b32 v221, a3\n\t"
    "v_accvgpr_read_b32 v222, a4\n\t"
    "v_accvgpr_read_b32 v223, a5\n\t"
    "v_accvgpr_read_b32 v224, a6\n\t"
    "v_accvgpr_read_b32 v225, a7\n\t"
    "v_mul_f32 v218, s37, v218\n\t"
    "v_mul_f32 v219, s37, v219\n\t"
    "v_mul_f32 v220, s37, v220\n\t"
    "v_mul_f32 v221, s37, v221\n\t"
    "v_mul_f32 v222, s37, v222\n\t"
    "v_mul_f32 v223, s37, v223\n\t"
    "v_mul_f32 v224, s37, v224\n\t"
    "v_mul_f32 v225, s37, v225\n\t"
    "buffer_load_dwordx4 v216, s[20:23], s70 offen offset:1024 sc1 lds\n\t"
    "v_cvt_pk_bf16_f32 v0, v218, v219\n\t"
    "v_cvt_pk_bf16_f32 v1, v220, v221\n\t"
    "v_cvt_pk_bf16_f32 v2, v222, v223\n\t"
    "v_cvt_pk_bf16_f32 v3, v224, v225\n\t"
    "v_accvgpr_read_b32 v226, a8\n\t"
    "v_accvgpr_read_b32 v227, a9\n\t"
    "v_accvgpr_read_b32 v228, a10\n\t"
    "v_accvgpr_read_b32 v229, a11\n\t"
    "v_accvgpr_read_b32 v230, a12\n\t"
    "v_accvgpr_read_b32 v231, a13\n\t"
    "v_accvgpr_read_b32 v232, a14\n\t"
    "v_accvgpr_read_b32 v233, a15\n\t"
    "v_mul_f32 v226, s37, v226\n\t"
    "v_mul_f32 v227, s37, v227\n\t"
    "v_mul_f32 v228, s37, v228\n\t"
    "v_mul_f32 v229, s37, v229\n\t"
    "v_mul_f32 v230, s37, v230\n\t"
    "buffer_load_dwordx4 v216, s[20:23], s70 offen offset:2048 sc1 lds\n\t"
    "v_mul_f32 v231, s37, v231\n\t"
    "v_mul_f32 v232, s37, v232\n\t"
    "v_mul_f32 v233, s37, v233\n\t"
    "v_cvt_pk_bf16_f32 v4, v226, v227\n\t"
    "v_cvt_pk_bf16_f32 v5, v228, v229\n\t"
    "v_cvt_pk_bf16_f32 v6, v230, v231\n\t"
    "v_cvt_pk_bf16_f32 v7, v232, v233\n\t"
    "v_accvgpr_read_b32 v218, a16\n\t"
    "v_accvgpr_read_b32 v219, a17\n\t"
    "v_accvgpr_read_b32 v220, a18\n\t"
    "v_accvgpr_read_b32 v221, a19\n\t"
    "v_accvgpr_read_b32 v222, a20\n\t"
    "v_accvgpr_read_b32 v223, a21\n\t"
    "v_accvgpr_read_b32 v224, a22\n\t"
    "v_accvgpr_read_b32 v225, a23\n\t"
    "v_mul_f32 v218, s37, v218\n\t"
    "v_mul_f32 v219, s37, v219\n\t"
    "v_mul_f32 v220, s37, v220\n\t"
    "buffer_load_dwordx4 v216, s[20:23], s70 offen offset:3072 sc1 lds\n\t"
    "s_add_u32 s70, s70, 0x8000\n\t"
    "v_mul_f32 v221, s37, v221\n\t"
    "v_mul_f32 v222, s37, v222\n\t"
    "v_mul_f32 v223, s37, v223\n\t"
    "v_mul_f32 v224, s37, v224\n\t"
    "v_mul_f32 v225, s37, v225\n\t"
    "v_cvt_pk_bf16_f32 v8, v218, v219\n\t"
    "v_cvt_pk_bf16_f32 v9, v220, v221\n\t"
    "v_cvt_pk_bf16_f32 v10, v222, v223\n\t"
    "v_cvt_pk_bf16_f32 v11, v224, v225\n\t"
    "v_accvgpr_read_b32 v226, a24\n\t"
    "v_accvgpr_read_b32 v227, a25\n\t"
    "v_accvgpr_read_b32 v228, a26\n\t"
    "v_accvgpr_read_b32 v229, a27\n\t"
    "v_accvgpr_read_b32 v230, a28\n\t"
    "v_accvgpr_read_b32 v231, a29\n\t"
    "v_accvgpr_read_b32 v232, a30\n\t"
    "v_accvgpr_read_b32 v233, a31\n\t"
    "s_and_b32 m0, s69, 0x1ffff\n\t"
    "s_add_u32 s69, s69, 0x8000\n\t"
    "s_nop 0\n\t"
    "buffer_load_dwordx4 v216, s[20:23], s71 offen offset:0 sc1 lds\n\t"
    "v_mul_f32 v226, s37, v226\n\t"
    "v_mul_f32 v227, s37, v227\n\t"
    "v_mul_f32 v228, s37, v228\n\t"
    "v_mul_f32 v229, s37, v229\n\t"
    "v_mul_f32 v230, s37, v230\n\t"
    "v_mul_f32 v231, s37, v231\n\t"
    "v_mul_f32 v232, s37, v232\n\t"
    "v_mul_f32 v233, s37, v233\n\t"
    "v_cvt_pk_bf16_f32 v12, v226, v227\n\t"
    "v_cvt_pk_bf16_f32 v13, v228, v229\n\t"
    "v_cvt_pk_bf16_f32 v14, v230, v231\n\t"
    "v_cvt_pk_bf16_f32 v15, v232, v233\n\t"
    "v_accvgpr_read_b32 v218, a32\n\t"
    "v_accvgpr_read_b32 v219, a33\n\t"
    "v_accvgpr_read_b32 v220, a34\n\t"
    "v_accvgpr_read_b32 v221, a35\n\t"
    "v_accvgpr_read_b32 v222, a36\n\t"
    "v_accvgpr_read_b32 v223, a37\n\t"
    "buffer_load_dwordx4 v216, s[20:23], s71 offen offset:1024 sc1 lds\n\t"
    "v_accvgpr_read_b32 v224, a38\n\t"
    "v_accvgpr_read_b32 v225, a39\n\t"
    "v_mul_f32 v218, s37, v218\n\t"
    "v_mul_f32 v219, s37, v219\n\t"
    "v_mul_f32 v220, s37, v220\n\t"
    "v_mul_f32 v221, s37, v221\n\t"
    "v_mul_f32 v222, s37, v222\n\t"
    "v_mul_f32 v223, s37, v223\n\t"
    "v_mul_f32 v224, s37, v224\n\t"
    "v_mul_f32 v225, s37, v225\n\t"
    "v_cvt_pk_bf16_f32 v16, v218, v219\n\t"
    "v_cvt_pk_bf16_f32 v17, v220, v221\n\t"
    "v_cvt_pk_bf16_f32 v18, v222, v223\n\t"
    "v_cvt_pk_bf16_f32 v19, v224, v225\n\t"
    "v_accvgpr_read_b32 v226, a40\n\t"
    "v_accvgpr_read_b32 v227, a41\n\t"
    "v_accvgpr_read_b32 v228, a42\n\t"
    "buffer_load_dwordx4 v216, s[20:23], s71 offen offset:2048 sc1 lds\n\t"
    "v_accvgpr_read_b32 v229, a43\n\t"
    "v_accvgpr_read_b32 v230, a44\n\t"
    "v_accvgpr_read_b32 v231, a45\n\t"
    "v_accvgpr_read_b32 v232, a46\n\t"
    "v_accvgpr_read_b32 v233, a47\n\t"
    "v_mul_f32 v226, s37, v226\n\t"
    "v_mul_f32 v227, s37, v227\n\t"
    "v_mul_f32 v228, s37, v228\n\t"
    "v_mul_f32 v229, s37, v229\n\t"
    "v_mul_f32 v230, s37, v230\n\t"
    "v_mul_f32 v231, s37, v231\n\t"
    "v_mul_f32 v232, s37, v232\n\t"
    "v_mul_f32 v233, s37, v233\n\t"
    "v_cvt_pk_bf16_f32 v20, v226, v227\n\t"
    "v_cvt_pk_bf16_f32 v21, v228, v229\n\t"
    "v_cvt_pk_bf16_f32 v22, v230, v231\n\t"
    "v_cvt_pk_bf16_f32 v23, v232, v233\n\t"
    "buffer_load_dwordx4 v216, s[20:23], s71 offen offset:3072 sc1 lds\n\t"
    "s_add_u32 s71, s71, 0x8000\n\t"
    "v_accvgpr_read_b32 v218, a48\n\t"
    "v_accvgpr_read_b32 v219, a49\n\t"
    "v_accvgpr_read_b32 v220, a50\n\t"
    "v_accvgpr_read_b32 v221, a51\n\t"
    "v_accvgpr_read_b32 v222, a52\n\t"
    "v_accvgpr_read_b32 v223, a53\n\t"
    "v_accvgpr_read_b32 v224, a54\n\t"
    "v_accvgpr_read_b32 v225, a55\n\t"
    "v_mul_f32 v218, s37, v218\n\t"
    "v_mul_f32 v219, s37, v219\n\t"
    "v_mul_f32 v220, s37, v220\n\t"
    "v_mul_f32 v221, s37, v221\n\t"
    "v_mul_f32 v222, s37, v222\n\t"
    "v_mul_f32 v223, s37, v223\n\t"
    "v_mul_f32 v224, s37, v224\n\t"
    "v_mul_f32 v225, s37, v225\n\t"
    "v_cvt_pk_bf16_f32 v24, v218, v219\n\t"
    "v_cvt_pk_bf16_f32 v25, v220, v221\n\t"
    "s_and_b32 m0, s68, 0x1ffff\n\t"
    "s_add_u32 s68, s68, 0x8000\n\t"
    "s_nop 0\n\t"
    "buffer_load_dwordx4 v216, s[20:23], s70 offen offset:0 sc1 lds\n\t"
    "v_cvt_pk_bf16_f32 v26, v222, v223\n\t"
    "v_cvt_pk_bf16_f32 v27, v224, v225\n\t"
    "v_accvgpr_read_b32 v226, a56\n\t"
    "v_accvgpr_read_b32 v227, a57\n\t"
    "v_accvgpr_read_b32 v228, a58\n\t"
    "v_accvgpr_read_b32 v229, a59\n\t"
    "v_accvgpr_read_b32 v230, a60\n\t"
    "v_accvgpr_read_b32 v231, a61\n\t"
    "v_accvgpr_read_b32 v232, a62\n\t"
    "v_accvgpr_read_b32 v233, a63\n\t"
    "v_mul_f32 v226, s37, v226\n\t"
    "v_mul_f32 v227, s37, v227\n\t"
    "v_mul_f32 v228, s37, v228\n\t"
    "v_mul_f32 v229, s37, v229\n\t"
    "v_mul_f32 v230, s37, v230\n\t"
    "v_mul_f32 v231, s37, v231\n\t"
    "v_mul_f32 v232, s37, v232\n\t"
    "buffer_load_dwordx4 v216, s[20:23], s70 offen offset:1024 sc1 lds\n\t"
    "v_mul_f32 v233, s37, v233\n\t"
    "v_cvt_pk_bf16_f32 v28, v226, v227\n\t"
    "v_cvt_pk_bf16_f32 v29, v228, v229\n\t"
    "v_cvt_pk_bf16_f32 v30, v230, v231\n\t"
    "v_cvt_pk_bf16_f32 v31, v232, v233\n\t"
    "v_accvgpr_read_b32 v218, a64\n\t"
    "v_accvgpr_read_b32 v219, a65\n\t"
    "v_accvgpr_read_b32 v220, a66\n\t"
    "v_accvgpr_read_b32 v221, a67\n\t"
    "v_accvgpr_read_b32 v222, a68\n\t"
    "v_accvgpr_read_b32 v223, a69\n\t"
    "v_accvgpr_read_b32 v224, a70\n\t"
    "v_accvgpr_read_b32 v225, a71\n\t"
    "v_mul_f32 v218, s37, v218\n\t"
    "v_mul_f32 v219, s37, v219\n\t"
    "v_mul_f32 v220, s37, v220\n\t"
    "v_mul_f32 v221, s37, v221\n\t"
    "v_mul_f32 v222, s37, v222\n\t"
    "buffer_load_dwordx4 v216, s[20:23], s70 offen offset:2048 sc1 lds\n\t"
    "v_mul_f32 v223, s37, v223\n\t"
    "v_mul_f32 v224, s37, v224\n\t"
    "v_mul_f32 v225, s37, v225\n\t"
    "v_cvt_pk_bf16_f32 v32, v218, v219\n\t"
    "v_cvt_pk_bf16_f32 v33, v220, v221\n\t"
    "v_cvt_pk_bf16_f32 v34, v222, v223\n\t"
    "v_cvt_pk_bf16_f32 v35, v224, v225\n\t"
    "v_accvgpr_read_b32 v226, a72\n\t"
    "v_accvgpr_read_b32 v227, a73\n\t"
    "v_accvgpr_read_b32 v228, a74\n\t"
    "v_accvgpr_read_b32 v229, a75\n\t"
    "v_accvgpr_read_b32 v230, a76\n\t"
    "v_accvgpr_read_b32 v231, a77\n\t"
    "v_accvgpr_read_b32 v232, a78\n\t"
    "v_accvgpr_read_b32 v233, a79\n\t"
    "v_mul_f32 v226, s37, v226\n\t"
    "v_mul_f32 v227, s37, v227\n\t"
    "buffer_load_dwordx4 v216, s[20:23], s70 offen offset:3072 sc1 lds\n\t"
    "s_add_u32 s70, s70, 0x8000\n\t"
    "v_mul_f32 v228, s37, v228\n\t"
    "v_mul_f32 v229, s37, v229\n\t"
    "v_mul_f32 v230, s37, v230\n\t"
    "v_mul_f32 v231, s37, v231\n\t"
    "v_mul_f32 v232, s37, v232\n\t"
    "v_mul_f32 v233, s37, v233\n\t"
    "v_cvt_pk_bf16_f32 v36, v226, v227\n\t"
    "v_cvt_pk_bf16_f32 v37, v228, v229\n\t"
    "v_cvt_pk_bf16_f32 v38, v230, v231\n\t"
    "v_cvt_pk_bf16_f32 v39, v232, v233\n\t"
    "v_accvgpr_read_b32 v218, a80\n\t"
    "v_accvgpr_read_b32 v219, a81\n\t"
    "v_accvgpr_read_b32 v220, a82\n\t"
    "v_accvgpr_read_b32 v221, a83\n\t"
    "v_accvgpr_read_b32 v222, a84\n\t"
    "v_accvgpr_read_b32 v223, a85\n\t"
    "v_accvgpr_read_b32 v224, a86\n\t"
    "s_and_b32 m0, s68, 0x1ffff\n\t"
    "s_add_u32 s68, s68, 0x8000\n\t"
    "s_nop 0\n\t"
    "buffer_load_dwordx4 v216, s[20:23], s70 offen offset:0 sc1 lds\n\t"
    "v_accvgpr_read_b32 v225, a87\n\t"
    "v_mul_f32 v218, s37, v218\n\t"
    "v_mul_f32 v219, s37, v219\n\t"
    "v_mul_f32 v220, s37, v220\n\t"
    "v_mul_f32 v221, s37, v221\n\t"
    "v_mul_f32 v222, s37, v222\n\t"
    "v_mul_f32 v223, s37, v223\n\t"
    "v_mul_f32 v224, s37, v224\n\t"
    "v_mul_f32 v225, s37, v225\n\t"
    "v_cvt_pk_bf16_f32 v40, v218, v219\n\t"
    "v_cvt_pk_bf16_f32 v41, v220, v221\n\t"
    "v_cvt_pk_bf16_f32 v42, v222, v223\n\t"
    "v_cvt_pk_bf16_f32 v43, v224, v225\n\t"
    "v_accvgpr_read_b32 v226, a88\n\t"
    "v_accvgpr_read_b32 v227, a89\n\t"
    "v_accvgpr_read_b32 v228, a90\n\t"
    "v_accvgpr_read_b32 v229, a91\n\t"
    "v_accvgpr_read_b32 v230, a92\n\t"
    "buffer_load_dwordx4 v216, s[20:23], s70 offen offset:1024 sc1 lds\n\t"
    "v_accvgpr_read_b32 v231, a93\n\t"
    "v_accvgpr_read_b32 v232, a94\n\t"
    "v_accvgpr_read_b32 v233, a95\n\t"
    "v_mul_f32 v226, s37, v226\n\t"
    "v_mul_f32 v227, s37, v227\n\t"
    "v_mul_f32 v228, s37, v228\n\t"
    "v_mul_f32 v229, s37, v229\n\t"
    "v_mul_f32 v230, s37, v230\n\t"
    "v_mul_f32 v231, s37, v231\n\t"
    "v_mul_f32 v232, s37, v232\n\t"
    "v_mul_f32 v233, s37, v233\n\t"
    "v_cvt_pk_bf16_f32 v44, v226, v227\n\t"
    "v_cvt_pk_bf16_f32 v45, v228, v229\n\t"
    "v_cvt_pk_bf16_f32 v46, v230, v231\n\t"
    "v_cvt_pk_bf16_f32 v47, v232, v233\n\t"
    "v_accvgpr_read_b32 v218, a96\n\t"
    "v_accvgpr_read_b32 v219, a97\n\t"
    "buffer_load_dwordx4 v216, s[20:23], s70 offen offset:2048 sc1 lds\n\t"
    "v_accvgpr_read_b32 v220, a98\n\t"
    "v_accvgpr_read_b32 v221, a99\n\t"
    "v_accvgpr_read_b32 v222, a100\n\t"
    "v_accvgpr_read_b32 v223, a101\n\t"
    "v_accvgpr_read_b32 v224, a102\n\t"
    "v_accvgpr_read_b32 v225, a103\n\t"
    "v_mul_f32 v218, s37, v218\n\t"
    "v_mul_f32 v219, s37, v219\n\t"
    "v_mul_f32 v220, s37, v220\n\t"
    "v_mul_f32 v221, s37, v221\n\t"
    "v_mul_f32 v222, s37, v222\n\t"
    "v_mul_f32 v223, s37, v223\n\t"
    "v_mul_f32 v224, s37, v224\n\t"
    "v_mul_f32 v225, s37, v225\n\t"
    "v_cvt_pk_bf16_f32 v48, v218, v219\n\t"
    "v_cvt_pk_bf16_f32 v49, v220, v221\n\t"
    "v_cvt_pk_bf16_f32 v50, v222, v223\n\t"
    "v_cvt_pk_bf16_f32 v51, v224, v225\n\t"
    "buffer_load_dwordx4 v216, s[20:23], s70 offen offset:3072 sc1 lds\n\t"
    "s_add_u32 s70, s70, 0x8000\n\t"
    "v_accvgpr_read_b32 v226, a104\n\t"
    "v_accvgpr_read_b32 v227, a105\n\t"
    "v_accvgpr_read_b32 v228, a106\n\t"
    "v_accvgpr_read_b32 v229, a107\n\t"
    "v_accvgpr_read_b32 v230, a108\n\t"
    "v_accvgpr_read_b32 v231, a109\n\t"
    "v_accvgpr_read_b32 v232, a110\n\t"
    "v_accvgpr_read_b32 v233, a111\n\t"
    "v_mul_f32 v226, s37, v226\n\t"
    "v_mul_f32 v227, s37, v227\n\t"
    "v_mul_f32 v228, s37, v228\n\t"
    "v_mul_f32 v229, s37, v229\n\t"
    "v_mul_f32 v230, s37, v230\n\t"
    "v_mul_f32 v231, s37, v231\n\t"
    "v_mul_f32 v232, s37, v232\n\t"
    "v_mul_f32 v233, s37, v233\n\t"
    "v_cvt_pk_bf16_f32 v52, v226, v227\n\t"
    "s_and_b32 m0, s69, 0x1ffff\n\t"
    "s_add_u32 s69, s69, 0x8000\n\t"
    "s_nop 0\n\t"
    "buffer_load_dwordx4 v216, s[20:23], s71 offen offset:0 sc1 lds\n\t"
    "v_cvt_pk_bf16_f32 v53, v228, v229\n\t"
    "v_cvt_pk_bf16_f32 v54, v230, v231\n\t"
    "v_cvt_pk_bf16_f32 v55, v232, v233\n\t"
    "v_accvgpr_read_b32 v218, a112\n\t"
    "v_accvgpr_read_b32 v219, a113\n\t"
    "v_accvgpr_read_b32 v220, a114\n\t"
    "v_accvgpr_read_b32 v221, a115\n\t"
    "v_accvgpr_read_b32 v222, a116\n\t"
    "v_accvgpr_read_b32 v223, a117\n\t"
    "v_accvgpr_read_b32 v224, a118\n\t"
    "v_accvgpr_read_b32 v225, a119\n\t"
    "v_mul_f32 v218, s37, v218\n\t"
    "v_mul_f32 v219, s37, v219\n\t"
    "v_mul_f32 v220, s37, v220\n\t"
    "v_mul_f32 v221, s37, v221\n\t"
    "v_mul_f32 v222, s37, v222\n\t"
    "v_mul_f32 v223, s37, v223\n\t"
    "buffer_load_dwordx4 v216, s[20:23], s71 offen offset:1024 sc1 lds\n\t"
    "v_mul_f32 v224, s37, v224\n\t"
    "v_mul_f32 v225, s37, v225\n\t"
    "v_cvt_pk_bf16_f32 v56, v218, v219\n\t"
    "v_cvt_pk_bf16_f32 v57, v220, v221\n\t"
    "v_cvt_pk_bf16_f32 v58, v222, v223\n\t"
    "v_cvt_pk_bf16_f32 v59, v224, v225\n\t"
    "v_accvgpr_read_b32 v226, a120\n\t"
    "v_accvgpr_read_b32 v227, a121\n\t"
    "v_accvgpr_read_b32 v228, a122\n\t"
    "v_accvgpr_read_b32 v229, a123\n\t"
    "v_accvgpr_read_b32 v230, a124\n\t"
    "v_accvgpr_read_b32 v231, a125\n\t"
    "v_accvgpr_read_b32 v232, a126\n\t"
    "v_accvgpr_read_b32 v233, a127\n\t"
    "v_mul_f32 v226, s37, v226\n\t"
    "v_mul_f32 v227, s37, v227\n\t"
    "v_mul_f32 v228, s37, v228\n\t"
    "v_mul_f32 v229, s37, v229\n\t"
    "buffer_load_dwordx4 v216, s[20:23], s71 offen offset:2048 sc1 lds\n\t"
    "v_mul_f32 v230, s37, v230\n\t"
    "v_mul_f32 v231, s37, v231\n\t"
    "v_mul_f32 v232, s37, v232\n\t"
    "v_mul_f32 v233, s37, v233\n\t"
    "v_cvt_pk_bf16_f32 v60, v226, v227\n\t"
    "v_cvt_pk_bf16_f32 v61, v228, v229\n\t"
    "v_cvt_pk_bf16_f32 v62, v230, v231\n\t"
    "v_cvt_pk_bf16_f32 v63, v232, v233\n\t"
    "v_and_b32 v218, 15, v217\n\t"
    "v_lshrrev_b32 v219, 4, v217\n\t"
    "v_lshlrev_b32 v219, 2, v219\n\t"
    "v_sub_u32 v219, v219, v218\n\t"
    "v_cvt_f32_i32 v219, v219\n\t"
    "v_sub_f32 v219, 0.5, v219\n\t"
    "v_mov_b32 v196, 0x71800000\n\t"
    "v_mul_f32 v195, v219, v196\n\t"
    "v_mov_b32 v197, 0x5f800000\n\t"
    "buffer_load_dwordx4 v216, s[20:23], s71 offen offset:3072 sc1 lds\n\t"
    "s_add_u32 s71, s71, 0x8000\n\t"
    "v_and_b32 v220, 3, v217\n\t"
    "v_cmp_eq_u32 vcc, 0, v220\n\t"
    "v_mov_b32 v221, 0x3f803f80\n\t"
    "s_nop 0\n\t"
    "v_cndmask_b32 v221, 0, v221, vcc\n\t"
    "v_accvgpr_write_b32 a144, v221\n\t"
    "v_accvgpr_write_b32 a145, v221\n\t"
    "v_accvgpr_write_b32 a146, v221\n\t"
    "v_accvgpr_write_b32 a147, v221\n\t"
    "v_mov_b32 v192, v216\n\t"
    "v_add_u32 v193, 0x4000, v216\n\t"
    "s_mov_b64 s[78:79], 0\n\t"
    "s_mov_b32 s51, 0x8000\n\t"
    "s_mov_b32 s52, 0x4000\n\t"
    "s_mov_b32 s45, 0\n\t"
    "s_mov_b32 s42, 0\n\t"
    "s_sub_u32 s43, s60, s33\n\t"
    "s_sub_u32 s44, s61, s33\n\t"
    "s_waitcnt vmcnt(8)\n\t"
    "s_barrier\n\t"
    "v_lshrrev_b32 v218, 4, v217\n\t"
    "v_lshlrev_b32 v218, 8, v218\n\t"
    "ds_read_b128 v[222:225], v218 offset:0\n\t"
    "ds_read_b128 v[226:229], v218 offset:1024\n\t"
    "ds_read_b128 v[230:233], v218 offset:2048\n\t"
    "ds_read_b128 v[234:237], v218 offset:3072\n\t"
    "ds_read_b128 a[148:151], v192 offset:0\n\t"
    "ds_read_b128 a[152:155], v192 offset:1024\n\t"
    "ds_read_b128 a[156:159], v192 offset:2048\n\t"
    "ds_read_b128 a[160:163], v192 offset:3072\n\t"
    "ds_read_b128 a[164:167], v192 offset:4096\n\t"
    "ds_read_b128 a[168:171], v192 offset:5120\n\t"
    "ds_read_b128 a[172:175], v192 offset:6144\n\t"
    "ds_read_b128 a[176:179], v192 offset:7168\n\t"
    "s_waitcnt lgkmcnt(8)\n\t"
    "v_mfma_f32_16x16x32_bf16 v[160:163], v[222:225], v[0:3], 0\n\t"
    "v_mfma_f32_16x16x32_bf16 v[164:167], v[222:225], v[16:19], 0\n\t"
    "v_mfma_f32_16x16x32_bf16 v[168:171], v[222:225], v[32:35], 0\n\t"
    "v_mfma_f32_16x16x32_bf16 v[172:175], v[222:225], v[48:51], 0\n\t"
    "v_mfma_f32_16x16x32_bf16 v[160:163], v[226:229], v[4:7], v[160:163]\n\t"
    "v_mfma_f32_16x16x32_bf16 v[164:167], v[226:229], v[20:23], v[164:167]\n\t"
    "v_mfma_f32_16x16x32_bf16 v[168:171], v[226:229], v[36:39], v[168:171]\n\t"
    "v_mfma_f32_16x16x32_bf16 v[172:175], v[226:229], v[52:55], v[172:175]\n\t"
    "v_mfma_f32_16x16x32_bf16 v[160:163], v[230:233], v[8:11], v[160:163]\n\t"
    "v_mfma_f32_16x16x32_bf16 v[164:167], v[230:233], v[24:27], v[164:167]\n\t"
    "v_mfma_f32_16x16x32_bf16 v[168:171], v[230:233], v[40:43], v[168:171]\n\t"
    "v_mfma_f32_16x16x32_bf16 v[172:175], v[230:233], v[56:59], v[172:175]\n\t"
    "v_mfma_f32_16x16x32_bf16 v[160:163], v[234:237], v[12:15], v[160:163]\n\t"
    "v_mfma_f32_16x16x32_bf16 v[164:167], v[234:237], v[28:31], v[164:167]\n\t"
    "v_mfma_f32_16x16x32_bf16 v[168:171], v[234:237], v[44:47], v[168:171]\n\t"
    "v_mfma_f32_16x16x32_bf16 v[172:175], v[234:237], v[60:63], v[172:175]\n\t"
    "s_nop 7\n\t"
    "v_sub_f32 v160, 0, v160\n\t"
    "v_sub_f32 v161, 0, v161\n\t"
    "v_sub_f32 v162, 0, v162\n\t"
    "v_sub_f32 v163, 0, v163\n\t"
    "v_sub_f32 v164, 0, v164\n\t"
    "v_sub_f32 v165, 0, v165\n\t"
    "v_sub_f32 v166, 0, v166\n\t"
    "v_sub_f32 v167, 0, v167\n\t"
    "v_sub_f32 v168, 0, v168\n\t"
    "v_sub_f32 v169, 0, v169\n\t"
    "v_sub_f32 v170, 0, v170\n\t"
    "v_sub_f32 v171, 0, v171\n\t"
    "v_sub_f32 v172, 0, v172\n\t"
    "v_sub_f32 v173, 0, v173\n\t"
    "v_sub_f32 v174, 0, v174\n\t"
    "v_sub_f32 v175, 0, v175\n\t"
    "L_att:\n\t"
    "s_lshl_b32 s37, s43, 4\n\t"
    "s_lshl_b32 s38, s45, 6\n\t"
    "s_sub_u32 s46, s37, s38\n\t"
    "v_cvt_f32_i32 v194, s46\n\t"
    "v_fma_f32 v194, v194, v196, v195\n\t"
    "v_mov_b32 v176, v194\n\t"
    "v_min_f32 v176, v176, v160\n\t"
    "v_add_f32 v177, 0xf1800000, v194\n\t"
    "v_min_f32 v177, v177, v160\n\t"
    "v_add_f32 v178, 0xf2000000, v194\n\t"
    "v_min_f32 v178, v178, v160\n\t"
    "v_add_f32 v179, 0xf2400000, v194\n\t"
    "v_min_f32 v179, v179, v160\n\t"
    "s_waitcnt lgkmcnt(7)\n\t"
    "v_mfma_f32_16x16x32_bf16 v[68:71], a[148:151], v[16:19], v[164:167]\n\t"
    "s_and_b32 s38, s52, 0x1ffff\n\t"
    "v_add_f32 v180, 0xf3800000, v194\n\t"
    "v_min_f32 v180, v180, v160\n\t"
    "v_mfma_f32_16x16x32_bf16 v[72:75], a[148:151], v[32:35], v[168:171]\n\t"
    "s_add_u32 s52, s52, 0x8000\n\t"
    "v_add_f32 v181, 0xf3880000, v194\n\t"
    "v_min_f32 v181, v181, v160\n\t"
    "v_mfma_f32_16x16x32_bf16 v[76:79], a[148:151], v[48:51], v[172:175]\n\t"
    "v_add_f32 v182, 0xf3900000, v194\n\t"
    "v_min_f32 v182, v182, v160\n\t"
    "v_mfma_f32_16x16x32_bf16 v[64:67], a[148:151], v[0:3], v[176:179]\n\t"
    "s_waitcnt lgkmcnt(6)\n\t"
    "ds_read_b128 a[148:151], v192 offset:8192\n\t"
    "v_add_u32 v193, s38, v216\n\t"
    "v_mfma_f32_16x16x32_bf16 v[68:71], a[152:155], v[20:23], v[68:71]\n\t"
    "v_add_f32 v183, 0xf3980000, v194\n\t"
    "v_min_f32 v183, v183, v160\n\t"
    "v_mfma_f32_16x16x32_bf16 v[72:75], a[152:155], v[36:39], v[72:75]\n\t"
    "v_add_f32 v184, 0xf4000000, v194\n\t"
    "v_min_f32 v184, v184, v160\n\t"
    "v_mfma_f32_16x16x32_bf16 v[76:79], a[152:155], v[52:55], v[76:79]\n\t"
    "v_add_f32 v185, 0xf4040000, v194\n\t"
    "v_min_f32 v185, v185, v160\n\t"
    "v_mfma_f32_16x16x32_bf16 v[64:67], a[152:155], v[4:7], v[64:67]\n\t"
    "s_waitcnt lgkmcnt(6)\n\t"
    "ds_read_b128 a[152:155], v192 offset:9216\n\t"
    "v_mfma_f32_16x16x32_bf16 v[68:71], a[156:159], v[24:27], v[68:71]\n\t"
    "v_add_f32 v186, 0xf4080000, v194\n\t"
    "v_min_f32 v186, v186, v160\n\t"
    "v_mfma_f32_16x16x32_bf16 v[72:75], a[156:159], v[40:43], v[72:75]\n\t"
    "v_add_f32 v187, 0xf40c0000, v194\n\t"
    "v_min_f32 v187, v187, v160\n\t"
    "v_mfma_f32_16x16x32_bf16 v[76:79], a[156:159], v[56:59], v[76:79]\n\t"
    "v_add_f32 v188, 0xf4400000, v194\n\t"
    "v_min_f32 v188, v188, v160\n\t"
    "v_mfma_f32_16x16x32_bf16 v[64:67], a[156:159], v[8:11], v[64:67]\n\t"
    "s_waitcnt lgkmcnt(6)\n\t"
    "ds_read_b128 a[156:159], v192 offset:10240\n\t"
    "v_mfma_f32_16x16x32_bf16 v[68:71], a[160:163], v[28:31], v[68:71]\n\t"
    "v_add_f32 v189, 0xf4440000, v194\n\t"
    "v_min_f32 v189, v189, v160\n\t"
    "v_mfma_f32_16x16x32_bf16 v[72:75], a[160:163], v[44:47], v[72:75]\n\t"
    "v_add_f32 v190, 0xf4480000, v194\n\t"
    "v_min_f32 v190, v190, v160\n\t"
    "v_mfma_f32_16x16x32_bf16 v[76:79], a[160:163], v[60:63], v[76:79]\n\t"
    "v_add_f32 v191, 0xf44c0000, v194\n\t"
    "v_min_f32 v191, v191, v160\n\t"
    "v_mfma_f32_16x16x32_bf16 v[64:67], a[160:163], v[12:15], v[64:67]\n\t"
    "s_waitcnt lgkmcnt(6)\n\t"
    "ds_read_b128 a[160:163], v192 offset:11264\n\t"
    "v_mfma_f32_16x16x32_bf16 v[84:87], a[164:167], v[16:19], v[164:167]\n\t"
    "v_exp_f32 v68, v68\n\t"
    "v_mfma_f32_16x16x32_bf16 v[88:91], a[164:167], v[32:35], v[168:171]\n\t"
    "v_exp_f32 v69, v69\n\t"
    "v_mfma_f32_16x16x32_bf16 v[92:95], a[164:167], v[48:51], v[172:175]\n\t"
    "v_exp_f32 v70, v70\n\t"
    "v_mfma_f32_16x16x32_bf16 v[80:83], a[164:167], v[0:3], v[180:183]\n\t"
    "s_waitcnt lgkmcnt(6)\n\t"
    "ds_read_b128 a[164:167], v192 offset:12288\n\t"
    "v_cvt_pk_bf16_f32 v136, v68, v69\n\t"
    "v_mfma_f32_16x16x32_bf16 v[84:87], a[168:171], v[20:23], v[84:87]\n\t"
    "v_exp_f32 v64, v64\n\t"
    "v_mfma_f32_16x16x32_bf16 v[88:91], a[168:171], v[36:39], v[88:91]\n\t"
    "v_exp_f32 v65, v65\n\t"
    "v_mfma_f32_16x16x32_bf16 v[92:95], a[168:171], v[52:55], v[92:95]\n\t"
    "v_exp_f32 v66, v66\n\t"
    "v_mfma_f32_16x16x32_bf16 v[80:83], a[168:171], v[4:7], v[80:83]\n\t"
    "s_waitcnt lgkmcnt(6)\n\t"
    "ds_read_b128 a[168:171], v192 offset:13312\n\t"
    "v_cvt_pk_bf16_f32 v128, v64, v65\n\t"
    "v_mfma_f32_16x16x32_bf16 v[84:87], a[172:175], v[24:27], v[84:87]\n\t"
    "v_exp_f32 v67, v67\n\t"
    "v_mfma_f32_16x16x32_bf16 v[88:91], a[172:175], v[40:43], v[88:91]\n\t"
    "v_exp_f32 v71, v71\n\t"
    "v_mfma_f32_16x16x32_bf16 v[92:95], a[172:175], v[56:59], v[92:95]\n\t"
    "v_exp_f32 v72, v72\n\t"
    "v_mfma_f32_16x16x32_bf16 v[80:83], a[172:175], v[8:11], v[80:83]\n\t"
    "s_waitcnt lgkmcnt(6)\n\t"
    "ds_read_b128 a[172:175], v192 offset:14336\n\t"
    "v_cvt_pk_bf16_f32 v129, v66, v67\n\t"
    "v_mfma_f32_16x16x32_bf16 v[84:87], a[176:179], v[28:31], v[84:87]\n\t"
    "v_exp_f32 v73, v73\n\t"
    "v_mfma_f32_16x16x32_bf16 v[88:91], a[176:179], v[44:47], v[88:91]\n\t"
    "s_waitcnt vmcnt(8)\n\t"
    "v_exp_f32 v74, v74\n\t"
    "v_mfma_f32_16x16x32_bf16 v[92:95], a[176:179], v[60:63], v[92:95]\n\t"
    "s_barrier\n\t"
    "v_exp_f32 v75, v75\n\t"
    "v_mfma_f32_16x16x32_bf16 v[80:83], a[176:179], v[12:15], v[80:83]\n\t"
    "s_waitcnt lgkmcnt(6)\n\t"
    "ds_read_b128 a[176:179], v192 offset:15360\n\t"
    "v_cvt_pk_bf16_f32 v137, v70, v71\n\t"
    "v_mfma_f32_16x16x32_bf16 v[100:103], a[148:151], v[16:19], v[164:167]\n\t"
    "s_and_b32 m0, s68, 0x1ffff\n\t"
    "v_exp_f32 v76, v76\n\t"
    "v_mfma_f32_16x16x32_bf16 v[104:107], a[148:151], v[32:35], v[168:171]\n\t"
    "buffer_load_dwordx4 v216, s[20:23], s70 offen offset:0 sc1 lds\n\t"
    "v_cvt_pk_bf16_f32 v144, v72, v73\n\t"
    "v_mfma_f32_16x16x32_bf16 v[108:111], a[148:151], v[48:51], v[172:175]\n\t"
    "s_add_u32 s68, s68, 0x8000\n\t"
    "v_exp_f32 v77, v77\n\t"
    "v_mfma_f32_16x16x32_bf16 v[96:99], a[148:151], v[0:3], v[184:187]\n\t"
    "s_waitcnt lgkmcnt(6)\n\t"
    "ds_read_b128 a[148:151], v193 offset:0\n\t"
    "v_cvt_pk_bf16_f32 v145, v74, v75\n\t"
    "v_mfma_f32_16x16x32_bf16 v[100:103], a[152:155], v[20:23], v[100:103]\n\t"
    "s_and_b32 s38, s51, 0x1ffff\n\t"
    "v_exp_f32 v78, v78\n\t"
    "v_mfma_f32_16x16x32_bf16 v[104:107], a[152:155], v[36:39], v[104:107]\n\t"
    "s_add_u32 s51, s51, 0x8000\n\t"
    "v_exp_f32 v79, v79\n\t"
    "v_mfma_f32_16x16x32_bf16 v[108:111], a[152:155], v[52:55], v[108:111]\n\t"
    "v_exp_f32 v80, v80\n\t"
    "v_mfma_f32_16x16x32_bf16 v[96:99], a[152:155], v[4:7], v[96:99]\n\t"
    "s_waitcnt lgkmcnt(6)\n\t"
    "ds_read_b128 a[152:155], v193 offset:1024\n\t"
    "v_add_u32 v192, s38, v216\n\t"
    "v_mfma_f32_16x16x32_bf16 v[100:103], a[156:159], v[24:27], v[100:103]\n\t"
    "v_exp_f32 v81, v81\n\t"
    "v_mfma_f32_16x16x32_bf16 v[104:107], a[156:159], v[40:43], v[104:107]\n\t"
    "buffer_load_dwordx4 v216, s[20:23], s70 offen offset:1024 sc1 lds\n\t"
    "v_cvt_pk_bf16_f32 v152, v76, v77\n\t"
    "v_mfma_f32_16x16x32_bf16 v[108:111], a[156:159], v[56:59], v[108:111]\n\t"
    "v_exp_f32 v82, v82\n\t"
    "v_mfma_f32_16x16x32_bf16 v[96:99], a[156:159], v[8:11], v[96:99]\n\t"
    "s_waitcnt lgkmcnt(6)\n\t"
    "ds_read_b128 a[156:159], v193 offset:2048\n\t"
    "v_cvt_pk_bf16_f32 v153, v78, v79\n\t"
    "v_mfma_f32_16x16x32_bf16 v[100:103], a[160:163], v[28:31], v[100:103]\n\t"
    "v_exp_f32 v83, v83\n\t"
    "v_mfma_f32_16x16x32_bf16 v[104:107], a[160:163], v[44:47], v[104:107]\n\t"
    "v_exp_f32 v84, v84\n\t"
    "v_mfma_f32_16x16x32_bf16 v[108:111], a[160:163], v[60:63], v[108:111]\n\t"
    "v_exp_f32 v85, v85\n\t"
    "v_mfma_f32_16x16x32_bf16 v[96:99], a[160:163], v[12:15], v[96:99]\n\t"
    "s_waitcnt lgkmcnt(6)\n\t"
    "ds_read_b128 a[160:163], v193 offset:3072\n\t"
    "v_cvt_pk_bf16_f32 v130, v80, v81\n\t"
    "v_mfma_f32_16x16x32_bf16 v[116:119], a[164:167], v[16:19], v[164:167]\n\t"
    "v_exp_f32 v86, v86\n\t"
    "v_mfma_f32_16x16x32_bf16 v[120:123], a[164:167], v[32:35], v[168:171]\n\t"
    "buffer_load_dwordx4 v216, s[20:23], s70 offen offset:2048 sc1 lds\n\t"
    "v_cvt_pk_bf16_f32 v131, v82, v83\n\t"
    "v_mfma_f32_16x16x32_bf16 v[124:127], a[164:167], v[48:51], v[172:175]\n\t"
    "v_exp_f32 v87, v87\n\t"
    "v_mfma_f32_16x16x32_bf16 v[112:115], a[164:167], v[0:3], v[188:191]\n\t"
    "s_waitcnt lgkmcnt(6)\n\t"
    "ds_read_b128 a[164:167], v193 offset:4096\n\t"
    "v_cvt_pk_bf16_f32 v138, v84, v85\n\t"
    "v_mfma_f32_16x16x32_bf16 v[116:119], a[168:171], v[20:23], v[116:119]\n\t"
    "v_exp_f32 v88, v88\n\t"
    "v_mfma_f32_16x16x32_bf16 v[120:123], a[168:171], v[36:39], v[120:123]\n\t"
    "v_exp_f32 v89, v89\n\t"
    "v_mfma_f32_16x16x32_bf16 v[124:127], a[168:171], v[52:55], v[124:127]\n\t"
    "v_exp_f32 v90, v90\n\t"
    "v_mfma_f32_16x16x32_bf16 v[112:115], a[168:171], v[4:7], v[112:115]\n\t"
    "s_waitcnt lgkmcnt(6)\n\t"
    "ds_read_b128 a[168:171], v193 offset:5120\n\t"
    "v_cvt_pk_bf16_f32 v139, v86, v87\n\t"
    "v_mfma_f32_16x16x32_bf16 v[116:119], a[172:175], v[24:27], v[116:119]\n\t"
    "v_exp_f32 v91, v91\n\t"
    "v_mfma_f32_16x16x32_bf16 v[120:123], a[172:175], v[40:43], v[120:123]\n\t"
    "buffer_load_dwordx4 v216, s[20:23], s70 offen offset:3072 sc1 lds\n\t"
    "v_cvt_pk_bf16_f32 v146, v88, v89\n\t"
    "v_mfma_f32_16x16x32_bf16 v[124:127], a[172:175], v[56:59], v[124:127]\n\t"
    "s_add_u32 s70, s70, 0x8000\n\t"
    "v_exp_f32 v92, v92\n\t"
    "v_mfma_f32_16x16x32_bf16 v[112:115], a[172:175], v[8:11], v[112:115]\n\t"
    "s_waitcnt lgkmcnt(6)\n\t"
    "ds_read_b128 a[172:175], v193 offset:6144\n\t"
    "v_cvt_pk_bf16_f32 v147, v90, v91\n\t"
    "v_mfma_f32_16x16x32_bf16 v[116:119], a[176:179], v[28:31], v[116:119]\n\t"
    "v_exp_f32 v93, v93\n\t"
    "v_mfma_f32_16x16x32_bf16 v[120:123], a[176:179], v[44:47], v[120:123]\n\t"
    "v_exp_f32 v94, v94\n\t"
    "v_exp_f32 v95, v95\n\t"
    "v_mfma_f32_16x16x32_bf16 v[124:127], a[176:179], v[60:63], v[124:127]\n\t"
    "s_and_b32 m0, s69, 0x1ffff\n\t"
    "v_cvt_pk_bf16_f32 v154, v92, v93\n\t"
    "v_cvt_pk_bf16_f32 v155, v94, v95\n\t"
    "v_mfma_f32_16x16x32_bf16 v[112:115], a[176:179], v[12:15], v[112:115]\n\t"
    "s_waitcnt lgkmcnt(6)\n\t"
    "ds_read_b128 a[176:179], v193 offset:7168\n\t"
    "v_mfma_f32_16x16x32_bf16 a[0:3], a[148:151], v[128:131], 0\n\t"
    "v_exp_f32 v96, v96\n\t"
    "v_mfma_f32_16x16x32_bf16 a[4:7], a[148:151], v[136:139], 0\n\t"
    "buffer_load_dwordx4 v216, s[20:23], s71 offen offset:0 sc1 lds\n\t"
    "v_mfma_f32_16x16x32_bf16 a[8:11], a[148:151], v[144:147], 0\n\t"
    "s_add_u32 s69, s69, 0x8000\n\t"
    "v_exp_f32 v97, v97\n\t"
    "v_mfma_f32_16x16x32_bf16 a[12:15], a[148:151], v[152:155], 0\n\t"
    "s_waitcnt lgkmcnt(6)\n\t"
    "ds_read_b128 a[148:151], v192 offset:0\n\t"
    "v_cvt_pk_bf16_f32 v132, v96, v97\n\t"
    "v_mfma_f32_16x16x32_bf16 a[16:19], a[152:155], v[128:131], 0\n\t"
    "v_exp_f32 v98, v98\n\t"
    "v_mfma_f32_16x16x32_bf16 a[20:23], a[152:155], v[136:139], 0\n\t"
    "v_exp_f32 v99, v99\n\t"
    "v_mfma_f32_16x16x32_bf16 a[24:27], a[152:155], v[144:147], 0\n\t"
    "v_exp_f32 v100, v100\n\t"
    "v_mfma_f32_16x16x32_bf16 a[28:31], a[152:155], v[152:155], 0\n\t"
    "s_waitcnt lgkmcnt(6)\n\t"
    "ds_read_b128 a[152:155], v192 offset:1024\n\t"
    "v_cvt_pk_bf16_f32 v133, v98, v99\n\t"
    "v_mfma_f32_16x16x32_bf16 a[32:35], a[156:159], v[128:131], 0\n\t"
    "v_exp_f32 v101, v101\n\t"
    "v_mfma_f32_16x16x32_bf16 a[36:39], a[156:159], v[136:139], 0\n\t"
    "buffer_load_dwordx4 v216, s[20:23], s71 offen offset:1024 sc1 lds\n\t"
    "v_cvt_pk_bf16_f32 v140, v100, v101\n\t"
    "v_mfma_f32_16x16x32_bf16 a[40:43], a[156:159], v[144:147], 0\n\t"
    "v_exp_f32 v102, v102\n\t"
    "v_mfma_f32_16x16x32_bf16 a[44:47], a[156:159], v[152:155], 0\n\t"
    "s_waitcnt lgkmcnt(6)\n\t"
    "ds_read_b128 a[156:159], v192 offset:2048\n\t"
    "v_mfma_f32_16x16x32_bf16 a[48:51], a[160:163], v[128:131], 0\n\t"
    "v_exp_f32 v103, v103\n\t"
    "v_mfma_f32_16x16x32_bf16 a[52:55], a[160:163], v[136:139], 0\n\t"
    "v_exp_f32 v104, v104\n\t"
    "v_mfma_f32_16x16x32_bf16 a[56:59], a[160:163], v[144:147], 0\n\t"
    "v_exp_f32 v105, v105\n\t"
    "v_mfma_f32_16x16x32_bf16 a[60:63], a[160:163], v[152:155], 0\n\t"
    "s_waitcnt lgkmcnt(6)\n\t"
    "ds_read_b128 a[160:163], v192 offset:3072\n\t"
    "v_cvt_pk_bf16_f32 v141, v102, v103\n\t"
    "v_mfma_f32_16x16x32_bf16 a[64:67], a[164:167], v[128:131], 0\n\t"
    "v_exp_f32 v106, v106\n\t"
    "v_mfma_f32_16x16x32_bf16 a[68:71], a[164:167], v[136:139], 0\n\t"
    "buffer_load_dwordx4 v216, s[20:23], s71 offen offset:2048 sc1 lds\n\t"
    "v_cvt_pk_bf16_f32 v148, v104, v105\n\t"
    "v_mfma_f32_16x16x32_bf16 a[72:75], a[164:167], v[144:147], 0\n\t"
    "v_exp_f32 v107, v107\n\t"
    "v_mfma_f32_16x16x32_bf16 a[76:79], a[164:167], v[152:155], 0\n\t"
    "s_waitcnt lgkmcnt(6)\n\t"
    "ds_read_b128 a[164:167], v192 offset:4096\n\t"
    "v_cvt_pk_bf16_f32 v149, v106, v107\n\t"
    "v_mfma_f32_16x16x32_bf16 a[80:83], a[168:171], v[128:131], 0\n\t"
    "v_exp_f32 v108, v108\n\t"
    "v_mfma_f32_16x16x32_bf16 a[84:87], a[168:171], v[136:139], 0\n\t"
    "v_exp_f32 v109, v109\n\t"
    "v_mfma_f32_16x16x32_bf16 a[88:91], a[168:171], v[144:147], 0\n\t"
    "v_exp_f32 v110, v110\n\t"
    "v_mfma_f32_16x16x32_bf16 a[92:95], a[168:171], v[152:155], 0\n\t"
    "s_waitcnt lgkmcnt(6)\n\t"
    "ds_read_b128 a[168:171], v192 offset:5120\n\t"
    "v_cvt_pk_bf16_f32 v156, v108, v109\n\t"
    "v_mfma_f32_16x16x32_bf16 a[96:99], a[172:175], v[128:131], 0\n\t"
    "v_exp_f32 v111, v111\n\t"
    "v_mfma_f32_16x16x32_bf16 a[100:103], a[172:175], v[136:139], 0\n\t"
    "buffer_load_dwordx4 v216, s[20:23], s71 offen offset:3072 sc1 lds\n\t"
    "v_cvt_pk_bf16_f32 v157, v110, v111\n\t"
    "v_mfma_f32_16x16x32_bf16 a[104:107], a[172:175], v[144:147], 0\n\t"
    "s_add_u32 s71, s71, 0x8000\n\t"
    "v_exp_f32 v112, v112\n\t"
    "v_mfma_f32_16x16x32_bf16 a[108:111], a[172:175], v[152:155], 0\n\t"
    "s_waitcnt lgkmcnt(6)\n\t"
    "ds_read_b128 a[172:175], v192 offset:6144\n\t"
    "v_mfma_f32_16x16x32_bf16 a[112:115], a[176:179], v[128:131], 0\n\t"
    "v_exp_f32 v113, v113\n\t"
    "v_mfma_f32_16x16x32_bf16 a[116:119], a[176:179], v[136:139], 0\n\t"
    "v_exp_f32 v114, v114\n\t"
    "v_mfma_f32_16x16x32_bf16 a[120:123], a[176:179], v[144:147], 0\n\t"
    "v_exp_f32 v115, v115\n\t"
    "v_mfma_f32_16x16x32_bf16 a[124:127], a[176:179], v[152:155], 0\n\t"
    "ds_read_b128 a[176:179], v192 offset:7168\n\t"
    "v_cvt_pk_bf16_f32 v134, v112, v113\n\t"
    "v_mfma_f32_16x16x32_bf16 a[128:131], a[144:147], v[128:131], 0\n\t"
    "s_add_u32 s45, s45, 1\n\t"
    "v_exp_f32 v116, v116\n\t"
    "v_mfma_f32_16x16x32_bf16 a[132:135], a[144:147], v[136:139], 0\n\t"
    "v_exp_f32 v117, v117\n\t"
    "v_mfma_f32_16x16x32_bf16 a[136:139], a[144:147], v[144:147], 0\n\t"
    "v_exp_f32 v118, v118\n\t"
    "v_mfma_f32_16x16x32_bf16 a[140:143], a[144:147], v[152:155], 0\n\t"
    "v_exp_f32 v119, v119\n\t"
    "s_nop 0\n\t"
    "v_cvt_pk_bf16_f32 v135, v114, v115\n\t"
    "v_cvt_pk_bf16_f32 v142, v116, v117\n\t"
    "v_cvt_pk_bf16_f32 v143, v118, v119\n\t"
    "s_cmp_eq_u32 s35, 0\n\t"
    "s_cbranch_scc1 L_p1_last\n\t"
    "s_lshl_b32 s40, s35, 1\n\t"
    "s_sub_u32 s40, s40, 1\n\t"
    "L_p1_loop:\n\t"
    "s_waitcnt lgkmcnt(7)\n\t"
    "v_mfma_f32_16x16x32_bf16 v[64:67], a[148:151], v[0:3], v[160:163]\n\t"
    "v_exp_f32 v120, v120\n\t"
    "v_mfma_f32_16x16x32_bf16 v[68:71], a[148:151], v[16:19], v[164:167]\n\t"
    "v_exp_f32 v121, v121\n\t"
    "v_mfma_f32_16x16x32_bf16 v[72:75], a[148:151], v[32:35], v[168:171]\n\t"
    "v_exp_f32 v122, v122\n\t"
    "v_mfma_f32_16x16x32_bf16 v[76:79], a[148:151], v[48:51], v[172:175]\n\t"
    "s_waitcnt lgkmcnt(6)\n\t"
    "ds_read_b128 a[148:151], v193 offset:8192\n\t"
    "v_cvt_pk_bf16_f32 v150, v120, v121\n\t"
    "v_mfma_f32_16x16x32_bf16 v[64:67], a[152:155], v[4:7], v[64:67]\n\t"
    "v_exp_f32 v123, v123\n\t"
    "v_mfma_f32_16x16x32_bf16 v[68:71], a[152:155], v[20:23], v[68:71]\n\t"
    "v_exp_f32 v124, v124\n\t"
    "v_mfma_f32_16x16x32_bf16 v[72:75], a[152:155], v[36:39], v[72:75]\n\t"
    "v_exp_f32 v125, v125\n\t"
    "v_mfma_f32_16x16x32_bf16 v[76:79], a[152:155], v[52:55], v[76:79]\n\t"
    "s_waitcnt lgkmcnt(6)\n\t"
    "ds_read_b128 a[152:155], v193 offset:9216\n\t"
    "v_cvt_pk_bf16_f32 v151, v122, v123\n\t"
    "v_mfma_f32_16x16x32_bf16 v[64:67], a[156:159], v[8:11], v[64:67]\n\t"
    "v_exp_f32 v126, v126\n\t"
    "v_mfma_f32_16x16x32_bf16 v[68:71], a[156:159], v[24:27], v[68:71]\n\t"
    "v_exp_f32 v127, v127\n\t"
    "v_mfma_f32_16x16x32_bf16 v[72:75], a[156:159], v[40:43], v[72:75]\n\t"
    "v_cvt_pk_bf16_f32 v158, v124, v125\n\t"
    "v_cvt_pk_bf16_f32 v159, v126, v127\n\t"
    "v_mfma_f32_16x16x32_bf16 v[76:79], a[156:159], v[56:59], v[76:79]\n\t"
    "s_waitcnt lgkmcnt(6)\n\t"
    "ds_read_b128 a[156:159], v193 offset:10240\n\t"
    "v_mfma_f32_16x16x32_bf16 v[64:67], a[160:163], v[12:15], v[64:67]\n\t"
    "v_mfma_f32_16x16x32_bf16 v[68:71], a[160:163], v[28:31], v[68:71]\n\t"
    "v_mfma_f32_16x16x32_bf16 v[72:75], a[160:163], v[44:47], v[72:75]\n\t"
    "v_mfma_f32_16x16x32_bf16 v[76:79], a[160:163], v[60:63], v[76:79]\n\t"
    "s_waitcnt lgkmcnt(6)\n\t"
    "ds_read_b128 a[160:163], v193 offset:11264\n\t"
    "v_mfma_f32_16x16x32_bf16 v[80:83], a[164:167], v[0:3], v[160:163]\n\t"
    "v_mfma_f32_16x16x32_bf16 v[84:87], a[164:167], v[16:19], v[164:167]\n\t"
    "v_mfma_f32_16x16x32_bf16 v[88:91], a[164:167], v[32:35], v[168:171]\n\t"
    "v_mfma_f32_16x16x32_bf16 v[92:95], a[164:167], v[48:51], v[172:175]\n\t"
    "s_waitcnt lgkmcnt(6)\n\t"
    "ds_read_b128 a[164:167], v193 offset:12288\n\t"
    "v_mfma_f32_16x16x32_bf16 v[80:83], a[168:171], v[4:7], v[80:83]\n\t"
    "v_exp_f32 v64, v64\n\t"
    "v_mfma_f32_16x16x32_bf16 v[84:87], a[168:171], v[20:23], v[84:87]\n\t"
    "v_exp_f32 v65, v65\n\t"
    "v_mfma_f32_16x16x32_bf16 v[88:91], a[168:171], v[36:39], v[88:91]\n\t"
    "v_exp_f32 v66, v66\n\t"
    "v_mfma_f32_16x16x32_bf16 v[92:95], a[168:171], v[52:55], v[92:95]\n\t"
    "s_waitcnt lgkmcnt(6)\n\t"
    "ds_read_b128 a[168:171], v193 offset:13312\n\t"
    "v_cvt_pk_bf16_f32 v128, v64, v65\n\t"
    "v_mfma_f32_16x16x32_bf16 v[80:83], a[172:175], v[8:11], v[80:83]\n\t"
    "v_exp_f32 v67, v67\n\t"
    "v_mfma_f32_16x16x32_bf16 v[84:87], a[172:175], v[24:27], v[84:87]\n\t"
    "v_exp_f32 v68, v68\n\t"
    "v_mfma_f32_16x16x32_bf16 v[88:91], a[172:175], v[40:43], v[88:91]\n\t"
    "v_exp_f32 v69, v69\n\t"
    "v_mfma_f32_16x16x32_bf16 v[92:95], a[172:175], v[56:59], v[92:95]\n\t"
    "s_waitcnt lgkmcnt(6)\n\t"
    "ds_read_b128 a[172:175], v193 offset:14336\n\t"
    "v_cvt_pk_bf16_f32 v129, v66, v67\n\t"
    "v_mfma_f32_16x16x32_bf16 v[80:83], a[176:179], v[12:15], v[80:83]\n\t"
    "v_exp_f32 v70, v70\n\t"
    "v_mfma_f32_16x16x32_bf16 v[84:87], a[176:179], v[28:31], v[84:87]\n\t"
    "v_exp_f32 v71, v71\n\t"
    "v_mfma_f32_16x16x32_bf16 v[88:91], a[176:179], v[44:47], v[88:91]\n\t"
    "v_exp_f32 v72, v72\n\t"
    "v_mfma_f32_16x16x32_bf16 v[92:95], a[176:179], v[60:63], v[92:95]\n\t"
    "s_waitcnt lgkmcnt(6)\n\t"
    "ds_read_b128 a[176:179], v193 offset:15360\n\t"
    "v_cvt_pk_bf16_f32 v136, v68, v69\n\t"
    "v_mfma_f32_16x16x32_bf16 a[0:3], a[148:151], v[132:135], a[0:3]\n\t"
    "s_and_b32 s38, s52, 0x1ffff\n\t"
    "v_exp_f32 v73, v73\n\t"
    "v_mfma_f32_16x16x32_bf16 a[4:7], a[148:151], v[140:143], a[4:7]\n\t"
    "s_add_u32 s52, s52, 0x8000\n\t"
    "v_exp_f32 v74, v74\n\t"
    "v_mfma_f32_16x16x32_bf16 a[8:11], a[148:151], v[148:151], a[8:11]\n\t"
    "v_exp_f32 v75, v75\n\t"
    "v_mfma_f32_16x16x32_bf16 a[12:15], a[148:151], v[156:159], a[12:15]\n\t"
    "s_waitcnt lgkmcnt(6)\n\t"
    "ds_read_b128 a[148:151], v192 offset:8192\n\t"
    "v_add_u32 v193, s38, v216\n\t"
    "v_mfma_f32_16x16x32_bf16 a[16:19], a[152:155], v[132:135], a[16:19]\n\t"
    "v_exp_f32 v76, v76\n\t"
    "v_mfma_f32_16x16x32_bf16 a[20:23], a[152:155], v[140:143], a[20:23]\n\t"
    "v_exp_f32 v77, v77\n\t"
    "v_mfma_f32_16x16x32_bf16 a[24:27], a[152:155], v[148:151], a[24:27]\n\t"
    "v_exp_f32 v78, v78\n\t"
    "v_mfma_f32_16x16x32_bf16 a[28:31], a[152:155], v[156:159], a[28:31]\n\t"
    "s_waitcnt lgkmcnt(6)\n\t"
    "ds_read_b128 a[152:155], v192 offset:9216\n\t"
    "v_cvt_pk_bf16_f32 v137, v70, v71\n\t"
    "v_mfma_f32_16x16x32_bf16 a[32:35], a[156:159], v[132:135], a[32:35]\n\t"
    "v_exp_f32 v79, v79\n\t"
    "v_mfma_f32_16x16x32_bf16 a[36:39], a[156:159], v[140:143], a[36:39]\n\t"
    "v_exp_f32 v80, v80\n\t"
    "v_mfma_f32_16x16x32_bf16 a[40:43], a[156:159], v[148:151], a[40:43]\n\t"
    "v_exp_f32 v81, v81\n\t"
    "v_mfma_f32_16x16x32_bf16 a[44:47], a[156:159], v[156:159], a[44:47]\n\t"
    "s_waitcnt lgkmcnt(6)\n\t"
    "ds_read_b128 a[156:159], v192 offset:10240\n\t"
    "v_cvt_pk_bf16_f32 v144, v72, v73\n\t"
    "v_mfma_f32_16x16x32_bf16 a[48:51], a[160:163], v[132:135], a[48:51]\n\t"
    "v_exp_f32 v82, v82\n\t"
    "v_mfma_f32_16x16x32_bf16 a[52:55], a[160:163], v[140:143], a[52:55]\n\t"
    "v_exp_f32 v83, v83\n\t"
    "v_mfma_f32_16x16x32_bf16 a[56:59], a[160:163], v[148:151], a[56:59]\n\t"
    "v_exp_f32 v84, v84\n\t"
    "v_mfma_f32_16x16x32_bf16 a[60:63], a[160:163], v[156:159], a[60:63]\n\t"
    "s_waitcnt lgkmcnt(6)\n\t"
    "ds_read_b128 a[160:163], v192 offset:11264\n\t"
    "v_cvt_pk_bf16_f32 v145, v74, v75\n\t"
    "v_mfma_f32_16x16x32_bf16 a[64:67], a[164:167], v[132:135], a[64:67]\n\t"
    "v_exp_f32 v85, v85\n\t"
    "v_mfma_f32_16x16x32_bf16 a[68:71], a[164:167], v[140:143], a[68:71]\n\t"
    "v_exp_f32 v86, v86\n\t"
    "v_mfma_f32_16x16x32_bf16 a[72:75], a[164:167], v[148:151], a[72:75]\n\t"
    "v_exp_f32 v87, v87\n\t"
    "v_mfma_f32_16x16x32_bf16 a[76:79], a[164:167], v[156:159], a[76:79]\n\t"
    "s_waitcnt lgkmcnt(6)\n\t"
    "ds_read_b128 a[164:167], v192 offset:12288\n\t"
    "v_cvt_pk_bf16_f32 v152, v76, v77\n\t"
    "v_mfma_f32_16x16x32_bf16 a[80:83], a[168:171], v[132:135], a[80:83]\n\t"
    "v_exp_f32 v88, v88\n\t"
    "v_mfma_f32_16x16x32_bf16 a[84:87], a[168:171], v[140:143], a[84:87]\n\t"
    "v_exp_f32 v89, v89\n\t"
    "v_mfma_f32_16x16x32_bf16 a[88:91], a[168:171], v[148:151], a[88:91]\n\t"
    "v_exp_f32 v90, v90\n\t"
    "v_mfma_f32_16x16x32_bf16 a[92:95], a[168:171], v[156:159], a[92:95]\n\t"
    "s_waitcnt lgkmcnt(6)\n\t"
    "ds_read_b128 a[168:171], v192 offset:13312\n\t"
    "v_cvt_pk_bf16_f32 v153, v78, v79\n\t"
    "v_mfma_f32_16x16x32_bf16 a[96:99], a[172:175], v[132:135], a[96:99]\n\t"
    "v_exp_f32 v91, v91\n\t"
    "v_mfma_f32_16x16x32_bf16 a[100:103], a[172:175], v[140:143], a[100:103]\n\t"
    "v_exp_f32 v92, v92\n\t"
    "v_mfma_f32_16x16x32_bf16 a[104:107], a[172:175], v[148:151], a[104:107]\n\t"
    "v_exp_f32 v93, v93\n\t"
    "v_mfma_f32_16x16x32_bf16 a[108:111], a[172:175], v[156:159], a[108:111]\n\t"
    "s_waitcnt lgkmcnt(6)\n\t"
    "ds_read_b128 a[172:175], v192 offset:14336\n\t"
    "v_cvt_pk_bf16_f32 v130, v80, v81\n\t"
    "v_mfma_f32_16x16x32_bf16 a[112:115], a[176:179], v[132:135], a[112:115]\n\t"
    "v_exp_f32 v94, v94\n\t"
    "v_mfma_f32_16x16x32_bf16 a[116:119], a[176:179], v[140:143], a[116:119]\n\t"
    "v_exp_f32 v95, v95\n\t"
    "v_mfma_f32_16x16x32_bf16 a[120:123], a[176:179], v[148:151], a[120:123]\n\t"
    "v_cvt_pk_bf16_f32 v131, v82, v83\n\t"
    "v_cvt_pk_bf16_f32 v138, v84, v85\n\t"
    "v_mfma_f32_16x16x32_bf16 a[124:127], a[176:179], v[156:159], a[124:127]\n\t"
    "ds_read_b128 a[176:179], v192 offset:15360\n\t"
    "v_cvt_pk_bf16_f32 v139, v86, v87\n\t"
    "v_mfma_f32_16x16x32_bf16 a[128:131], a[144:147], v[132:135], a[128:131]\n\t"
    "v_cvt_pk_bf16_f32 v146, v88, v89\n\t"
    "v_cvt_pk_bf16_f32 v147, v90, v91\n\t"
    "v_mfma_f32_16x16x32_bf16 a[132:135], a[144:147], v[140:143], a[132:135]\n\t"
    "s_waitcnt vmcnt(8)\n\t"
    "v_cvt_pk_bf16_f32 v154, v92, v93\n\t"
    "v_cvt_pk_bf16_f32 v155, v94, v95\n\t"
    "v_mfma_f32_16x16x32_bf16 a[136:139], a[144:147], v[148:151], a[136:139]\n\t"
    "s_barrier\n\t"
    "v_mfma_f32_16x16x32_bf16 a[140:143], a[144:147], v[156:159], a[140:143]\n\t"
    "s_waitcnt lgkmcnt(7)\n\t"
    "v_mfma_f32_16x16x32_bf16 v[96:99], a[148:151], v[0:3], v[160:163]\n\t"
    "s_and_b32 m0, s68, 0x1ffff\n\t"
    "v_mfma_f32_16x16x32_bf16 v[100:103], a[148:151], v[16:19], v[164:167]\n\t"
    "buffer_load_dwordx4 v216, s[20:23], s70 offen offset:0 sc1 lds\n\t"
    "v_mfma_f32_16x16x32_bf16 v[104:107], a[148:151], v[32:35], v[168:171]\n\t"
    "s_add_u32 s68, s68, 0x8000\n\t"
    "v_mfma_f32_16x16x32_bf16 v[108:111], a[148:151], v[48:51], v[172:175]\n\t"
    "s_waitcnt lgkmcnt(6)\n\t"
    "ds_read_b128 a[148:151], v193 offset:0\n\t"
    "v_mfma_f32_16x16x32_bf16 v[96:99], a[152:155], v[4:7], v[96:99]\n\t"
    "s_and_b32 s38, s51, 0x1ffff\n\t"
    "v_mfma_f32_16x16x32_bf16 v[100:103], a[152:155], v[20:23], v[100:103]\n\t"
    "s_add_u32 s51, s51, 0x8000\n\t"
    "v_mfma_f32_16x16x32_bf16 v[104:107], a[152:155], v[36:39], v[104:107]\n\t"
    "v_mfma_f32_16x16x32_bf16 v[108:111], a[152:155], v[52:55], v[108:111]\n\t"
    "s_waitcnt lgkmcnt(6)\n\t"
    "ds_read_b128 a[152:155], v193 offset:1024\n\t"
    "v_add_u32 v192, s38, v216\n\t"
    "v_mfma_f32_16x16x32_bf16 v[96:99], a[156:159], v[8:11], v[96:99]\n\t"
    "v_mfma_f32_16x16x32_bf16 v[100:103], a[156:159], v[24:27], v[100:103]\n\t"
    "buffer_load_dwordx4 v216, s[20:23], s70 offen offset:1024 sc1 lds\n\t"
    "v_mfma_f32_16x16x32_bf16 v[104:107], a[156:159], v[40:43], v[104:107]\n\t"
    "v_mfma_f32_16x16x32_bf16 v[108:111], a[156:159], v[56:59], v[108:111]\n\t"
    "s_waitcnt lgkmcnt(6)\n\t"
    "ds_read_b128 a[156:159], v193 offset:2048\n\t"
    "v_mfma_f32_16x16x32_bf16 v[96:99], a[160:163], v[12:15], v[96:99]\n\t"
    "v_mfma_f32_16x16x32_bf16 v[100:103], a[160:163], v[28:31], v[100:103]\n\t"
    "v_mfma_f32_16x16x32_bf16 v[104:107], a[160:163], v[44:47], v[104:107]\n\t"
    "v_mfma_f32_16x16x32_bf16 v[108:111], a[160:163], v[60:63], v[108:111]\n\t"
    "s_waitcnt lgkmcnt(6)\n\t"
    "ds_read_b128 a[160:163], v193 offset:3072\n\t"
    "v_mfma_f32_16x16x32_bf16 v[112:115], a[164:167], v[0:3], v[160:163]\n\t"
    "v_mfma_f32_16x16x32_bf16 v[116:119], a[164:167], v[16:19], v[164:167]\n\t"
    "buffer_load_dwordx4 v216, s[20:23], s70 offen offset:2048 sc1 lds\n\t"
    "v_mfma_f32_16x16x32_bf16 v[120:123], a[164:167], v[32:35], v[168:171]\n\t"
    "v_exp_f32 v96, v96\n\t"
    "v_mfma_f32_16x16x32_bf16 v[124:127], a[164:167], v[48:51], v[172:175]\n\t"
    "s_waitcnt lgkmcnt(6)\n\t"
    "ds_read_b128 a[164:167], v193 offset:4096\n\t"
    "v_mfma_f32_16x16x32_bf16 v[112:115], a[168:171], v[4:7], v[112:115]\n\t"
    "v_exp_f32 v97, v97\n\t"
    "v_mfma_f32_16x16x32_bf16 v[116:119], a[168:171], v[20:23], v[116:119]\n\t"
    "v_exp_f32 v98, v98\n\t"
    "v_mfma_f32_16x16x32_bf16 v[120:123], a[168:171], v[36:39], v[120:123]\n\t"
    "v_exp_f32 v99, v99\n\t"
    "v_mfma_f32_16x16x32_bf16 v[124:127], a[168:171], v[52:55], v[124:127]\n\t"
    "s_waitcnt lgkmcnt(6)\n\t"
    "ds_read_b128 a[168:171], v193 offset:5120\n\t"
    "v_cvt_pk_bf16_f32 v132, v96, v97\n\t"
    "v_mfma_f32_16x16x32_bf16 v[112:115], a[172:175], v[8:11], v[112:115]\n\t"
    "v_exp_f32 v100, v100\n\t"
    "v_mfma_f32_16x16x32_bf16 v[116:119], a[172:175], v[24:27], v[116:119]\n\t"
    "buffer_load_dwordx4 v216, s[20:23], s70 offen offset:3072 sc1 lds\n\t"
    "v_cvt_pk_bf16_f32 v133, v98, v99\n\t"
    "v_mfma_f32_16x16x32_bf16 v[120:123], a[172:175], v[40:43], v[120:123]\n\t"
    "s_add_u32 s70, s70, 0x8000\n\t"
    "v_exp_f32 v101, v101\n\t"
    "v_mfma_f32_16x16x32_bf16 v[124:127], a[172:175], v[56:59], v[124:127]\n\t"
    "s_waitcnt lgkmcnt(6)\n\t"
    "ds_read_b128 a[172:175], v193 offset:6144\n\t"
    "v_cvt_pk_bf16_f32 v140, v100, v101\n\t"
    "v_mfma_f32_16x16x32_bf16 v[112:115], a[176:179], v[12:15], v[112:115]\n\t"
    "v_exp_f32 v102, v102\n\t"
    "v_mfma_f32_16x16x32_bf16 v[116:119], a[176:179], v[28:31], v[116:119]\n\t"
    "v_exp_f32 v103, v103\n\t"
    "v_mfma_f32_16x16x32_bf16 v[120:123], a[176:179], v[44:47], v[120:123]\n\t"
    "s_and_b32 m0, s69, 0x1ffff\n\t"
    "v_exp_f32 v104, v104\n\t"
    "v_mfma_f32_16x16x32_bf16 v[124:127], a[176:179], v[60:63], v[124:127]\n\t"
    "s_waitcnt lgkmcnt(6)\n\t"
    "ds_read_b128 a[176:179], v193 offset:7168\n\t"
    "v_cvt_pk_bf16_f32 v141, v102, v103\n\t"
    "v_mfma_f32_16x16x32_bf16 a[0:3], a[148:151], v[128:131], a[0:3]\n\t"
    "v_exp_f32 v105, v105\n\t"
    "v_mfma_f32_16x16x32_bf16 a[4:7], a[148:151], v[136:139], a[4:7]\n\t"
    "buffer_load_dwordx4 v216, s[20:23], s71 offen offset:0 sc1 lds\n\t"
    "v_cvt_pk_bf16_f32 v148, v104, v105\n\t"
    "v_mfma_f32_16x16x32_bf16 a[8:11], a[148:151], v[144:147], a[8:11]\n\t"
    "s_add_u32 s69, s69, 0x8000\n\t"
    "v_exp_f32 v106, v106\n\t"
    "v_mfma_f32_16x16x32_bf16 a[12:15], a[148:151], v[152:155], a[12:15]\n\t"
    "s_waitcnt lgkmcnt(6)\n\t"
    "ds_read_b128 a[148:151], v192 offset:0\n\t"
    "v_mfma_f32_16x16x32_bf16 a[16:19], a[152:155], v[128:131], a[16:19]\n\t"
    "v_exp_f32 v107, v107\n\t"
    "v_mfma_f32_16x16x32_bf16 a[20:23], a[152:155], v[136:139], a[20:23]\n\t"
    "v_exp_f32 v108, v108\n\t"
    "v_mfma_f32_16x16x32_bf16 a[24:27], a[152:155], v[144:147], a[24:27]\n\t"
    "v_exp_f32 v109, v109\n\t"
    "v_mfma_f32_16x16x32_bf16 a[28:31], a[152:155], v[152:155], a[28:31]\n\t"
    "s_waitcnt lgkmcnt(6)\n\t"
    "ds_read_b128 a[152:155], v192 offset:1024\n\t"
    "v_cvt_pk_bf16_f32 v149, v106, v107\n\t"
    "v_mfma_f32_16x16x32_bf16 a[32:35], a[156:159], v[128:131], a[32:35]\n\t"
    "v_exp_f32 v110, v110\n\t"
    "v_mfma_f32_16x16x32_bf16 a[36:39], a[156:159], v[136:139], a[36:39]\n\t"
    "buffer_load_dwordx4 v216, s[20:23], s71 offen offset:1024 sc1 lds\n\t"
    "v_cvt_pk_bf16_f32 v156, v108, v109\n\t"
    "v_mfma_f32_16x16x32_bf16 a[40:43], a[156:159], v[144:147], a[40:43]\n\t"
    "v_exp_f32 v111, v111\n\t"
    "v_mfma_f32_16x16x32_bf16 a[44:47], a[156:159], v[152:155], a[44:47]\n\t"
    "s_waitcnt lgkmcnt(6)\n\t"
    "ds_read_b128 a[156:159], v192 offset:2048\n\t"
    "v_cvt_pk_bf16_f32 v157, v110, v111\n\t"
    "v_mfma_f32_16x16x32_bf16 a[48:51], a[160:163], v[128:131], a[48:51]\n\t"
    "v_exp_f32 v112, v112\n\t"
    "v_mfma_f32_16x16x32_bf16 a[52:55], a[160:163], v[136:139], a[52:55]\n\t"
    "v_exp_f32 v113, v113\n\t"
    "v_mfma_f32_16x16x32_bf16 a[56:59], a[160:163], v[144:147], a[56:59]\n\t"
    "v_exp_f32 v114, v114\n\t"
    "v_mfma_f32_16x16x32_bf16 a[60:63], a[160:163], v[152:155], a[60:63]\n\t"
    "s_waitcnt lgkmcnt(6)\n\t"
    "ds_read_b128 a[160:163], v192 offset:3072\n\t"
    "v_cvt_pk_bf16_f32 v134, v112, v113\n\t"
    "v_mfma_f32_16x16x32_bf16 a[64:67], a[164:167], v[128:131], a[64:67]\n\t"
    "v_exp_f32 v115, v115\n\t"
    "v_mfma_f32_16x16x32_bf16 a[68:71], a[164:167], v[136:139], a[68:71]\n\t"
    "buffer_load_dwordx4 v216, s[20:23], s71 offen offset:2048 sc1 lds\n\t"
    "v_cvt_pk_bf16_f32 v135, v114, v115\n\t"
    "v_mfma_f32_16x16x32_bf16 a[72:75], a[164:167], v[144:147], a[72:75]\n\t"
    "v_exp_f32 v116, v116\n\t"
    "v_mfma_f32_16x16x32_bf16 a[76:79], a[164:167], v[152:155], a[76:79]\n\t"
    "s_waitcnt lgkmcnt(6)\n\t"
    "ds_read_b128 a[164:167], v192 offset:4096\n\t"
    "v_mfma_f32_16x16x32_bf16 a[80:83], a[168:171], v[128:131], a[80:83]\n\t"
    "v_exp_f32 v117, v117\n\t"
    "v_mfma_f32_16x16x32_bf16 a[84:87], a[168:171], v[136:139], a[84:87]\n\t"
    "v_exp_f32 v118, v118\n\t"
    "v_mfma_f32_16x16x32_bf16 a[88:91], a[168:171], v[144:147], a[88:91]\n\t"
    "v_exp_f32 v119, v119\n\t"
    "v_mfma_f32_16x16x32_bf16 a[92:95], a[168:171], v[152:155], a[92:95]\n\t"
    "s_waitcnt lgkmcnt(6)\n\t"
    "ds_read_b128 a[168:171], v192 offset:5120\n\t"
    "v_cvt_pk_bf16_f32 v142, v116, v117\n\t"
    "v_mfma_f32_16x16x32_bf16 a[96:99], a[172:175], v[128:131], a[96:99]\n\t"
    "v_cvt_pk_bf16_f32 v143, v118, v119\n\t"
    "v_mfma_f32_16x16x32_bf16 a[100:103], a[172:175], v[136:139], a[100:103]\n\t"
    "buffer_load_dwordx4 v216, s[20:23], s71 offen offset:3072 sc1 lds\n\t"
    "v_mfma_f32_16x16x32_bf16 a[104:107], a[172:175], v[144:147], a[104:107]\n\t"
    "s_add_u32 s71, s71, 0x8000\n\t"
    "v_mfma_f32_16x16x32_bf16 a[108:111], a[172:175], v[152:155], a[108:111]\n\t"
    "s_waitcnt lgkmcnt(6)\n\t"
    "ds_read_b128 a[172:175], v192 offset:6144\n\t"
    "v_mfma_f32_16x16x32_bf16 a[112:115], a[176:179], v[128:131], a[112:115]\n\t"
    "v_mfma_f32_16x16x32_bf16 a[116:119], a[176:179], v[136:139], a[116:119]\n\t"
    "v_mfma_f32_16x16x32_bf16 a[120:123], a[176:179], v[144:147], a[120:123]\n\t"
    "v_mfma_f32_16x16x32_bf16 a[124:127], a[176:179], v[152:155], a[124:127]\n\t"
    "ds_read_b128 a[176:179], v192 offset:7168\n\t"
    "v_mfma_f32_16x16x32_bf16 a[128:131], a[144:147], v[128:131], a[128:131]\n\t"
    "s_add_u32 s45, s45, 1\n\t"
    "v_mfma_f32_16x16x32_bf16 a[132:135], a[144:147], v[136:139], a[132:135]\n\t"
    "v_mfma_f32_16x16x32_bf16 a[136:139], a[144:147], v[144:147], a[136:139]\n\t"
    "v_mfma_f32_16x16x32_bf16 a[140:143], a[144:147], v[152:155], a[140:143]\n\t"
    "s_sub_u32 s40, s40, 1\n\t"
    "s_cmp_lg_u32 s40, 0\n\t"
    "s_cbranch_scc1 L_p1_loop\n\t"
    "s_lshl_b32 s37, s43, 4\n\t"
    "s_lshl_b32 s38, s45, 6\n\t"
    "s_sub_u32 s46, s37, s38\n\t"
    "v_cvt_f32_i32 v194, s46\n\t"
    "v_fma_f32 v194, v194, v196, v195\n\t"
    "v_mov_b32 v176, v194\n\t"
    "v_min_f32 v176, v176, v160\n\t"
    "v_add_f32 v177, 0xf1800000, v194\n\t"
    "v_min_f32 v177, v177, v160\n\t"
    "v_add_f32 v178, 0xf2000000, v194\n\t"
    "v_min_f32 v178, v178, v160\n\t"
    "v_add_f32 v179, 0xf2400000, v194\n\t"
    "v_min_f32 v179, v179, v160\n\t"
    "s_waitcnt lgkmcnt(7)\n\t"
    "v_mfma_f32_16x16x32_bf16 v[68:71], a[148:151], v[16:19], v[164:167]\n\t"
    "v_add_f32 v180, 0xf3800000, v194\n\t"
    "v_min_f32 v180, v180, v160\n\t"
    "v_mfma_f32_16x16x32_bf16 v[72:75], a[148:151], v[32:35], v[168:171]\n\t"
    "v_add_f32 v181, 0xf3880000, v194\n\t"
    "v_min_f32 v181, v181, v160\n\t"
    "v_mfma_f32_16x16x32_bf16 v[76:79], a[148:151], v[48:51], v[172:175]\n\t"
    "v_add_f32 v182, 0xf3900000, v194\n\t"
    "v_min_f32 v182, v182, v160\n\t"
    "v_mfma_f32_16x16x32_bf16 v[64:67], a[148:151], v[0:3], v[176:179]\n\t"
    "s_waitcnt lgkmcnt(6)\n\t"
    "ds_read_b128 a[148:151], v193 offset:8192\n\t"
    "v_mfma_f32_16x16x32_bf16 v[68:71], a[152:155], v[20:23], v[68:71]\n\t"
    "v_add_f32 v183, 0xf3980000, v194\n\t"
    "v_min_f32 v183, v183, v160\n\t"
    "v_mfma_f32_16x16x32_bf16 v[72:75], a[152:155], v[36:39], v[72:75]\n\t"
    "v_exp_f32 v120, v120\n\t"
    "v_mfma_f32_16x16x32_bf16 v[76:79], a[152:155], v[52:55], v[76:79]\n\t"
    "v_exp_f32 v121, v121\n\t"
    "v_mfma_f32_16x16x32_bf16 v[64:67], a[152:155], v[4:7], v[64:67]\n\t"
    "s_waitcnt lgkmcnt(6)\n\t"
    "ds_read_b128 a[152:155], v193 offset:9216\n\t"
    "v_cvt_pk_bf16_f32 v150, v120, v121\n\t"
    "v_mfma_f32_16x16x32_bf16 v[68:71], a[156:159], v[24:27], v[68:71]\n\t"
    "v_exp_f32 v122, v122\n\t"
    "v_mfma_f32_16x16x32_bf16 v[72:75], a[156:159], v[40:43], v[72:75]\n\t"
    "v_exp_f32 v123, v123\n\t"
    "v_mfma_f32_16x16x32_bf16 v[76:79], a[156:159], v[56:59], v[76:79]\n\t"
    "v_exp_f32 v124, v124\n\t"
    "v_mfma_f32_16x16x32_bf16 v[64:67], a[156:159], v[8:11], v[64:67]\n\t"
    "s_waitcnt lgkmcnt(6)\n\t"
    "ds_read_b128 a[156:159], v193 offset:10240\n\t"
    "v_cvt_pk_bf16_f32 v151, v122, v123\n\t"
    "v_mfma_f32_16x16x32_bf16 v[68:71], a[160:163], v[28:31], v[68:71]\n\t"
    "v_exp_f32 v125, v125\n\t"
    "v_mfma_f32_16x16x32_bf16 v[72:75], a[160:163], v[44:47], v[72:75]\n\t"
    "v_exp_f32 v126, v126\n\t"
    "v_mfma_f32_16x16x32_bf16 v[76:79], a[160:163], v[60:63], v[76:79]\n\t"
    "v_exp_f32 v127, v127\n\t"
    "v_mfma_f32_16x16x32_bf16 v[64:67], a[160:163], v[12:15], v[64:67]\n\t"
    "s_waitcnt lgkmcnt(6)\n\t"
    "ds_read_b128 a[160:163], v193 offset:11264\n\t"
    "v_cvt_pk_bf16_f32 v158, v124, v125\n\t"
    "v_mfma_f32_16x16x32_bf16 v[84:87], a[164:167], v[16:19], v[164:167]\n\t"
    "v_cvt_pk_bf16_f32 v159, v126, v127\n\t"
    "v_mfma_f32_16x16x32_bf16 v[88:91], a[164:167], v[32:35], v[168:171]\n\t"
    "v_add_f32 v184, 0xf4000000, v194\n\t"
    "v_min_f32 v184, v184, v160\n\t"
    "v_mfma_f32_16x16x32_bf16 v[92:95], a[164:167], v[48:51], v[172:175]\n\t"
    "v_add_f32 v185, 0xf4040000, v194\n\t"
    "v_min_f32 v185, v185, v160\n\t"
    "v_mfma_f32_16x16x32_bf16 v[80:83], a[164:167], v[0:3], v[180:183]\n\t"
    "s_waitcnt lgkmcnt(6)\n\t"
    "ds_read_b128 a[164:167], v193 offset:12288\n\t"
    "v_mfma_f32_16x16x32_bf16 v[84:87], a[168:171], v[20:23], v[84:87]\n\t"
    "v_add_f32 v186, 0xf4080000, v194\n\t"
    "v_min_f32 v186, v186, v160\n\t"
    "v_mfma_f32_16x16x32_bf16 v[88:91], a[168:171], v[36:39], v[88:91]\n\t"
    "v_add_f32 v187, 0xf40c0000, v194\n\t"
    "v_min_f32 v187, v187, v160\n\t"
    "v_mfma_f32_16x16x32_bf16 v[92:95], a[168:171], v[52:55], v[92:95]\n\t"
    "v_add_f32 v188, 0xf4400000, v194\n\t"
    "v_min_f32 v188, v188, v160\n\t"
    "v_mfma_f32_16x16x32_bf16 v[80:83], a[168:171], v[4:7], v[80:83]\n\t"
    "s_waitcnt lgkmcnt(6)\n\t"
    "ds_read_b128 a[168:171], v193 offset:13312\n\t"
    "v_mfma_f32_16x16x32_bf16 v[84:87], a[172:175], v[24:27], v[84:87]\n\t"
    "v_add_f32 v189, 0xf4440000, v194\n\t"
    "v_min_f32 v189, v189, v160\n\t"
    "v_mfma_f32_16x16x32_bf16 v[88:91], a[172:175], v[40:43], v[88:91]\n\t"
    "v_add_f32 v190, 0xf4480000, v194\n\t"
    "v_min_f32 v190, v190, v160\n\t"
    "v_mfma_f32_16x16x32_bf16 v[92:95], a[172:175], v[56:59], v[92:95]\n\t"
    "v_add_f32 v191, 0xf44c0000, v194\n\t"
    "v_min_f32 v191, v191, v160\n\t"
    "v_mfma_f32_16x16x32_bf16 v[80:83], a[172:175], v[8:11], v[80:83]\n\t"
    "s_waitcnt lgkmcnt(6)\n\t"
    "ds_read_b128 a[172:175], v193 offset:14336\n\t"
    "v_mfma_f32_16x16x32_bf16 v[84:87], a[176:179], v[28:31], v[84:87]\n\t"
    "v_exp_f32 v64, v64\n\t"
    "v_mfma_f32_16x16x32_bf16 v[88:91], a[176:179], v[44:47], v[88:91]\n\t"
    "v_exp_f32 v65, v65\n\t"
    "v_mfma_f32_16x16x32_bf16 v[92:95], a[176:179], v[60:63], v[92:95]\n\t"
    "v_exp_f32 v66, v66\n\t"
    "v_mfma_f32_16x16x32_bf16 v[80:83], a[176:179], v[12:15], v[80:83]\n\t"
    "s_waitcnt lgkmcnt(6)\n\t"
    "ds_read_b128 a[176:179], v193 offset:15360\n\t"
    "v_cvt_pk_bf16_f32 v128, v64, v65\n\t"
    "v_mfma_f32_16x16x32_bf16 a[0:3], a[148:151], v[132:135], a[0:3]\n\t"
    "s_and_b32 s38, s52, 0x1ffff\n\t"
    "v_exp_f32 v67, v67\n\t"
    "v_mfma_f32_16x16x32_bf16 a[4:7], a[148:151], v[140:143], a[4:7]\n\t"
    "s_add_u32 s52, s52, 0x8000\n\t"
    "v_exp_f32 v68, v68\n\t"
    "v_mfma_f32_16x16x32_bf16 a[8:11], a[148:151], v[148:151], a[8:11]\n\t"
    "v_exp_f32 v69, v69\n\t"
    "v_mfma_f32_16x16x32_bf16 a[12:15], a[148:151], v[156:159], a[12:15]\n\t"
    "s_waitcnt lgkmcnt(6)\n\t"
    "ds_read_b128 a[148:151], v192 offset:8192\n\t"
    "v_add_u32 v193, s38, v216\n\t"
    "v_mfma_f32_16x16x32_bf16 a[16:19], a[152:155], v[132:135], a[16:19]\n\t"
    "v_exp_f32 v70, v70\n\t"
    "v_mfma_f32_16x16x32_bf16 a[20:23], a[152:155], v[140:143], a[20:23]\n\t"
    "v_exp_f32 v71, v71\n\t"
    "v_mfma_f32_16x16x32_bf16 a[24:27], a[152:155], v[148:151], a[24:27]\n\t"
    "v_exp_f32 v72, v72\n\t"
    "v_mfma_f32_16x16x32_bf16 a[28:31], a[152:155], v[156:159], a[28:31]\n\t"
    "s_waitcnt lgkmcnt(6)\n\t"
    "ds_read_b128 a[152:155], v192 offset:9216\n\t"
    "v_cvt_pk_bf16_f32 v129, v66, v67\n\t"
    "v_mfma_f32_16x16x32_bf16 a[32:35], a[156:159], v[132:135], a[32:35]\n\t"
    "v_exp_f32 v73, v73\n\t"
    "v_mfma_f32_16x16x32_bf16 a[36:39], a[156:159], v[140:143], a[36:39]\n\t"
    "v_exp_f32 v74, v74\n\t"
    "v_mfma_f32_16x16x32_bf16 a[40:43], a[156:159], v[148:151], a[40:43]\n\t"
    "v_exp_f32 v75, v75\n\t"
    "v_mfma_f32_16x16x32_bf16 a[44:47], a[156:159], v[156:159], a[44:47]\n\t"
    "s_waitcnt lgkmcnt(6)\n\t"
    "ds_read_b128 a[156:159], v192 offset:10240\n\t"
    "v_cvt_pk_bf16_f32 v136, v68, v69\n\t"
    "v_mfma_f32_16x16x32_bf16 a[48:51], a[160:163], v[132:135], a[48:51]\n\t"
    "v_exp_f32 v76, v76\n\t"
    "v_mfma_f32_16x16x32_bf16 a[52:55], a[160:163], v[140:143], a[52:55]\n\t"
    "v_exp_f32 v77, v77\n\t"
    "v_mfma_f32_16x16x32_bf16 a[56:59], a[160:163], v[148:151], a[56:59]\n\t"
    "v_exp_f32 v78, v78\n\t"
    "v_mfma_f32_16x16x32_bf16 a[60:63], a[160:163], v[156:159], a[60:63]\n\t"
    "s_waitcnt lgkmcnt(6)\n\t"
    "ds_read_b128 a[160:163], v192 offset:11264\n\t"
    "v_cvt_pk_bf16_f32 v137, v70, v71\n\t"
    "v_mfma_f32_16x16x32_bf16 a[64:67], a[164:167], v[132:135], a[64:67]\n\t"
    "v_exp_f32 v79, v79\n\t"
    "v_mfma_f32_16x16x32_bf16 a[68:71], a[164:167], v[140:143], a[68:71]\n\t"
    "v_exp_f32 v80, v80\n\t"
    "v_mfma_f32_16x16x32_bf16 a[72:75], a[164:167], v[148:151], a[72:75]\n\t"
    "v_exp_f32 v81, v81\n\t"
    "v_mfma_f32_16x16x32_bf16 a[76:79], a[164:167], v[156:159], a[76:79]\n\t"
    "s_waitcnt lgkmcnt(6)\n\t"
    "ds_read_b128 a[164:167], v192 offset:12288\n\t"
    "v_cvt_pk_bf16_f32 v144, v72, v73\n\t"
    "v_mfma_f32_16x16x32_bf16 a[80:83], a[168:171], v[132:135], a[80:83]\n\t"
    "v_exp_f32 v82, v82\n\t"
    "v_mfma_f32_16x16x32_bf16 a[84:87], a[168:171], v[140:143], a[84:87]\n\t"
    "v_exp_f32 v83, v83\n\t"
    "v_mfma_f32_16x16x32_bf16 a[88:91], a[168:171], v[148:151], a[88:91]\n\t"
    "v_exp_f32 v84, v84\n\t"
    "v_mfma_f32_16x16x32_bf16 a[92:95], a[168:171], v[156:159], a[92:95]\n\t"
    "s_waitcnt lgkmcnt(6)\n\t"
    "ds_read_b128 a[168:171], v192 offset:13312\n\t"
    "v_cvt_pk_bf16_f32 v145, v74, v75\n\t"
    "v_mfma_f32_16x16x32_bf16 a[96:99], a[172:175], v[132:135], a[96:99]\n\t"
    "v_exp_f32 v85, v85\n\t"
    "v_mfma_f32_16x16x32_bf16 a[100:103], a[172:175], v[140:143], a[100:103]\n\t"
    "v_exp_f32 v86, v86\n\t"
    "v_mfma_f32_16x16x32_bf16 a[104:107], a[172:175], v[148:151], a[104:107]\n\t"
    "v_exp_f32 v87, v87\n\t"
    "v_mfma_f32_16x16x32_bf16 a[108:111], a[172:175], v[156:159], a[108:111]\n\t"
    "s_waitcnt lgkmcnt(6)\n\t"
    "ds_read_b128 a[172:175], v192 offset:14336\n\t"
    "v_cvt_pk_bf16_f32 v152, v76, v77\n\t"
    "v_mfma_f32_16x16x32_bf16 a[112:115], a[176:179], v[132:135], a[112:115]\n\t"
    "v_exp_f32 v88, v88\n\t"
    "v_mfma_f32_16x16x32_bf16 a[116:119], a[176:179], v[140:143], a[116:119]\n\t"
    "v_exp_f32 v89, v89\n\t"
    "v_mfma_f32_16x16x32_bf16 a[120:123], a[176:179], v[148:151], a[120:123]\n\t"
    "v_exp_f32 v90, v90\n\t"
    "v_mfma_f32_16x16x32_bf16 a[124:127], a[176:179], v[156:159], a[124:127]\n\t"
    "ds_read_b128 a[176:179], v192 offset:15360\n\t"
    "v_cvt_pk_bf16_f32 v153, v78, v79\n\t"
    "v_mfma_f32_16x16x32_bf16 a[128:131], a[144:147], v[132:135], a[128:131]\n\t"
    "v_exp_f32 v91, v91\n\t"
    "v_mfma_f32_16x16x32_bf16 a[132:135], a[144:147], v[140:143], a[132:135]\n\t"
    "s_waitcnt vmcnt(8)\n\t"
    "v_exp_f32 v92, v92\n\t"
    "v_mfma_f32_16x16x32_bf16 a[136:139], a[144:147], v[148:151], a[136:139]\n\t"
    "s_barrier\n\t"
    "v_exp_f32 v93, v93\n\t"
    "v_mfma_f32_16x16x32_bf16 a[140:143], a[144:147], v[156:159], a[140:143]\n\t"
    "s_waitcnt lgkmcnt(7)\n\t"
    "v_exp_f32 v94, v94\n\t"
    "v_mfma_f32_16x16x32_bf16 v[100:103], a[148:151], v[16:19], v[164:167]\n\t"
    "s_and_b32 m0, s68, 0x1ffff\n\t"
    "v_exp_f32 v95, v95\n\t"
    "v_mfma_f32_16x16x32_bf16 v[104:107], a[148:151], v[32:35], v[168:171]\n\t"
    "buffer_load_dwordx4 v216, s[20:23], s70 offen offset:0 sc1 lds\n\t"
    "v_cvt_pk_bf16_f32 v130, v80, v81\n\t"
    "v_mfma_f32_16x16x32_bf16 v[108:111], a[148:151], v[48:51], v[172:175]\n\t"
    "s_add_u32 s68, s68, 0x8000\n\t"
    "v_cvt_pk_bf16_f32 v131, v82, v83\n\t"
    "v_cvt_pk_bf16_f32 v138, v84, v85\n\t"
    "v_mfma_f32_16x16x32_bf16 v[96:99], a[148:151], v[0:3], v[184:187]\n\t"
    "s_waitcnt lgkmcnt(6)\n\t"
    "ds_read_b128 a[148:151], v193 offset:0\n\t"
    "v_cvt_pk_bf16_f32 v139, v86, v87\n\t"
    "v_mfma_f32_16x16x32_bf16 v[100:103], a[152:155], v[20:23], v[100:103]\n\t"
    "s_and_b32 s38, s51, 0x1ffff\n\t"
    "v_cvt_pk_bf16_f32 v146, v88, v89\n\t"
    "v_cvt_pk_bf16_f32 v147, v90, v91\n\t"
    "v_mfma_f32_16x16x32_bf16 v[104:107], a[152:155], v[36:39], v[104:107]\n\t"
    "s_add_u32 s51, s51, 0x8000\n\t"
    "v_cvt_pk_bf16_f32 v154, v92, v93\n\t"
    "v_cvt_pk_bf16_f32 v155, v94, v95\n\t"
    "v_mfma_f32_16x16x32_bf16 v[108:111], a[152:155], v[52:55], v[108:111]\n\t"
    "v_mfma_f32_16x16x32_bf16 v[96:99], a[152:155], v[4:7], v[96:99]\n\t"
    "s_waitcnt lgkmcnt(6)\n\t"
    "ds_read_b128 a[152:155], v193 offset:1024\n\t"
    "v_add_u32 v192, s38, v216\n\t"
    "v_mfma_f32_16x16x32_bf16 v[100:103], a[156:159], v[24:27], v[100:103]\n\t"
    "v_mfma_f32_16x16x32_bf16 v[104:107], a[156:159], v[40:43], v[104:107]\n\t"
    "buffer_load_dwordx4 v216, s[20:23], s70 offen offset:1024 sc1 lds\n\t"
    "v_mfma_f32_16x16x32_bf16 v[108:111], a[156:159], v[56:59], v[108:111]\n\t"
    "v_mfma_f32_16x16x32_bf16 v[96:99], a[156:159], v[8:11], v[96:99]\n\t"
    "s_waitcnt lgkmcnt(6)\n\t"
    "ds_read_b128 a[156:159], v193 offset:2048\n\t"
    "v_mfma_f32_16x16x32_bf16 v[100:103], a[160:163], v[28:31], v[100:103]\n\t"
    "v_mfma_f32_16x16x32_bf16 v[104:107], a[160:163], v[44:47], v[104:107]\n\t"
    "v_mfma_f32_16x16x32_bf16 v[108:111], a[160:163], v[60:63], v[108:111]\n\t"
    "v_mfma_f32_16x16x32_bf16 v[96:99], a[160:163], v[12:15], v[96:99]\n\t"
    "s_waitcnt lgkmcnt(6)\n\t"
    "ds_read_b128 a[160:163], v193 offset:3072\n\t"
    "v_mfma_f32_16x16x32_bf16 v[116:119], a[164:167], v[16:19], v[164:167]\n\t"
    "v_mfma_f32_16x16x32_bf16 v[120:123], a[164:167], v[32:35], v[168:171]\n\t"
    "buffer_load_dwordx4 v216, s[20:23], s70 offen offset:2048 sc1 lds\n\t"
    "v_mfma_f32_16x16x32_bf16 v[124:127], a[164:167], v[48:51], v[172:175]\n\t"
    "v_exp_f32 v100, v100\n\t"
    "v_mfma_f32_16x16x32_bf16 v[112:115], a[164:167], v[0:3], v[188:191]\n\t"
    "s_waitcnt lgkmcnt(6)\n\t"
    "ds_read_b128 a[164:167], v193 offset:4096\n\t"
    "v_mfma_f32_16x16x32_bf16 v[116:119], a[168:171], v[20:23], v[116:119]\n\t"
    "v_exp_f32 v96, v96\n\t"
    "v_mfma_f32_16x16x32_bf16 v[120:123], a[168:171], v[36:39], v[120:123]\n\t"
    "v_exp_f32 v97, v97\n\t"
    "v_mfma_f32_16x16x32_bf16 v[124:127], a[168:171], v[52:55], v[124:127]\n\t"
    "v_exp_f32 v98, v98\n\t"
    "v_mfma_f32_16x16x32_bf16 v[112:115], a[168:171], v[4:7], v[112:115]\n\t"
    "s_waitcnt lgkmcnt(6)\n\t"
    "ds_read_b128 a[168:171], v193 offset:5120\n\t"
    "v_cvt_pk_bf16_f32 v132, v96, v97\n\t"
    "v_mfma_f32_16x16x32_bf16 v[116:119], a[172:175], v[24:27], v[116:119]\n\t"
    "v_exp_f32 v99, v99\n\t"
    "v_mfma_f32_16x16x32_bf16 v[120:123], a[172:175], v[40:43], v[120:123]\n\t"
    "buffer_load_dwordx4 v216, s[20:23], s70 offen offset:3072 sc1 lds\n\t"
    "v_cvt_pk_bf16_f32 v133, v98, v99\n\t"
    "v_mfma_f32_16x16x32_bf16 v[124:127], a[172:175], v[56:59], v[124:127]\n\t"
    "s_add_u32 s70, s70, 0x8000\n\t"
    "v_exp_f32 v101, v101\n\t"
    "v_mfma_f32_16x16x32_bf16 v[112:115], a[172:175], v[8:11], v[112:115]\n\t"
    "s_waitcnt lgkmcnt(6)\n\t"
    "ds_read_b128 a[172:175], v193 offset:6144\n\t"
    "v_cvt_pk_bf16_f32 v140, v100, v101\n\t"
    "v_mfma_f32_16x16x32_bf16 v[116:119], a[176:179], v[28:31], v[116:119]\n\t"
    "v_exp_f32 v102, v102\n\t"
    "v_mfma_f32_16x16x32_bf16 v[120:123], a[176:179], v[44:47], v[120:123]\n\t"
    "v_exp_f32 v103, v103\n\t"
    "v_mfma_f32_16x16x32_bf16 v[124:127], a[176:179], v[60:63], v[124:127]\n\t"
    "s_and_b32 m0, s69, 0x1ffff\n\t"
    "v_exp_f32 v104, v104\n\t"
    "v_mfma_f32_16x16x32_bf16 v[112:115], a[176:179], v[12:15], v[112:115]\n\t"
    "s_waitcnt lgkmcnt(6)\n\t"
    "ds_read_b128 a[176:179], v193 offset:7168\n\t"
    "v_cvt_pk_bf16_f32 v141, v102, v103\n\t"
    "v_mfma_f32_16x16x32_bf16 a[0:3], a[148:151], v[128:131], a[0:3]\n\t"
    "v_exp_f32 v105, v105\n\t"
    "v_mfma_f32_16x16x32_bf16 a[4:7], a[148:151], v[136:139], a[4:7]\n\t"
    "buffer_load_dwordx4 v216, s[20:23], s71 offen offset:0 sc1 lds\n\t"
    "v_cvt_pk_bf16_f32 v148, v104, v105\n\t"
    "v_mfma_f32_16x16x32_bf16 a[8:11], a[148:151], v[144:147], a[8:11]\n\t"
    "s_add_u32 s69, s69, 0x8000\n\t"
    "v_exp_f32 v106, v106\n\t"
    "v_mfma_f32_16x16x32_bf16 a[12:15], a[148:151], v[152:155], a[12:15]\n\t"
    "s_waitcnt lgkmcnt(6)\n\t"
    "ds_read_b128 a[148:151], v192 offset:0\n\t"
    "v_mfma_f32_16x16x32_bf16 a[16:19], a[152:155], v[128:131], a[16:19]\n\t"
    "v_exp_f32 v107, v107\n\t"
    "v_mfma_f32_16x16x32_bf16 a[20:23], a[152:155], v[136:139], a[20:23]\n\t"
    "v_exp_f32 v108, v108\n\t"
    "v_mfma_f32_16x16x32_bf16 a[24:27], a[152:155], v[144:147], a[24:27]\n\t"
    "v_exp_f32 v109, v109\n\t"
    "v_mfma_f32_16x16x32_bf16 a[28:31], a[152:155], v[152:155], a[28:31]\n\t"
    "s_waitcnt lgkmcnt(6)\n\t"
    "ds_read_b128 a[152:155], v192 offset:1024\n\t"
    "v_cvt_pk_bf16_f32 v149, v106, v107\n\t"
    "v_mfma_f32_16x16x32_bf16 a[32:35], a[156:159], v[128:131], a[32:35]\n\t"
    "v_exp_f32 v110, v110\n\t"
    "v_mfma_f32_16x16x32_bf16 a[36:39], a[156:159], v[136:139], a[36:39]\n\t"
    "buffer_load_dwordx4 v216, s[20:23], s71 offen offset:1024 sc1 lds\n\t"
    "v_cvt_pk_bf16_f32 v156, v108, v109\n\t"
    "v_mfma_f32_16x16x32_bf16 a[40:43], a[156:159], v[144:147], a[40:43]\n\t"
    "v_exp_f32 v111, v111\n\t"
    "v_mfma_f32_16x16x32_bf16 a[44:47], a[156:159], v[152:155], a[44:47]\n\t"
    "s_waitcnt lgkmcnt(6)\n\t"
    "ds_read_b128 a[156:159], v192 offset:2048\n\t"
    "v_cvt_pk_bf16_f32 v157, v110, v111\n\t"
    "v_mfma_f32_16x16x32_bf16 a[48:51], a[160:163], v[128:131], a[48:51]\n\t"
    "v_exp_f32 v112, v112\n\t"
    "v_mfma_f32_16x16x32_bf16 a[52:55], a[160:163], v[136:139], a[52:55]\n\t"
    "v_exp_f32 v113, v113\n\t"
    "v_mfma_f32_16x16x32_bf16 a[56:59], a[160:163], v[144:147], a[56:59]\n\t"
    "v_exp_f32 v114, v114\n\t"
    "v_mfma_f32_16x16x32_bf16 a[60:63], a[160:163], v[152:155], a[60:63]\n\t"
    "s_waitcnt lgkmcnt(6)\n\t"
    "ds_read_b128 a[160:163], v192 offset:3072\n\t"
    "v_cvt_pk_bf16_f32 v134, v112, v113\n\t"
    "v_mfma_f32_16x16x32_bf16 a[64:67], a[164:167], v[128:131], a[64:67]\n\t"
    "v_exp_f32 v115, v115\n\t"
    "v_mfma_f32_16x16x32_bf16 a[68:71], a[164:167], v[136:139], a[68:71]\n\t"
    "buffer_load_dwordx4 v216, s[20:23], s71 offen offset:2048 sc1 lds\n\t"
    "v_cvt_pk_bf16_f32 v135, v114, v115\n\t"
    "v_mfma_f32_16x16x32_bf16 a[72:75], a[164:167], v[144:147], a[72:75]\n\t"
    "v_exp_f32 v116, v116\n\t"
    "v_mfma_f32_16x16x32_bf16 a[76:79], a[164:167], v[152:155], a[76:79]\n\t"
    "s_waitcnt lgkmcnt(6)\n\t"
    "ds_read_b128 a[164:167], v192 offset:4096\n\t"
    "v_mfma_f32_16x16x32_bf16 a[80:83], a[168:171], v[128:131], a[80:83]\n\t"
    "v_exp_f32 v117, v117\n\t"
    "v_mfma_f32_16x16x32_bf16 a[84:87], a[168:171], v[136:139], a[84:87]\n\t"
    "v_exp_f32 v118, v118\n\t"
    "v_mfma_f32_16x16x32_bf16 a[88:91], a[168:171], v[144:147], a[88:91]\n\t"
    "v_exp_f32 v119, v119\n\t"
    "v_mfma_f32_16x16x32_bf16 a[92:95], a[168:171], v[152:155], a[92:95]\n\t"
    "s_waitcnt lgkmcnt(6)\n\t"
    "ds_read_b128 a[168:171], v192 offset:5120\n\t"
    "v_cvt_pk_bf16_f32 v142, v116, v117\n\t"
    "v_mfma_f32_16x16x32_bf16 a[96:99], a[172:175], v[128:131], a[96:99]\n\t"
    "v_cvt_pk_bf16_f32 v143, v118, v119\n\t"
    "v_mfma_f32_16x16x32_bf16 a[100:103], a[172:175], v[136:139], a[100:103]\n\t"
    "buffer_load_dwordx4 v216, s[20:23], s71 offen offset:3072 sc1 lds\n\t"
    "v_mfma_f32_16x16x32_bf16 a[104:107], a[172:175], v[144:147], a[104:107]\n\t"
    "s_add_u32 s71, s71, 0x8000\n\t"
    "v_mfma_f32_16x16x32_bf16 a[108:111], a[172:175], v[152:155], a[108:111]\n\t"
    "s_waitcnt lgkmcnt(6)\n\t"
    "ds_read_b128 a[172:175], v192 offset:6144\n\t"
    "v_mfma_f32_16x16x32_bf16 a[112:115], a[176:179], v[128:131], a[112:115]\n\t"
    "v_mfma_f32_16x16x32_bf16 a[116:119], a[176:179], v[136:139], a[116:119]\n\t"
    "v_mfma_f32_16x16x32_bf16 a[120:123], a[176:179], v[144:147], a[120:123]\n\t"
    "v_mfma_f32_16x16x32_bf16 a[124:127], a[176:179], v[152:155], a[124:127]\n\t"
    "ds_read_b128 a[176:179], v192 offset:7168\n\t"
    "v_mfma_f32_16x16x32_bf16 a[128:131], a[144:147], v[128:131], a[128:131]\n\t"
    "s_add_u32 s45, s45, 1\n\t"
    "v_mfma_f32_16x16x32_bf16 a[132:135], a[144:147], v[136:139], a[132:135]\n\t"
    "v_mfma_f32_16x16x32_bf16 a[136:139], a[144:147], v[144:147], a[136:139]\n\t"
    "v_mfma_f32_16x16x32_bf16 a[140:143], a[144:147], v[152:155], a[140:143]\n\t"
    "L_p1_last:\n\t"
    "s_lshl_b32 s37, s43, 4\n\t"
    "s_lshl_b32 s38, s45, 6\n\t"
    "s_sub_u32 s46, s37, s38\n\t"
    "v_cvt_f32_i32 v194, s46\n\t"
    "v_fma_f32 v194, v194, v196, v195\n\t"
    "v_add_f32 v176, 0x74800000, v194\n\t"
    "v_min_f32 v176, v176, v164\n\t"
    "v_add_f32 v177, 0x747c0000, v194\n\t"
    "v_min_f32 v177, v177, v164\n\t"
    "v_add_f32 v178, 0x74780000, v194\n\t"
    "v_min_f32 v178, v178, v164\n\t"
    "v_add_f32 v179, 0x74740000, v194\n\t"
    "v_min_f32 v179, v179, v164\n\t"
    "s_waitcnt lgkmcnt(7)\n\t"
    "v_mfma_f32_16x16x32_bf16 v[72:75], a[148:151], v[32:35], v[168:171]\n\t"
    "v_add_f32 v180, 0x74400000, v194\n\t"
    "v_min_f32 v180, v180, v164\n\t"
    "v_mfma_f32_16x16x32_bf16 v[76:79], a[148:151], v[48:51], v[172:175]\n\t"
    "v_add_f32 v181, 0x743c0000, v194\n\t"
    "v_min_f32 v181, v181, v164\n\t"
    "v_mfma_f32_16x16x32_bf16 v[68:71], a[148:151], v[16:19], v[176:179]\n\t"
    "s_waitcnt lgkmcnt(6)\n\t"
    "ds_read_b128 a[148:151], v193 offset:8192\n\t"
    "v_mfma_f32_16x16x32_bf16 v[72:75], a[152:155], v[36:39], v[72:75]\n\t"
    "v_add_f32 v182, 0x74380000, v194\n\t"
    "v_min_f32 v182, v182, v164\n\t"
    "v_mfma_f32_16x16x32_bf16 v[76:79], a[152:155], v[52:55], v[76:79]\n\t"
    "v_add_f32 v183, 0x74340000, v194\n\t"
    "v_min_f32 v183, v183, v164\n\t"
    "v_mfma_f32_16x16x32_bf16 v[68:71], a[152:155], v[20:23], v[68:71]\n\t"
    "s_waitcnt lgkmcnt(6)\n\t"
    "ds_read_b128 a[152:155], v193 offset:9216\n\t"
    "v_mfma_f32_16x16x32_bf16 v[72:75], a[156:159], v[40:43], v[72:75]\n\t"
    "v_exp_f32 v120, v120\n\t"
    "v_mfma_f32_16x16x32_bf16 v[76:79], a[156:159], v[56:59], v[76:79]\n\t"
    "v_exp_f32 v121, v121\n\t"
    "v_mfma_f32_16x16x32_bf16 v[68:71], a[156:159], v[24:27], v[68:71]\n\t"
    "s_waitcnt lgkmcnt(6)\n\t"
    "ds_read_b128 a[156:159], v193 offset:10240\n\t"
    "v_cvt_pk_bf16_f32 v150, v120, v121\n\t"
    "v_mfma_f32_16x16x32_bf16 v[72:75], a[160:163], v[44:47], v[72:75]\n\t"
    "v_exp_f32 v122, v122\n\t"
    "v_mfma_f32_16x16x32_bf16 v[76:79], a[160:163], v[60:63], v[76:79]\n\t"
    "v_exp_f32 v123, v123\n\t"
    "v_mfma_f32_16x16x32_bf16 v[68:71], a[160:163], v[28:31], v[68:71]\n\t"
    "s_waitcnt lgkmcnt(6)\n\t"
    "ds_read_b128 a[160:163], v193 offset:11264\n\t"
    "v_cvt_pk_bf16_f32 v151, v122, v123\n\t"
    "v_mfma_f32_16x16x32_bf16 v[88:91], a[164:167], v[32:35], v[168:171]\n\t"
    "v_exp_f32 v124, v124\n\t"
    "v_mfma_f32_16x16x32_bf16 v[92:95], a[164:167], v[48:51], v[172:175]\n\t"
    "v_exp_f32 v125, v125\n\t"
    "v_mfma_f32_16x16x32_bf16 v[84:87], a[164:167], v[16:19], v[180:183]\n\t"
    "s_waitcnt lgkmcnt(6)\n\t"
    "ds_read_b128 a[164:167], v193 offset:12288\n\t"
    "v_cvt_pk_bf16_f32 v158, v124, v125\n\t"
    "v_mfma_f32_16x16x32_bf16 v[88:91], a[168:171], v[36:39], v[88:91]\n\t"
    "v_exp_f32 v126, v126\n\t"
    "v_mfma_f32_16x16x32_bf16 v[92:95], a[168:171], v[52:55], v[92:95]\n\t"
    "v_exp_f32 v127, v127\n\t"
    "v_mfma_f32_16x16x32_bf16 v[84:87], a[168:171], v[20:23], v[84:87]\n\t"
    "s_waitcnt lgkmcnt(6)\n\t"
    "ds_read_b128 a[168:171], v193 offset:13312\n\t"
    "v_cvt_pk_bf16_f32 v159, v126, v127\n\t"
    "v_mfma_f32_16x16x32_bf16 v[88:91], a[172:175], v[40:43], v[88:91]\n\t"
    "v_add_f32 v184, 0x74000000, v194\n\t"
    "v_min_f32 v184, v184, v164\n\t"
    "v_mfma_f32_16x16x32_bf16 v[92:95], a[172:175], v[56:59], v[92:95]\n\t"
    "v_add_f32 v185, 0x73f80000, v194\n\t"
    "v_min_f32 v185, v185, v164\n\t"
    "v_mfma_f32_16x16x32_bf16 v[84:87], a[172:175], v[24:27], v[84:87]\n\t"
    "s_waitcnt lgkmcnt(6)\n\t"
    "ds_read_b128 a[172:175], v193 offset:14336\n\t"
    "v_mfma_f32_16x16x32_bf16 v[88:91], a[176:179], v[44:47], v[88:91]\n\t"
    "v_add_f32 v186, 0x73f00000, v194\n\t"
    "v_min_f32 v186, v186, v164\n\t"
    "v_mfma_f32_16x16x32_bf16 v[92:95], a[176:179], v[60:63], v[92:95]\n\t"
    "v_add_f32 v187, 0x73e80000, v194\n\t"
    "v_min_f32 v187, v187, v164\n\t"
    "v_mfma_f32_16x16x32_bf16 v[84:87], a[176:179], v[28:31], v[84:87]\n\t"
    "s_waitcnt lgkmcnt(6)\n\t"
    "ds_read_b128 a[176:179], v193 offset:15360\n\t"
    "v_mfma_f32_16x16x32_bf16 a[0:3], a[148:151], v[132:135], a[0:3]\n\t"
    "s_and_b32 s38, s52, 0x1ffff\n\t"
    "v_add_f32 v188, 0x73800000, v194\n\t"
    "v_min_f32 v188, v188, v164\n\t"
    "v_mfma_f32_16x16x32_bf16 a[4:7], a[148:151], v[140:143], a[4:7]\n\t"
    "s_add_u32 s52, s52, 0x8000\n\t"
    "v_add_f32 v189, 0x73700000, v194\n\t"
    "v_min_f32 v189, v189, v164\n\t"
    "v_mfma_f32_16x16x32_bf16 a[8:11], a[148:151], v[148:151], a[8:11]\n\t"
    "v_add_f32 v190, 0x73600000, v194\n\t"
    "v_min_f32 v190, v190, v164\n\t"
    "v_mfma_f32_16x16x32_bf16 a[12:15], a[148:151], v[156:159], a[12:15]\n\t"
    "s_waitcnt lgkmcnt(6)\n\t"
    "ds_read_b128 a[148:151], v192 offset:8192\n\t"
    "v_add_u32 v193, s38, v216\n\t"
    "v_mfma_f32_16x16x32_bf16 a[16:19], a[152:155], v[132:135], a[16:19]\n\t"
    "v_add_f32 v191, 0x73500000, v194\n\t"
    "v_min_f32 v191, v191, v164\n\t"
    "v_mfma_f32_16x16x32_bf16 a[20:23], a[152:155], v[140:143], a[20:23]\n\t"
    "v_exp_f32 v68, v68\n\t"
    "v_mfma_f32_16x16x32_bf16 a[24:27], a[152:155], v[148:151], a[24:27]\n\t"
    "v_exp_f32 v69, v69\n\t"
    "v_mfma_f32_16x16x32_bf16 a[28:31], a[152:155], v[156:159], a[28:31]\n\t"
    "s_waitcnt lgkmcnt(6)\n\t"
    "ds_read_b128 a[152:155], v192 offset:9216\n\t"
    "v_cvt_pk_bf16_f32 v136, v68, v69\n\t"
    "v_mfma_f32_16x16x32_bf16 a[32:35], a[156:159], v[132:135], a[32:35]\n\t"
    "v_exp_f32 v70, v70\n\t"
    "v_mfma_f32_16x16x32_bf16 a[36:39], a[156:159], v[140:143], a[36:39]\n\t"
    "v_exp_f32 v71, v71\n\t"
    "v_mfma_f32_16x16x32_bf16 a[40:43], a[156:159], v[148:151], a[40:43]\n\t"
    "v_exp_f32 v72, v72\n\t"
    "v_mfma_f32_16x16x32_bf16 a[44:47], a[156:159], v[156:159], a[44:47]\n\t"
    "s_waitcnt lgkmcnt(6)\n\t"
    "ds_read_b128 a[156:159], v192 offset:10240\n\t"
    "v_cvt_pk_bf16_f32 v137, v70, v71\n\t"
    "v_mfma_f32_16x16x32_bf16 a[48:51], a[160:163], v[132:135], a[48:51]\n\t"
    "v_exp_f32 v73, v73\n\t"
    "v_mfma_f32_16x16x32_bf16 a[52:55], a[160:163], v[140:143], a[52:55]\n\t"
    "v_exp_f32 v74, v74\n\t"
    "v_mfma_f32_16x16x32_bf16 a[56:59], a[160:163], v[148:151], a[56:59]\n\t"
    "v_exp_f32 v75, v75\n\t"
    "v_mfma_f32_16x16x32_bf16 a[60:63], a[160:163], v[156:159], a[60:63]\n\t"
    "s_waitcnt lgkmcnt(6)\n\t"
    "ds_read_b128 a[160:163], v192 offset:11264\n\t"
    "v_cvt_pk_bf16_f32 v144, v72, v73\n\t"
    "v_mfma_f32_16x16x32_bf16 a[64:67], a[164:167], v[132:135], a[64:67]\n\t"
    "v_exp_f32 v76, v76\n\t"
    "v_mfma_f32_16x16x32_bf16 a[68:71], a[164:167], v[140:143], a[68:71]\n\t"
    "v_exp_f32 v77, v77\n\t"
    "v_mfma_f32_16x16x32_bf16 a[72:75], a[164:167], v[148:151], a[72:75]\n\t"
    "v_exp_f32 v78, v78\n\t"
    "v_mfma_f32_16x16x32_bf16 a[76:79], a[164:167], v[156:159], a[76:79]\n\t"
    "s_waitcnt lgkmcnt(6)\n\t"
    "ds_read_b128 a[164:167], v192 offset:12288\n\t"
    "v_cvt_pk_bf16_f32 v145, v74, v75\n\t"
    "v_mfma_f32_16x16x32_bf16 a[80:83], a[168:171], v[132:135], a[80:83]\n\t"
    "v_exp_f32 v79, v79\n\t"
    "v_mfma_f32_16x16x32_bf16 a[84:87], a[168:171], v[140:143], a[84:87]\n\t"
    "v_exp_f32 v84, v84\n\t"
    "v_mfma_f32_16x16x32_bf16 a[88:91], a[168:171], v[148:151], a[88:91]\n\t"
    "v_exp_f32 v85, v85\n\t"
    "v_mfma_f32_16x16x32_bf16 a[92:95], a[168:171], v[156:159], a[92:95]\n\t"
    "s_waitcnt lgkmcnt(6)\n\t"
    "ds_read_b128 a[168:171], v192 offset:13312\n\t"
    "v_cvt_pk_bf16_f32 v152, v76, v77\n\t"
    "v_mfma_f32_16x16x32_bf16 a[96:99], a[172:175], v[132:135], a[96:99]\n\t"
    "v_exp_f32 v86, v86\n\t"
    "v_mfma_f32_16x16x32_bf16 a[100:103], a[172:175], v[140:143], a[100:103]\n\t"
    "v_exp_f32 v87, v87\n\t"
    "v_mfma_f32_16x16x32_bf16 a[104:107], a[172:175], v[148:151], a[104:107]\n\t"
    "v_exp_f32 v88, v88\n\t"
    "v_mfma_f32_16x16x32_bf16 a[108:111], a[172:175], v[156:159], a[108:111]\n\t"
    "s_waitcnt lgkmcnt(6)\n\t"
    "ds_read_b128 a[172:175], v192 offset:14336\n\t"
    "v_cvt_pk_bf16_f32 v153, v78, v79\n\t"
    "v_mfma_f32_16x16x32_bf16 a[112:115], a[176:179], v[132:135], a[112:115]\n\t"
    "v_exp_f32 v89, v89\n\t"
    "v_mfma_f32_16x16x32_bf16 a[116:119], a[176:179], v[140:143], a[116:119]\n\t"
    "v_exp_f32 v90, v90\n\t"
    "v_mfma_f32_16x16x32_bf16 a[120:123], a[176:179], v[148:151], a[120:123]\n\t"
    "v_exp_f32 v91, v91\n\t"
    "v_mfma_f32_16x16x32_bf16 a[124:127], a[176:179], v[156:159], a[124:127]\n\t"
    "ds_read_b128 a[176:179], v192 offset:15360\n\t"
    "v_cvt_pk_bf16_f32 v138, v84, v85\n\t"
    "v_mfma_f32_16x16x32_bf16 a[128:131], a[144:147], v[132:135], a[128:131]\n\t"
    "v_exp_f32 v92, v92\n\t"
    "v_mfma_f32_16x16x32_bf16 a[132:135], a[144:147], v[140:143], a[132:135]\n\t"
    "s_waitcnt vmcnt(8)\n\t"
    "v_exp_f32 v93, v93\n\t"
    "v_mfma_f32_16x16x32_bf16 a[136:139], a[144:147], v[148:151], a[136:139]\n\t"
    "s_barrier\n\t"
    "v_exp_f32 v94, v94\n\t"
    "v_mfma_f32_16x16x32_bf16 a[140:143], a[144:147], v[156:159], a[140:143]\n\t"
    "s_waitcnt lgkmcnt(7)\n\t"
    "v_exp_f32 v95, v95\n\t"
    "v_mfma_f32_16x16x32_bf16 v[104:107], a[148:151], v[32:35], v[168:171]\n\t"
    "s_and_b32 m0, s68, 0x1ffff\n\t"
    "v_cvt_pk_bf16_f32 v139, v86, v87\n\t"
    "v_cvt_pk_bf16_f32 v146, v88, v89\n\t"
    "v_mfma_f32_16x16x32_bf16 v[108:111], a[148:151], v[48:51], v[172:175]\n\t"
    "v_cvt_pk_bf16_f32 v147, v90, v91\n\t"
    "v_cvt_pk_bf16_f32 v154, v92, v93\n\t"
    "v_mfma_f32_16x16x32_bf16 v[100:103], a[148:151], v[16:19], v[184:187]\n\t"
    "s_waitcnt lgkmcnt(6)\n\t"
    "ds_read_b128 a[148:151], v193 offset:0\n\t"
    "buffer_load_dwordx4 v216, s[20:23], s70 offen offset:0 sc1 lds\n\t"
    "v_mfma_f32_16x16x32_bf16 v[104:107], a[152:155], v[36:39], v[104:107]\n\t"
    "s_and_b32 s38, s51, 0x1ffff\n\t"
    "v_cvt_pk_bf16_f32 v155, v94, v95\n\t"
    "v_mfma_f32_16x16x32_bf16 v[108:111], a[152:155], v[52:55], v[108:111]\n\t"
    "s_add_u32 s51, s51, 0x8000\n\t"
    "v_mfma_f32_16x16x32_bf16 v[100:103], a[152:155], v[20:23], v[100:103]\n\t"
    "s_waitcnt lgkmcnt(6)\n\t"
    "ds_read_b128 a[152:155], v193 offset:1024\n\t"
    "v_add_u32 v192, s38, v216\n\t"
    "v_mfma_f32_16x16x32_bf16 v[104:107], a[156:159], v[40:43], v[104:107]\n\t"
    "s_add_u32 s68, s68, 0x8000\n\t"
    "v_mfma_f32_16x16x32_bf16 v[108:111], a[156:159], v[56:59], v[108:111]\n\t"
    "v_mfma_f32_16x16x32_bf16 v[100:103], a[156:159], v[24:27], v[100:103]\n\t"
    "s_waitcnt lgkmcnt(6)\n\t"
    "ds_read_b128 a[156:159], v193 offset:2048\n\t"
    "buffer_load_dwordx4 v216, s[20:23], s70 offen offset:1024 sc1 lds\n\t"
    "v_mfma_f32_16x16x32_bf16 v[104:107], a[160:163], v[44:47], v[104:107]\n\t"
    "v_mfma_f32_16x16x32_bf16 v[108:111], a[160:163], v[60:63], v[108:111]\n\t"
    "v_mfma_f32_16x16x32_bf16 v[100:103], a[160:163], v[28:31], v[100:103]\n\t"
    "s_waitcnt lgkmcnt(6)\n\t"
    "ds_read_b128 a[160:163], v193 offset:3072\n\t"
    "v_mfma_f32_16x16x32_bf16 v[120:123], a[164:167], v[32:35], v[168:171]\n\t"
    "v_mfma_f32_16x16x32_bf16 v[124:127], a[164:167], v[48:51], v[172:175]\n\t"
    "v_mfma_f32_16x16x32_bf16 v[116:119], a[164:167], v[16:19], v[188:191]\n\t"
    "s_waitcnt lgkmcnt(6)\n\t"
    "ds_read_b128 a[164:167], v193 offset:4096\n\t"
    "buffer_load_dwordx4 v216, s[20:23], s70 offen offset:2048 sc1 lds\n\t"
    "v_mfma_f32_16x16x32_bf16 v[120:123], a[168:171], v[36:39], v[120:123]\n\t"
    "v_exp_f32 v104, v104\n\t"
    "v_mfma_f32_16x16x32_bf16 v[124:127], a[168:171], v[52:55], v[124:127]\n\t"
    "v_exp_f32 v100, v100\n\t"
    "v_mfma_f32_16x16x32_bf16 v[116:119], a[168:171], v[20:23], v[116:119]\n\t"
    "s_waitcnt lgkmcnt(6)\n\t"
    "ds_read_b128 a[168:171], v193 offset:5120\n\t"
    "v_mfma_f32_16x16x32_bf16 v[120:123], a[172:175], v[40:43], v[120:123]\n\t"
    "v_exp_f32 v101, v101\n\t"
    "v_mfma_f32_16x16x32_bf16 v[124:127], a[172:175], v[56:59], v[124:127]\n\t"
    "v_exp_f32 v102, v102\n\t"
    "v_mfma_f32_16x16x32_bf16 v[116:119], a[172:175], v[24:27], v[116:119]\n\t"
    "s_waitcnt lgkmcnt(6)\n\t"
    "ds_read_b128 a[172:175], v193 offset:6144\n\t"
    "buffer_load_dwordx4 v216, s[20:23], s70 offen offset:3072 sc1 lds\n\t"
    "v_mfma_f32_16x16x32_bf16 v[120:123], a[176:179], v[44:47], v[120:123]\n\t"
    "s_add_u32 s70, s70, 0x8000\n\t"
    "v_exp_f32 v103, v103\n\t"
    "v_mfma_f32_16x16x32_bf16 v[124:127], a[176:179], v[60:63], v[124:127]\n\t"
    "s_and_b32 m0, s69, 0x1ffff\n\t"
    "v_exp_f32 v105, v105\n\t"
    "v_mfma_f32_16x16x32_bf16 v[116:119], a[176:179], v[28:31], v[116:119]\n\t"
    "s_waitcnt lgkmcnt(6)\n\t"
    "ds_read_b128 a[176:179], v193 offset:7168\n\t"
    "v_cvt_pk_bf16_f32 v140, v100, v101\n\t"
    "v_mfma_f32_16x16x32_bf16 a[4:7], a[148:151], v[136:139], a[4:7]\n\t"
    "v_exp_f32 v106, v106\n\t"
    "v_mfma_f32_16x16x32_bf16 a[8:11], a[148:151], v[144:147], a[8:11]\n\t"
    "v_exp_f32 v107, v107\n\t"
    "v_mfma_f32_16x16x32_bf16 a[12:15], a[148:151], v[152:155], a[12:15]\n\t"
    "s_waitcnt lgkmcnt(6)\n\t"
    "ds_read_b128 a[148:151], v193 offset:8192\n\t"
    "buffer_load_dwordx4 v216, s[20:23], s71 offen offset:0 sc1 lds\n\t"
    "v_mfma_f32_16x16x32_bf16 a[20:23], a[152:155], v[136:139], a[20:23]\n\t"
    "s_add_u32 s69, s69, 0x8000\n\t"
    "v_exp_f32 v108, v108\n\t"
    "v_mfma_f32_16x16x32_bf16 a[24:27], a[152:155], v[144:147], a[24:27]\n\t"
    "v_exp_f32 v109, v109\n\t"
    "v_mfma_f32_16x16x32_bf16 a[28:31], a[152:155], v[152:155], a[28:31]\n\t"
    "s_waitcnt lgkmcnt(6)\n\t"
    "ds_read_b128 a[152:155], v193 offset:9216\n\t"
    "v_cvt_pk_bf16_f32 v141, v102, v103\n\t"
    "v_mfma_f32_16x16x32_bf16 a[36:39], a[156:159], v[136:139], a[36:39]\n\t"
    "v_exp_f32 v110, v110\n\t"
    "v_mfma_f32_16x16x32_bf16 a[40:43], a[156:159], v[144:147], a[40:43]\n\t"
    "v_exp_f32 v111, v111\n\t"
    "v_mfma_f32_16x16x32_bf16 a[44:47], a[156:159], v[152:155], a[44:47]\n\t"
    "s_waitcnt lgkmcnt(6)\n\t"
    "ds_read_b128 a[156:159], v193 offset:10240\n\t"
    "buffer_load_dwordx4 v216, s[20:23], s71 offen offset:1024 sc1 lds\n\t"
    "v_mfma_f32_16x16x32_bf16 a[52:55], a[160:163], v[136:139], a[52:55]\n\t"
    "v_cvt_pk_bf16_f32 v148, v104, v105\n\t"
    "v_cvt_pk_bf16_f32 v149, v106, v107\n\t"
    "v_mfma_f32_16x16x32_bf16 a[56:59], a[160:163], v[144:147], a[56:59]\n\t"
    "v_cvt_pk_bf16_f32 v156, v108, v109\n\t"
    "v_cvt_pk_bf16_f32 v157, v110, v111\n\t"
    "v_mfma_f32_16x16x32_bf16 a[60:63], a[160:163], v[152:155], a[60:63]\n\t"
    "s_waitcnt lgkmcnt(6)\n\t"
    "ds_read_b128 a[160:163], v193 offset:11264\n\t"
    "v_mfma_f32_16x16x32_bf16 a[68:71], a[164:167], v[136:139], a[68:71]\n\t"
    "v_exp_f32 v116, v116\n\t"
    "v_mfma_f32_16x16x32_bf16 a[72:75], a[164:167], v[144:147], a[72:75]\n\t"
    "v_exp_f32 v117, v117\n\t"
    "v_mfma_f32_16x16x32_bf16 a[76:79], a[164:167], v[152:155], a[76:79]\n\t"
    "s_waitcnt lgkmcnt(6)\n\t"
    "ds_read_b128 a[164:167], v193 offset:12288\n\t"
    "buffer_load_dwordx4 v216, s[20:23], s71 offen offset:2048 sc1 lds\n\t"
    "v_mfma_f32_16x16x32_bf16 a[84:87], a[168:171], v[136:139], a[84:87]\n\t"
    "v_exp_f32 v118, v118\n\t"
    "v_mfma_f32_16x16x32_bf16 a[88:91], a[168:171], v[144:147], a[88:91]\n\t"
    "v_exp_f32 v119, v119\n\t"
    "v_mfma_f32_16x16x32_bf16 a[92:95], a[168:171], v[152:155], a[92:95]\n\t"
    "s_waitcnt lgkmcnt(6)\n\t"
    "ds_read_b128 a[168:171], v193 offset:13312\n\t"
    "v_cvt_pk_bf16_f32 v142, v116, v117\n\t"
    "v_mfma_f32_16x16x32_bf16 a[100:103], a[172:175], v[136:139], a[100:103]\n\t"
    "v_exp_f32 v120, v120\n\t"
    "v_mfma_f32_16x16x32_bf16 a[104:107], a[172:175], v[144:147], a[104:107]\n\t"
    "v_exp_f32 v121, v121\n\t"
    "v_mfma_f32_16x16x32_bf16 a[108:111], a[172:175], v[152:155], a[108:111]\n\t"
    "s_waitcnt lgkmcnt(6)\n\t"
    "ds_read_b128 a[172:175], v193 offset:14336\n\t"
    "buffer_load_dwordx4 v216, s[20:23], s71 offen offset:3072 sc1 lds\n\t"
    "v_mfma_f32_16x16x32_bf16 a[116:119], a[176:179], v[136:139], a[116:119]\n\t"
    "s_add_u32 s71, s71, 0x8000\n\t"
    "v_exp_f32 v122, v122\n\t"
    "v_mfma_f32_16x16x32_bf16 a[120:123], a[176:179], v[144:147], a[120:123]\n\t"
    "v_exp_f32 v123, v123\n\t"
    "v_mfma_f32_16x16x32_bf16 a[124:127], a[176:179], v[152:155], a[124:127]\n\t"
    "s_add_u32 s45, s45, 1\n\t"
    "ds_read_b128 a[176:179], v193 offset:15360\n\t"
    "v_cvt_pk_bf16_f32 v143, v118, v119\n\t"
    "v_mfma_f32_16x16x32_bf16 a[132:135], a[144:147], v[136:139], a[132:135]\n\t"
    "v_exp_f32 v124, v124\n\t"
    "v_mfma_f32_16x16x32_bf16 a[136:139], a[144:147], v[144:147], a[136:139]\n\t"
    "v_exp_f32 v125, v125\n\t"
    "v_mfma_f32_16x16x32_bf16 a[140:143], a[144:147], v[152:155], a[140:143]\n\t"
    "v_exp_f32 v126, v126\n\t"
    "v_exp_f32 v127, v127\n\t"
    "s_nop 0\n\t"
    "v_cvt_pk_bf16_f32 v150, v120, v121\n\t"
    "v_cvt_pk_bf16_f32 v151, v122, v123\n\t"
    "v_cvt_pk_bf16_f32 v158, v124, v125\n\t"
    "v_cvt_pk_bf16_f32 v159, v126, v127\n\t"
    "s_waitcnt lgkmcnt(7)\n\t"
    "v_mfma_f32_16x16x32_bf16 a[4:7], a[148:151], v[140:143], a[4:7]\n\t"
    "v_mfma_f32_16x16x32_bf16 a[8:11], a[148:151], v[148:151], a[8:11]\n\t"
    "v_mfma_f32_16x16x32_bf16 a[12:15], a[148:151], v[156:159], a[12:15]\n\t"
    "s_waitcnt lgkmcnt(6)\n\t"
    "ds_read_b128 a[148:151], v192 offset:0\n\t"
    "v_mfma_f32_16x16x32_bf16 a[20:23], a[152:155], v[140:143], a[20:23]\n\t"
    "v_mfma_f32_16x16x32_bf16 a[24:27], a[152:155], v[148:151], a[24:27]\n\t"
    "v_mfma_f32_16x16x32_bf16 a[28:31], a[152:155], v[156:159], a[28:31]\n\t"
    "s_waitcnt lgkmcnt(6)\n\t"
    "ds_read_b128 a[152:155], v192 offset:1024\n\t"
    "v_mfma_f32_16x16x32_bf16 a[36:39], a[156:159], v[140:143], a[36:39]\n\t"
    "v_mfma_f32_16x16x32_bf16 a[40:43], a[156:159], v[148:151], a[40:43]\n\t"
    "v_mfma_f32_16x16x32_bf16 a[44:47], a[156:159], v[156:159], a[44:47]\n\t"
    "s_waitcnt lgkmcnt(6)\n\t"
    "ds_read_b128 a[156:159], v192 offset:2048\n\t"
    "v_mfma_f32_16x16x32_bf16 a[52:55], a[160:163], v[140:143], a[52:55]\n\t"
    "v_mfma_f32_16x16x32_bf16 a[56:59], a[160:163], v[148:151], a[56:59]\n\t"
    "v_mfma_f32_16x16x32_bf16 a[60:63], a[160:163], v[156:159], a[60:63]\n\t"
    "s_waitcnt lgkmcnt(6)\n\t"
    "ds_read_b128 a[160:163], v192 offset:3072\n\t"
    "v_mfma_f32_16x16x32_bf16 a[68:71], a[164:167], v[140:143], a[68:71]\n\t"
    "v_mfma_f32_16x16x32_bf16 a[72:75], a[164:167], v[148:151], a[72:75]\n\t"
    "v_mfma_f32_16x16x32_bf16 a[76:79], a[164:167], v[156:159], a[76:79]\n\t"
    "s_waitcnt lgkmcnt(6)\n\t"
    "ds_read_b128 a[164:167], v192 offset:4096\n\t"
    "v_mfma_f32_16x16x32_bf16 a[84:87], a[168:171], v[140:143], a[84:87]\n\t"
    "v_mfma_f32_16x16x32_bf16 a[88:91], a[168:171], v[148:151], a[88:91]\n\t"
    "v_mfma_f32_16x16x32_bf16 a[92:95], a[168:171], v[156:159], a[92:95]\n\t"
    "s_waitcnt lgkmcnt(6)\n\t"
    "ds_read_b128 a[168:171], v192 offset:5120\n\t"
    "v_mfma_f32_16x16x32_bf16 a[100:103], a[172:175], v[140:143], a[100:103]\n\t"
    "v_mfma_f32_16x16x32_bf16 a[104:107], a[172:175], v[148:151], a[104:107]\n\t"
    "v_mfma_f32_16x16x32_bf16 a[108:111], a[172:175], v[156:159], a[108:111]\n\t"
    "s_waitcnt lgkmcnt(6)\n\t"
    "ds_read_b128 a[172:175], v192 offset:6144\n\t"
    "v_mfma_f32_16x16x32_bf16 a[116:119], a[176:179], v[140:143], a[116:119]\n\t"
    "v_mfma_f32_16x16x32_bf16 a[120:123], a[176:179], v[148:151], a[120:123]\n\t"
    "v_mfma_f32_16x16x32_bf16 a[124:127], a[176:179], v[156:159], a[124:127]\n\t"
    "ds_read_b128 a[176:179], v192 offset:7168\n\t"
    "v_mfma_f32_16x16x32_bf16 a[132:135], a[144:147], v[140:143], a[132:135]\n\t"
    "v_mfma_f32_16x16x32_bf16 a[136:139], a[144:147], v[148:151], a[136:139]\n\t"
    "v_mfma_f32_16x16x32_bf16 a[140:143], a[144:147], v[156:159], a[140:143]\n\t"
    "s_nop 7\n\t"
    "v_accvgpr_read_b32 v250, a128\n\t"
    "v_cmp_nle_f32 vcc, v250, v197\n\t"
    "s_or_b64 s[78:79], s[78:79], vcc\n\t"
    "v_rcp_f32 v250, v250\n\t"
    "v_accvgpr_read_b32 v218, a0\n\t"
    "v_accvgpr_read_b32 v219, a1\n\t"
    "v_accvgpr_read_b32 v220, a2\n\t"
    "v_accvgpr_read_b32 v221, a3\n\t"
    "v_accvgpr_read_b32 v222, a16\n\t"
    "v_accvgpr_read_b32 v223, a17\n\t"
    "v_accvgpr_read_b32 v224, a18\n\t"
    "v_accvgpr_read_b32 v225, a19\n\t"
    "v_mul_f32 v218, v218, v250\n\t"
    "v_mul_f32 v219, v219, v250\n\t"
    "v_mul_f32 v220, v220, v250\n\t"
    "v_mul_f32 v221, v221, v250\n\t"
    "v_mul_f32 v222, v222, v250\n\t"
    "v_mul_f32 v223, v223, v250\n\t"
    "v_mul_f32 v224, v224, v250\n\t"
    "v_mul_f32 v225, v225, v250\n\t"
    "v_cvt_pk_bf16_f32 v226, v218, v219\n\t"
    "v_cvt_pk_bf16_f32 v227, v220, v221\n\t"
    "v_cvt_pk_bf16_f32 v228, v222, v223\n\t"
    "v_cvt_pk_bf16_f32 v229, v224, v225\n\t"
    "s_add_u32 s39, s66, 0x0\n\t"
    "buffer_store_dwordx4 v[226:229], v216, s[24:27], s39 offen offset:0 sc1\n\t"
    "v_accvgpr_read_b32 v230, a32\n\t"
    "v_accvgpr_read_b32 v231, a33\n\t"
    "v_accvgpr_read_b32 v232, a34\n\t"
    "v_accvgpr_read_b32 v233, a35\n\t"
    "v_accvgpr_read_b32 v234, a48\n\t"
    "v_accvgpr_read_b32 v235, a49\n\t"
    "v_accvgpr_read_b32 v236, a50\n\t"
    "v_accvgpr_read_b32 v237, a51\n\t"
    "v_mul_f32 v230, v230, v250\n\t"
    "v_mul_f32 v231, v231, v250\n\t"
    "v_mul_f32 v232, v232, v250\n\t"
    "v_mul_f32 v233, v233, v250\n\t"
    "v_mul_f32 v234, v234, v250\n\t"
    "v_mul_f32 v235, v235, v250\n\t"
    "v_mul_f32 v236, v236, v250\n\t"
    "v_mul_f32 v237, v237, v250\n\t"
    "v_cvt_pk_bf16_f32 v238, v230, v231\n\t"
    "v_cvt_pk_bf16_f32 v239, v232, v233\n\t"
    "v_cvt_pk_bf16_f32 v240, v234, v235\n\t"
    "v_cvt_pk_bf16_f32 v241, v236, v237\n\t"
    "buffer_store_dwordx4 v[238:241], v216, s[24:27], s39 offen offset:1024 sc1\n\t"
    "v_accvgpr_read_b32 v218, a64\n\t"
    "v_accvgpr_read_b32 v219, a65\n\t"
    "v_accvgpr_read_b32 v220, a66\n\t"
    "v_accvgpr_read_b32 v221, a67\n\t"
    "v_accvgpr_read_b32 v222, a80\n\t"
    "v_accvgpr_read_b32 v223, a81\n\t"
    "v_accvgpr_read_b32 v224, a82\n\t"
    "v_accvgpr_read_b32 v225, a83\n\t"
    "v_mul_f32 v218, v218, v250\n\t"
    "v_mul_f32 v219, v219, v250\n\t"
    "v_mul_f32 v220, v220, v250\n\t"
    "v_mul_f32 v221, v221, v250\n\t"
    "v_mul_f32 v222, v222, v250\n\t"
    "v_mul_f32 v223, v223, v250\n\t"
    "v_mul_f32 v224, v224, v250\n\t"
    "v_mul_f32 v225, v225, v250\n\t"
    "v_cvt_pk_bf16_f32 v226, v218, v219\n\t"
    "v_cvt_pk_bf16_f32 v227, v220, v221\n\t"
    "v_cvt_pk_bf16_f32 v228, v222, v223\n\t"
    "v_cvt_pk_bf16_f32 v229, v224, v225\n\t"
    "buffer_store_dwordx4 v[226:229], v216, s[24:27], s39 offen offset:2048 sc1\n\t"
    "v_accvgpr_read_b32 v230, a96\n\t"
    "v_accvgpr_read_b32 v231, a97\n\t"
    "v_accvgpr_read_b32 v232, a98\n\t"
    "v_accvgpr_read_b32 v233, a99\n\t"
    "v_accvgpr_read_b32 v234, a112\n\t"
    "v_accvgpr_read_b32 v235, a113\n\t"
    "v_accvgpr_read_b32 v236, a114\n\t"
    "v_accvgpr_read_b32 v237, a115\n\t"
    "v_mul_f32 v230, v230, v250\n\t"
    "v_mul_f32 v231, v231, v250\n\t"
    "v_mul_f32 v232, v232, v250\n\t"
    "v_mul_f32 v233, v233, v250\n\t"
    "v_mul_f32 v234, v234, v250\n\t"
    "v_mul_f32 v235, v235, v250\n\t"
    "v_mul_f32 v236, v236, v250\n\t"
    "v_mul_f32 v237, v237, v250\n\t"
    "v_cvt_pk_bf16_f32 v238, v230, v231\n\t"
    "v_cvt_pk_bf16_f32 v239, v232, v233\n\t"
    "v_cvt_pk_bf16_f32 v240, v234, v235\n\t"
    "v_cvt_pk_bf16_f32 v241, v236, v237\n\t"
    "buffer_store_dwordx4 v[238:241], v216, s[24:27], s39 offen offset:3072 sc1\n\t"
    "v_accvgpr_read_b32 v251, a132\n\t"
    "v_cmp_nle_f32 vcc, v251, v197\n\t"
    "s_or_b64 s[78:79], s[78:79], vcc\n\t"
    "v_rcp_f32 v251, v251\n\t"
    "v_accvgpr_read_b32 v218, a4\n\t"
    "v_accvgpr_read_b32 v219, a5\n\t"
    "v_accvgpr_read_b32 v220, a6\n\t"
    "v_accvgpr_read_b32 v221, a7\n\t"
    "v_accvgpr_read_b32 v222, a20\n\t"
    "v_accvgpr_read_b32 v223, a21\n\t"
    "v_accvgpr_read_b32 v224, a22\n\t"
    "v_accvgpr_read_b32 v225, a23\n\t"
    "v_mul_f32 v218, v218, v251\n\t"
    "v_mul_f32 v219, v219, v251\n\t"
    "v_mul_f32 v220, v220, v251\n\t"
    "v_mul_f32 v221, v221, v251\n\t"
    "v_mul_f32 v222, v222, v251\n\t"
    "v_mul_f32 v223, v223, v251\n\t"
    "v_mul_f32 v224, v224, v251\n\t"
    "v_mul_f32 v225, v225, v251\n\t"
    "v_cvt_pk_bf16_f32 v226, v218, v219\n\t"
    "v_cvt_pk_bf16_f32 v227, v220, v221\n\t"
    "v_cvt_pk_bf16_f32 v228, v222, v223\n\t"
    "v_cvt_pk_bf16_f32 v229, v224, v225\n\t"
    "s_add_u32 s39, s66, 0x80000\n\t"
    "buffer_store_dwordx4 v[226:229], v216, s[24:27], s39 offen offset:0 sc1\n\t"
    "v_accvgpr_read_b32 v230, a36\n\t"
    "v_accvgpr_read_b32 v231, a37\n\t"
    "v_accvgpr_read_b32 v232, a38\n\t"
    "v_accvgpr_read_b32 v233, a39\n\t"
    "v_accvgpr_read_b32 v234, a52\n\t"
    "v_accvgpr_read_b32 v235, a53\n\t"
    "v_accvgpr_read_b32 v236, a54\n\t"
    "v_accvgpr_read_b32 v237, a55\n\t"
    "v_mul_f32 v230, v230, v251\n\t"
    "v_mul_f32 v231, v231, v251\n\t"
    "v_mul_f32 v232, v232, v251\n\t"
    "v_mul_f32 v233, v233, v251\n\t"
    "v_mul_f32 v234, v234, v251\n\t"
    "v_mul_f32 v235, v235, v251\n\t"
    "v_mul_f32 v236, v236, v251\n\t"
    "v_mul_f32 v237, v237, v251\n\t"
    "v_cvt_pk_bf16_f32 v238, v230, v231\n\t"
    "v_cvt_pk_bf16_f32 v239, v232, v233\n\t"
    "v_cvt_pk_bf16_f32 v240, v234, v235\n\t"
    "v_cvt_pk_bf16_f32 v241, v236, v237\n\t"
    "buffer_store_dwordx4 v[238:241], v216, s[24:27], s39 offen offset:1024 sc1\n\t"
    "v_accvgpr_read_b32 v218, a68\n\t"
    "v_accvgpr_read_b32 v219, a69\n\t"
    "v_accvgpr_read_b32 v220, a70\n\t"
    "v_accvgpr_read_b32 v221, a71\n\t"
    "v_accvgpr_read_b32 v222, a84\n\t"
    "v_accvgpr_read_b32 v223, a85\n\t"
    "v_accvgpr_read_b32 v224, a86\n\t"
    "v_accvgpr_read_b32 v225, a87\n\t"
    "v_mul_f32 v218, v218, v251\n\t"
    "v_mul_f32 v219, v219, v251\n\t"
    "v_mul_f32 v220, v220, v251\n\t"
    "v_mul_f32 v221, v221, v251\n\t"
    "v_mul_f32 v222, v222, v251\n\t"
    "v_mul_f32 v223, v223, v251\n\t"
    "v_mul_f32 v224, v224, v251\n\t"
    "v_mul_f32 v225, v225, v251\n\t"
    "v_cvt_pk_bf16_f32 v226, v218, v219\n\t"
    "v_cvt_pk_bf16_f32 v227, v220, v221\n\t"
    "v_cvt_pk_bf16_f32 v228, v222, v223\n\t"
    "v_cvt_pk_bf16_f32 v229, v224, v225\n\t"
    "buffer_store_dwordx4 v[226:229], v216, s[24:27], s39 offen offset:2048 sc1\n\t"
    "v_accvgpr_read_b32 v230, a100\n\t"
    "v_accvgpr_read_b32 v231, a101\n\t"
    "v_accvgpr_read_b32 v232, a102\n\t"
    "v_accvgpr_read_b32 v233, a103\n\t"
    "v_accvgpr_read_b32 v234, a116\n\t"
    "v_accvgpr_read_b32 v235, a117\n\t"
    "v_accvgpr_read_b32 v236, a118\n\t"
    "v_accvgpr_read_b32 v237, a119\n\t"
    "v_mul_f32 v230, v230, v251\n\t"
    "v_mul_f32 v231, v231, v251\n\t"
    "v_mul_f32 v232, v232, v251\n\t"
    "v_mul_f32 v233, v233, v251\n\t"
    "v_mul_f32 v234, v234, v251\n\t"
    "v_mul_f32 v235, v235, v251\n\t"
    "v_mul_f32 v236, v236, v251\n\t"
    "v_mul_f32 v237, v237, v251\n\t"
    "v_cvt_pk_bf16_f32 v238, v230, v231\n\t"
    "v_cvt_pk_bf16_f32 v239, v232, v233\n\t"
    "v_cvt_pk_bf16_f32 v240, v234, v235\n\t"
    "v_cvt_pk_bf16_f32 v241, v236, v237\n\t"
    "buffer_store_dwordx4 v[238:241], v216, s[24:27], s39 offen offset:3072 sc1\n\t"
    "s_lshl_b32 s37, s44, 4\n\t"
    "s_lshl_b32 s38, s45, 6\n\t"
    "s_sub_u32 s46, s37, s38\n\t"
    "v_cvt_f32_i32 v194, s46\n\t"
    "v_fma_f32 v194, v194, v196, v195\n\t"
    "v_mov_b32 v176, v194\n\t"
    "v_min_f32 v176, v176, v168\n\t"
    "v_add_f32 v177, 0xf1800000, v194\n\t"
    "v_min_f32 v177, v177, v168\n\t"
    "v_add_f32 v178, 0xf2000000, v194\n\t"
    "v_min_f32 v178, v178, v168\n\t"
    "v_add_f32 v179, 0xf2400000, v194\n\t"
    "v_min_f32 v179, v179, v168\n\t"
    "s_waitcnt lgkmcnt(7)\n\t"
    "v_mfma_f32_16x16x32_bf16 v[76:79], a[148:151], v[48:51], v[172:175]\n\t"
    "s_and_b32 s38, s52, 0x1ffff\n\t"
    "v_add_f32 v180, 0xf3800000, v194\n\t"
    "v_min_f32 v180, v180, v168\n\t"
    "v_mfma_f32_16x16x32_bf16 v[72:75], a[148:151], v[32:35], v[176:179]\n\t"
    "s_waitcnt lgkmcnt(6)\n\t"
    "ds_read_b128 a[148:151], v192 offset:8192\n\t"
    "v_add_u32 v193, s38, v216\n\t"
    "v_mfma_f32_16x16x32_bf16 v[76:79], a[152:155], v[52:55], v[76:79]\n\t"
    "s_add_u32 s52, s52, 0x8000\n\t"
    "v_add_f32 v181, 0xf3880000, v194\n\t"
    "v_min_f32 v181, v181, v168\n\t"
    "v_mfma_f32_16x16x32_bf16 v[72:75], a[152:155], v[36:39], v[72:75]\n\t"
    "s_waitcnt lgkmcnt(6)\n\t"
    "ds_read_b128 a[152:155], v192 offset:9216\n\t"
    "v_mfma_f32_16x16x32_bf16 v[76:79], a[156:159], v[56:59], v[76:79]\n\t"
    "v_add_f32 v182, 0xf3900000, v194\n\t"
    "v_min_f32 v182, v182, v168\n\t"
    "v_mfma_f32_16x16x32_bf16 v[72:75], a[156:159], v[40:43], v[72:75]\n\t"
    "s_waitcnt lgkmcnt(6)\n\t"
    "ds_read_b128 a[156:159], v192 offset:10240\n\t"
    "v_mfma_f32_16x16x32_bf16 v[76:79], a[160:163], v[60:63], v[76:79]\n\t"
    "v_add_f32 v183, 0xf3980000, v194\n\t"
    "v_min_f32 v183, v183, v168\n\t"
    "v_mfma_f32_16x16x32_bf16 v[72:75], a[160:163], v[44:47], v[72:75]\n\t"
    "s_waitcnt lgkmcnt(6)\n\t"
    "ds_read_b128 a[160:163], v192 offset:11264\n\t"
    "v_mfma_f32_16x16x32_bf16 v[92:95], a[164:167], v[48:51], v[172:175]\n\t"
    "v_add_f32 v184, 0xf4000000, v194\n\t"
    "v_min_f32 v184, v184, v168\n\t"
    "v_mfma_f32_16x16x32_bf16 v[88:91], a[164:167], v[32:35], v[180:183]\n\t"
    "s_waitcnt lgkmcnt(6)\n\t"
    "ds_read_b128 a[164:167], v192 offset:12288\n\t"
    "v_mfma_f32_16x16x32_bf16 v[92:95], a[168:171], v[52:55], v[92:95]\n\t"
    "v_add_f32 v185, 0xf4040000, v194\n\t"
    "v_min_f32 v185, v185, v168\n\t"
    "v_mfma_f32_16x16x32_bf16 v[88:91], a[168:171], v[36:39], v[88:91]\n\t"
    "s_waitcnt lgkmcnt(6)\n\t"
    "ds_read_b128 a[168:171], v192 offset:13312\n\t"
    "v_mfma_f32_16x16x32_bf16 v[92:95], a[172:175], v[56:59], v[92:95]\n\t"
    "s_waitcnt vmcnt(16)\n\t"
    "v_add_f32 v186, 0xf4080000, v194\n\t"
    "v_min_f32 v186, v186, v168\n\t"
    "v_mfma_f32_16x16x32_bf16 v[88:91], a[172:175], v[40:43], v[88:91]\n\t"
    "s_waitcnt lgkmcnt(6)\n\t"
    "ds_read_b128 a[172:175], v192 offset:14336\n\t"
    "v_mfma_f32_16x16x32_bf16 v[92:95], a[176:179], v[60:63], v[92:95]\n\t"
    "s_barrier\n\t"
    "v_add_f32 v187, 0xf40c0000, v194\n\t"
    "v_min_f32 v187, v187, v168\n\t"
    "v_mfma_f32_16x16x32_bf16 v[88:91], a[176:179], v[44:47], v[88:91]\n\t"
    "s_waitcnt lgkmcnt(6)\n\t"
    "ds_read_b128 a[176:179], v192 offset:15360\n\t"
    "v_mfma_f32_16x16x32_bf16 v[108:111], a[148:151], v[48:51], v[172:175]\n\t"
    "s_and_b32 m0, s68, 0x1ffff\n\t"
    "v_add_f32 v188, 0xf4400000, v194\n\t"
    "v_min_f32 v188, v188, v168\n\t"
    "v_mfma_f32_16x16x32_bf16 v[104:107], a[148:151], v[32:35], v[184:187]\n\t"
    "s_waitcnt lgkmcnt(6)\n\t"
    "ds_read_b128 a[148:151], v193 offset:0\n\t"
    "buffer_load_dwordx4 v216, s[20:23], s70 offen offset:0 sc1 lds\n\t"
    "v_mfma_f32_16x16x32_bf16 v[108:111], a[152:155], v[52:55], v[108:111]\n\t"
    "s_and_b32 s38, s51, 0x1ffff\n\t"
    "v_add_f32 v189, 0xf4440000, v194\n\t"
    "v_min_f32 v189, v189, v168\n\t"
    "v_mfma_f32_16x16x32_bf16 v[104:107], a[152:155], v[36:39], v[104:107]\n\t"
    "s_waitcnt lgkmcnt(6)\n\t"
    "ds_read_b128 a[152:155], v193 offset:1024\n\t"
    "v_add_u32 v192, s38, v216\n\t"
    "v_mfma_f32_16x16x32_bf16 v[108:111], a[156:159], v[56:59], v[108:111]\n\t"
    "s_add_u32 s51, s51, 0x8000\n\t"
    "v_add_f32 v190, 0xf4480000, v194\n\t"
    "v_min_f32 v190, v190, v168\n\t"
    "v_mfma_f32_16x16x32_bf16 v[104:107], a[156:159], v[40:43], v[104:107]\n\t"
    "s_waitcnt lgkmcnt(6)\n\t"
    "ds_read_b128 a[156:159], v193 offset:2048\n\t"
    "buffer_load_dwordx4 v216, s[20:23], s70 offen offset:1024 sc1 lds\n\t"
    "v_mfma_f32_16x16x32_bf16 v[108:111], a[160:163], v[60:63], v[108:111]\n\t"
    "s_add_u32 s68, s68, 0x8000\n\t"
    "v_add_f32 v191, 0xf44c0000, v194\n\t"
    "v_min_f32 v191, v191, v168\n\t"
    "v_mfma_f32_16x16x32_bf16 v[104:107], a[160:163], v[44:47], v[104:107]\n\t"
    "s_waitcnt lgkmcnt(6)\n\t"
    "ds_read_b128 a[160:163], v193 offset:3072\n\t"
    "v_mfma_f32_16x16x32_bf16 v[124:127], a[164:167], v[48:51], v[172:175]\n\t"
    "v_exp_f32 v72, v72\n\t"
    "v_mfma_f32_16x16x32_bf16 v[120:123], a[164:167], v[32:35], v[188:191]\n\t"
    "s_waitcnt lgkmcnt(6)\n\t"
    "ds_read_b128 a[164:167], v193 offset:4096\n\t"
    "buffer_load_dwordx4 v216, s[20:23], s70 offen offset:2048 sc1 lds\n\t"
    "v_mfma_f32_16x16x32_bf16 v[124:127], a[168:171], v[52:55], v[124:127]\n\t"
    "v_exp_f32 v73, v73\n\t"
    "v_mfma_f32_16x16x32_bf16 v[120:123], a[168:171], v[36:39], v[120:123]\n\t"
    "s_waitcnt lgkmcnt(6)\n\t"
    "ds_read_b128 a[168:171], v193 offset:5120\n\t"
    "v_cvt_pk_bf16_f32 v144, v72, v73\n\t"
    "v_mfma_f32_16x16x32_bf16 v[124:127], a[172:175], v[56:59], v[124:127]\n\t"
    "v_exp_f32 v74, v74\n\t"
    "v_mfma_f32_16x16x32_bf16 v[120:123], a[172:175], v[40:43], v[120:123]\n\t"
    "s_waitcnt lgkmcnt(6)\n\t"
    "ds_read_b128 a[172:175], v193 offset:6144\n\t"
    "buffer_load_dwordx4 v216, s[20:23], s70 offen offset:3072 sc1 lds\n\t"
    "v_exp_f32 v75, v75\n\t"
    "v_exp_f32 v76, v76\n\t"
    "v_exp_f32 v77, v77\n\t"
    "v_exp_f32 v78, v78\n\t"
    "v_exp_f32 v79, v79\n\t"
    "v_exp_f32 v88, v88\n\t"
    "v_exp_f32 v89, v89\n\t"
    "v_exp_f32 v90, v90\n\t"
    "v_exp_f32 v91, v91\n\t"
    "v_exp_f32 v92, v92\n\t"
    "v_exp_f32 v93, v93\n\t"
    "v_exp_f32 v94, v94\n\t"
    "v_exp_f32 v95, v95\n\t"
    "v_mfma_f32_16x16x32_bf16 v[124:127], a[176:179], v[60:63], v[124:127]\n\t"
    "s_add_u32 s70, s70, 0x8000\n\t"
    "v_cvt_pk_bf16_f32 v145, v74, v75\n\t"
    "v_cvt_pk_bf16_f32 v152, v76, v77\n\t"
    "v_cvt_pk_bf16_f32 v153, v78, v79\n\t"
    "v_cvt_pk_bf16_f32 v146, v88, v89\n\t"
    "v_cvt_pk_bf16_f32 v147, v90, v91\n\t"
    "v_cvt_pk_bf16_f32 v154, v92, v93\n\t"
    "v_cvt_pk_bf16_f32 v155, v94, v95\n\t"
    "v_mfma_f32_16x16x32_bf16 v[120:123], a[176:179], v[44:47], v[120:123]\n\t"
    "s_waitcnt lgkmcnt(6)\n\t"
    "ds_read_b128 a[176:179], v193 offset:7168\n\t"
    "v_mfma_f32_16x16x32_bf16 a[8:11], a[148:151], v[144:147], a[8:11]\n\t"
    "s_and_b32 m0, s69, 0x1ffff\n\t"
    "v_exp_f32 v104, v104\n\t"
    "v_mfma_f32_16x16x32_bf16 a[12:15], a[148:151], v[152:155], a[12:15]\n\t"
    "s_waitcnt lgkmcnt(6)\n\t"
    "ds_read_b128 a[148:151], v192 offset:0\n\t"
    "buffer_load_dwordx4 v216, s[20:23], s71 offen offset:0 sc1 lds\n\t"
    "v_mfma_f32_16x16x32_bf16 a[24:27], a[152:155], v[144:147], a[24:27]\n\t"
    "s_add_u32 s69, s69, 0x8000\n\t"
    "v_exp_f32 v105, v105\n\t"
    "v_mfma_f32_16x16x32_bf16 a[28:31], a[152:155], v[152:155], a[28:31]\n\t"
    "s_waitcnt lgkmcnt(6)\n\t"
    "ds_read_b128 a[152:155], v192 offset:1024\n\t"
    "v_cvt_pk_bf16_f32 v148, v104, v105\n\t"
    "v_mfma_f32_16x16x32_bf16 a[40:43], a[156:159], v[144:147], a[40:43]\n\t"
    "v_exp_f32 v106, v106\n\t"
    "v_mfma_f32_16x16x32_bf16 a[44:47], a[156:159], v[152:155], a[44:47]\n\t"
    "s_waitcnt lgkmcnt(6)\n\t"
    "ds_read_b128 a[156:159], v192 offset:2048\n\t"
    "buffer_load_dwordx4 v216, s[20:23], s71 offen offset:1024 sc1 lds\n\t"
    "v_mfma_f32_16x16x32_bf16 a[56:59], a[160:163], v[144:147], a[56:59]\n\t"
    "v_exp_f32 v107, v107\n\t"
    "v_mfma_f32_16x16x32_bf16 a[60:63], a[160:163], v[152:155], a[60:63]\n\t"
    "s_waitcnt lgkmcnt(6)\n\t"
    "ds_read_b128 a[160:163], v192 offset:3072\n\t"
    "v_cvt_pk_bf16_f32 v149, v106, v107\n\t"
    "v_mfma_f32_16x16x32_bf16 a[72:75], a[164:167], v[144:147], a[72:75]\n\t"
    "v_exp_f32 v108, v108\n\t"
    "v_mfma_f32_16x16x32_bf16 a[76:79], a[164:167], v[152:155], a[76:79]\n\t"
    "s_waitcnt lgkmcnt(6)\n\t"
    "ds_read_b128 a[164:167], v192 offset:4096\n\t"
    "buffer_load_dwordx4 v216, s[20:23], s71 offen offset:2048 sc1 lds\n\t"
    "v_mfma_f32_16x16x32_bf16 a[88:91], a[168:171], v[144:147], a[88:91]\n\t"
    "v_exp_f32 v109, v109\n\t"
    "v_mfma_f32_16x16x32_bf16 a[92:95], a[168:171], v[152:155], a[92:95]\n\t"
    "s_waitcnt lgkmcnt(6)\n\t"
    "ds_read_b128 a[168:171], v192 offset:5120\n\t"
    "v_cvt_pk_bf16_f32 v156, v108, v109\n\t"
    "v_mfma_f32_16x16x32_bf16 a[104:107], a[172:175], v[144:147], a[104:107]\n\t"
    "v_exp_f32 v110, v110\n\t"
    "v_mfma_f32_16x16x32_bf16 a[108:111], a[172:175], v[152:155], a[108:111]\n\t"
    "s_waitcnt lgkmcnt(6)\n\t"
    "ds_read_b128 a[172:175], v192 offset:6144\n\t"
    "buffer_load_dwordx4 v216, s[20:23], s71 offen offset:3072 sc1 lds\n\t"
    "v_mfma_f32_16x16x32_bf16 a[120:123], a[176:179], v[144:147], a[120:123]\n\t"
    "s_add_u32 s71, s71, 0x8000\n\t"
    "v_exp_f32 v111, v111\n\t"
    "v_mfma_f32_16x16x32_bf16 a[124:127], a[176:179], v[152:155], a[124:127]\n\t"
    "s_add_u32 s45, s45, 1\n\t"
    "ds_read_b128 a[176:179], v192 offset:7168\n\t"
    "v_cvt_pk_bf16_f32 v157, v110, v111\n\t"
    "v_mfma_f32_16x16x32_bf16 a[136:139], a[144:147], v[144:147], a[136:139]\n\t"
    "v_exp_f32 v120, v120\n\t"
    "v_mfma_f32_16x16x32_bf16 a[140:143], a[144:147], v[152:155], a[140:143]\n\t"
    "v_exp_f32 v121, v121\n\t"
    "s_nop 0\n\t"
    "v_cvt_pk_bf16_f32 v150, v120, v121\n\t"
    "s_cmp_eq_u32 s35, 7\n\t"
    "s_cbranch_scc1 L_p2_last\n\t"
    "s_lshl_b32 s40, s35, 2\n\t"
    "s_sub_u32 s40, 27, s40\n\t"
    "L_p2_loop:\n\t"
    "s_waitcnt lgkmcnt(7)\n\t"
    "v_mfma_f32_16x16x32_bf16 v[72:75], a[148:151], v[32:35], v[168:171]\n\t"
    "v_exp_f32 v122, v122\n\t"
    "v_mfma_f32_16x16x32_bf16 v[76:79], a[148:151], v[48:51], v[172:175]\n\t"
    "s_waitcnt lgkmcnt(6)\n\t"
    "ds_read_b128 a[148:151], v193 offset:8192\n\t"
    "v_mfma_f32_16x16x32_bf16 v[72:75], a[152:155], v[36:39], v[72:75]\n\t"
    "v_exp_f32 v123, v123\n\t"
    "v_mfma_f32_16x16x32_bf16 v[76:79], a[152:155], v[52:55], v[76:79]\n\t"
    "s_waitcnt lgkmcnt(6)\n\t"
    "ds_read_b128 a[152:155], v193 offset:9216\n\t"
    "v_cvt_pk_bf16_f32 v151, v122, v123\n\t"
    "v_mfma_f32_16x16x32_bf16 v[72:75], a[156:159], v[40:43], v[72:75]\n\t"
    "v_exp_f32 v124, v124\n\t"
    "v_mfma_f32_16x16x32_bf16 v[76:79], a[156:159], v[56:59], v[76:79]\n\t"
    "s_waitcnt lgkmcnt(6)\n\t"
    "ds_read_b128 a[156:159], v193 offset:10240\n\t"
    "v_mfma_f32_16x16x32_bf16 v[72:75], a[160:163], v[44:47], v[72:75]\n\t"
    "v_exp_f32 v125, v125\n\t"
    "v_mfma_f32_16x16x32_bf16 v[76:79], a[160:163], v[60:63], v[76:79]\n\t"
    "s_waitcnt lgkmcnt(6)\n\t"
    "ds_read_b128 a[160:163], v193 offset:11264\n\t"
    "v_cvt_pk_bf16_f32 v158, v124, v125\n\t"
    "v_mfma_f32_16x16x32_bf16 v[88:91], a[164:167], v[32:35], v[168:171]\n\t"
    "v_exp_f32 v126, v126\n\t"
    "v_mfma_f32_16x16x32_bf16 v[92:95], a[164:167], v[48:51], v[172:175]\n\t"
    "s_waitcnt lgkmcnt(6)\n\t"
    "ds_read_b128 a[164:167], v193 offset:12288\n\t"
    "v_mfma_f32_16x16x32_bf16 v[88:91], a[168:171], v[36:39], v[88:91]\n\t"
    "v_exp_f32 v127, v127\n\t"
    "v_mfma_f32_16x16x32_bf16 v[92:95], a[168:171], v[52:55], v[92:95]\n\t"
    "s_waitcnt lgkmcnt(6)\n\t"
    "ds_read_b128 a[168:171], v193 offset:13312\n\t"
    "v_cvt_pk_bf16_f32 v159, v126, v127\n\t"
    "v_mfma_f32_16x16x32_bf16 v[88:91], a[172:175], v[40:43], v[88:91]\n\t"
    "v_exp_f32 v72, v72\n\t"
    "v_mfma_f32_16x16x32_bf16 v[92:95], a[172:175], v[56:59], v[92:95]\n\t"
    "s_waitcnt lgkmcnt(6)\n\t"
    "ds_read_b128 a[172:175], v193 offset:14336\n\t"
    "v_mfma_f32_16x16x32_bf16 v[88:91], a[176:179], v[44:47], v[88:91]\n\t"
    "v_exp_f32 v73, v73\n\t"
    "v_mfma_f32_16x16x32_bf16 v[92:95], a[176:179], v[60:63], v[92:95]\n\t"
    "s_waitcnt lgkmcnt(6)\n\t"
    "ds_read_b128 a[176:179], v193 offset:15360\n\t"
    "v_cvt_pk_bf16_f32 v144, v72, v73\n\t"
    "v_mfma_f32_16x16x32_bf16 a[8:11], a[148:151], v[148:151], a[8:11]\n\t"
    "s_and_b32 s38, s52, 0x1ffff\n\t"
    "v_exp_f32 v74, v74\n\t"
    "v_mfma_f32_16x16x32_bf16 a[12:15], a[148:151], v[156:159], a[12:15]\n\t"
    "s_waitcnt lgkmcnt(6)\n\t"
    "ds_read_b128 a[148:151], v192 offset:8192\n\t"
    "v_add_u32 v193, s38, v216\n\t"
    "v_mfma_f32_16x16x32_bf16 a[24:27], a[152:155], v[148:151], a[24:27]\n\t"
    "s_add_u32 s52, s52, 0x8000\n\t"
    "v_exp_f32 v75, v75\n\t"
    "v_mfma_f32_16x16x32_bf16 a[28:31], a[152:155], v[156:159], a[28:31]\n\t"
    "s_waitcnt lgkmcnt(6)\n\t"
    "ds_read_b128 a[152:155], v192 offset:9216\n\t"
    "v_cvt_pk_bf16_f32 v145, v74, v75\n\t"
    "v_mfma_f32_16x16x32_bf16 a[40:43], a[156:159], v[148:151], a[40:43]\n\t"
    "v_exp_f32 v76, v76\n\t"
    "v_mfma_f32_16x16x32_bf16 a[44:47], a[156:159], v[156:159], a[44:47]\n\t"
    "s_waitcnt lgkmcnt(6)\n\t"
    "ds_read_b128 a[156:159], v192 offset:10240\n\t"
    "v_mfma_f32_16x16x32_bf16 a[56:59], a[160:163], v[148:151], a[56:59]\n\t"
    "v_exp_f32 v77, v77\n\t"
    "v_mfma_f32_16x16x32_bf16 a[60:63], a[160:163], v[156:159], a[60:63]\n\t"
    "s_waitcnt lgkmcnt(6)\n\t"
    "ds_read_b128 a[160:163], v192 offset:11264\n\t"
    "v_cvt_pk_bf16_f32 v152, v76, v77\n\t"
    "v_mfma_f32_16x16x32_bf16 a[72:75], a[164:167], v[148:151], a[72:75]\n\t"
    "v_exp_f32 v78, v78\n\t"
    "v_mfma_f32_16x16x32_bf16 a[76:79], a[164:167], v[156:159], a[76:79]\n\t"
    "s_waitcnt lgkmcnt(6)\n\t"
    "ds_read_b128 a[164:167], v192 offset:12288\n\t"
    "v_mfma_f32_16x16x32_bf16 a[88:91], a[168:171], v[148:151], a[88:91]\n\t"
    "v_exp_f32 v79, v79\n\t"
    "v_mfma_f32_16x16x32_bf16 a[92:95], a[168:171], v[156:159], a[92:95]\n\t"
    "s_waitcnt lgkmcnt(6)\n\t"
    "ds_read_b128 a[168:171], v192 offset:13312\n\t"
    "v_cvt_pk_bf16_f32 v153, v78, v79\n\t"
    "v_mfma_f32_16x16x32_bf16 a[104:107], a[172:175], v[148:151], a[104:107]\n\t"
    "v_exp_f32 v88, v88\n\t"
    "v_mfma_f32_16x16x32_bf16 a[108:111], a[172:175], v[156:159], a[108:111]\n\t"
    "s_waitcnt lgkmcnt(6)\n\t"
    "ds_read_b128 a[172:175], v192 offset:14336\n\t"
    "v_mfma_f32_16x16x32_bf16 a[120:123], a[176:179], v[148:151], a[120:123]\n\t"
    "v_exp_f32 v89, v89\n\t"
    "v_mfma_f32_16x16x32_bf16 a[124:127], a[176:179], v[156:159], a[124:127]\n\t"
    "s_waitcnt vmcnt(8)\n\t"
    "ds_read_b128 a[176:179], v192 offset:15360\n\t"
    "v_cvt_pk_bf16_f32 v146, v88, v89\n\t"
    "v_mfma_f32_16x16x32_bf16 a[136:139], a[144:147], v[148:151], a[136:139]\n\t"
    "s_barrier\n\t"
    "v_exp_f32 v90, v90\n\t"
    "v_mfma_f32_16x16x32_bf16 a[140:143], a[144:147], v[156:159], a[140:143]\n\t"
    "s_waitcnt lgkmcnt(7)\n\t"
    "v_exp_f32 v91, v91\n\t"
    "v_mfma_f32_16x16x32_bf16 v[104:107], a[148:151], v[32:35], v[168:171]\n\t"
    "s_and_b32 m0, s68, 0x1ffff\n\t"
    "v_exp_f32 v92, v92\n\t"
    "v_mfma_f32_16x16x32_bf16 v[108:111], a[148:151], v[48:51], v[172:175]\n\t"
    "s_waitcnt lgkmcnt(6)\n\t"
    "ds_read_b128 a[148:151], v193 offset:0\n\t"
    "buffer_load_dwordx4 v216, s[20:23], s70 offen offset:0 sc1 lds\n\t"
    "v_mfma_f32_16x16x32_bf16 v[104:107], a[152:155], v[36:39], v[104:107]\n\t"
    "s_and_b32 s38, s51, 0x1ffff\n\t"
    "v_exp_f32 v93, v93\n\t"
    "v_mfma_f32_16x16x32_bf16 v[108:111], a[152:155], v[52:55], v[108:111]\n\t"
    "s_waitcnt lgkmcnt(6)\n\t"
    "ds_read_b128 a[152:155], v193 offset:1024\n\t"
    "v_add_u32 v192, s38, v216\n\t"
    "v_mfma_f32_16x16x32_bf16 v[104:107], a[156:159], v[40:43], v[104:107]\n\t"
    "s_add_u32 s51, s51, 0x8000\n\t"
    "v_exp_f32 v94, v94\n\t"
    "v_mfma_f32_16x16x32_bf16 v[108:111], a[156:159], v[56:59], v[108:111]\n\t"
    "s_waitcnt lgkmcnt(6)\n\t"
    "ds_read_b128 a[156:159], v193 offset:2048\n\t"
    "buffer_load_dwordx4 v216, s[20:23], s70 offen offset:1024 sc1 lds\n\t"
    "v_mfma_f32_16x16x32_bf16 v[104:107], a[160:163], v[44:47], v[104:107]\n\t"
    "s_add_u32 s68, s68, 0x8000\n\t"
    "v_exp_f32 v95, v95\n\t"
    "v_mfma_f32_16x16x32_bf16 v[108:111], a[160:163], v[60:63], v[108:111]\n\t"
    "s_waitcnt lgkmcnt(6)\n\t"
    "ds_read_b128 a[160:163], v193 offset:3072\n\t"
    "v_cvt_pk_bf16_f32 v147, v90, v91\n\t"
    "v_mfma_f32_16x16x32_bf16 v[120:123], a[164:167], v[32:35], v[168:171]\n\t"
    "v_cvt_pk_bf16_f32 v154, v92, v93\n\t"
    "v_cvt_pk_bf16_f32 v155, v94, v95\n\t"
    "v_mfma_f32_16x16x32_bf16 v[124:127], a[164:167], v[48:51], v[172:175]\n\t"
    "s_waitcnt lgkmcnt(6)\n\t"
    "ds_read_b128 a[164:167], v193 offset:4096\n\t"
    "buffer_load_dwordx4 v216, s[20:23], s70 offen offset:2048 sc1 lds\n\t"
    "v_mfma_f32_16x16x32_bf16 v[120:123], a[168:171], v[36:39], v[120:123]\n\t"
    "v_exp_f32 v104, v104\n\t"
    "v_mfma_f32_16x16x32_bf16 v[124:127], a[168:171], v[52:55], v[124:127]\n\t"
    "s_waitcnt lgkmcnt(6)\n\t"
    "ds_read_b128 a[168:171], v193 offset:5120\n\t"
    "v_mfma_f32_16x16x32_bf16 v[120:123], a[172:175], v[40:43], v[120:123]\n\t"
    "v_exp_f32 v105, v105\n\t"
    "v_mfma_f32_16x16x32_bf16 v[124:127], a[172:175], v[56:59], v[124:127]\n\t"
    "s_waitcnt lgkmcnt(6)\n\t"
    "ds_read_b128 a[172:175], v193 offset:6144\n\t"
    "buffer_load_dwordx4 v216, s[20:23], s70 offen offset:3072 sc1 lds\n\t"
    "v_mfma_f32_16x16x32_bf16 v[120:123], a[176:179], v[44:47], v[120:123]\n\t"
    "s_add_u32 s70, s70, 0x8000\n\t"
    "v_exp_f32 v106, v106\n\t"
    "v_mfma_f32_16x16x32_bf16 v[124:127], a[176:179], v[60:63], v[124:127]\n\t"
    "s_waitcnt lgkmcnt(6)\n\t"
    "ds_read_b128 a[176:179], v193 offset:7168\n\t"
    "v_cvt_pk_bf16_f32 v148, v104, v105\n\t"
    "v_mfma_f32_16x16x32_bf16 a[8:11], a[148:151], v[144:147], a[8:11]\n\t"
    "s_and_b32 m0, s69, 0x1ffff\n\t"
    "v_exp_f32 v107, v107\n\t"
    "v_mfma_f32_16x16x32_bf16 a[12:15], a[148:151], v[152:155], a[12:15]\n\t"
    "s_waitcnt lgkmcnt(6)\n\t"
    "ds_read_b128 a[148:151], v192 offset:0\n\t"
    "buffer_load_dwordx4 v216, s[20:23], s71 offen offset:0 sc1 lds\n\t"
    "v_mfma_f32_16x16x32_bf16 a[24:27], a[152:155], v[144:147], a[24:27]\n\t"
    "s_add_u32 s69, s69, 0x8000\n\t"
    "v_exp_f32 v108, v108\n\t"
    "v_mfma_f32_16x16x32_bf16 a[28:31], a[152:155], v[152:155], a[28:31]\n\t"
    "s_waitcnt lgkmcnt(6)\n\t"
    "ds_read_b128 a[152:155], v192 offset:1024\n\t"
    "v_cvt_pk_bf16_f32 v149, v106, v107\n\t"
    "v_mfma_f32_16x16x32_bf16 a[40:43], a[156:159], v[144:147], a[40:43]\n\t"
    "v_exp_f32 v109, v109\n\t"
    "v_mfma_f32_16x16x32_bf16 a[44:47], a[156:159], v[152:155], a[44:47]\n\t"
    "s_waitcnt lgkmcnt(6)\n\t"
    "ds_read_b128 a[156:159], v192 offset:2048\n\t"
    "buffer_load_dwordx4 v216, s[20:23], s71 offen offset:1024 sc1 lds\n\t"
    "v_mfma_f32_16x16x32_bf16 a[56:59], a[160:163], v[144:147], a[56:59]\n\t"
    "v_exp_f32 v110, v110\n\t"
    "v_mfma_f32_16x16x32_bf16 a[60:63], a[160:163], v[152:155], a[60:63]\n\t"
    "s_waitcnt lgkmcnt(6)\n\t"
    "ds_read_b128 a[160:163], v192 offset:3072\n\t"
    "v_cvt_pk_bf16_f32 v156, v108, v109\n\t"
    "v_mfma_f32_16x16x32_bf16 a[72:75], a[164:167], v[144:147], a[72:75]\n\t"
    "v_exp_f32 v111, v111\n\t"
    "v_mfma_f32_16x16x32_bf16 a[76:79], a[164:167], v[152:155], a[76:79]\n\t"
    "s_waitcnt lgkmcnt(6)\n\t"
    "ds_read_b128 a[164:167], v192 offset:4096\n\t"
    "buffer_load_dwordx4 v216, s[20:23], s71 offen offset:2048 sc1 lds\n\t"
    "v_mfma_f32_16x16x32_bf16 a[88:91], a[168:171], v[144:147], a[88:91]\n\t"
    "v_cvt_pk_bf16_f32 v157, v110, v111\n\t"
    "v_mfma_f32_16x16x32_bf16 a[92:95], a[168:171], v[152:155], a[92:95]\n\t"
    "s_waitcnt lgkmcnt(6)\n\t"
    "ds_read_b128 a[168:171], v192 offset:5120\n\t"
    "v_mfma_f32_16x16x32_bf16 a[104:107], a[172:175], v[144:147], a[104:107]\n\t"
    "v_exp_f32 v120, v120\n\t"
    "v_mfma_f32_16x16x32_bf16 a[108:111], a[172:175], v[152:155], a[108:111]\n\t"
    "s_waitcnt lgkmcnt(6)\n\t"
    "ds_read_b128 a[172:175], v192 offset:6144\n\t"
    "buffer_load_dwordx4 v216, s[20:23], s71 offen offset:3072 sc1 lds\n\t"
    "v_mfma_f32_16x16x32_bf16 a[120:123], a[176:179], v[144:147], a[120:123]\n\t"
    "s_add_u32 s71, s71, 0x8000\n\t"
    "v_exp_f32 v121, v121\n\t"
    "v_mfma_f32_16x16x32_bf16 a[124:127], a[176:179], v[152:155], a[124:127]\n\t"
    "s_add_u32 s45, s45, 1\n\t"
    "ds_read_b128 a[176:179], v192 offset:7168\n\t"
    "v_cvt_pk_bf16_f32 v150, v120, v121\n\t"
    "v_mfma_f32_16x16x32_bf16 a[136:139], a[144:147], v[144:147], a[136:139]\n\t"
    "v_mfma_f32_16x16x32_bf16 a[140:143], a[144:147], v[152:155], a[140:143]\n\t"
    "s_sub_u32 s40, s40, 1\n\t"
    "s_cmp_lg_u32 s40, 0\n\t"
    "s_cbranch_scc1 L_p2_loop\n\t"
    "s_lshl_b32 s37, s44, 4\n\t"
    "s_lshl_b32 s38, s45, 6\n\t"
    "s_sub_u32 s46, s37, s38\n\t"
    "v_cvt_f32_i32 v194, s46\n\t"
    "v_fma_f32 v194, v194, v196, v195\n\t"
    "v_mov_b32 v176, v194\n\t"
    "v_min_f32 v176, v176, v168\n\t"
    "v_add_f32 v177, 0xf1800000, v194\n\t"
    "v_min_f32 v177, v177, v168\n\t"
    "v_add_f32 v178, 0xf2000000, v194\n\t"
    "v_min_f32 v178, v178, v168\n\t"
    "v_add_f32 v179, 0xf2400000, v194\n\t"
    "v_min_f32 v179, v179, v168\n\t"
    "s_waitcnt lgkmcnt(7)\n\t"
    "v_mfma_f32_16x16x32_bf16 v[76:79], a[148:151], v[48:51], v[172:175]\n\t"
    "v_add_f32 v180, 0xf3800000, v194\n\t"
    "v_min_f32 v180, v180, v168\n\t"
    "v_mfma_f32_16x16x32_bf16 v[72:75], a[148:151], v[32:35], v[176:179]\n\t"
    "s_waitcnt lgkmcnt(6)\n\t"
    "ds_read_b128 a[148:151], v193 offset:8192\n\t"
    "v_mfma_f32_16x16x32_bf16 v[76:79], a[152:155], v[52:55], v[76:79]\n\t"
    "v_add_f32 v181, 0xf3880000, v194\n\t"
    "v_min_f32 v181, v181, v168\n\t"
    "v_mfma_f32_16x16x32_bf16 v[72:75], a[152:155], v[36:39], v[72:75]\n\t"
    "s_waitcnt lgkmcnt(6)\n\t"
    "ds_read_b128 a[152:155], v193 offset:9216\n\t"
    "v_mfma_f32_16x16x32_bf16 v[76:79], a[156:159], v[56:59], v[76:79]\n\t"
    "v_add_f32 v182, 0xf3900000, v194\n\t"
    "v_min_f32 v182, v182, v168\n\t"
    "v_mfma_f32_16x16x32_bf16 v[72:75], a[156:159], v[40:43], v[72:75]\n\t"
    "s_waitcnt lgkmcnt(6)\n\t"
    "ds_read_b128 a[156:159], v193 offset:10240\n\t"
    "v_mfma_f32_16x16x32_bf16 v[76:79], a[160:163], v[60:63], v[76:79]\n\t"
    "v_add_f32 v183, 0xf3980000, v194\n\t"
    "v_min_f32 v183, v183, v168\n\t"
    "v_mfma_f32_16x16x32_bf16 v[72:75], a[160:163], v[44:47], v[72:75]\n\t"
    "s_waitcnt lgkmcnt(6)\n\t"
    "ds_read_b128 a[160:163], v193 offset:11264\n\t"
    "v_mfma_f32_16x16x32_bf16 v[92:95], a[164:167], v[48:51], v[172:175]\n\t"
    "v_exp_f32 v122, v122\n\t"
    "v_mfma_f32_16x16x32_bf16 v[88:91], a[164:167], v[32:35], v[180:183]\n\t"
    "s_waitcnt lgkmcnt(6)\n\t"
    "ds_read_b128 a[164:167], v193 offset:12288\n\t"
    "v_mfma_f32_16x16x32_bf16 v[92:95], a[168:171], v[52:55], v[92:95]\n\t"
    "v_exp_f32 v123, v123\n\t"
    "v_mfma_f32_16x16x32_bf16 v[88:91], a[168:171], v[36:39], v[88:91]\n\t"
    "s_waitcnt lgkmcnt(6)\n\t"
    "ds_read_b128 a[168:171], v193 offset:13312\n\t"
    "v_cvt_pk_bf16_f32 v151, v122, v123\n\t"
    "v_mfma_f32_16x16x32_bf16 v[92:95], a[172:175], v[56:59], v[92:95]\n\t"
    "v_exp_f32 v124, v124\n\t"
    "v_mfma_f32_16x16x32_bf16 v[88:91], a[172:175], v[40:43], v[88:91]\n\t"
    "s_waitcnt lgkmcnt(6)\n\t"
    "ds_read_b128 a[172:175], v193 offset:14336\n\t"
    "v_exp_f32 v125, v125\n\t"
    "v_exp_f32 v126, v126\n\t"
    "v_exp_f32 v127, v127\n\t"
    "v_mfma_f32_16x16x32_bf16 v[92:95], a[176:179], v[60:63], v[92:95]\n\t"
    "v_cvt_pk_bf16_f32 v158, v124, v125\n\t"
    "v_cvt_pk_bf16_f32 v159, v126, v127\n\t"
    "v_mfma_f32_16x16x32_bf16 v[88:91], a[176:179], v[44:47], v[88:91]\n\t"
    "s_waitcnt lgkmcnt(6)\n\t"
    "ds_read_b128 a[176:179], v193 offset:15360\n\t"
    "v_mfma_f32_16x16x32_bf16 a[8:11], a[148:151], v[148:151], a[8:11]\n\t"
    "s_and_b32 s38, s52, 0x1ffff\n\t"
    "v_add_f32 v184, 0xf4000000, v194\n\t"
    "v_min_f32 v184, v184, v168\n\t"
    "v_mfma_f32_16x16x32_bf16 a[12:15], a[148:151], v[156:159], a[12:15]\n\t"
    "s_waitcnt lgkmcnt(6)\n\t"
    "ds_read_b128 a[148:151], v192 offset:8192\n\t"
    "v_add_u32 v193, s38, v216\n\t"
    "v_mfma_f32_16x16x32_bf16 a[24:27], a[152:155], v[148:151], a[24:27]\n\t"
    "s_add_u32 s52, s52, 0x8000\n\t"
    "v_add_f32 v185, 0xf4040000, v194\n\t"
    "v_min_f32 v185, v185, v168\n\t"
    "v_mfma_f32_16x16x32_bf16 a[28:31], a[152:155], v[156:159], a[28:31]\n\t"
    "s_waitcnt lgkmcnt(6)\n\t"
    "ds_read_b128 a[152:155], v192 offset:9216\n\t"
    "v_mfma_f32_16x16x32_bf16 a[40:43], a[156:159], v[148:151], a[40:43]\n\t"
    "v_add_f32 v186, 0xf4080000, v194\n\t"
    "v_min_f32 v186, v186, v168\n\t"
    "v_mfma_f32_16x16x32_bf16 a[44:47], a[156:159], v[156:159], a[44:47]\n\t"
    "s_waitcnt lgkmcnt(6)\n\t"
    "ds_read_b128 a[156:159], v192 offset:10240\n\t"
    "v_mfma_f32_16x16x32_bf16 a[56:59], a[160:163], v[148:151], a[56:59]\n\t"
    "v_add_f32 v187, 0xf40c0000, v194\n\t"
    "v_min_f32 v187, v187, v168\n\t"
    "v_mfma_f32_16x16x32_bf16 a[60:63], a[160:163], v[156:159], a[60:63]\n\t"
    "s_waitcnt lgkmcnt(6)\n\t"
    "ds_read_b128 a[160:163], v192 offset:11264\n\t"
    "v_mfma_f32_16x16x32_bf16 a[72:75], a[164:167], v[148:151], a[72:75]\n\t"
    "v_add_f32 v188, 0xf4400000, v194\n\t"
    "v_min_f32 v188, v188, v168\n\t"
    "v_mfma_f32_16x16x32_bf16 a[76:79], a[164:167], v[156:159], a[76:79]\n\t"
    "s_waitcnt lgkmcnt(6)\n\t"
    "ds_read_b128 a[164:167], v192 offset:12288\n\t"
    "v_mfma_f32_16x16x32_bf16 a[88:91], a[168:171], v[148:151], a[88:91]\n\t"
    "v_add_f32 v189, 0xf4440000, v194\n\t"
    "v_min_f32 v189, v189, v168\n\t"
    "v_mfma_f32_16x16x32_bf16 a[92:95], a[168:171], v[156:159], a[92:95]\n\t"
    "s_waitcnt lgkmcnt(6)\n\t"
    "ds_read_b128 a[168:171], v192 offset:13312\n\t"
    "v_mfma_f32_16x16x32_bf16 a[104:107], a[172:175], v[148:151], a[104:107]\n\t"
    "v_add_f32 v190, 0xf4480000, v194\n\t"
    "v_min_f32 v190, v190, v168\n\t"
    "v_mfma_f32_16x16x32_bf16 a[108:111], a[172:175], v[156:159], a[108:111]\n\t"
    "s_waitcnt lgkmcnt(6)\n\t"
    "ds_read_b128 a[172:175], v192 offset:14336\n\t"
    "v_mfma_f32_16x16x32_bf16 a[120:123], a[176:179], v[148:151], a[120:123]\n\t"
    "v_add_f32 v191, 0xf44c0000, v194\n\t"
    "v_min_f32 v191, v191, v168\n\t"
    "v_mfma_f32_16x16x32_bf16 a[124:127], a[176:179], v[156:159], a[124:127]\n\t"
    "s_waitcnt vmcnt(8)\n\t"
    "ds_read_b128 a[176:179], v192 offset:15360\n\t"
    "v_mfma_f32_16x16x32_bf16 a[136:139], a[144:147], v[148:151], a[136:139]\n\t"
    "s_barrier\n\t"
    "v_exp_f32 v72, v72\n\t"
    "v_mfma_f32_16x16x32_bf16 a[140:143], a[144:147], v[156:159], a[140:143]\n\t"
    "s_waitcnt lgkmcnt(7)\n\t"
    "v_exp_f32 v73, v73\n\t"
    "v_mfma_f32_16x16x32_bf16 v[108:111], a[148:151], v[48:51], v[172:175]\n\t"
    "v_exp_f32 v74, v74\n\t"
    "v_mfma_f32_16x16x32_bf16 v[104:107], a[148:151], v[32:35], v[184:187]\n\t"
    "s_waitcnt lgkmcnt(6)\n\t"
    "ds_read_b128 a[148:151], v193 offset:0\n\t"
    "v_cvt_pk_bf16_f32 v144, v72, v73\n\t"
    "v_mfma_f32_16x16x32_bf16 v[108:111], a[152:155], v[52:55], v[108:111]\n\t"
    "s_and_b32 s38, s51, 0x1ffff\n\t"
    "v_exp_f32 v75, v75\n\t"
    "v_mfma_f32_16x16x32_bf16 v[104:107], a[152:155], v[36:39], v[104:107]\n\t"
    "s_waitcnt lgkmcnt(6)\n\t"
    "ds_read_b128 a[152:155], v193 offset:1024\n\t"
    "v_add_u32 v192, s38, v216\n\t"
    "v_mfma_f32_16x16x32_bf16 v[108:111], a[156:159], v[56:59], v[108:111]\n\t"
    "s_add_u32 s51, s51, 0x8000\n\t"
    "v_exp_f32 v76, v76\n\t"
    "v_mfma_f32_16x16x32_bf16 v[104:107], a[156:159], v[40:43], v[104:107]\n\t"
    "s_waitcnt lgkmcnt(6)\n\t"
    "ds_read_b128 a[156:159], v193 offset:2048\n\t"
    "v_cvt_pk_bf16_f32 v145, v74, v75\n\t"
    "v_mfma_f32_16x16x32_bf16 v[108:111], a[160:163], v[60:63], v[108:111]\n\t"
    "v_exp_f32 v77, v77\n\t"
    "v_mfma_f32_16x16x32_bf16 v[104:107], a[160:163], v[44:47], v[104:107]\n\t"
    "s_waitcnt lgkmcnt(6)\n\t"
    "ds_read_b128 a[160:163], v193 offset:3072\n\t"
    "v_cvt_pk_bf16_f32 v152, v76, v77\n\t"
    "v_mfma_f32_16x16x32_bf16 v[124:127], a[164:167], v[48:51], v[172:175]\n\t"
    "v_exp_f32 v78, v78\n\t"
    "v_mfma_f32_16x16x32_bf16 v[120:123], a[164:167], v[32:35], v[188:191]\n\t"
    "s_waitcnt lgkmcnt(6)\n\t"
    "ds_read_b128 a[164:167], v193 offset:4096\n\t"
    "v_mfma_f32_16x16x32_bf16 v[124:127], a[168:171], v[52:55], v[124:127]\n\t"
    "v_exp_f32 v79, v79\n\t"
    "v_mfma_f32_16x16x32_bf16 v[120:123], a[168:171], v[36:39], v[120:123]\n\t"
    "s_waitcnt lgkmcnt(6)\n\t"
    "ds_read_b128 a[168:171], v193 offset:5120\n\t"
    "v_cvt_pk_bf16_f32 v153, v78, v79\n\t"
    "v_mfma_f32_16x16x32_bf16 v[124:127], a[172:175], v[56:59], v[124:127]\n\t"
    "v_exp_f32 v88, v88\n\t"
    "v_mfma_f32_16x16x32_bf16 v[120:123], a[172:175], v[40:43], v[120:123]\n\t"
    "s_waitcnt lgkmcnt(6)\n\t"
    "ds_read_b128 a[172:175], v193 offset:6144\n\t"
    "v_exp_f32 v89, v89\n\t"
    "v_exp_f32 v90, v90\n\t"
    "v_exp_f32 v91, v91\n\t"
    "v_exp_f32 v92, v92\n\t"
    "v_exp_f32 v93, v93\n\t"
    "v_exp_f32 v94, v94\n\t"
    "v_exp_f32 v95, v95\n\t"
    "v_mfma_f32_16x16x32_bf16 v[124:127], a[176:179], v[60:63], v[124:127]\n\t"
    "v_cvt_pk_bf16_f32 v146, v88, v89\n\t"
    "v_cvt_pk_bf16_f32 v147, v90, v91\n\t"
    "v_cvt_pk_bf16_f32 v154, v92, v93\n\t"
    "v_cvt_pk_bf16_f32 v155, v94, v95\n\t"
    "v_mfma_f32_16x16x32_bf16 v[120:123], a[176:179], v[44:47], v[120:123]\n\t"
    "s_waitcnt lgkmcnt(6)\n\t"
    "ds_read_b128 a[176:179], v193 offset:7168\n\t"
    "v_mfma_f32_16x16x32_bf16 a[8:11], a[148:151], v[144:147], a[8:11]\n\t"
    "v_exp_f32 v104, v104\n\t"
    "v_mfma_f32_16x16x32_bf16 a[12:15], a[148:151], v[152:155], a[12:15]\n\t"
    "s_waitcnt lgkmcnt(6)\n\t"
    "ds_read_b128 a[148:151], v192 offset:0\n\t"
    "v_mfma_f32_16x16x32_bf16 a[24:27], a[152:155], v[144:147], a[24:27]\n\t"
    "v_exp_f32 v105, v105\n\t"
    "v_mfma_f32_16x16x32_bf16 a[28:31], a[152:155], v[152:155], a[28:31]\n\t"
    "s_waitcnt lgkmcnt(6)\n\t"
    "ds_read_b128 a[152:155], v192 offset:1024\n\t"
    "v_cvt_pk_bf16_f32 v148, v104, v105\n\t"
    "v_mfma_f32_16x16x32_bf16 a[40:43], a[156:159], v[144:147], a[40:43]\n\t"
    "v_exp_f32 v106, v106\n\t"
    "v_mfma_f32_16x16x32_bf16 a[44:47], a[156:159], v[152:155], a[44:47]\n\t"
    "s_waitcnt lgkmcnt(6)\n\t"
    "ds_read_b128 a[156:159], v192 offset:2048\n\t"
    "v_mfma_f32_16x16x32_bf16 a[56:59], a[160:163], v[144:147], a[56:59]\n\t"
    "v_exp_f32 v107, v107\n\t"
    "v_mfma_f32_16x16x32_bf16 a[60:63], a[160:163], v[152:155], a[60:63]\n\t"
    "s_waitcnt lgkmcnt(6)\n\t"
    "ds_read_b128 a[160:163], v192 offset:3072\n\t"
    "v_cvt_pk_bf16_f32 v149, v106, v107\n\t"
    "v_mfma_f32_16x16x32_bf16 a[72:75], a[164:167], v[144:147], a[72:75]\n\t"
    "v_exp_f32 v108, v108\n\t"
    "v_mfma_f32_16x16x32_bf16 a[76:79], a[164:167], v[152:155], a[76:79]\n\t"
    "s_waitcnt lgkmcnt(6)\n\t"
    "ds_read_b128 a[164:167], v192 offset:4096\n\t"
    "v_mfma_f32_16x16x32_bf16 a[88:91], a[168:171], v[144:147], a[88:91]\n\t"
    "v_exp_f32 v109, v109\n\t"
    "v_mfma_f32_16x16x32_bf16 a[92:95], a[168:171], v[152:155], a[92:95]\n\t"
    "s_waitcnt lgkmcnt(6)\n\t"
    "ds_read_b128 a[168:171], v192 offset:5120\n\t"
    "v_cvt_pk_bf16_f32 v156, v108, v109\n\t"
    "v_mfma_f32_16x16x32_bf16 a[104:107], a[172:175], v[144:147], a[104:107]\n\t"
    "v_exp_f32 v110, v110\n\t"
    "v_mfma_f32_16x16x32_bf16 a[108:111], a[172:175], v[152:155], a[108:111]\n\t"
    "s_waitcnt lgkmcnt(6)\n\t"
    "ds_read_b128 a[172:175], v192 offset:6144\n\t"
    "v_mfma_f32_16x16x32_bf16 a[120:123], a[176:179], v[144:147], a[120:123]\n\t"
    "s_add_u32 s45, s45, 1\n\t"
    "v_exp_f32 v111, v111\n\t"
    "v_mfma_f32_16x16x32_bf16 a[124:127], a[176:179], v[152:155], a[124:127]\n\t"
    "ds_read_b128 a[176:179], v192 offset:7168\n\t"
    "v_cvt_pk_bf16_f32 v157, v110, v111\n\t"
    "v_mfma_f32_16x16x32_bf16 a[136:139], a[144:147], v[144:147], a[136:139]\n\t"
    "v_exp_f32 v120, v120\n\t"
    "v_mfma_f32_16x16x32_bf16 a[140:143], a[144:147], v[152:155], a[140:143]\n\t"
    "v_exp_f32 v121, v121\n\t"
    "s_nop 0\n\t"
    "v_cvt_pk_bf16_f32 v150, v120, v121\n\t"
    "L_p2_last:\n\t"
    "s_lshl_b32 s37, s44, 4\n\t"
    "s_lshl_b32 s38, s45, 6\n\t"
    "s_sub_u32 s46, s37, s38\n\t"
    "v_cvt_f32_i32 v194, s46\n\t"
    "v_fma_f32 v194, v194, v196, v195\n\t"
    "v_add_f32 v176, 0x74800000, v194\n\t"
    "v_min_f32 v176, v176, v172\n\t"
    "v_add_f32 v177, 0x747c0000, v194\n\t"
    "v_min_f32 v177, v177, v172\n\t"
    "v_add_f32 v178, 0x74780000, v194\n\t"
    "v_min_f32 v178, v178, v172\n\t"
    "v_add_f32 v179, 0x74740000, v194\n\t"
    "v_min_f32 v179, v179, v172\n\t"
    "s_nop 0\n\t"
    "s_waitcnt lgkmcnt(7)\n\t"
    "v_mfma_f32_16x16x32_bf16 v[76:79], a[148:151], v[48:51], v[176:179]\n\t"
    "s_waitcnt lgkmcnt(6)\n\t"
    "ds_read_b128 a[148:151], v193 offset:8192\n\t"
    "v_mfma_f32_16x16x32_bf16 v[76:79], a[152:155], v[52:55], v[76:79]\n\t"
    "s_waitcnt lgkmcnt(6)\n\t"
    "ds_read_b128 a[152:155], v193 offset:9216\n\t"
    "v_mfma_f32_16x16x32_bf16 v[76:79], a[156:159], v[56:59], v[76:79]\n\t"
    "s_waitcnt lgkmcnt(6)\n\t"
    "ds_read_b128 a[156:159], v193 offset:10240\n\t"
    "v_add_f32 v180, 0x74400000, v194\n\t"
    "v_min_f32 v180, v180, v172\n\t"
    "v_add_f32 v181, 0x743c0000, v194\n\t"
    "v_min_f32 v181, v181, v172\n\t"
    "v_add_f32 v182, 0x74380000, v194\n\t"
    "v_min_f32 v182, v182, v172\n\t"
    "v_add_f32 v183, 0x74340000, v194\n\t"
    "v_min_f32 v183, v183, v172\n\t"
    "v_mfma_f32_16x16x32_bf16 v[76:79], a[160:163], v[60:63], v[76:79]\n\t"
    "s_waitcnt lgkmcnt(6)\n\t"
    "ds_read_b128 a[160:163], v193 offset:11264\n\t"
    "v_mfma_f32_16x16x32_bf16 v[92:95], a[164:167], v[48:51], v[180:183]\n\t"
    "s_waitcnt lgkmcnt(6)\n\t"
    "ds_read_b128 a[164:167], v193 offset:12288\n\t"
    "v_mfma_f32_16x16x32_bf16 v[92:95], a[168:171], v[52:55], v[92:95]\n\t"
    "s_waitcnt lgkmcnt(6)\n\t"
    "ds_read_b128 a[168:171], v193 offset:13312\n\t"
    "v_exp_f32 v122, v122\n\t"
    "v_exp_f32 v123, v123\n\t"
    "v_exp_f32 v124, v124\n\t"
    "v_exp_f32 v125, v125\n\t"
    "v_exp_f32 v126, v126\n\t"
    "v_exp_f32 v127, v127\n\t"
    "v_mfma_f32_16x16x32_bf16 v[92:95], a[172:175], v[56:59], v[92:95]\n\t"
    "s_waitcnt lgkmcnt(6)\n\t"
    "ds_read_b128 a[172:175], v193 offset:14336\n\t"
    "v_cvt_pk_bf16_f32 v151, v122, v123\n\t"
    "v_cvt_pk_bf16_f32 v158, v124, v125\n\t"
    "v_cvt_pk_bf16_f32 v159, v126, v127\n\t"
    "v_mfma_f32_16x16x32_bf16 v[92:95], a[176:179], v[60:63], v[92:95]\n\t"
    "s_waitcnt lgkmcnt(6)\n\t"
    "ds_read_b128 a[176:179], v193 offset:15360\n\t"
    "v_mfma_f32_16x16x32_bf16 a[8:11], a[148:151], v[148:151], a[8:11]\n\t"
    "s_and_b32 s38, s52, 0x1ffff\n\t"
    "v_add_f32 v184, 0x74000000, v194\n\t"
    "v_min_f32 v184, v184, v172\n\t"
    "v_mfma_f32_16x16x32_bf16 a[12:15], a[148:151], v[156:159], a[12:15]\n\t"
    "s_waitcnt lgkmcnt(6)\n\t"
    "ds_read_b128 a[148:151], v192 offset:8192\n\t"
    "v_add_u32 v193, s38, v216\n\t"
    "v_mfma_f32_16x16x32_bf16 a[24:27], a[152:155], v[148:151], a[24:27]\n\t"
    "s_add_u32 s52, s52, 0x8000\n\t"
    "v_add_f32 v185, 0x73f80000, v194\n\t"
    "v_min_f32 v185, v185, v172\n\t"
    "v_mfma_f32_16x16x32_bf16 a[28:31], a[152:155], v[156:159], a[28:31]\n\t"
    "s_waitcnt lgkmcnt(6)\n\t"
    "ds_read_b128 a[152:155], v192 offset:9216\n\t"
    "v_mfma_f32_16x16x32_bf16 a[40:43], a[156:159], v[148:151], a[40:43]\n\t"
    "v_add_f32 v186, 0x73f00000, v194\n\t"
    "v_min_f32 v186, v186, v172\n\t"
    "v_mfma_f32_16x16x32_bf16 a[44:47], a[156:159], v[156:159], a[44:47]\n\t"
    "s_waitcnt lgkmcnt(6)\n\t"
    "ds_read_b128 a[156:159], v192 offset:10240\n\t"
    "v_mfma_f32_16x16x32_bf16 a[56:59], a[160:163], v[148:151], a[56:59]\n\t"
    "v_add_f32 v187, 0x73e80000, v194\n\t"
    "v_min_f32 v187, v187, v172\n\t"
    "v_mfma_f32_16x16x32_bf16 a[60:63], a[160:163], v[156:159], a[60:63]\n\t"
    "s_waitcnt lgkmcnt(6)\n\t"
    "ds_read_b128 a[160:163], v192 offset:11264\n\t"
    "v_mfma_f32_16x16x32_bf16 a[72:75], a[164:167], v[148:151], a[72:75]\n\t"
    "v_add_f32 v188, 0x73800000, v194\n\t"
    "v_min_f32 v188, v188, v172\n\t"
    "v_mfma_f32_16x16x32_bf16 a[76:79], a[164:167], v[156:159], a[76:79]\n\t"
    "s_waitcnt lgkmcnt(6)\n\t"
    "ds_read_b128 a[164:167], v192 offset:12288\n\t"
    "v_mfma_f32_16x16x32_bf16 a[88:91], a[168:171], v[148:151], a[88:91]\n\t"
    "v_add_f32 v189, 0x73700000, v194\n\t"
    "v_min_f32 v189, v189, v172\n\t"
    "v_mfma_f32_16x16x32_bf16 a[92:95], a[168:171], v[156:159], a[92:95]\n\t"
    "s_waitcnt lgkmcnt(6)\n\t"
    "ds_read_b128 a[168:171], v192 offset:13312\n\t"
    "v_mfma_f32_16x16x32_bf16 a[104:107], a[172:175], v[148:151], a[104:107]\n\t"
    "v_add_f32 v190, 0x73600000, v194\n\t"
    "v_min_f32 v190, v190, v172\n\t"
    "v_mfma_f32_16x16x32_bf16 a[108:111], a[172:175], v[156:159], a[108:111]\n\t"
    "s_waitcnt lgkmcnt(6)\n\t"
    "ds_read_b128 a[172:175], v192 offset:14336\n\t"
    "v_mfma_f32_16x16x32_bf16 a[120:123], a[176:179], v[148:151], a[120:123]\n\t"
    "v_add_f32 v191, 0x73500000, v194\n\t"
    "v_min_f32 v191, v191, v172\n\t"
    "v_mfma_f32_16x16x32_bf16 a[124:127], a[176:179], v[156:159], a[124:127]\n\t"
    "s_waitcnt vmcnt(0)\n\t"
    "ds_read_b128 a[176:179], v192 offset:15360\n\t"
    "v_mfma_f32_16x16x32_bf16 a[136:139], a[144:147], v[148:151], a[136:139]\n\t"
    "s_barrier\n\t"
    "v_exp_f32 v76, v76\n\t"
    "v_mfma_f32_16x16x32_bf16 a[140:143], a[144:147], v[156:159], a[140:143]\n\t"
    "s_waitcnt lgkmcnt(7)\n\t"
    "v_exp_f32 v77, v77\n\t"
    "v_mfma_f32_16x16x32_bf16 v[108:111], a[148:151], v[48:51], v[184:187]\n\t"
    "s_waitcnt lgkmcnt(6)\n\t"
    "ds_read_b128 a[148:151], v193 offset:0\n\t"
    "v_cvt_pk_bf16_f32 v152, v76, v77\n\t"
    "v_mfma_f32_16x16x32_bf16 v[108:111], a[152:155], v[52:55], v[108:111]\n\t"
    "s_waitcnt lgkmcnt(6)\n\t"
    "ds_read_b128 a[152:155], v193 offset:1024\n\t"
    "v_mfma_f32_16x16x32_bf16 v[108:111], a[156:159], v[56:59], v[108:111]\n\t"
    "s_waitcnt lgkmcnt(6)\n\t"
    "ds_read_b128 a[156:159], v193 offset:2048\n\t"
    "v_mfma_f32_16x16x32_bf16 v[108:111], a[160:163], v[60:63], v[108:111]\n\t"
    "s_waitcnt lgkmcnt(6)\n\t"
    "ds_read_b128 a[160:163], v193 offset:3072\n\t"
    "v_mfma_f32_16x16x32_bf16 v[124:127], a[164:167], v[48:51], v[188:191]\n\t"
    "s_waitcnt lgkmcnt(6)\n\t"
    "ds_read_b128 a[164:167], v193 offset:4096\n\t"
    "v_mfma_f32_16x16x32_bf16 v[124:127], a[168:171], v[52:55], v[124:127]\n\t"
    "s_waitcnt lgkmcnt(6)\n\t"
    "ds_read_b128 a[168:171], v193 offset:5120\n\t"
    "v_exp_f32 v78, v78\n\t"
    "v_exp_f32 v79, v79\n\t"
    "v_exp_f32 v92, v92\n\t"
    "v_exp_f32 v93, v93\n\t"
    "v_exp_f32 v94, v94\n\t"
    "v_exp_f32 v95, v95\n\t"
    "v_mfma_f32_16x16x32_bf16 v[124:127], a[172:175], v[56:59], v[124:127]\n\t"
    "s_waitcnt lgkmcnt(6)\n\t"
    "ds_read_b128 a[172:175], v193 offset:6144\n\t"
    "v_cvt_pk_bf16_f32 v153, v78, v79\n\t"
    "v_cvt_pk_bf16_f32 v154, v92, v93\n\t"
    "v_cvt_pk_bf16_f32 v155, v94, v95\n\t"
    "v_mfma_f32_16x16x32_bf16 v[124:127], a[176:179], v[60:63], v[124:127]\n\t"
    "s_waitcnt lgkmcnt(6)\n\t"
    "ds_read_b128 a[176:179], v193 offset:7168\n\t"
    "v_add_u32 v192, s38, v216\n\t"
    "v_mfma_f32_16x16x32_bf16 a[12:15], a[148:151], v[152:155], a[12:15]\n\t"
    "s_waitcnt lgkmcnt(6)\n\t"
    "ds_read_b128 a[148:151], v193 offset:8192\n\t"
    "v_mfma_f32_16x16x32_bf16 a[28:31], a[152:155], v[152:155], a[28:31]\n\t"
    "s_waitcnt lgkmcnt(6)\n\t"
    "ds_read_b128 a[152:155], v193 offset:9216\n\t"
    "v_mfma_f32_16x16x32_bf16 a[44:47], a[156:159], v[152:155], a[44:47]\n\t"
    "s_waitcnt lgkmcnt(6)\n\t"
    "ds_read_b128 a[156:159], v193 offset:10240\n\t"
    "v_mfma_f32_16x16x32_bf16 a[60:63], a[160:163], v[152:155], a[60:63]\n\t"
    "s_waitcnt lgkmcnt(6)\n\t"
    "ds_read_b128 a[160:163], v193 offset:11264\n\t"
    "v_mfma_f32_16x16x32_bf16 a[76:79], a[164:167], v[152:155], a[76:79]\n\t"
    "s_waitcnt lgkmcnt(6)\n\t"
    "ds_read_b128 a[164:167], v193 offset:12288\n\t"
    "v_mfma_f32_16x16x32_bf16 a[92:95], a[168:171], v[152:155], a[92:95]\n\t"
    "s_waitcnt lgkmcnt(6)\n\t"
    "ds_read_b128 a[168:171], v193 offset:13312\n\t"
    "v_mfma_f32_16x16x32_bf16 a[108:111], a[172:175], v[152:155], a[108:111]\n\t"
    "s_waitcnt lgkmcnt(6)\n\t"
    "ds_read_b128 a[172:175], v193 offset:14336\n\t"
    "v_mfma_f32_16x16x32_bf16 a[124:127], a[176:179], v[152:155], a[124:127]\n\t"
    "s_and_b32 s38, s51, 0x1ffff\n\t"
    "ds_read_b128 a[176:179], v193 offset:15360\n\t"
    "v_mfma_f32_16x16x32_bf16 a[140:143], a[144:147], v[152:155], a[140:143]\n\t"
    "s_add_u32 s51, s51, 0x8000\n\t"
    "s_add_u32 s45, s45, 1\n\t"
    "v_exp_f32 v108, v108\n\t"
    "v_exp_f32 v109, v109\n\t"
    "v_exp_f32 v110, v110\n\t"
    "v_exp_f32 v111, v111\n\t"
    "v_exp_f32 v124, v124\n\t"
    "v_exp_f32 v125, v125\n\t"
    "v_exp_f32 v126, v126\n\t"
    "v_exp_f32 v127, v127\n\t"
    "s_nop 0\n\t"
    "v_cvt_pk_bf16_f32 v156, v108, v109\n\t"
    "v_cvt_pk_bf16_f32 v157, v110, v111\n\t"
    "v_cvt_pk_bf16_f32 v158, v124, v125\n\t"
    "v_cvt_pk_bf16_f32 v159, v126, v127\n\t"
    "s_nop 1\n\t"
    "s_waitcnt lgkmcnt(7)\n\t"
    "v_mfma_f32_16x16x32_bf16 a[12:15], a[148:151], v[156:159], a[12:15]\n\t"
    "s_waitcnt lgkmcnt(6)\n\t"
    "v_mfma_f32_16x16x32_bf16 a[28:31], a[152:155], v[156:159], a[28:31]\n\t"
    "s_waitcnt lgkmcnt(5)\n\t"
    "v_mfma_f32_16x16x32_bf16 a[44:47], a[156:159], v[156:159], a[44:47]\n\t"
    "s_waitcnt lgkmcnt(4)\n\t"
    "v_mfma_f32_16x16x32_bf16 a[60:63], a[160:163], v[156:159], a[60:63]\n\t"
    "s_waitcnt lgkmcnt(3)\n\t"
    "v_mfma_f32_16x16x32_bf16 a[76:79], a[164:167], v[156:159], a[76:79]\n\t"
    "s_waitcnt lgkmcnt(2)\n\t"
    "v_mfma_f32_16x16x32_bf16 a[92:95], a[168:171], v[156:159], a[92:95]\n\t"
    "s_waitcnt lgkmcnt(1)\n\t"
    "v_mfma_f32_16x16x32_bf16 a[108:111], a[172:175], v[156:159], a[108:111]\n\t"
    "s_waitcnt lgkmcnt(0)\n\t"
    "v_mfma_f32_16x16x32_bf16 a[124:127], a[176:179], v[156:159], a[124:127]\n\t"
    "v_mfma_f32_16x16x32_bf16 a[140:143], a[144:147], v[156:159], a[140:143]\n\t"
    "s_nop 7\n\t"
    "v_accvgpr_read_b32 v218, a136\n\t"
    "v_accvgpr_read_b32 v219, a140\n\t"
    "v_cmp_nle_f32 vcc, v218, v197\n\t"
    "s_or_b64 s[78:79], s[78:79], vcc\n\t"
    "v_cmp_nle_f32 vcc, v219, v197\n\t"
    "s_or_b64 s[78:79], s[78:79], vcc\n\t"
    "s_or_b32 s37, s78, s79\n\t"
    "v_mov_b32 v220, s37\n\t"
    "s_lshl_b32 s37, s33, 2\n\t"
    "s_add_u32 s37, s37, 0x20000\n\t"
    "v_mov_b32 v221, s37\n\t"
    "ds_write_b32 v221, v220\n\t"
    "s_waitcnt lgkmcnt(0)\n\t"
    "s_barrier\n\t"
    "v_mov_b32 v245, 0x20000\n\t"
    "ds_read_b128 v[246:249], v245\n\t"
    "s_cmp_lg_u32 s33, 0\n\t"
    "s_cbranch_scc1 L_pk2\n\t"
    "v_and_b32 v218, 31, v217\n\t"
    "v_lshl_add_u32 v219, v218, 3, s35\n\t"
    "v_lshlrev_b32 v200, 6, v219\n\t"
    "v_add_u32 v200, 0x210000, v200\n\t"
    "v_lshlrev_b32 v201, 9, v219\n\t"
    "s_lshl_b32 s37, s34, 4\n\t"
    "s_add_u32 s37, s37, 0x220000\n\t"
    "v_add_u32 v201, s37, v201\n\t"
    "v_lshlrev_b32 v202, 4, v218\n\t"
    "s_lshl_b32 s37, s36, 9\n\t"
    "s_add_u32 s37, s37, 0x220000\n\t"
    "v_add_u32 v202, s37, v202\n\t"
    "buffer_load_dwordx2 v[198:199], v200, s[28:31], 0 offen sc1\n\t"
    "L_pk2:\n\t"
    "s_waitcnt lgkmcnt(0)\n\t"
    "v_or_b32 v246, v246, v247\n\t"
    "v_or3_b32 v246, v246, v248, v249\n\t"
    "s_nop 0\n\t"
    "v_readfirstlane_b32 s37, v246\n\t"
    "s_nop 1\n\t"
    "s_cmp_lg_u32 s37, 0\n\t"
    "s_cselect_b32 s37, 1, 0\n\t"
    "s_andn2_b32 s37, s37, s42\n\t"
    "s_cmp_lg_u32 s37, 0\n\t"
    "s_cbranch_scc1 L_redo\n\t"
    "s_nop 7\n\t"
    "v_accvgpr_read_b32 v250, a136\n\t"
    "v_cmp_nle_f32 vcc, v250, v197\n\t"
    "s_or_b64 s[78:79], s[78:79], vcc\n\t"
    "v_rcp_f32 v250, v250\n\t"
    "v_accvgpr_read_b32 v218, a8\n\t"
    "v_accvgpr_read_b32 v219, a9\n\t"
    "v_accvgpr_read_b32 v220, a10\n\t"
    "v_accvgpr_read_b32 v221, a11\n\t"
    "v_accvgpr_read_b32 v222, a24\n\t"
    "v_accvgpr_read_b32 v223, a25\n\t"
    "v_accvgpr_read_b32 v224, a26\n\t"
    "v_accvgpr_read_b32 v225, a27\n\t"
    "v_mul_f32 v218, v218, v250\n\t"
    "v_mul_f32 v219, v219, v250\n\t"
    "v_mul_f32 v220, v220, v250\n\t"
    "v_mul_f32 v221, v221, v250\n\t"
    "v_mul_f32 v222, v222, v250\n\t"
    "v_mul_f32 v223, v223, v250\n\t"
    "v_mul_f32 v224, v224, v250\n\t"
    "v_mul_f32 v225, v225, v250\n\t"
    "v_cvt_pk_bf16_f32 v226, v218, v219\n\t"
    "v_cvt_pk_bf16_f32 v227, v220, v221\n\t"
    "v_cvt_pk_bf16_f32 v228, v222, v223\n\t"
    "v_cvt_pk_bf16_f32 v229, v224, v225\n\t"
    "s_add_u32 s39, s67, 0x0\n\t"
    "buffer_store_dwordx4 v[226:229], v216, s[24:27], s39 offen offset:0 sc1\n\t"
    "v_accvgpr_read_b32 v230, a40\n\t"
    "v_accvgpr_read_b32 v231, a41\n\t"
    "v_accvgpr_read_b32 v232, a42\n\t"
    "v_accvgpr_read_b32 v233, a43\n\t"
    "v_accvgpr_read_b32 v234, a56\n\t"
    "v_accvgpr_read_b32 v235, a57\n\t"
    "v_accvgpr_read_b32 v236, a58\n\t"
    "v_accvgpr_read_b32 v237, a59\n\t"
    "v_mul_f32 v230, v230, v250\n\t"
    "v_mul_f32 v231, v231, v250\n\t"
    "v_mul_f32 v232, v232, v250\n\t"
    "v_mul_f32 v233, v233, v250\n\t"
    "v_mul_f32 v234, v234, v250\n\t"
    "v_mul_f32 v235, v235, v250\n\t"
    "v_mul_f32 v236, v236, v250\n\t"
    "v_mul_f32 v237, v237, v250\n\t"
    "v_cvt_pk_bf16_f32 v238, v230, v231\n\t"
    "v_cvt_pk_bf16_f32 v239, v232, v233\n\t"
    "v_cvt_pk_bf16_f32 v240, v234, v235\n\t"
    "v_cvt_pk_bf16_f32 v241, v236, v237\n\t"
    "buffer_store_dwordx4 v[238:241], v216, s[24:27], s39 offen offset:1024 sc1\n\t"
    "v_accvgpr_read_b32 v218, a72\n\t"
    "v_accvgpr_read_b32 v219, a73\n\t"
    "v_accvgpr_read_b32 v220, a74\n\t"
    "v_accvgpr_read_b32 v221, a75\n\t"
    "v_accvgpr_read_b32 v222, a88\n\t"
    "v_accvgpr_read_b32 v223, a89\n\t"
    "v_accvgpr_read_b32 v224, a90\n\t"
    "v_accvgpr_read_b32 v225, a91\n\t"
    "v_mul_f32 v218, v218, v250\n\t"
    "v_mul_f32 v219, v219, v250\n\t"
    "v_mul_f32 v220, v220, v250\n\t"
    "v_mul_f32 v221, v221, v250\n\t"
    "v_mul_f32 v222, v222, v250\n\t"
    "v_mul_f32 v223, v223, v250\n\t"
    "v_mul_f32 v224, v224, v250\n\t"
    "v_mul_f32 v225, v225, v250\n\t"
    "v_cvt_pk_bf16_f32 v226, v218, v219\n\t"
    "v_cvt_pk_bf16_f32 v227, v220, v221\n\t"
    "v_cvt_pk_bf16_f32 v228, v222, v223\n\t"
    "v_cvt_pk_bf16_f32 v229, v224, v225\n\t"
    "buffer_store_dwordx4 v[226:229], v216, s[24:27], s39 offen offset:2048 sc1\n\t"
    "v_accvgpr_read_b32 v230, a104\n\t"
    "v_accvgpr_read_b32 v231, a105\n\t"
    "v_accvgpr_read_b32 v232, a106\n\t"
    "v_accvgpr_read_b32 v233, a107\n\t"
    "v_accvgpr_read_b32 v234, a120\n\t"
    "v_accvgpr_read_b32 v235, a121\n\t"
    "v_accvgpr_read_b32 v236, a122\n\t"
    "v_accvgpr_read_b32 v237, a123\n\t"
    "v_mul_f32 v230, v230, v250\n\t"
    "v_mul_f32 v231, v231, v250\n\t"
    "v_mul_f32 v232, v232, v250\n\t"
    "v_mul_f32 v233, v233, v250\n\t"
    "v_mul_f32 v234, v234, v250\n\t"
    "v_mul_f32 v235, v235, v250\n\t"
    "v_mul_f32 v236, v236, v250\n\t"
    "v_mul_f32 v237, v237, v250\n\t"
    "v_cvt_pk_bf16_f32 v238, v230, v231\n\t"
    "v_cvt_pk_bf16_f32 v239, v232, v233\n\t"
    "v_cvt_pk_bf16_f32 v240, v234, v235\n\t"
    "v_cvt_pk_bf16_f32 v241, v236, v237\n\t"
    "buffer_store_dwordx4 v[238:241], v216, s[24:27], s39 offen offset:3072 sc1\n\t"
    "v_accvgpr_read_b32 v251, a140\n\t"
    "v_cmp_nle_f32 vcc, v251, v197\n\t"
    "s_or_b64 s[78:79], s[78:79], vcc\n\t"
    "v_rcp_f32 v251, v251\n\t"
    "v_accvgpr_read_b32 v218, a12\n\t"
    "v_accvgpr_read_b32 v219, a13\n\t"
    "v_accvgpr_read_b32 v220, a14\n\t"
    "v_accvgpr_read_b32 v221, a15\n\t"
    "v_accvgpr_read_b32 v222, a28\n\t"
    "v_accvgpr_read_b32 v223, a29\n\t"
    "v_accvgpr_read_b32 v224, a30\n\t"
    "v_accvgpr_read_b32 v225, a31\n\t"
    "v_mul_f32 v218, v218, v251\n\t"
    "v_mul_f32 v219, v219, v251\n\t"
    "v_mul_f32 v220, v220, v251\n\t"
    "v_mul_f32 v221, v221, v251\n\t"
    "v_mul_f32 v222, v222, v251\n\t"
    "v_mul_f32 v223, v223, v251\n\t"
    "v_mul_f32 v224, v224, v251\n\t"
    "v_mul_f32 v225, v225, v251\n\t"
    "v_cvt_pk_bf16_f32 v226, v218, v219\n\t"
    "v_cvt_pk_bf16_f32 v227, v220, v221\n\t"
    "v_cvt_pk_bf16_f32 v228, v222, v223\n\t"
    "v_cvt_pk_bf16_f32 v229, v224, v225\n\t"
    "s_add_u32 s39, s67, 0x80000\n\t"
    "buffer_store_dwordx4 v[226:229], v216, s[24:27], s39 offen offset:0 sc1\n\t"
    "v_accvgpr_read_b32 v230, a44\n\t"
    "v_accvgpr_read_b32 v231, a45\n\t"
    "v_accvgpr_read_b32 v232, a46\n\t"
    "v_accvgpr_read_b32 v233, a47\n\t"
    "v_accvgpr_read_b32 v234, a60\n\t"
    "v_accvgpr_read_b32 v235, a61\n\t"
    "v_accvgpr_read_b32 v236, a62\n\t"
    "v_accvgpr_read_b32 v237, a63\n\t"
    "v_mul_f32 v230, v230, v251\n\t"
    "v_mul_f32 v231, v231, v251\n\t"
    "v_mul_f32 v232, v232, v251\n\t"
    "v_mul_f32 v233, v233, v251\n\t"
    "v_mul_f32 v234, v234, v251\n\t"
    "v_mul_f32 v235, v235, v251\n\t"
    "v_mul_f32 v236, v236, v251\n\t"
    "v_mul_f32 v237, v237, v251\n\t"
    "v_cvt_pk_bf16_f32 v238, v230, v231\n\t"
    "v_cvt_pk_bf16_f32 v239, v232, v233\n\t"
    "v_cvt_pk_bf16_f32 v240, v234, v235\n\t"
    "v_cvt_pk_bf16_f32 v241, v236, v237\n\t"
    "buffer_store_dwordx4 v[238:241], v216, s[24:27], s39 offen offset:1024 sc1\n\t"
    "v_accvgpr_read_b32 v218, a76\n\t"
    "v_accvgpr_read_b32 v219, a77\n\t"
    "v_accvgpr_read_b32 v220, a78\n\t"
    "v_accvgpr_read_b32 v221, a79\n\t"
    "v_accvgpr_read_b32 v222, a92\n\t"
    "v_accvgpr_read_b32 v223, a93\n\t"
    "v_accvgpr_read_b32 v224, a94\n\t"
    "v_accvgpr_read_b32 v225, a95\n\t"
    "v_mul_f32 v218, v218, v251\n\t"
    "v_mul_f32 v219, v219, v251\n\t"
    "v_mul_f32 v220, v220, v251\n\t"
    "v_mul_f32 v221, v221, v251\n\t"
    "v_mul_f32 v222, v222, v251\n\t"
    "v_mul_f32 v223, v223, v251\n\t"
    "v_mul_f32 v224, v224, v251\n\t"
    "v_mul_f32 v225, v225, v251\n\t"
    "v_cvt_pk_bf16_f32 v226, v218, v219\n\t"
    "v_cvt_pk_bf16_f32 v227, v220, v221\n\t"
    "v_cvt_pk_bf16_f32 v228, v222, v223\n\t"
    "v_cvt_pk_bf16_f32 v229, v224, v225\n\t"
    "buffer_store_dwordx4 v[226:229], v216, s[24:27], s39 offen offset:2048 sc1\n\t"
    "v_accvgpr_read_b32 v230, a108\n\t"
    "v_accvgpr_read_b32 v231, a109\n\t"
    "v_accvgpr_read_b32 v232, a110\n\t"
    "v_accvgpr_read_b32 v233, a111\n\t"
    "v_accvgpr_read_b32 v234, a124\n\t"
    "v_accvgpr_read_b32 v235, a125\n\t"
    "v_accvgpr_read_b32 v236, a126\n\t"
    "v_accvgpr_read_b32 v237, a127\n\t"
    "v_mul_f32 v230, v230, v251\n\t"
    "v_mul_f32 v231, v231, v251\n\t"
    "v_mul_f32 v232, v232, v251\n\t"
    "v_mul_f32 v233, v233, v251\n\t"
    "v_mul_f32 v234, v234, v251\n\t"
    "v_mul_f32 v235, v235, v251\n\t"
    "v_mul_f32 v236, v236, v251\n\t"
    "v_mul_f32 v237, v237, v251\n\t"
    "v_cvt_pk_bf16_f32 v238, v230, v231\n\t"
    "v_cvt_pk_bf16_f32 v239, v232, v233\n\t"
    "v_cvt_pk_bf16_f32 v240, v234, v235\n\t"
    "v_cvt_pk_bf16_f32 v241, v236, v237\n\t"
    "buffer_store_dwordx4 v[238:241], v216, s[24:27], s39 offen offset:3072 sc1\n\t"
    "s_add_u32 m0, s97, 0x2000\n\t"
    "s_nop 0\n\t"
    "buffer_load_dwordx4 v203, s[12:15], s82 offen lds\n\t"
    "buffer_load_dwordx4 v204, s[12:15], s82 offen offset:1024 lds\n\t"
    "s_add_u32 s82, s82, 0x8000\n\t"
    "s_add_u32 m0, s97, 0x6000\n\t"
    "s_nop 0\n\t"
    "buffer_load_dwordx4 v203, s[12:15], s82 offen lds\n\t"
    "buffer_load_dwordx4 v204, s[12:15], s82 offen offset:1024 lds\n\t"
    "s_add_u32 s82, s82, 0x8000\n\t"
    "s_add_u32 m0, s97, 0xa000\n\t"
    "s_nop 0\n\t"
    "buffer_load_dwordx4 v203, s[12:15], s82 offen lds\n\t"
    "buffer_load_dwordx4 v204, s[12:15], s82 offen offset:1024 lds\n\t"
    "s_add_u32 s82, s82, 0x8000\n\t"
    "s_add_u32 m0, s97, 0xe000\n\t"
    "s_nop 0\n\t"
    "buffer_load_dwordx4 v203, s[12:15], s82 offen lds\n\t"
    "buffer_load_dwordx4 v204, s[12:15], s82 offen offset:1024 lds\n\t"
    "s_add_u32 s82, s82, 0x8000\n\t"
    "s_add_u32 m0, s97, 0x12000\n\t"
    "s_nop 0\n\t"
    "buffer_load_dwordx4 v203, s[12:15], s82 offen lds\n\t"
    "buffer_load_dwordx4 v204, s[12:15], s82 offen offset:1024 lds\n\t"
    "s_add_u32 s82, s82, 0x8000\n\t"
    "s_add_u32 m0, s97, 0x16000\n\t"
    "s_nop 0\n\t"
    "buffer_load_dwordx4 v203, s[12:15], s82 offen lds\n\t"
    "buffer_load_dwordx4 v204, s[12:15], s82 offen offset:1024 lds\n\t"
    "s_add_u32 s82, s82, 0x8000\n\t"
    "s_add_u32 m0, s97, 0x1a000\n\t"
    "s_nop 0\n\t"
    "buffer_load_dwordx4 v203, s[12:15], s82 offen lds\n\t"
    "buffer_load_dwordx4 v204, s[12:15], s82 offen offset:1024 lds\n\t"
    "s_add_u32 s82, s82, 0x8000\n\t"
    "s_add_u32 m0, s97, 0x1e000\n\t"
    "s_nop 0\n\t"
    "buffer_load_dwordx4 v203, s[12:15], s82 offen lds\n\t"
    "buffer_load_dwordx4 v204, s[12:15], s82 offen offset:1024 lds\n\t"
    "s_add_u32 s82, s82, 0x8000\n\t"
    "s_waitcnt vmcnt(16)\n\t"
    "s_barrier\n\t"
    "s_cmp_lg_u32 s33, 0\n\t"
    "s_cbranch_scc1 L_s2\n\t"
    "v_mov_b32 v226, v198\n\t"
    "v_mov_b32 v227, v199\n\t"
    "v_mov_b32 v228, s76\n\t"
    "v_mov_b32 v229, s77\n\t"
    "buffer_store_dwordx4 v[226:229], v201, s[28:31], 0 offen sc1\n\t"
    "s_nop 1\n\t"
    "v_mov_b32 v228, s76\n\t"
    "v_mov_b32 v229, s77\n\t"
    "v_mov_b32 v234, v198\n\t"
    "v_mov_b32 v235, v199\n\t"
    "s_mov_b32 s40, 0x40000\n\t"
    "L_poll2:\n\t"
    "buffer_load_dwordx4 v[230:233], v202, s[28:31], 0 offen sc1\n\t"
    "s_waitcnt vmcnt(0)\n\t"
    "v_cmp_eq_u64 vcc, s[76:77], v[230:231]\n\t"
    "s_nop 1\n\t"
    "s_cmp_eq_u64 vcc, -1\n\t"
    "s_cbranch_scc1 L_poll2_done\n\t"
    "s_sub_u32 s40, s40, 1\n\t"
    "s_cmp_eq_u32 s40, 0\n\t"
    "s_cbranch_scc1 L_poll2_done\n\t"
    "s_and_b32 s37, s40, 15\n\t"
    "s_cmp_lg_u32 s37, 0\n\t"
    "s_cbranch_scc1 L_poll2\n\t"
    "buffer_load_dwordx2 v[234:235], v200, s[28:31], 0 offen sc1\n\t"
    "s_waitcnt vmcnt(0)\n\t"
    "v_cndmask_b32 v226, v234, v232, vcc\n\t"
    "v_cndmask_b32 v227, v235, v233, vcc\n\t"
    "buffer_store_dwordx4 v[226:229], v201, s[28:31], 0 offen sc1\n\t"
    "s_branch L_poll2\n\t"
    "L_poll2_done:\n\t"
    "s_nop 0\n\t"
    "v_cndmask_b32 v226, v234, v232, vcc\n\t"
    "v_cndmask_b32 v227, v235, v233, vcc\n\t"
    "buffer_store_dwordx4 v[226:229], v201, s[28:31], 0 offen sc1\n\t"
    "L_s2:\n\t"
    "s_barrier\n\t"
    "s_add_u32 m0, s97, 0x0\n\t"
    "s_nop 0\n\t"
    "buffer_load_dwordx4 v216, s[24:27], s80 offen sc1 lds\n\t"
    "buffer_load_dwordx4 v216, s[24:27], s81 offen offset:1024 sc1 lds\n\t"
    "s_add_u32 s80, s80, 0x400\n\t"
    "s_add_u32 s81, s81, 0x400\n\t"
    "s_add_u32 m0, s97, 0x4000\n\t"
    "s_nop 0\n\t"
    "buffer_load_dwordx4 v216, s[24:27], s80 offen sc1 lds\n\t"
    "buffer_load_dwordx4 v216, s[24:27], s81 offen offset:1024 sc1 lds\n\t"
    "s_add_u32 s80, s80, 0x400\n\t"
    "s_add_u32 s81, s81, 0x400\n\t"
    "s_add_u32 m0, s97, 0x8000\n\t"
    "s_nop 0\n\t"
    "buffer_load_dwordx4 v216, s[24:27], s80 offen sc1 lds\n\t"
    "buffer_load_dwordx4 v216, s[24:27], s81 offen offset:1024 sc1 lds\n\t"
    "s_add_u32 s80, s80, 0x400\n\t"
    "s_add_u32 s81, s81, 0x400\n\t"
    "s_add_u32 m0, s97, 0xc000\n\t"
    "s_nop 0\n\t"
    "buffer_load_dwordx4 v216, s[24:27], s80 offen sc1 lds\n\t"
    "buffer_load_dwordx4 v216, s[24:27], s81 offen offset:1024 sc1 lds\n\t"
    "s_add_u32 s80, s80, 0x400\n\t"
    "s_add_u32 s81, s81, 0x400\n\t"
    "s_add_u32 m0, s97, 0x10000\n\t"
    "s_nop 0\n\t"
    "buffer_load_dwordx4 v216, s[24:27], s80 offen sc1 lds\n\t"
    "buffer_load_dwordx4 v216, s[24:27], s81 offen offset:1024 sc1 lds\n\t"
    "s_add_u32 s80, s80, 0x400\n\t"
    "s_add_u32 s81, s81, 0x400\n\t"
    "s_add_u32 m0, s97, 0x14000\n\t"
    "s_nop 0\n\t"
    "buffer_load_dwordx4 v216, s[24:27], s80 offen sc1 lds\n\t"
    "buffer_load_dwordx4 v216, s[24:27], s81 offen offset:1024 sc1 lds\n\t"
    "s_add_u32 s80, s80, 0x400\n\t"
    "s_add_u32 s81, s81, 0x400\n\t"
    "s_add_u32 m0, s97, 0x18000\n\t"
    "s_nop 0\n\t"
    "buffer_load_dwordx4 v216, s[24:27], s80 offen sc1 lds\n\t"
    "buffer_load_dwordx4 v216, s[24:27], s81 offen offset:1024 sc1 lds\n\t"
    "s_add_u32 s80, s80, 0x400\n\t"
    "s_add_u32 s81, s81, 0x400\n\t"
    "s_add_u32 m0, s97, 0x1c000\n\t"
    "s_nop 0\n\t"
    "buffer_load_dwordx4 v216, s[24:27], s80 offen sc1 lds\n\t"
    "buffer_load_dwordx4 v216, s[24:27], s81 offen offset:1024 sc1 lds\n\t"
    "s_add_u32 s80, s80, 0x400\n\t"
    "s_add_u32 s81, s81, 0x400\n\t"
    "s_mov_b32 s51, 0xfffdc000\n\t"
    "s_mov_b32 s52, 0x4000\n\t"
    "s_waitcnt vmcnt(10)\n\t"
    "s_barrier\n\t"
    "ds_read_b64_tr_b16 v[16:17], v207 offset:0\n\t"
    "ds_read_b64_tr_b16 v[18:19], v207 offset:4096\n\t"
    "ds_read_b128 v[0:3], v205 offset:0\n\t"
    "ds_read_b128 v[4:7], v205 offset:1024\n\t"
    "ds_read_b128 v[8:11], v205 offset:2048\n\t"
    "ds_read_b128 v[12:15], v205 offset:3072\n\t"
    "ds_read_b64_tr_b16 v[20:21], v208 offset:0\n\t"
    "ds_read_b64_tr_b16 v[22:23], v208 offset:4096\n\t"
    "ds_read_b64_tr_b16 v[24:25], v209 offset:0\n\t"
    "ds_read_b64_tr_b16 v[26:27], v209 offset:4096\n\t"
    "ds_read_b64_tr_b16 v[28:29], v210 offset:0\n\t"
    "ds_read_b64_tr_b16 v[30:31], v210 offset:4096\n\t"
    "s_waitcnt lgkmcnt(9)\n\t"
    "v_mfma_f32_16x16x32_bf16 v[64:67], v[16:19], v[0:3], 0\n\t"
    "s_waitcnt lgkmcnt(8)\n\t"
    "v_add_u32 v205, 0x4000, v205\n\t"
    "v_add_u32 v206, 0x4000, v206\n\t"
    "v_mfma_f32_16x16x32_bf16 v[68:71], v[16:19], v[4:7], 0\n\t"
    "s_waitcnt lgkmcnt(7)\n\t"
    "s_mov_b32 m0, s86\n\t"
    "v_add_u32 v207, 0x4000, v207\n\t"
    "v_add_u32 v208, 0x4000, v208\n\t"
    "v_mfma_f32_16x16x32_bf16 v[72:75], v[16:19], v[8:11], 0\n\t"
    "s_waitcnt lgkmcnt(6)\n\t"
    "buffer_load_dwordx4 v216, s[24:27], s80 offen sc1 lds\n\t"
    "v_add_u32 v209, 0x4000, v209\n\t"
    "v_add_u32 v210, 0x4000, v210\n\t"
    "v_mfma_f32_16x16x32_bf16 v[76:79], v[16:19], v[12:15], 0\n\t"
    "s_waitcnt lgkmcnt(4)\n\t"
    "ds_read_b64_tr_b16 v[48:49], v207 offset:0\n\t"
    "v_mfma_f32_16x16x32_bf16 v[80:83], v[20:23], v[0:3], 0\n\t"
    "ds_read_b64_tr_b16 v[50:51], v207 offset:4096\n\t"
    "v_mfma_f32_16x16x32_bf16 v[84:87], v[20:23], v[4:7], 0\n\t"
    "ds_read_b128 v[32:35], v205 offset:0\n\t"
    "v_mfma_f32_16x16x32_bf16 v[88:91], v[20:23], v[8:11], 0\n\t"
    "ds_read_b128 v[36:39], v205 offset:1024\n\t"
    "buffer_load_dwordx4 v216, s[24:27], s81 offen offset:1024 sc1 lds\n\t"
    "v_mfma_f32_16x16x32_bf16 v[92:95], v[20:23], v[12:15], 0\n\t"
    "s_waitcnt lgkmcnt(6)\n\t"
    "s_add_u32 s80, s80, 0x400\n\t"
    "ds_read_b128 v[40:43], v205 offset:2048\n\t"
    "v_mfma_f32_16x16x32_bf16 v[96:99], v[24:27], v[0:3], 0\n\t"
    "s_add_u32 s81, s81, 0x400\n\t"
    "ds_read_b128 v[44:47], v205 offset:3072\n\t"
    "v_mfma_f32_16x16x32_bf16 v[100:103], v[24:27], v[4:7], 0\n\t"
    "s_add_u32 m0, s86, 0x2000\n\t"
    "ds_read_b64_tr_b16 v[52:53], v208 offset:0\n\t"
    "v_mfma_f32_16x16x32_bf16 v[104:107], v[24:27], v[8:11], 0\n\t"
    "ds_read_b64_tr_b16 v[54:55], v208 offset:4096\n\t"
    "buffer_load_dwordx4 v203, s[12:15], s82 offen lds\n\t"
    "v_mfma_f32_16x16x32_bf16 v[108:111], v[24:27], v[12:15], 0\n\t"
    "s_waitcnt lgkmcnt(8)\n\t"
    "ds_read_b64_tr_b16 v[56:57], v209 offset:0\n\t"
    "v_mfma_f32_16x16x32_bf16 v[112:115], v[28:31], v[0:3], 0\n\t"
    "ds_read_b64_tr_b16 v[58:59], v209 offset:4096\n\t"
    "v_mfma_f32_16x16x32_bf16 v[116:119], v[28:31], v[4:7], 0\n\t"
    "ds_read_b64_tr_b16 v[60:61], v210 offset:0\n\t"
    "v_mfma_f32_16x16x32_bf16 v[120:123], v[28:31], v[8:11], 0\n\t"
    "ds_read_b64_tr_b16 v[62:63], v210 offset:4096\n\t"
    "buffer_load_dwordx4 v204, s[12:15], s82 offen offset:1024 lds\n\t"
    "v_mfma_f32_16x16x32_bf16 v[124:127], v[28:31], v[12:15], 0\n\t"
    "s_waitcnt lgkmcnt(9)\n\t"
    "s_add_u32 s82, s82, 0x8000\n\t"
    "v_mfma_f32_16x16x32_bf16 v[64:67], v[48:51], v[32:35], v[64:67]\n\t"
    "s_waitcnt lgkmcnt(8)\n\t"
    "s_add_u32 s87, s87, 0x8000\n\t"
    "s_cmp_eq_u32 s87, 0x28000\n\t"
    "s_cselect_b32 s88, s51, s52\n\t"
    "v_mfma_f32_16x16x32_bf16 v[68:71], v[48:51], v[36:39], v[68:71]\n\t"
    "s_waitcnt lgkmcnt(7)\n\t"
    "s_cselect_b32 s87, 0, s87\n\t"
    "s_add_u32 m0, s86, 0x4000\n\t"
    "v_add_u32 v205, s88, v205\n\t"
    "v_add_u32 v206, s88, v206\n\t"
    "v_mfma_f32_16x16x32_bf16 v[72:75], v[48:51], v[40:43], v[72:75]\n\t"
    "s_waitcnt lgkmcnt(6)\n\t"
    "buffer_load_dwordx4 v216, s[24:27], s80 offen sc1 lds\n\t"
    "v_add_u32 v207, s88, v207\n\t"
    "v_add_u32 v208, s88, v208\n\t"
    "v_mfma_f32_16x16x32_bf16 v[76:79], v[48:51], v[44:47], v[76:79]\n\t"
    "s_waitcnt lgkmcnt(4)\n\t"
    "v_add_u32 v209, s88, v209\n\t"
    "v_add_u32 v210, s88, v210\n\t"
    "v_mfma_f32_16x16x32_bf16 v[80:83], v[52:55], v[32:35], v[80:83]\n\t"
    "ds_read_b64_tr_b16 v[16:17], v207 offset:0\n\t"
    "v_mfma_f32_16x16x32_bf16 v[84:87], v[52:55], v[36:39], v[84:87]\n\t"
    "ds_read_b64_tr_b16 v[18:19], v207 offset:4096\n\t"
    "v_mfma_f32_16x16x32_bf16 v[88:91], v[52:55], v[40:43], v[88:91]\n\t"
    "ds_read_b128 v[0:3], v205 offset:0\n\t"
    "buffer_load_dwordx4 v216, s[24:27], s81 offen offset:1024 sc1 lds\n\t"
    "v_mfma_f32_16x16x32_bf16 v[92:95], v[52:55], v[44:47], v[92:95]\n\t"
    "s_waitcnt lgkmcnt(5)\n\t"
    "s_add_u32 s80, s80, 0x400\n\t"
    "ds_read_b128 v[4:7], v205 offset:1024\n\t"
    "v_mfma_f32_16x16x32_bf16 v[96:99], v[56:59], v[32:35], v[96:99]\n\t"
    "s_add_u32 s81, s81, 0x400\n\t"
    "ds_read_b128 v[8:11], v205 offset:2048\n\t"
    "v_mfma_f32_16x16x32_bf16 v[100:103], v[56:59], v[36:39], v[100:103]\n\t"
    "s_add_u32 m0, s86, 0x6000\n\t"
    "ds_read_b128 v[12:15], v205 offset:3072\n\t"
    "v_mfma_f32_16x16x32_bf16 v[104:107], v[56:59], v[40:43], v[104:107]\n\t"
    "s_add_u32 s86, s86, 0x8000\n\t"
    "s_cmp_ge_u32 s86, 0x28000\n\t"
    "s_cselect_b32 s37, 0x28000, 0\n\t"
    "ds_read_b64_tr_b16 v[20:21], v208 offset:0\n\t"
    "buffer_load_dwordx4 v203, s[12:15], s82 offen lds\n\t"
    "v_mfma_f32_16x16x32_bf16 v[108:111], v[56:59], v[44:47], v[108:111]\n\t"
    "s_waitcnt lgkmcnt(7)\n\t"
    "ds_read_b64_tr_b16 v[22:23], v208 offset:4096\n\t"
    "v_mfma_f32_16x16x32_bf16 v[112:115], v[60:63], v[32:35], v[112:115]\n\t"
    "s_sub_u32 s86, s86, s37\n\t"
    "ds_read_b64_tr_b16 v[24:25], v209 offset:0\n\t"
    "v_mfma_f32_16x16x32_bf16 v[116:119], v[60:63], v[36:39], v[116:119]\n\t"
    "s_waitcnt vmcnt(13)\n\t"
    "ds_read_b64_tr_b16 v[26:27], v209 offset:4096\n\t"
    "v_mfma_f32_16x16x32_bf16 v[120:123], v[60:63], v[40:43], v[120:123]\n\t"
    "s_barrier\n\t"
    "ds_read_b64_tr_b16 v[28:29], v210 offset:0\n\t"
    "buffer_load_dwordx4 v204, s[12:15], s82 offen offset:1024 lds\n\t"
    "v_mfma_f32_16x16x32_bf16 v[124:127], v[60:63], v[44:47], v[124:127]\n\t"
    "s_add_u32 s82, s82, 0x8000\n\t"
    "ds_read_b64_tr_b16 v[30:31], v210 offset:4096\n\t"
    "s_waitcnt lgkmcnt(9)\n\t"
    "v_mfma_f32_16x16x32_bf16 v[64:67], v[16:19], v[0:3], v[64:67]\n\t"
    "s_waitcnt lgkmcnt(8)\n\t"
    "v_add_u32 v205, 0x4000, v205\n\t"
    "v_add_u32 v206, 0x4000, v206\n\t"
    "v_mfma_f32_16x16x32_bf16 v[68:71], v[16:19], v[4:7], v[68:71]\n\t"
    "s_waitcnt lgkmcnt(7)\n\t"
    "s_mov_b32 m0, s86\n\t"
    "v_add_u32 v207, 0x4000, v207\n\t"
    "v_add_u32 v208, 0x4000, v208\n\t"
    "v_mfma_f32_16x16x32_bf16 v[72:75], v[16:19], v[8:11], v[72:75]\n\t"
    "s_waitcnt lgkmcnt(6)\n\t"
    "buffer_load_dwordx4 v216, s[24:27], s80 offen sc1 lds\n\t"
    "v_add_u32 v209, 0x4000, v209\n\t"
    "v_add_u32 v210, 0x4000, v210\n\t"
    "v_mfma_f32_16x16x32_bf16 v[76:79], v[16:19], v[12:15], v[76:79]\n\t"
    "s_waitcnt lgkmcnt(4)\n\t"
    "ds_read_b64_tr_b16 v[48:49], v207 offset:0\n\t"
    "v_mfma_f32_16x16x32_bf16 v[80:83], v[20:23], v[0:3], v[80:83]\n\t"
    "ds_read_b64_tr_b16 v[50:51], v207 offset:4096\n\t"
    "v_mfma_f32_16x16x32_bf16 v[84:87], v[20:23], v[4:7], v[84:87]\n\t"
    "ds_read_b128 v[32:35], v205 offset:0\n\t"
    "v_mfma_f32_16x16x32_bf16 v[88:91], v[20:23], v[8:11], v[88:91]\n\t"
    "ds_read_b128 v[36:39], v205 offset:1024\n\t"
    "buffer_load_dwordx4 v216, s[24:27], s81 offen offset:1024 sc1 lds\n\t"
    "v_mfma_f32_16x16x32_bf16 v[92:95], v[20:23], v[12:15], v[92:95]\n\t"
    "s_waitcnt lgkmcnt(6)\n\t"
    "s_add_u32 s80, s80, 0x400\n\t"
    "ds_read_b128 v[40:43], v205 offset:2048\n\t"
    "v_mfma_f32_16x16x32_bf16 v[96:99], v[24:27], v[0:3], v[96:99]\n\t"
    "s_add_u32 s81, s81, 0x400\n\t"
    "ds_read_b128 v[44:47], v205 offset:3072\n\t"
    "v_mfma_f32_16x16x32_bf16 v[100:103], v[24:27], v[4:7], v[100:103]\n\t"
    "s_add_u32 m0, s86, 0x2000\n\t"
    "ds_read_b64_tr_b16 v[52:53], v208 offset:0\n\t"
    "v_mfma_f32_16x16x32_bf16 v[104:107], v[24:27], v[8:11], v[104:107]\n\t"
    "ds_read_b64_tr_b16 v[54:55], v208 offset:4096\n\t"
    "buffer_load_dwordx4 v203, s[12:15], s82 offen lds\n\t"
    "v_mfma_f32_16x16x32_bf16 v[108:111], v[24:27], v[12:15], v[108:111]\n\t"
    "s_waitcnt lgkmcnt(8)\n\t"
    "ds_read_b64_tr_b16 v[56:57], v209 offset:0\n\t"
    "v_mfma_f32_16x16x32_bf16 v[112:115], v[28:31], v[0:3], v[112:115]\n\t"
    "ds_read_b64_tr_b16 v[58:59], v209 offset:4096\n\t"
    "v_mfma_f32_16x16x32_bf16 v[116:119], v[28:31], v[4:7], v[116:119]\n\t"
    "ds_read_b64_tr_b16 v[60:61], v210 offset:0\n\t"
    "v_mfma_f32_16x16x32_bf16 v[120:123], v[28:31], v[8:11], v[120:123]\n\t"
    "ds_read_b64_tr_b16 v[62:63], v210 offset:4096\n\t"
    "buffer_load_dwordx4 v204, s[12:15], s82 offen offset:1024 lds\n\t"
    "v_mfma_f32_16x16x32_bf16 v[124:127], v[28:31], v[12:15], v[124:127]\n\t"
    "s_waitcnt lgkmcnt(9)\n\t"
    "s_add_u32 s82, s82, 0x8000\n\t"
    "v_mfma_f32_16x16x32_bf16 v[64:67], v[48:51], v[32:35], v[64:67]\n\t"
    "s_waitcnt lgkmcnt(8)\n\t"
    "s_add_u32 s87, s87, 0x8000\n\t"
    "s_cmp_eq_u32 s87, 0x28000\n\t"
    "s_cselect_b32 s88, s51, s52\n\t"
    "v_mfma_f32_16x16x32_bf16 v[68:71], v[48:51], v[36:39], v[68:71]\n\t"
    "s_waitcnt lgkmcnt(7)\n\t"
    "s_cselect_b32 s87, 0, s87\n\t"
    "s_add_u32 m0, s86, 0x4000\n\t"
    "v_add_u32 v205, s88, v205\n\t"
    "v_add_u32 v206, s88, v206\n\t"
    "v_mfma_f32_16x16x32_bf16 v[72:75], v[48:51], v[40:43], v[72:75]\n\t"
    "s_waitcnt lgkmcnt(6)\n\t"
    "buffer_load_dwordx4 v216, s[24:27], s80 offen sc1 lds\n\t"
    "v_add_u32 v207, s88, v207\n\t"
    "v_add_u32 v208, s88, v208\n\t"
    "v_mfma_f32_16x16x32_bf16 v[76:79], v[48:51], v[44:47], v[76:79]\n\t"
    "s_waitcnt lgkmcnt(4)\n\t"
    "v_add_u32 v209, s88, v209\n\t"
    "v_add_u32 v210, s88, v210\n\t"
    "v_mfma_f32_16x16x32_bf16 v[80:83], v[52:55], v[32:35], v[80:83]\n\t"
    "ds_read_b64_tr_b16 v[16:17], v207 offset:0\n\t"
    "v_mfma_f32_16x16x32_bf16 v[84:87], v[52:55], v[36:39], v[84:87]\n\t"
    "ds_read_b64_tr_b16 v[18:19], v207 offset:4096\n\t"
    "v_mfma_f32_16x16x32_bf16 v[88:91], v[52:55], v[40:43], v[88:91]\n\t"
    "ds_read_b128 v[0:3], v205 offset:0\n\t"
    "buffer_load_dwordx4 v216, s[24:27], s81 offen offset:1024 sc1 lds\n\t"
    "v_mfma_f32_16x16x32_bf16 v[92:95], v[52:55], v[44:47], v[92:95]\n\t"
    "s_waitcnt lgkmcnt(5)\n\t"
    "s_add_u32 s80, s80, 0x400\n\t"
    "ds_read_b128 v[4:7], v205 offset:1024\n\t"
    "v_mfma_f32_16x16x32_bf16 v[96:99], v[56:59], v[32:35], v[96:99]\n\t"
    "s_add_u32 s81, s81, 0x400\n\t"
    "ds_read_b128 v[8:11], v205 offset:2048\n\t"
    "v_mfma_f32_16x16x32_bf16 v[100:103], v[56:59], v[36:39], v[100:103]\n\t"
    "s_add_u32 m0, s86, 0x6000\n\t"
    "ds_read_b128 v[12:15], v205 offset:3072\n\t"
    "v_mfma_f32_16x16x32_bf16 v[104:107], v[56:59], v[40:43], v[104:107]\n\t"
    "s_add_u32 s86, s86, 0x8000\n\t"
    "s_cmp_ge_u32 s86, 0x28000\n\t"
    "s_cselect_b32 s37, 0x28000, 0\n\t"
    "ds_read_b64_tr_b16 v[20:21], v208 offset:0\n\t"
    "buffer_load_dwordx4 v203, s[12:15], s82 offen lds\n\t"
    "v_mfma_f32_16x16x32_bf16 v[108:111], v[56:59], v[44:47], v[108:111]\n\t"
    "s_waitcnt lgkmcnt(7)\n\t"
    "ds_read_b64_tr_b16 v[22:23], v208 offset:4096\n\t"
    "v_mfma_f32_16x16x32_bf16 v[112:115], v[60:63], v[32:35], v[112:115]\n\t"
    "s_sub_u32 s86, s86, s37\n\t"
    "ds_read_b64_tr_b16 v[24:25], v209 offset:0\n\t"
    "v_mfma_f32_16x16x32_bf16 v[116:119], v[60:63], v[36:39], v[116:119]\n\t"
    "s_waitcnt vmcnt(17)\n\t"
    "ds_read_b64_tr_b16 v[26:27], v209 offset:4096\n\t"
    "v_mfma_f32_16x16x32_bf16 v[120:123], v[60:63], v[40:43], v[120:123]\n\t"
    "s_barrier\n\t"
    "ds_read_b64_tr_b16 v[28:29], v210 offset:0\n\t"
    "buffer_load_dwordx4 v204, s[12:15], s82 offen offset:1024 lds\n\t"
    "v_mfma_f32_16x16x32_bf16 v[124:127], v[60:63], v[44:47], v[124:127]\n\t"
    "s_add_u32 s82, s82, 0x8000\n\t"
    "ds_read_b64_tr_b16 v[30:31], v210 offset:4096\n\t"
    "s_mov_b32 s40, 10\n\t"
    "L_o4_loopb:\n\t"
    "s_waitcnt lgkmcnt(9)\n\t"
    "v_mfma_f32_16x16x32_bf16 v[64:67], v[16:19], v[0:3], v[64:67]\n\t"
    "s_waitcnt lgkmcnt(8)\n\t"
    "v_add_u32 v205, 0x4000, v205\n\t"
    "v_add_u32 v206, 0x4000, v206\n\t"
    "v_mfma_f32_16x16x32_bf16 v[68:71], v[16:19], v[4:7], v[68:71]\n\t"
    "s_waitcnt lgkmcnt(7)\n\t"
    "s_mov_b32 m0, s86\n\t"
    "v_add_u32 v207, 0x4000, v207\n\t"
    "v_add_u32 v208, 0x4000, v208\n\t"
    "v_mfma_f32_16x16x32_bf16 v[72:75], v[16:19], v[8:11], v[72:75]\n\t"
    "s_waitcnt lgkmcnt(6)\n\t"
    "buffer_load_dwordx4 v216, s[24:27], s80 offen sc1 lds\n\t"
    "v_add_u32 v209, 0x4000, v209\n\t"
    "v_add_u32 v210, 0x4000, v210\n\t"
    "v_mfma_f32_16x16x32_bf16 v[76:79], v[16:19], v[12:15], v[76:79]\n\t"
    "s_waitcnt lgkmcnt(4)\n\t"
    "ds_read_b64_tr_b16 v[48:49], v207 offset:0\n\t"
    "v_mfma_f32_16x16x32_bf16 v[80:83], v[20:23], v[0:3], v[80:83]\n\t"
    "ds_read_b64_tr_b16 v[50:51], v207 offset:4096\n\t"
    "v_mfma_f32_16x16x32_bf16 v[84:87], v[20:23], v[4:7], v[84:87]\n\t"
    "ds_read_b128 v[32:35], v205 offset:0\n\t"
    "v_mfma_f32_16x16x32_bf16 v[88:91], v[20:23], v[8:11], v[88:91]\n\t"
    "ds_read_b128 v[36:39], v205 offset:1024\n\t"
    "buffer_load_dwordx4 v216, s[24:27], s81 offen offset:1024 sc1 lds\n\t"
    "v_mfma_f32_16x16x32_bf16 v[92:95], v[20:23], v[12:15], v[92:95]\n\t"
    "s_waitcnt lgkmcnt(6)\n\t"
    "s_add_u32 s80, s80, 0x400\n\t"
    "ds_read_b128 v[40:43], v205 offset:2048\n\t"
    "v_mfma_f32_16x16x32_bf16 v[96:99], v[24:27], v[0:3], v[96:99]\n\t"
    "s_add_u32 s81, s81, 0x400\n\t"
    "ds_read_b128 v[44:47], v205 offset:3072\n\t"
    "v_mfma_f32_16x16x32_bf16 v[100:103], v[24:27], v[4:7], v[100:103]\n\t"
    "s_add_u32 m0, s86, 0x2000\n\t"
    "ds_read_b64_tr_b16 v[52:53], v208 offset:0\n\t"
    "v_mfma_f32_16x16x32_bf16 v[104:107], v[24:27], v[8:11], v[104:107]\n\t"
    "ds_read_b64_tr_b16 v[54:55], v208 offset:4096\n\t"
    "buffer_load_dwordx4 v203, s[12:15], s82 offen lds\n\t"
    "v_mfma_f32_16x16x32_bf16 v[108:111], v[24:27], v[12:15], v[108:111]\n\t"
    "s_waitcnt lgkmcnt(8)\n\t"
    "ds_read_b64_tr_b16 v[56:57], v209 offset:0\n\t"
    "v_mfma_f32_16x16x32_bf16 v[112:115], v[28:31], v[0:3], v[112:115]\n\t"
    "ds_read_b64_tr_b16 v[58:59], v209 offset:4096\n\t"
    "v_mfma_f32_16x16x32_bf16 v[116:119], v[28:31], v[4:7], v[116:119]\n\t"
    "ds_read_b64_tr_b16 v[60:61], v210 offset:0\n\t"
    "v_mfma_f32_16x16x32_bf16 v[120:123], v[28:31], v[8:11], v[120:123]\n\t"
    "ds_read_b64_tr_b16 v[62:63], v210 offset:4096\n\t"
    "buffer_load_dwordx4 v204, s[12:15], s82 offen offset:1024 lds\n\t"
    "v_mfma_f32_16x16x32_bf16 v[124:127], v[28:31], v[12:15], v[124:127]\n\t"
    "s_waitcnt lgkmcnt(9)\n\t"
    "s_add_u32 s82, s82, 0x8000\n\t"
    "v_mfma_f32_16x16x32_bf16 v[64:67], v[48:51], v[32:35], v[64:67]\n\t"
    "s_waitcnt lgkmcnt(8)\n\t"
    "s_add_u32 s87, s87, 0x8000\n\t"
    "s_cmp_eq_u32 s87, 0x28000\n\t"
    "s_cselect_b32 s88, s51, s52\n\t"
    "v_mfma_f32_16x16x32_bf16 v[68:71], v[48:51], v[36:39], v[68:71]\n\t"
    "s_waitcnt lgkmcnt(7)\n\t"
    "s_cselect_b32 s87, 0, s87\n\t"
    "s_add_u32 m0, s86, 0x4000\n\t"
    "v_add_u32 v205, s88, v205\n\t"
    "v_add_u32 v206, s88, v206\n\t"
    "v_mfma_f32_16x16x32_bf16 v[72:75], v[48:51], v[40:43], v[72:75]\n\t"
    "s_waitcnt lgkmcnt(6)\n\t"
    "buffer_load_dwordx4 v216, s[24:27], s80 offen sc1 lds\n\t"
    "v_add_u32 v207, s88, v207\n\t"
    "v_add_u32 v208, s88, v208\n\t"
    "v_mfma_f32_16x16x32_bf16 v[76:79], v[48:51], v[44:47], v[76:79]\n\t"
    "s_waitcnt lgkmcnt(4)\n\t"
    "v_add_u32 v209, s88, v209\n\t"
    "v_add_u32 v210, s88, v210\n\t"
    "v_mfma_f32_16x16x32_bf16 v[80:83], v[52:55], v[32:35], v[80:83]\n\t"
    "ds_read_b64_tr_b16 v[16:17], v207 offset:0\n\t"
    "v_mfma_f32_16x16x32_bf16 v[84:87], v[52:55], v[36:39], v[84:87]\n\t"
    "ds_read_b64_tr_b16 v[18:19], v207 offset:4096\n\t"
    "v_mfma_f32_16x16x32_bf16 v[88:91], v[52:55], v[40:43], v[88:91]\n\t"
    "ds_read_b128 v[0:3], v205 offset:0\n\t"
    "buffer_load_dwordx4 v216, s[24:27], s81 offen offset:1024 sc1 lds\n\t"
    "v_mfma_f32_16x16x32_bf16 v[92:95], v[52:55], v[44:47], v[92:95]\n\t"
    "s_waitcnt lgkmcnt(5)\n\t"
    "s_add_u32 s80, s80, 0x400\n\t"
    "ds_read_b128 v[4:7], v205 offset:1024\n\t"
    "v_mfma_f32_16x16x32_bf16 v[96:99], v[56:59], v[32:35], v[96:99]\n\t"
    "s_add_u32 s81, s81, 0x400\n\t"
    "ds_read_b128 v[8:11], v205 offset:2048\n\t"
    "v_mfma_f32_16x16x32_bf16 v[100:103], v[56:59], v[36:39], v[100:103]\n\t"
    "s_add_u32 m0, s86, 0x6000\n\t"
    "ds_read_b128 v[12:15], v205 offset:3072\n\t"
    "v_mfma_f32_16x16x32_bf16 v[104:107], v[56:59], v[40:43], v[104:107]\n\t"
    "s_add_u32 s86, s86, 0x8000\n\t"
    "s_cmp_ge_u32 s86, 0x28000\n\t"
    "s_cselect_b32 s37, 0x28000, 0\n\t"
    "ds_read_b64_tr_b16 v[20:21], v208 offset:0\n\t"
    "buffer_load_dwordx4 v203, s[12:15], s82 offen lds\n\t"
    "v_mfma_f32_16x16x32_bf16 v[108:111], v[56:59], v[44:47], v[108:111]\n\t"
    "s_waitcnt lgkmcnt(7)\n\t"
    "ds_read_b64_tr_b16 v[22:23], v208 offset:4096\n\t"
    "v_mfma_f32_16x16x32_bf16 v[112:115], v[60:63], v[32:35], v[112:115]\n\t"
    "s_sub_u32 s86, s86, s37\n\t"
    "ds_read_b64_tr_b16 v[24:25], v209 offset:0\n\t"
    "v_mfma_f32_16x16x32_bf16 v[116:119], v[60:63], v[36:39], v[116:119]\n\t"
    "s_waitcnt vmcnt(19)\n\t"
    "ds_read_b64_tr_b16 v[26:27], v209 offset:4096\n\t"
    "v_mfma_f32_16x16x32_bf16 v[120:123], v[60:63], v[40:43], v[120:123]\n\t"
    "s_barrier\n\t"
    "ds_read_b64_tr_b16 v[28:29], v210 offset:0\n\t"
    "buffer_load_dwordx4 v204, s[12:15], s82 offen offset:1024 lds\n\t"
    "v_mfma_f32_16x16x32_bf16 v[124:127], v[60:63], v[44:47], v[124:127]\n\t"
    "s_add_u32 s82, s82, 0x8000\n\t"
    "ds_read_b64_tr_b16 v[30:31], v210 offset:4096\n\t"
    "s_sub_u32 s40, s40, 1\n\t"
    "s_cmp_lg_u32 s40, 0\n\t"
    "s_cbranch_scc1 L_o4_loopb\n\t"
    "s_mov_b32 s40, 2\n\t"
    "L_o4_loopc:\n\t"
    "s_waitcnt lgkmcnt(9)\n\t"
    "v_mfma_f32_16x16x32_bf16 v[64:67], v[16:19], v[0:3], v[64:67]\n\t"
    "s_waitcnt lgkmcnt(8)\n\t"
    "v_add_u32 v205, 0x4000, v205\n\t"
    "v_add_u32 v206, 0x4000, v206\n\t"
    "v_mfma_f32_16x16x32_bf16 v[68:71], v[16:19], v[4:7], v[68:71]\n\t"
    "s_waitcnt lgkmcnt(7)\n\t"
    "s_mov_b32 m0, s86\n\t"
    "v_add_u32 v207, 0x4000, v207\n\t"
    "v_add_u32 v208, 0x4000, v208\n\t"
    "v_mfma_f32_16x16x32_bf16 v[72:75], v[16:19], v[8:11], v[72:75]\n\t"
    "s_waitcnt lgkmcnt(6)\n\t"
    "buffer_load_dwordx4 v216, s[28:31], s83 offen sc1 lds\n\t"
    "v_add_u32 v209, 0x4000, v209\n\t"
    "v_add_u32 v210, 0x4000, v210\n\t"
    "v_mfma_f32_16x16x32_bf16 v[76:79], v[16:19], v[12:15], v[76:79]\n\t"
    "s_waitcnt lgkmcnt(4)\n\t"
    "ds_read_b64_tr_b16 v[48:49], v207 offset:0\n\t"
    "v_mfma_f32_16x16x32_bf16 v[80:83], v[20:23], v[0:3], v[80:83]\n\t"
    "ds_read_b64_tr_b16 v[50:51], v207 offset:4096\n\t"
    "v_mfma_f32_16x16x32_bf16 v[84:87], v[20:23], v[4:7], v[84:87]\n\t"
    "ds_read_b128 v[32:35], v205 offset:0\n\t"
    "v_mfma_f32_16x16x32_bf16 v[88:91], v[20:23], v[8:11], v[88:91]\n\t"
    "ds_read_b128 v[36:39], v205 offset:1024\n\t"
    "buffer_load_dwordx4 v216, s[28:31], s84 offen offset:1024 sc1 lds\n\t"
    "v_mfma_f32_16x16x32_bf16 v[92:95], v[20:23], v[12:15], v[92:95]\n\t"
    "s_waitcnt lgkmcnt(6)\n\t"
    "s_add_u32 s83, s83, 0x400\n\t"
    "ds_read_b128 v[40:43], v205 offset:2048\n\t"
    "v_mfma_f32_16x16x32_bf16 v[96:99], v[24:27], v[0:3], v[96:99]\n\t"
    "s_add_u32 s84, s84, 0x400\n\t"
    "ds_read_b128 v[44:47], v205 offset:3072\n\t"
    "v_mfma_f32_16x16x32_bf16 v[100:103], v[24:27], v[4:7], v[100:103]\n\t"
    "s_add_u32 m0, s86, 0x2000\n\t"
    "ds_read_b64_tr_b16 v[52:53], v208 offset:0\n\t"
    "v_mfma_f32_16x16x32_bf16 v[104:107], v[24:27], v[8:11], v[104:107]\n\t"
    "ds_read_b64_tr_b16 v[54:55], v208 offset:4096\n\t"
    "buffer_load_dwordx4 v203, s[16:19], s85 offen lds\n\t"
    "v_mfma_f32_16x16x32_bf16 v[108:111], v[24:27], v[12:15], v[108:111]\n\t"
    "s_waitcnt lgkmcnt(8)\n\t"
    "ds_read_b64_tr_b16 v[56:57], v209 offset:0\n\t"
    "v_mfma_f32_16x16x32_bf16 v[112:115], v[28:31], v[0:3], v[112:115]\n\t"
    "ds_read_b64_tr_b16 v[58:59], v209 offset:4096\n\t"
    "v_mfma_f32_16x16x32_bf16 v[116:119], v[28:31], v[4:7], v[116:119]\n\t"
    "ds_read_b64_tr_b16 v[60:61], v210 offset:0\n\t"
    "v_mfma_f32_16x16x32_bf16 v[120:123], v[28:31], v[8:11], v[120:123]\n\t"
    "ds_read_b64_tr_b16 v[62:63], v210 offset:4096\n\t"
    "buffer_load_dwordx4 v204, s[16:19], s85 offen offset:1024 lds\n\t"
    "v_mfma_f32_16x16x32_bf16 v[124:127], v[28:31], v[12:15], v[124:127]\n\t"
    "s_waitcnt lgkmcnt(9)\n\t"
    "s_add_u32 s85, s85, 0x8000\n\t"
    "v_mfma_f32_16x16x32_bf16 v[64:67], v[48:51], v[32:35], v[64:67]\n\t"
    "s_waitcnt lgkmcnt(8)\n\t"
    "s_add_u32 s87, s87, 0x8000\n\t"
    "s_cmp_eq_u32 s87, 0x28000\n\t"
    "s_cselect_b32 s88, s51, s52\n\t"
    "v_mfma_f32_16x16x32_bf16 v[68:71], v[48:51], v[36:39], v[68:71]\n\t"
    "s_waitcnt lgkmcnt(7)\n\t"
    "s_cselect_b32 s87, 0, s87\n\t"
    "s_add_u32 m0, s86, 0x4000\n\t"
    "v_add_u32 v205, s88, v205\n\t"
    "v_add_u32 v206, s88, v206\n\t"
    "v_mfma_f32_16x16x32_bf16 v[72:75], v[48:51], v[40:43], v[72:75]\n\t"
    "s_waitcnt lgkmcnt(6)\n\t"
    "buffer_load_dwordx4 v216, s[28:31], s83 offen sc1 lds\n\t"
    "v_add_u32 v207, s88, v207\n\t"
    "v_add_u32 v208, s88, v208\n\t"
    "v_mfma_f32_16x16x32_bf16 v[76:79], v[48:51], v[44:47], v[76:79]\n\t"
    "s_waitcnt lgkmcnt(4)\n\t"
    "v_add_u32 v209, s88, v209\n\t"
    "v_add_u32 v210, s88, v210\n\t"
    "v_mfma_f32_16x16x32_bf16 v[80:83], v[52:55], v[32:35], v[80:83]\n\t"
    "ds_read_b64_tr_b16 v[16:17], v207 offset:0\n\t"
    "v_mfma_f32_16x16x32_bf16 v[84:87], v[52:55], v[36:39], v[84:87]\n\t"
    "ds_read_b64_tr_b16 v[18:19], v207 offset:4096\n\t"
    "v_mfma_f32_16x16x32_bf16 v[88:91], v[52:55], v[40:43], v[88:91]\n\t"
    "ds_read_b128 v[0:3], v205 offset:0\n\t"
    "buffer_load_dwordx4 v216, s[28:31], s84 offen offset:1024 sc1 lds\n\t"
    "v_mfma_f32_16x16x32_bf16 v[92:95], v[52:55], v[44:47], v[92:95]\n\t"
    "s_waitcnt lgkmcnt(5)\n\t"
    "s_add_u32 s83, s83, 0x400\n\t"
    "ds_read_b128 v[4:7], v205 offset:1024\n\t"
    "v_mfma_f32_16x16x32_bf16 v[96:99], v[56:59], v[32:35], v[96:99]\n\t"
    "s_add_u32 s84, s84, 0x400\n\t"
    "ds_read_b128 v[8:11], v205 offset:2048\n\t"
    "v_mfma_f32_16x16x32_bf16 v[100:103], v[56:59], v[36:39], v[100:103]\n\t"
    "s_add_u32 m0, s86, 0x6000\n\t"
    "ds_read_b128 v[12:15], v205 offset:3072\n\t"
    "v_mfma_f32_16x16x32_bf16 v[104:107], v[56:59], v[40:43], v[104:107]\n\t"
    "s_add_u32 s86, s86, 0x8000\n\t"
    "s_cmp_ge_u32 s86, 0x28000\n\t"
    "s_cselect_b32 s37, 0x28000, 0\n\t"
    "ds_read_b64_tr_b16 v[20:21], v208 offset:0\n\t"
    "buffer_load_dwordx4 v203, s[16:19], s85 offen lds\n\t"
    "v_mfma_f32_16x16x32_bf16 v[108:111], v[56:59], v[44:47], v[108:111]\n\t"
    "s_waitcnt lgkmcnt(7)\n\t"
    "ds_read_b64_tr_b16 v[22:23], v208 offset:4096\n\t"
    "v_mfma_f32_16x16x32_bf16 v[112:115], v[60:63], v[32:35], v[112:115]\n\t"
    "s_sub_u32 s86, s86, s37\n\t"
    "ds_read_b64_tr_b16 v[24:25], v209 offset:0\n\t"
    "v_mfma_f32_16x16x32_bf16 v[116:119], v[60:63], v[36:39], v[116:119]\n\t"
    "s_waitcnt vmcnt(19)\n\t"
    "ds_read_b64_tr_b16 v[26:27], v209 offset:4096\n\t"
    "v_mfma_f32_16x16x32_bf16 v[120:123], v[60:63], v[40:43], v[120:123]\n\t"
    "s_barrier\n\t"
    "ds_read_b64_tr_b16 v[28:29], v210 offset:0\n\t"
    "buffer_load_dwordx4 v204, s[16:19], s85 offen offset:1024 lds\n\t"
    "v_mfma_f32_16x16x32_bf16 v[124:127], v[60:63], v[44:47], v[124:127]\n\t"
    "s_add_u32 s85, s85, 0x8000\n\t"
    "ds_read_b64_tr_b16 v[30:31], v210 offset:4096\n\t"
    "s_sub_u32 s40, s40, 1\n\t"
    "s_cmp_lg_u32 s40, 0\n\t"
    "s_cbranch_scc1 L_o4_loopc\n\t"
    "s_waitcnt lgkmcnt(9)\n\t"
    "v_mfma_f32_16x16x32_bf16 v[64:67], v[16:19], v[0:3], v[64:67]\n\t"
    "s_waitcnt lgkmcnt(8)\n\t"
    "v_add_u32 v205, 0x4000, v205\n\t"
    "v_add_u32 v206, 0x4000, v206\n\t"
    "v_mfma_f32_16x16x32_bf16 v[68:71], v[16:19], v[4:7], v[68:71]\n\t"
    "s_waitcnt lgkmcnt(7)\n\t"
    "v_add_u32 v207, 0x4000, v207\n\t"
    "v_add_u32 v208, 0x4000, v208\n\t"
    "v_mfma_f32_16x16x32_bf16 v[72:75], v[16:19], v[8:11], v[72:75]\n\t"
    "s_waitcnt lgkmcnt(6)\n\t"
    "buffer_load_dword v192, v212, s[28:31], s96 offen offset:0 sc1\n\t"
    "v_add_u32 v209, 0x4000, v209\n\t"
    "v_add_u32 v210, 0x4000, v210\n\t"
    "v_mfma_f32_16x16x32_bf16 v[76:79], v[16:19], v[12:15], v[76:79]\n\t"
    "s_waitcnt lgkmcnt(4)\n\t"
    "ds_read_b64_tr_b16 v[48:49], v207 offset:0\n\t"
    "v_mfma_f32_16x16x32_bf16 v[80:83], v[20:23], v[0:3], v[80:83]\n\t"
    "ds_read_b64_tr_b16 v[50:51], v207 offset:4096\n\t"
    "v_mfma_f32_16x16x32_bf16 v[84:87], v[20:23], v[4:7], v[84:87]\n\t"
    "ds_read_b128 v[32:35], v205 offset:0\n\t"
    "buffer_load_dword v193, v212, s[28:31], s96 offen offset:64 sc1\n\t"
    "v_mfma_f32_16x16x32_bf16 v[88:91], v[20:23], v[8:11], v[88:91]\n\t"
    "ds_read_b128 v[36:39], v205 offset:1024\n\t"
    "v_mfma_f32_16x16x32_bf16 v[92:95], v[20:23], v[12:15], v[92:95]\n\t"
    "s_waitcnt lgkmcnt(6)\n\t"
    "ds_read_b128 v[40:43], v205 offset:2048\n\t"
    "v_mfma_f32_16x16x32_bf16 v[96:99], v[24:27], v[0:3], v[96:99]\n\t"
    "ds_read_b128 v[44:47], v205 offset:3072\n\t"
    "buffer_load_dword v194, v212, s[28:31], s96 offen offset:128 sc1\n\t"
    "v_mfma_f32_16x16x32_bf16 v[100:103], v[24:27], v[4:7], v[100:103]\n\t"
    "ds_read_b64_tr_b16 v[52:53], v208 offset:0\n\t"
    "v_mfma_f32_16x16x32_bf16 v[104:107], v[24:27], v[8:11], v[104:107]\n\t"
    "ds_read_b64_tr_b16 v[54:55], v208 offset:4096\n\t"
    "v_mfma_f32_16x16x32_bf16 v[108:111], v[24:27], v[12:15], v[108:111]\n\t"
    "s_waitcnt lgkmcnt(8)\n\t"
    "ds_read_b64_tr_b16 v[56:57], v209 offset:0\n\t"
    "buffer_load_dword v195, v212, s[28:31], s96 offen offset:192 sc1\n\t"
    "v_mfma_f32_16x16x32_bf16 v[112:115], v[28:31], v[0:3], v[112:115]\n\t"
    "ds_read_b64_tr_b16 v[58:59], v209 offset:4096\n\t"
    "v_mfma_f32_16x16x32_bf16 v[116:119], v[28:31], v[4:7], v[116:119]\n\t"
    "ds_read_b64_tr_b16 v[60:61], v210 offset:0\n\t"
    "v_mfma_f32_16x16x32_bf16 v[120:123], v[28:31], v[8:11], v[120:123]\n\t"
    "ds_read_b64_tr_b16 v[62:63], v210 offset:4096\n\t"
    "v_mfma_f32_16x16x32_bf16 v[124:127], v[28:31], v[12:15], v[124:127]\n\t"
    "s_waitcnt lgkmcnt(9)\n\t"
    "v_mfma_f32_16x16x32_bf16 v[64:67], v[48:51], v[32:35], v[64:67]\n\t"
    "s_waitcnt lgkmcnt(8)\n\t"
    "s_add_u32 s87, s87, 0x8000\n\t"
    "s_cmp_eq_u32 s87, 0x28000\n\t"
    "s_cselect_b32 s88, s51, s52\n\t"
    "v_mfma_f32_16x16x32_bf16 v[68:71], v[48:51], v[36:39], v[68:71]\n\t"
    "s_waitcnt lgkmcnt(7)\n\t"
    "s_cselect_b32 s87, 0, s87\n\t"
    "v_add_u32 v205, s88, v205\n\t"
    "v_add_u32 v206, s88, v206\n\t"
    "v_mfma_f32_16x16x32_bf16 v[72:75], v[48:51], v[40:43], v[72:75]\n\t"
    "s_waitcnt lgkmcnt(6)\n\t"
    "v_add_u32 v207, s88, v207\n\t"
    "v_add_u32 v208, s88, v208\n\t"
    "v_mfma_f32_16x16x32_bf16 v[76:79], v[48:51], v[44:47], v[76:79]\n\t"
    "s_waitcnt lgkmcnt(4)\n\t"
    "v_add_u32 v209, s88, v209\n\t"
    "v_add_u32 v210, s88, v210\n\t"
    "v_mfma_f32_16x16x32_bf16 v[80:83], v[52:55], v[32:35], v[80:83]\n\t"
    "ds_read_b64_tr_b16 v[16:17], v207 offset:0\n\t"
    "v_mfma_f32_16x16x32_bf16 v[84:87], v[52:55], v[36:39], v[84:87]\n\t"
    "ds_read_b64_tr_b16 v[18:19], v207 offset:4096\n\t"
    "v_mfma_f32_16x16x32_bf16 v[88:91], v[52:55], v[40:43], v[88:91]\n\t"
    "ds_read_b128 v[0:3], v205 offset:0\n\t"
    "v_mfma_f32_16x16x32_bf16 v[92:95], v[52:55], v[44:47], v[92:95]\n\t"
    "s_waitcnt lgkmcnt(5)\n\t"
    "ds_read_b128 v[4:7], v205 offset:1024\n\t"
    "v_mfma_f32_16x16x32_bf16 v[96:99], v[56:59], v[32:35], v[96:99]\n\t"
    "ds_read_b128 v[8:11], v205 offset:2048\n\t"
    "v_mfma_f32_16x16x32_bf16 v[100:103], v[56:59], v[36:39], v[100:103]\n\t"
    "ds_read_b128 v[12:15], v205 offset:3072\n\t"
    "v_mfma_f32_16x16x32_bf16 v[104:107], v[56:59], v[40:43], v[104:107]\n\t"
    "ds_read_b64_tr_b16 v[20:21], v208 offset:0\n\t"
    "v_mfma_f32_16x16x32_bf16 v[108:111], v[56:59], v[44:47], v[108:111]\n\t"
    "s_waitcnt lgkmcnt(7)\n\t"
    "ds_read_b64_tr_b16 v[22:23], v208 offset:4096\n\t"
    "v_mfma_f32_16x16x32_bf16 v[112:115], v[60:63], v[32:35], v[112:115]\n\t"
    "ds_read_b64_tr_b16 v[24:25], v209 offset:0\n\t"
    "v_mfma_f32_16x16x32_bf16 v[116:119], v[60:63], v[36:39], v[116:119]\n\t"
    "s_waitcnt vmcnt(12)\n\t"
    "ds_read_b64_tr_b16 v[26:27], v209 offset:4096\n\t"
    "v_mfma_f32_16x16x32_bf16 v[120:123], v[60:63], v[40:43], v[120:123]\n\t"
    "s_barrier\n\t"
    "ds_read_b64_tr_b16 v[28:29], v210 offset:0\n\t"
    "v_mfma_f32_16x16x32_bf16 v[124:127], v[60:63], v[44:47], v[124:127]\n\t"
    "ds_read_b64_tr_b16 v[30:31], v210 offset:4096\n\t"
    "s_waitcnt lgkmcnt(9)\n\t"
    "v_mfma_f32_16x16x32_bf16 v[64:67], v[16:19], v[0:3], v[64:67]\n\t"
    "s_waitcnt lgkmcnt(8)\n\t"
    "v_add_u32 v205, 0x4000, v205\n\t"
    "v_add_u32 v206, 0x4000, v206\n\t"
    "v_mfma_f32_16x16x32_bf16 v[68:71], v[16:19], v[4:7], v[68:71]\n\t"
    "s_waitcnt lgkmcnt(7)\n\t"
    "v_add_u32 v207, 0x4000, v207\n\t"
    "v_add_u32 v208, 0x4000, v208\n\t"
    "v_mfma_f32_16x16x32_bf16 v[72:75], v[16:19], v[8:11], v[72:75]\n\t"
    "s_waitcnt lgkmcnt(6)\n\t"
    "v_add_u32 v209, 0x4000, v209\n\t"
    "v_add_u32 v210, 0x4000, v210\n\t"
    "v_mfma_f32_16x16x32_bf16 v[76:79], v[16:19], v[12:15], v[76:79]\n\t"
    "s_waitcnt lgkmcnt(4)\n\t"
    "ds_read_b64_tr_b16 v[48:49], v207 offset:0\n\t"
    "v_mfma_f32_16x16x32_bf16 v[80:83], v[20:23], v[0:3], v[80:83]\n\t"
    "ds_read_b64_tr_b16 v[50:51], v207 offset:4096\n\t"
    "v_mfma_f32_16x16x32_bf16 v[84:87], v[20:23], v[4:7], v[84:87]\n\t"
    "ds_read_b128 v[32:35], v205 offset:0\n\t"
    "v_mfma_f32_16x16x32_bf16 v[88:91], v[20:23], v[8:11], v[88:91]\n\t"
    "ds_read_b128 v[36:39], v205 offset:1024\n\t"
    "v_mfma_f32_16x16x32_bf16 v[92:95], v[20:23], v[12:15], v[92:95]\n\t"
    "s_waitcnt lgkmcnt(6)\n\t"
    "ds_read_b128 v[40:43], v205 offset:2048\n\t"
    "v_mfma_f32_16x16x32_bf16 v[96:99], v[24:27], v[0:3], v[96:99]\n\t"
    "ds_read_b128 v[44:47], v205 offset:3072\n\t"
    "v_mfma_f32_16x16x32_bf16 v[100:103], v[24:27], v[4:7], v[100:103]\n\t"
    "ds_read_b64_tr_b16 v[52:53], v208 offset:0\n\t"
    "v_mfma_f32_16x16x32_bf16 v[104:107], v[24:27], v[8:11], v[104:107]\n\t"
    "ds_read_b64_tr_b16 v[54:55], v208 offset:4096\n\t"
    "v_mfma_f32_16x16x32_bf16 v[108:111], v[24:27], v[12:15], v[108:111]\n\t"
    "s_waitcnt lgkmcnt(8)\n\t"
    "ds_read_b64_tr_b16 v[56:57], v209 offset:0\n\t"
    "v_mfma_f32_16x16x32_bf16 v[112:115], v[28:31], v[0:3], v[112:115]\n\t"
    "ds_read_b64_tr_b16 v[58:59], v209 offset:4096\n\t"
    "v_mfma_f32_16x16x32_bf16 v[116:119], v[28:31], v[4:7], v[116:119]\n\t"
    "ds_read_b64_tr_b16 v[60:61], v210 offset:0\n\t"
    "v_mfma_f32_16x16x32_bf16 v[120:123], v[28:31], v[8:11], v[120:123]\n\t"
    "ds_read_b64_tr_b16 v[62:63], v210 offset:4096\n\t"
    "v_mfma_f32_16x16x32_bf16 v[124:127], v[28:31], v[12:15], v[124:127]\n\t"
    "s_waitcnt lgkmcnt(9)\n\t"
    "v_mfma_f32_16x16x32_bf16 v[64:67], v[48:51], v[32:35], v[64:67]\n\t"
    "s_waitcnt lgkmcnt(8)\n\t"
    "s_add_u32 s87, s87, 0x8000\n\t"
    "s_cmp_eq_u32 s87, 0x28000\n\t"
    "s_cselect_b32 s88, s51, s52\n\t"
    "v_mfma_f32_16x16x32_bf16 v[68:71], v[48:51], v[36:39], v[68:71]\n\t"
    "s_waitcnt lgkmcnt(7)\n\t"
    "s_cselect_b32 s87, 0, s87\n\t"
    "v_add_u32 v205, s88, v205\n\t"
    "v_add_u32 v206, s88, v206\n\t"
    "v_mfma_f32_16x16x32_bf16 v[72:75], v[48:51], v[40:43], v[72:75]\n\t"
    "s_waitcnt lgkmcnt(6)\n\t"
    "v_add_u32 v207, s88, v207\n\t"
    "v_add_u32 v208, s88, v208\n\t"
    "v_mfma_f32_16x16x32_bf16 v[76:79], v[48:51], v[44:47], v[76:79]\n\t"
    "s_waitcnt lgkmcnt(4)\n\t"
    "v_add_u32 v209, s88, v209\n\t"
    "v_add_u32 v210, s88, v210\n\t"
    "v_mfma_f32_16x16x32_bf16 v[80:83], v[52:55], v[32:35], v[80:83]\n\t"
    "ds_read_b64_tr_b16 v[16:17], v207 offset:0\n\t"
    "v_mfma_f32_16x16x32_bf16 v[84:87], v[52:55], v[36:39], v[84:87]\n\t"
    "ds_read_b64_tr_b16 v[18:19], v207 offset:4096\n\t"
    "v_mfma_f32_16x16x32_bf16 v[88:91], v[52:55], v[40:43], v[88:91]\n\t"
    "ds_read2st64_b64 v[0:3], v206 offset0:0 offset1:1\n\t"
    "v_mfma_f32_16x16x32_bf16 v[92:95], v[52:55], v[44:47], v[92:95]\n\t"
    "s_waitcnt lgkmcnt(5)\n\t"
    "ds_read2st64_b64 v[4:7], v206 offset0:2 offset1:3\n\t"
    "v_mfma_f32_16x16x32_bf16 v[96:99], v[56:59], v[32:35], v[96:99]\n\t"
    "ds_read2st64_b64 v[8:11], v206 offset0:4 offset1:5\n\t"
    "v_mfma_f32_16x16x32_bf16 v[100:103], v[56:59], v[36:39], v[100:103]\n\t"
    "ds_read2st64_b64 v[12:15], v206 offset0:6 offset1:7\n\t"
    "v_mfma_f32_16x16x32_bf16 v[104:107], v[56:59], v[40:43], v[104:107]\n\t"
    "ds_read_b64_tr_b16 v[20:21], v208 offset:0\n\t"
    "v_mfma_f32_16x16x32_bf16 v[108:111], v[56:59], v[44:47], v[108:111]\n\t"
    "s_waitcnt lgkmcnt(7)\n\t"
    "ds_read_b64_tr_b16 v[22:23], v208 offset:4096\n\t"
    "v_mfma_f32_16x16x32_bf16 v[112:115], v[60:63], v[32:35], v[112:115]\n\t"
    "ds_read_b64_tr_b16 v[24:25], v209 offset:0\n\t"
    "v_mfma_f32_16x16x32_bf16 v[116:119], v[60:63], v[36:39], v[116:119]\n\t"
    "s_waitcnt vmcnt(4)\n\t"
    "ds_read_b64_tr_b16 v[26:27], v209 offset:4096\n\t"
    "v_mfma_f32_16x16x32_bf16 v[120:123], v[60:63], v[40:43], v[120:123]\n\t"
    "s_barrier\n\t"
    "ds_read_b64_tr_b16 v[28:29], v210 offset:0\n\t"
    "v_mfma_f32_16x16x32_bf16 v[124:127], v[60:63], v[44:47], v[124:127]\n\t"
    "ds_read_b64_tr_b16 v[30:31], v210 offset:4096\n\t"
    "s_waitcnt lgkmcnt(9)\n\t"
    "v_mfma_f32_16x16x32_bf16 v[128:131], v[16:19], v[0:3], 0\n\t"
    "s_waitcnt lgkmcnt(8)\n\t"
    "v_add_u32 v205, 0x4000, v205\n\t"
    "v_add_u32 v206, 0x4000, v206\n\t"
    "v_mfma_f32_16x16x32_bf16 v[132:135], v[16:19], v[4:7], 0\n\t"
    "s_waitcnt lgkmcnt(7)\n\t"
    "v_add_u32 v207, 0x4000, v207\n\t"
    "v_add_u32 v208, 0x4000, v208\n\t"
    "v_mfma_f32_16x16x32_bf16 v[136:139], v[16:19], v[8:11], 0\n\t"
    "s_waitcnt lgkmcnt(6)\n\t"
    "v_add_u32 v209, 0x4000, v209\n\t"
    "v_add_u32 v210, 0x4000, v210\n\t"
    "v_mfma_f32_16x16x32_bf16 v[140:143], v[16:19], v[12:15], 0\n\t"
    "s_waitcnt lgkmcnt(4)\n\t"
    "ds_read_b64_tr_b16 v[48:49], v207 offset:0\n\t"
    "v_mfma_f32_16x16x32_bf16 v[144:147], v[20:23], v[0:3], 0\n\t"
    "ds_read_b64_tr_b16 v[50:51], v207 offset:4096\n\t"
    "v_mfma_f32_16x16x32_bf16 v[148:151], v[20:23], v[4:7], 0\n\t"
    "ds_read2st64_b64 v[32:35], v206 offset0:0 offset1:1\n\t"
    "v_mfma_f32_16x16x32_bf16 v[152:155], v[20:23], v[8:11], 0\n\t"
    "ds_read2st64_b64 v[36:39], v206 offset0:2 offset1:3\n\t"
    "v_mfma_f32_16x16x32_bf16 v[156:159], v[20:23], v[12:15], 0\n\t"
    "s_waitcnt lgkmcnt(6)\n\t"
    "ds_read2st64_b64 v[40:43], v206 offset0:4 offset1:5\n\t"
    "v_mfma_f32_16x16x32_bf16 v[160:163], v[24:27], v[0:3], 0\n\t"
    "ds_read2st64_b64 v[44:47], v206 offset0:6 offset1:7\n\t"
    "v_mfma_f32_16x16x32_bf16 v[164:167], v[24:27], v[4:7], 0\n\t"
    "ds_read_b64_tr_b16 v[52:53], v208 offset:0\n\t"
    "v_mfma_f32_16x16x32_bf16 v[168:171], v[24:27], v[8:11], 0\n\t"
    "ds_read_b64_tr_b16 v[54:55], v208 offset:4096\n\t"
    "v_mfma_f32_16x16x32_bf16 v[172:175], v[24:27], v[12:15], 0\n\t"
    "s_waitcnt lgkmcnt(8)\n\t"
    "ds_read_b64_tr_b16 v[56:57], v209 offset:0\n\t"
    "v_mfma_f32_16x16x32_bf16 v[176:179], v[28:31], v[0:3], 0\n\t"
    "ds_read_b64_tr_b16 v[58:59], v209 offset:4096\n\t"
    "v_mfma_f32_16x16x32_bf16 v[180:183], v[28:31], v[4:7], 0\n\t"
    "ds_read_b64_tr_b16 v[60:61], v210 offset:0\n\t"
    "v_mfma_f32_16x16x32_bf16 v[184:187], v[28:31], v[8:11], 0\n\t"
    "ds_read_b64_tr_b16 v[62:63], v210 offset:4096\n\t"
    "v_mfma_f32_16x16x32_bf16 v[188:191], v[28:31], v[12:15], 0\n\t"
    "s_waitcnt lgkmcnt(9)\n\t"
    "v_mfma_f32_16x16x32_bf16 v[128:131], v[48:51], v[32:35], v[128:131]\n\t"
    "s_waitcnt lgkmcnt(8)\n\t"
    "s_add_u32 s87, s87, 0x8000\n\t"
    "s_cmp_eq_u32 s87, 0x28000\n\t"
    "s_cselect_b32 s88, s51, s52\n\t"
    "v_mfma_f32_16x16x32_bf16 v[132:135], v[48:51], v[36:39], v[132:135]\n\t"
    "s_waitcnt lgkmcnt(7)\n\t"
    "s_cselect_b32 s87, 0, s87\n\t"
    "v_add_u32 v205, s88, v205\n\t"
    "v_add_u32 v206, s88, v206\n\t"
    "v_mfma_f32_16x16x32_bf16 v[136:139], v[48:51], v[40:43], v[136:139]\n\t"
    "s_waitcnt lgkmcnt(6)\n\t"
    "v_add_u32 v207, s88, v207\n\t"
    "v_add_u32 v208, s88, v208\n\t"
    "v_mfma_f32_16x16x32_bf16 v[140:143], v[48:51], v[44:47], v[140:143]\n\t"
    "s_waitcnt lgkmcnt(4)\n\t"
    "v_add_u32 v209, s88, v209\n\t"
    "v_add_u32 v210, s88, v210\n\t"
    "v_mfma_f32_16x16x32_bf16 v[144:147], v[52:55], v[32:35], v[144:147]\n\t"
    "ds_read_b64_tr_b16 v[16:17], v207 offset:0\n\t"
    "v_mfma_f32_16x16x32_bf16 v[148:151], v[52:55], v[36:39], v[148:151]\n\t"
    "ds_read_b64_tr_b16 v[18:19], v207 offset:4096\n\t"
    "v_mfma_f32_16x16x32_bf16 v[152:155], v[52:55], v[40:43], v[152:155]\n\t"
    "ds_read2st64_b64 v[0:3], v206 offset0:0 offset1:1\n\t"
    "v_mfma_f32_16x16x32_bf16 v[156:159], v[52:55], v[44:47], v[156:159]\n\t"
    "s_waitcnt lgkmcnt(5)\n\t"
    "ds_read2st64_b64 v[4:7], v206 offset0:2 offset1:3\n\t"
    "v_mfma_f32_16x16x32_bf16 v[160:163], v[56:59], v[32:35], v[160:163]\n\t"
    "ds_read2st64_b64 v[8:11], v206 offset0:4 offset1:5\n\t"
    "v_mfma_f32_16x16x32_bf16 v[164:167], v[56:59], v[36:39], v[164:167]\n\t"
    "ds_read2st64_b64 v[12:15], v206 offset0:6 offset1:7\n\t"
    "v_mfma_f32_16x16x32_bf16 v[168:171], v[56:59], v[40:43], v[168:171]\n\t"
    "ds_read_b64_tr_b16 v[20:21], v208 offset:0\n\t"
    "v_mfma_f32_16x16x32_bf16 v[172:175], v[56:59], v[44:47], v[172:175]\n\t"
    "s_waitcnt lgkmcnt(7)\n\t"
    "ds_read_b64_tr_b16 v[22:23], v208 offset:4096\n\t"
    "v_mfma_f32_16x16x32_bf16 v[176:179], v[60:63], v[32:35], v[176:179]\n\t"
    "ds_read_b64_tr_b16 v[24:25], v209 offset:0\n\t"
    "v_mfma_f32_16x16x32_bf16 v[180:183], v[60:63], v[36:39], v[180:183]\n\t"
    "s_waitcnt vmcnt(0)\n\t"
    "ds_read_b64_tr_b16 v[26:27], v209 offset:4096\n\t"
    "v_mfma_f32_16x16x32_bf16 v[184:187], v[60:63], v[40:43], v[184:187]\n\t"
    "s_barrier\n\t"
    "ds_read_b64_tr_b16 v[28:29], v210 offset:0\n\t"
    "v_mfma_f32_16x16x32_bf16 v[188:191], v[60:63], v[44:47], v[188:191]\n\t"
    "ds_read_b64_tr_b16 v[30:31], v210 offset:4096\n\t"
    "s_waitcnt lgkmcnt(9)\n\t"
    "v_mfma_f32_16x16x32_bf16 v[128:131], v[16:19], v[0:3], v[128:131]\n\t"
    "s_waitcnt lgkmcnt(8)\n\t"
    "v_add_u32 v205, 0x4000, v205\n\t"
    "v_add_u32 v206, 0x4000, v206\n\t"
    "v_mfma_f32_16x16x32_bf16 v[132:135], v[16:19], v[4:7], v[132:135]\n\t"
    "s_waitcnt lgkmcnt(7)\n\t"
    "v_add_u32 v207, 0x4000, v207\n\t"
    "v_add_u32 v208, 0x4000, v208\n\t"
    "v_mfma_f32_16x16x32_bf16 v[136:139], v[16:19], v[8:11], v[136:139]\n\t"
    "s_waitcnt lgkmcnt(6)\n\t"
    "v_add_u32 v209, 0x4000, v209\n\t"
    "v_add_u32 v210, 0x4000, v210\n\t"
    "v_mfma_f32_16x16x32_bf16 v[140:143], v[16:19], v[12:15], v[140:143]\n\t"
    "s_waitcnt lgkmcnt(4)\n\t"
    "ds_read_b64_tr_b16 v[48:49], v207 offset:0\n\t"
    "v_mfma_f32_16x16x32_bf16 v[144:147], v[20:23], v[0:3], v[144:147]\n\t"
    "ds_read_b64_tr_b16 v[50:51], v207 offset:4096\n\t"
    "v_mfma_f32_16x16x32_bf16 v[148:151], v[20:23], v[4:7], v[148:151]\n\t"
    "ds_read2st64_b64 v[32:35], v206 offset0:0 offset1:1\n\t"
    "v_mfma_f32_16x16x32_bf16 v[152:155], v[20:23], v[8:11], v[152:155]\n\t"
    "ds_read2st64_b64 v[36:39], v206 offset0:2 offset1:3\n\t"
    "v_mfma_f32_16x16x32_bf16 v[156:159], v[20:23], v[12:15], v[156:159]\n\t"
    "s_waitcnt lgkmcnt(6)\n\t"
    "ds_read2st64_b64 v[40:43], v206 offset0:4 offset1:5\n\t"
    "v_mfma_f32_16x16x32_bf16 v[160:163], v[24:27], v[0:3], v[160:163]\n\t"
    "ds_read2st64_b64 v[44:47], v206 offset0:6 offset1:7\n\t"
    "v_mfma_f32_16x16x32_bf16 v[164:167], v[24:27], v[4:7], v[164:167]\n\t"
    "ds_read_b64_tr_b16 v[52:53], v208 offset:0\n\t"
    "v_mfma_f32_16x16x32_bf16 v[168:171], v[24:27], v[8:11], v[168:171]\n\t"
    "ds_read_b64_tr_b16 v[54:55], v208 offset:4096\n\t"
    "v_mfma_f32_16x16x32_bf16 v[172:175], v[24:27], v[12:15], v[172:175]\n\t"
    "s_waitcnt lgkmcnt(8)\n\t"
    "ds_read_b64_tr_b16 v[56:57], v209 offset:0\n\t"
    "v_mfma_f32_16x16x32_bf16 v[176:179], v[28:31], v[0:3], v[176:179]\n\t"
    "ds_read_b64_tr_b16 v[58:59], v209 offset:4096\n\t"
    "v_mfma_f32_16x16x32_bf16 v[180:183], v[28:31], v[4:7], v[180:183]\n\t"
    "ds_read_b64_tr_b16 v[60:61], v210 offset:0\n\t"
    "v_mfma_f32_16x16x32_bf16 v[184:187], v[28:31], v[8:11], v[184:187]\n\t"
    "ds_read_b64_tr_b16 v[62:63], v210 offset:4096\n\t"
    "v_mfma_f32_16x16x32_bf16 v[188:191], v[28:31], v[12:15], v[188:191]\n\t"
    "s_waitcnt lgkmcnt(9)\n\t"
    "v_mfma_f32_16x16x32_bf16 v[128:131], v[48:51], v[32:35], v[128:131]\n\t"
    "s_waitcnt lgkmcnt(8)\n\t"
    "v_mfma_f32_16x16x32_bf16 v[132:135], v[48:51], v[36:39], v[132:135]\n\t"
    "s_waitcnt lgkmcnt(7)\n\t"
    "v_mfma_f32_16x16x32_bf16 v[136:139], v[48:51], v[40:43], v[136:139]\n\t"
    "s_waitcnt lgkmcnt(6)\n\t"
    "v_mfma_f32_16x16x32_bf16 v[140:143], v[48:51], v[44:47], v[140:143]\n\t"
    "s_waitcnt lgkmcnt(4)\n\t"
    "v_mfma_f32_16x16x32_bf16 v[144:147], v[52:55], v[32:35], v[144:147]\n\t"
    "v_mfma_f32_16x16x32_bf16 v[148:151], v[52:55], v[36:39], v[148:151]\n\t"
    "v_mfma_f32_16x16x32_bf16 v[152:155], v[52:55], v[40:43], v[152:155]\n\t"
    "v_mfma_f32_16x16x32_bf16 v[156:159], v[52:55], v[44:47], v[156:159]\n\t"
    "s_waitcnt lgkmcnt(2)\n\t"
    "v_mfma_f32_16x16x32_bf16 v[160:163], v[56:59], v[32:35], v[160:163]\n\t"
    "v_mfma_f32_16x16x32_bf16 v[164:167], v[56:59], v[36:39], v[164:167]\n\t"
    "v_mfma_f32_16x16x32_bf16 v[168:171], v[56:59], v[40:43], v[168:171]\n\t"
    "v_mfma_f32_16x16x32_bf16 v[172:175], v[56:59], v[44:47], v[172:175]\n\t"
    "s_waitcnt lgkmcnt(0)\n\t"
    "v_mfma_f32_16x16x32_bf16 v[176:179], v[60:63], v[32:35], v[176:179]\n\t"
    "v_mfma_f32_16x16x32_bf16 v[180:183], v[60:63], v[36:39], v[180:183]\n\t"
    "v_mfma_f32_16x16x32_bf16 v[184:187], v[60:63], v[40:43], v[184:187]\n\t"
    "v_mfma_f32_16x16x32_bf16 v[188:191], v[60:63], v[44:47], v[188:191]\n\t"
    "s_nop 7\n\t"
    "s_cmp_lg_u32 s33, 0\n\t"
    "s_cbranch_scc1 L_pk3\n\t"
    "v_and_b32 v218, 3, v217\n\t"
    "s_and_b32 s37, s34, 7\n\t"
    "v_lshl_add_u32 v219, v218, 3, s37\n\t"
    "v_lshl_add_u32 v219, v219, 3, s35\n\t"
    "v_lshlrev_b32 v196, 6, v219\n\t"
    "v_add_u32 v197, 0x240000, v196\n\t"
    "v_add_u32 v196, 0x210000, v196\n\t"
    "s_lshr_b32 s37, s34, 3\n\t"
    "s_lshl_b32 s37, s37, 4\n\t"
    "v_add_u32 v197, s37, v197\n\t"
    "v_lshlrev_b32 v198, 4, v218\n\t"
    "s_lshl_b32 s37, s36, 6\n\t"
    "s_add_u32 s37, s37, 0x240000\n\t"
    "v_add_u32 v198, s37, v198\n\t"
    "buffer_load_dwordx2 v[200:201], v196, s[28:31], 0 offen sc1\n\t"
    "L_pk3:\n\t"
    "v_sub_f32 v60, 1.0, v192\n\t"
    "v_sub_f32 v61, 1.0, v193\n\t"
    "v_sub_f32 v62, 1.0, v194\n\t"
    "v_sub_f32 v63, 1.0, v195\n\t"
    "v_mul_f32 v64, v64, v60\n\t"
    "v_mul_f32 v65, v65, v60\n\t"
    "v_mul_f32 v66, v66, v60\n\t"
    "v_mul_f32 v67, v67, v60\n\t"
    "v_fmac_f32 v64, v128, v192\n\t"
    "v_fmac_f32 v65, v129, v192\n\t"
    "v_fmac_f32 v66, v130, v192\n\t"
    "v_fmac_f32 v67, v131, v192\n\t"
    "s_add_u32 s39, s89, 0x0\n\t"
    "v_cvt_pk_bf16_f32 v0, v64, v65\n\t"
    "v_cvt_pk_bf16_f32 v1, v66, v67\n\t"
    "v_mul_f32 v80, v80, v60\n\t"
    "v_mul_f32 v81, v81, v60\n\t"
    "v_mul_f32 v82, v82, v60\n\t"
    "v_mul_f32 v83, v83, v60\n\t"
    "v_fmac_f32 v80, v144, v192\n\t"
    "v_fmac_f32 v81, v145, v192\n\t"
    "v_fmac_f32 v82, v146, v192\n\t"
    "v_fmac_f32 v83, v147, v192\n\t"
    "v_cvt_pk_bf16_f32 v2, v80, v81\n\t"
    "v_cvt_pk_bf16_f32 v3, v82, v83\n\t"
    "buffer_store_dwordx4 v[0:3], v216, s[92:95], s39 offen offset:0 sc1\n\t"
    "v_mul_f32 v96, v96, v60\n\t"
    "v_mul_f32 v97, v97, v60\n\t"
    "v_mul_f32 v98, v98, v60\n\t"
    "v_mul_f32 v99, v99, v60\n\t"
    "v_fmac_f32 v96, v160, v192\n\t"
    "v_fmac_f32 v97, v161, v192\n\t"
    "v_fmac_f32 v98, v162, v192\n\t"
    "v_fmac_f32 v99, v163, v192\n\t"
    "v_cvt_pk_bf16_f32 v4, v96, v97\n\t"
    "v_cvt_pk_bf16_f32 v5, v98, v99\n\t"
    "v_mul_f32 v112, v112, v60\n\t"
    "v_mul_f32 v113, v113, v60\n\t"
    "v_mul_f32 v114, v114, v60\n\t"
    "v_mul_f32 v115, v115, v60\n\t"
    "v_fmac_f32 v112, v176, v192\n\t"
    "v_fmac_f32 v113, v177, v192\n\t"
    "v_fmac_f32 v114, v178, v192\n\t"
    "v_fmac_f32 v115, v179, v192\n\t"
    "v_cvt_pk_bf16_f32 v6, v112, v113\n\t"
    "v_cvt_pk_bf16_f32 v7, v114, v115\n\t"
    "buffer_store_dwordx4 v[4:7], v216, s[92:95], s39 offen offset:1024 sc1\n\t"
    "v_mul_f32 v68, v68, v61\n\t"
    "v_mul_f32 v69, v69, v61\n\t"
    "v_mul_f32 v70, v70, v61\n\t"
    "v_mul_f32 v71, v71, v61\n\t"
    "v_fmac_f32 v68, v132, v193\n\t"
    "v_fmac_f32 v69, v133, v193\n\t"
    "v_fmac_f32 v70, v134, v193\n\t"
    "v_fmac_f32 v71, v135, v193\n\t"
    "s_add_u32 s39, s89, 0x2000\n\t"
    "v_cvt_pk_bf16_f32 v8, v68, v69\n\t"
    "v_cvt_pk_bf16_f32 v9, v70, v71\n\t"
    "v_mul_f32 v84, v84, v61\n\t"
    "v_mul_f32 v85, v85, v61\n\t"
    "v_mul_f32 v86, v86, v61\n\t"
    "v_mul_f32 v87, v87, v61\n\t"
    "v_fmac_f32 v84, v148, v193\n\t"
    "v_fmac_f32 v85, v149, v193\n\t"
    "v_fmac_f32 v86, v150, v193\n\t"
    "v_fmac_f32 v87, v151, v193\n\t"
    "v_cvt_pk_bf16_f32 v10, v84, v85\n\t"
    "v_cvt_pk_bf16_f32 v11, v86, v87\n\t"
    "buffer_store_dwordx4 v[8:11], v216, s[92:95], s39 offen offset:0 sc1\n\t"
    "v_mul_f32 v100, v100, v61\n\t"
    "v_mul_f32 v101, v101, v61\n\t"
    "v_mul_f32 v102, v102, v61\n\t"
    "v_mul_f32 v103, v103, v61\n\t"
    "v_fmac_f32 v100, v164, v193\n\t"
    "v_fmac_f32 v101, v165, v193\n\t"
    "v_fmac_f32 v102, v166, v193\n\t"
    "v_fmac_f32 v103, v167, v193\n\t"
    "v_cvt_pk_bf16_f32 v12, v100, v101\n\t"
    "v_cvt_pk_bf16_f32 v13, v102, v103\n\t"
    "v_mul_f32 v116, v116, v61\n\t"
    "v_mul_f32 v117, v117, v61\n\t"
    "v_mul_f32 v118, v118, v61\n\t"
    "v_mul_f32 v119, v119, v61\n\t"
    "v_fmac_f32 v116, v180, v193\n\t"
    "v_fmac_f32 v117, v181, v193\n\t"
    "v_fmac_f32 v118, v182, v193\n\t"
    "v_fmac_f32 v119, v183, v193\n\t"
    "v_cvt_pk_bf16_f32 v14, v116, v117\n\t"
    "v_cvt_pk_bf16_f32 v15, v118, v119\n\t"
    "buffer_store_dwordx4 v[12:15], v216, s[92:95], s39 offen offset:1024 sc1\n\t"
    "v_mul_f32 v72, v72, v62\n\t"
    "v_mul_f32 v73, v73, v62\n\t"
    "v_mul_f32 v74, v74, v62\n\t"
    "v_mul_f32 v75, v75, v62\n\t"
    "v_fmac_f32 v72, v136, v194\n\t"
    "v_fmac_f32 v73, v137, v194\n\t"
    "v_fmac_f32 v74, v138, v194\n\t"
    "v_fmac_f32 v75, v139, v194\n\t"
    "s_add_u32 s39, s89, 0x10000\n\t"
    "v_cvt_pk_bf16_f32 v16, v72, v73\n\t"
    "v_cvt_pk_bf16_f32 v17, v74, v75\n\t"
    "v_mul_f32 v88, v88, v62\n\t"
    "v_mul_f32 v89, v89, v62\n\t"
    "v_mul_f32 v90, v90, v62\n\t"
    "v_mul_f32 v91, v91, v62\n\t"
    "v_fmac_f32 v88, v152, v194\n\t"
    "v_fmac_f32 v89, v153, v194\n\t"
    "v_fmac_f32 v90, v154, v194\n\t"
    "v_fmac_f32 v91, v155, v194\n\t"
    "v_cvt_pk_bf16_f32 v18, v88, v89\n\t"
    "v_cvt_pk_bf16_f32 v19, v90, v91\n\t"
    "buffer_store_dwordx4 v[16:19], v216, s[92:95], s39 offen offset:0 sc1\n\t"
    "v_mul_f32 v104, v104, v62\n\t"
    "v_mul_f32 v105, v105, v62\n\t"
    "v_mul_f32 v106, v106, v62\n\t"
    "v_mul_f32 v107, v107, v62\n\t"
    "v_fmac_f32 v104, v168, v194\n\t"
    "v_fmac_f32 v105, v169, v194\n\t"
    "v_fmac_f32 v106, v170, v194\n\t"
    "v_fmac_f32 v107, v171, v194\n\t"
    "v_cvt_pk_bf16_f32 v20, v104, v105\n\t"
    "v_cvt_pk_bf16_f32 v21, v106, v107\n\t"
    "v_mul_f32 v120, v120, v62\n\t"
    "v_mul_f32 v121, v121, v62\n\t"
    "v_mul_f32 v122, v122, v62\n\t"
    "v_mul_f32 v123, v123, v62\n\t"
    "v_fmac_f32 v120, v184, v194\n\t"
    "v_fmac_f32 v121, v185, v194\n\t"
    "v_fmac_f32 v122, v186, v194\n\t"
    "v_fmac_f32 v123, v187, v194\n\t"
    "v_cvt_pk_bf16_f32 v22, v120, v121\n\t"
    "v_cvt_pk_bf16_f32 v23, v122, v123\n\t"
    "buffer_store_dwordx4 v[20:23], v216, s[92:95], s39 offen offset:1024 sc1\n\t"
    "v_mul_f32 v76, v76, v63\n\t"
    "v_mul_f32 v77, v77, v63\n\t"
    "v_mul_f32 v78, v78, v63\n\t"
    "v_mul_f32 v79, v79, v63\n\t"
    "v_fmac_f32 v76, v140, v195\n\t"
    "v_fmac_f32 v77, v141, v195\n\t"
    "v_fmac_f32 v78, v142, v195\n\t"
    "v_fmac_f32 v79, v143, v195\n\t"
    "s_add_u32 s39, s89, 0x12000\n\t"
    "v_cvt_pk_bf16_f32 v24, v76, v77\n\t"
    "v_cvt_pk_bf16_f32 v25, v78, v79\n\t"
    "v_mul_f32 v92, v92, v63\n\t"
    "v_mul_f32 v93, v93, v63\n\t"
    "v_mul_f32 v94, v94, v63\n\t"
    "v_mul_f32 v95, v95, v63\n\t"
    "v_fmac_f32 v92, v156, v195\n\t"
    "v_fmac_f32 v93, v157, v195\n\t"
    "v_fmac_f32 v94, v158, v195\n\t"
    "v_fmac_f32 v95, v159, v195\n\t"
    "v_cvt_pk_bf16_f32 v26, v92, v93\n\t"
    "v_cvt_pk_bf16_f32 v27, v94, v95\n\t"
    "buffer_store_dwordx4 v[24:27], v216, s[92:95], s39 offen offset:0 sc1\n\t"
    "v_mul_f32 v108, v108, v63\n\t"
    "v_mul_f32 v109, v109, v63\n\t"
    "v_mul_f32 v110, v110, v63\n\t"
    "v_mul_f32 v111, v111, v63\n\t"
    "v_fmac_f32 v108, v172, v195\n\t"
    "v_fmac_f32 v109, v173, v195\n\t"
    "v_fmac_f32 v110, v174, v195\n\t"
    "v_fmac_f32 v111, v175, v195\n\t"
    "v_cvt_pk_bf16_f32 v28, v108, v109\n\t"
    "v_cvt_pk_bf16_f32 v29, v110, v111\n\t"
    "v_mul_f32 v124, v124, v63\n\t"
    "v_mul_f32 v125, v125, v63\n\t"
    "v_mul_f32 v126, v126, v63\n\t"
    "v_mul_f32 v127, v127, v63\n\t"
    "v_fmac_f32 v124, v188, v195\n\t"
    "v_fmac_f32 v125, v189, v195\n\t"
    "v_fmac_f32 v126, v190, v195\n\t"
    "v_fmac_f32 v127, v191, v195\n\t"
    "v_cvt_pk_bf16_f32 v30, v124, v125\n\t"
    "v_cvt_pk_bf16_f32 v31, v126, v127\n\t"
    "buffer_store_dwordx4 v[28:31], v216, s[92:95], s39 offen offset:1024 sc1\n\t"
    "s_waitcnt vmcnt(0)\n\t"
    "s_barrier\n\t"
    "s_cmp_lg_u32 s33, 0\n\t"
    "s_cbranch_scc1 L_s3\n\t"
    "v_mov_b32 v226, v200\n\t"
    "v_mov_b32 v227, v201\n\t"
    "v_mov_b32 v228, s76\n\t"
    "v_mov_b32 v229, s77\n\t"
    "buffer_store_dwordx4 v[226:229], v197, s[28:31], 0 offen sc1\n\t"
    "s_nop 1\n\t"
    "v_mov_b32 v228, s76\n\t"
    "v_mov_b32 v229, s77\n\t"
    "v_mov_b32 v234, v200\n\t"
    "v_mov_b32 v235, v201\n\t"
    "s_mov_b32 s40, 0x40000\n\t"
    "L_poll3:\n\t"
    "buffer_load_dwordx4 v[230:233], v198, s[28:31], 0 offen sc1\n\t"
    "s_waitcnt vmcnt(0)\n\t"
    "v_cmp_eq_u64 vcc, s[76:77], v[230:231]\n\t"
    "s_nop 1\n\t"
    "s_cmp_eq_u64 vcc, -1\n\t"
    "s_cbranch_scc1 L_poll3_done\n\t"
    "s_sub_u32 s40, s40, 1\n\t"
    "s_cmp_eq_u32 s40, 0\n\t"
    "s_cbranch_scc1 L_poll3_done\n\t"
    "s_and_b32 s37, s40, 15\n\t"
    "s_cmp_lg_u32 s37, 0\n\t"
    "s_cbranch_scc1 L_poll3\n\t"
    "buffer_load_dwordx2 v[234:235], v196, s[28:31], 0 offen sc1\n\t"
    "s_waitcnt vmcnt(0)\n\t"
    "v_cndmask_b32 v226, v234, v232, vcc\n\t"
    "v_cndmask_b32 v227, v235, v233, vcc\n\t"
    "buffer_store_dwordx4 v[226:229], v197, s[28:31], 0 offen sc1\n\t"
    "s_branch L_poll3\n\t"
    "L_poll3_done:\n\t"
    "s_nop 0\n\t"
    "v_cndmask_b32 v226, v234, v232, vcc\n\t"
    "v_cndmask_b32 v227, v235, v233, vcc\n\t"
    "buffer_store_dwordx4 v[226:229], v197, s[28:31], 0 offen sc1\n\t"
    "L_s3:\n\t"
    "s_barrier\n\t"
    "s_add_u32 s39, s90, 0x0\n\t"
    "buffer_load_dwordx4 v[0:3], v216, s[92:95], s39 offen offset:0 sc1\n\t"
    "buffer_load_dwordx4 v[4:7], v216, s[92:95], s39 offen offset:1024 sc1\n\t"
    "s_add_u32 s39, s90, 0x4000\n\t"
    "buffer_load_dwordx4 v[8:11], v216, s[92:95], s39 offen offset:0 sc1\n\t"
    "buffer_load_dwordx4 v[12:15], v216, s[92:95], s39 offen offset:1024 sc1\n\t"
    "s_add_u32 s39, s90, 0x8000\n\t"
    "buffer_load_dwordx4 v[16:19], v216, s[92:95], s39 offen offset:0 sc1\n\t"
    "buffer_load_dwordx4 v[20:23], v216, s[92:95], s39 offen offset:1024 sc1\n\t"
    "s_add_u32 s39, s90, 0xc000\n\t"
    "buffer_load_dwordx4 v[24:27], v216, s[92:95], s39 offen offset:0 sc1\n\t"
    "buffer_load_dwordx4 v[28:31], v216, s[92:95], s39 offen offset:1024 sc1\n\t"
    "v_and_b32 v64, 15, v217\n\t"
    "v_mul_u32_u24 v64, 144, v64\n\t"
    "v_lshrrev_b32 v65, 4, v217\n\t"
    "v_lshl_add_u32 v64, v65, 3, v64\n\t"
    "v_add_u32 v64, s41, v64\n\t"
    "v_lshrrev_b32 v65, 2, v217\n\t"
    "v_and_b32 v67, 3, v217\n\t"
    "v_mul_u32_u24 v66, 144, v65\n\t"
    "v_lshl_add_u32 v66, v67, 5, v66\n\t"
    "v_add_u32 v66, s41, v66\n\t"
    "v_lshlrev_b32 v65, 10, v65\n\t"
    "v_lshl_add_u32 v65, v67, 5, v65\n\t"
    "s_waitcnt vmcnt(6)\n\t"
    "v_lshlrev_b32 v72, 16, v0\n\t"
    "v_and_b32 v73, 0xffff0000, v0\n\t"
    "v_lshlrev_b32 v74, 16, v1\n\t"
    "v_and_b32 v75, 0xffff0000, v1\n\t"
    "v_lshlrev_b32 v76, 16, v2\n\t"
    "v_and_b32 v77, 0xffff0000, v2\n\t"
    "v_lshlrev_b32 v78, 16, v3\n\t"
    "v_and_b32 v79, 0xffff0000, v3\n\t"
    "v_lshlrev_b32 v80, 16, v4\n\t"
    "v_and_b32 v81, 0xffff0000, v4\n\t"
    "v_lshlrev_b32 v82, 16, v5\n\t"
    "v_and_b32 v83, 0xffff0000, v5\n\t"
    "v_lshlrev_b32 v84, 16, v6\n\t"
    "v_and_b32 v85, 0xffff0000, v6\n\t"
    "v_lshlrev_b32 v86, 16, v7\n\t"
    "v_and_b32 v87, 0xffff0000, v7\n\t"
    "s_waitcnt vmcnt(4)\n\t"
    "v_lshlrev_b32 v32, 16, v8\n\t"
    "v_and_b32 v33, 0xffff0000, v8\n\t"
    "v_lshlrev_b32 v34, 16, v9\n\t"
    "v_and_b32 v35, 0xffff0000, v9\n\t"
    "v_lshlrev_b32 v36, 16, v10\n\t"
    "v_and_b32 v37, 0xffff0000, v10\n\t"
    "v_lshlrev_b32 v38, 16, v11\n\t"
    "v_and_b32 v39, 0xffff0000, v11\n\t"
    "v_lshlrev_b32 v40, 16, v12\n\t"
    "v_and_b32 v41, 0xffff0000, v12\n\t"
    "v_lshlrev_b32 v42, 16, v13\n\t"
    "v_and_b32 v43, 0xffff0000, v13\n\t"
    "v_lshlrev_b32 v44, 16, v14\n\t"
    "v_and_b32 v45, 0xffff0000, v14\n\t"
    "v_lshlrev_b32 v46, 16, v15\n\t"
    "v_and_b32 v47, 0xffff0000, v15\n\t"
    "v_add_f32 v72, v72, v32\n\t"
    "v_add_f32 v73, v73, v33\n\t"
    "v_add_f32 v74, v74, v34\n\t"
    "v_add_f32 v75, v75, v35\n\t"
    "v_add_f32 v76, v76, v36\n\t"
    "v_add_f32 v77, v77, v37\n\t"
    "v_add_f32 v78, v78, v38\n\t"
    "v_add_f32 v79, v79, v39\n\t"
    "v_add_f32 v80, v80, v40\n\t"
    "v_add_f32 v81, v81, v41\n\t"
    "v_add_f32 v82, v82, v42\n\t"
    "v_add_f32 v83, v83, v43\n\t"
    "v_add_f32 v84, v84, v44\n\t"
    "v_add_f32 v85, v85, v45\n\t"
    "v_add_f32 v86, v86, v46\n\t"
    "v_add_f32 v87, v87, v47\n\t"
    "s_waitcnt vmcnt(2)\n\t"
    "v_lshlrev_b32 v32, 16, v16\n\t"
    "v_and_b32 v33, 0xffff0000, v16\n\t"
    "v_lshlrev_b32 v34, 16, v17\n\t"
    "v_and_b32 v35, 0xffff0000, v17\n\t"
    "v_lshlrev_b32 v36, 16, v18\n\t"
    "v_and_b32 v37, 0xffff0000, v18\n\t"
    "v_lshlrev_b32 v38, 16, v19\n\t"
    "v_and_b32 v39, 0xffff0000, v19\n\t"
    "v_lshlrev_b32 v40, 16, v20\n\t"
    "v_and_b32 v41, 0xffff0000, v20\n\t"
    "v_lshlrev_b32 v42, 16, v21\n\t"
    "v_and_b32 v43, 0xffff0000, v21\n\t"
    "v_lshlrev_b32 v44, 16, v22\n\t"
    "v_and_b32 v45, 0xffff0000, v22\n\t"
    "v_lshlrev_b32 v46, 16, v23\n\t"
    "v_and_b32 v47, 0xffff0000, v23\n\t"
    "v_add_f32 v72, v72, v32\n\t"
    "v_add_f32 v73, v73, v33\n\t"
    "v_add_f32 v74, v74, v34\n\t"
    "v_add_f32 v75, v75, v35\n\t"
    "v_add_f32 v76, v76, v36\n\t"
    "v_add_f32 v77, v77, v37\n\t"
    "v_add_f32 v78, v78, v38\n\t"
    "v_add_f32 v79, v79, v39\n\t"
    "v_add_f32 v80, v80, v40\n\t"
    "v_add_f32 v81, v81, v41\n\t"
    "v_add_f32 v82, v82, v42\n\t"
    "v_add_f32 v83, v83, v43\n\t"
    "v_add_f32 v84, v84, v44\n\t"
    "v_add_f32 v85, v85, v45\n\t"
    "v_add_f32 v86, v86, v46\n\t"
    "v_add_f32 v87, v87, v47\n\t"
    "s_waitcnt vmcnt(0)\n\t"
    "v_lshlrev_b32 v32, 16, v24\n\t"
    "v_and_b32 v33, 0xffff0000, v24\n\t"
    "v_lshlrev_b32 v34, 16, v25\n\t"
    "v_and_b32 v35, 0xffff0000, v25\n\t"
    "v_lshlrev_b32 v36, 16, v26\n\t"
    "v_and_b32 v37, 0xffff0000, v26\n\t"
    "v_lshlrev_b32 v38, 16, v27\n\t"
    "v_and_b32 v39, 0xffff0000, v27\n\t"
    "v_lshlrev_b32 v40, 16, v28\n\t"
    "v_and_b32 v41, 0xffff0000, v28\n\t"
    "v_lshlrev_b32 v42, 16, v29\n\t"
    "v_and_b32 v43, 0xffff0000, v29\n\t"
    "v_lshlrev_b32 v44, 16, v30\n\t"
    "v_and_b32 v45, 0xffff0000, v30\n\t"
    "v_lshlrev_b32 v46, 16, v31\n\t"
    "v_and_b32 v47, 0xffff0000, v31\n\t"
    "v_add_f32 v72, v72, v32\n\t"
    "v_add_f32 v73, v73, v33\n\t"
    "v_add_f32 v74, v74, v34\n\t"
    "v_add_f32 v75, v75, v35\n\t"
    "v_add_f32 v76, v76, v36\n\t"
    "v_add_f32 v77, v77, v37\n\t"
    "v_add_f32 v78, v78, v38\n\t"
    "v_add_f32 v79, v79, v39\n\t"
    "v_add_f32 v80, v80, v40\n\t"
    "v_add_f32 v81, v81, v41\n\t"
    "v_add_f32 v82, v82, v42\n\t"
    "v_add_f32 v83, v83, v43\n\t"
    "v_add_f32 v84, v84, v44\n\t"
    "v_add_f32 v85, v85, v45\n\t"
    "v_add_f32 v86, v86, v46\n\t"
    "v_add_f32 v87, v87, v47\n\t"
    "v_cvt_pk_bf16_f32 v100, v72, v73\n\t"
    "v_cvt_pk_bf16_f32 v101, v74, v75\n\t"
    "ds_write_b64 v64, v[100:101] offset:0\n\t"
    "v_cvt_pk_bf16_f32 v102, v76, v77\n\t"
    "v_cvt_pk_bf16_f32 v103, v78, v79\n\t"
    "ds_write_b64 v64, v[102:103] offset:32\n\t"
    "v_cvt_pk_bf16_f32 v104, v80, v81\n\t"
    "v_cvt_pk_bf16_f32 v105, v82, v83\n\t"
    "ds_write_b64 v64, v[104:105] offset:64\n\t"
    "v_cvt_pk_bf16_f32 v106, v84, v85\n\t"
    "v_cvt_pk_bf16_f32 v107, v86, v87\n\t"
    "ds_write_b64 v64, v[106:107] offset:96\n\t"
    "s_waitcnt lgkmcnt(0)\n\t"
    "ds_read_b128 v[0:3], v66\n\t"
    "ds_read_b128 v[4:7], v66 offset:16\n\t"
    "s_waitcnt lgkmcnt(0)\n\t"
    "buffer_store_dwordx4 v[0:3], v65, s[72:75], s91 offen\n\t"
    "buffer_store_dwordx4 v[4:7], v65, s[72:75], s91 offen offset:16\n\t"
    "s_branch L_exit\n\t"
    "L_redo:\n\t"
    "s_waitcnt vmcnt(0)\n\t"
    "s_barrier\n\t"
    "s_mov_b32 s42, 1\n\t"
    "s_mov_b32 s45, 0\n\t"
    "s_mov_b32 s70, s41\n\t"
    "s_lshl_b32 s40, s35, 1\n\t"
    "s_sub_u32 s40, 32, s40\n\t"
    "v_mov_b32 v176, 0xff800000\n\t"
    "v_mov_b32 v177, 0xff800000\n\t"
    "v_mov_b32 v178, 0xff800000\n\t"
    "v_mov_b32 v179, 0xff800000\n\t"
    "L_redo_loop:\n\t"
    "s_mov_b32 m0, s41\n\t"
    "s_nop 0\n\t"
    "buffer_load_dwordx4 v216, s[20:23], s70 offen offset:0 sc1 lds\n\t"
    "buffer_load_dwordx4 v216, s[20:23], s70 offen offset:1024 sc1 lds\n\t"
    "buffer_load_dwordx4 v216, s[20:23], s70 offen offset:2048 sc1 lds\n\t"
    "buffer_load_dwordx4 v216, s[20:23], s70 offen offset:3072 sc1 lds\n\t"
    "s_add_u32 s70, s70, 0x8000\n\t"
    "s_waitcnt vmcnt(0)\n\t"
    "s_barrier\n\t"
    "ds_read_b128 a[180:183], v216 offset:0\n\t"
    "ds_read_b128 a[184:187], v216 offset:1024\n\t"
    "ds_read_b128 a[188:191], v216 offset:2048\n\t"
    "ds_read_b128 a[192:195], v216 offset:3072\n\t"
    "ds_read_b128 a[196:199], v216 offset:4096\n\t"
    "ds_read_b128 a[200:203], v216 offset:5120\n\t"
    "ds_read_b128 a[204:207], v216 offset:6144\n\t"
    "ds_read_b128 a[208:211], v216 offset:7168\n\t"
    "ds_read_b128 a[212:215], v216 offset:8192\n\t"
    "ds_read_b128 a[216:219], v216 offset:9216\n\t"
    "ds_read_b128 a[220:223], v216 offset:10240\n\t"
    "ds_read_b128 a[224:227], v216 offset:11264\n\t"
    "ds_read_b128 a[228:231], v216 offset:12288\n\t"
    "ds_read_b128 a[232:235], v216 offset:13312\n\t"
    "ds_read_b128 a[236:239], v216 offset:14336\n\t"
    "ds_read_b128 a[240:243], v216 offset:15360\n\t"
    "s_lshl_b32 s38, s45, 6\n\t"
    "s_lshl_b32 s37, s43, 4\n\t"
    "s_sub_u32 s37, s37, s38\n\t"
    "v_cvt_f32_i32 v184, s37\n\t"
    "s_lshl_b32 s37, s44, 4\n\t"
    "s_sub_u32 s37, s37, s38\n\t"
    "v_cvt_f32_i32 v185, s37\n\t"
    "v_fma_f32 v184, v184, v196, v195\n\t"
    "v_fma_f32 v185, v185, v196, v195\n\t"
    "s_waitcnt lgkmcnt(0)\n\t"
    "v_mov_b32 v180, v184\n\t"
    "v_min_f32 v180, 0, v180\n\t"
    "v_add_f32 v181, 0xf1800000, v184\n\t"
    "v_min_f32 v181, 0, v181\n\t"
    "v_add_f32 v182, 0xf2000000, v184\n\t"
    "v_min_f32 v182, 0, v182\n\t"
    "v_add_f32 v183, 0xf2400000, v184\n\t"
    "v_min_f32 v183, 0, v183\n\t"
    "s_nop 1\n\t"
    "v_mfma_f32_16x16x32_bf16 v[64:67], a[180:183], v[0:3], v[180:183]\n\t"
    "v_mfma_f32_16x16x32_bf16 v[64:67], a[184:187], v[4:7], v[64:67]\n\t"
    "v_mfma_f32_16x16x32_bf16 v[64:67], a[188:191], v[8:11], v[64:67]\n\t"
    "v_mfma_f32_16x16x32_bf16 v[64:67], a[192:195], v[12:15], v[64:67]\n\t"
    "s_nop 7\n\t"
    "v_max3_f32 v176, v176, v64, v65\n\t"
    "v_max3_f32 v176, v176, v66, v67\n\t"
    "v_add_f32 v180, 0x74800000, v184\n\t"
    "v_min_f32 v180, 0, v180\n\t"
    "v_add_f32 v181, 0x747c0000, v184\n\t"
    "v_min_f32 v181, 0, v181\n\t"
    "v_add_f32 v182, 0x74780000, v184\n\t"
    "v_min_f32 v182, 0, v182\n\t"
    "v_add_f32 v183, 0x74740000, v184\n\t"
    "v_min_f32 v183, 0, v183\n\t"
    "s_nop 1\n\t"
    "v_mfma_f32_16x16x32_bf16 v[64:67], a[180:183], v[16:19], v[180:183]\n\t"
    "v_mfma_f32_16x16x32_bf16 v[64:67], a[184:187], v[20:23], v[64:67]\n\t"
    "v_mfma_f32_16x16x32_bf16 v[64:67], a[188:191], v[24:27], v[64:67]\n\t"
    "v_mfma_f32_16x16x32_bf16 v[64:67], a[192:195], v[28:31], v[64:67]\n\t"
    "s_nop 7\n\t"
    "v_max3_f32 v177, v177, v64, v65\n\t"
    "v_max3_f32 v177, v177, v66, v67\n\t"
    "v_mov_b32 v180, v185\n\t"
    "v_min_f32 v180, 0, v180\n\t"
    "v_add_f32 v181, 0xf1800000, v185\n\t"
    "v_min_f32 v181, 0, v181\n\t"
    "v_add_f32 v182, 0xf2000000, v185\n\t"
    "v_min_f32 v182, 0, v182\n\t"
    "v_add_f32 v183, 0xf2400000, v185\n\t"
    "v_min_f32 v183, 0, v183\n\t"
    "s_nop 1\n\t"
    "v_mfma_f32_16x16x32_bf16 v[64:67], a[180:183], v[32:35], v[180:183]\n\t"
    "v_mfma_f32_16x16x32_bf16 v[64:67], a[184:187], v[36:39], v[64:67]\n\t"
    "v_mfma_f32_16x16x32_bf16 v[64:67], a[188:191], v[40:43], v[64:67]\n\t"
    "v_mfma_f32_16x16x32_bf16 v[64:67], a[192:195], v[44:47], v[64:67]\n\t"
    "s_nop 7\n\t"
    "v_max3_f32 v178, v178, v64, v65\n\t"
    "v_max3_f32 v178, v178, v66, v67\n\t"
    "v_add_f32 v180, 0x74800000, v185\n\t"
    "v_min_f32 v180, 0, v180\n\t"
    "v_add_f32 v181, 0x747c0000, v185\n\t"
    "v_min_f32 v181, 0, v181\n\t"
    "v_add_f32 v182, 0x74780000, v185\n\t"
    "v_min_f32 v182, 0, v182\n\t"
    "v_add_f32 v183, 0x74740000, v185\n\t"
    "v_min_f32 v183, 0, v183\n\t"
    "s_nop 1\n\t"
    "v_mfma_f32_16x16x32_bf16 v[64:67], a[180:183], v[48:51], v[180:183]\n\t"
    "v_mfma_f32_16x16x32_bf16 v[64:67], a[184:187], v[52:55], v[64:67]\n\t"
    "v_mfma_f32_16x16x32_bf16 v[64:67], a[188:191], v[56:59], v[64:67]\n\t"
    "v_mfma_f32_16x16x32_bf16 v[64:67], a[192:195], v[60:63], v[64:67]\n\t"
    "s_nop 7\n\t"
    "v_max3_f32 v179, v179, v64, v65\n\t"
    "v_max3_f32 v179, v179, v66, v67\n\t"
    "v_add_f32 v180, 0xf3800000, v184\n\t"
    "v_min_f32 v180, 0, v180\n\t"
    "v_add_f32 v181, 0xf3880000, v184\n\t"
    "v_min_f32 v181, 0, v181\n\t"
    "v_add_f32 v182, 0xf3900000, v184\n\t"
    "v_min_f32 v182, 0, v182\n\t"
    "v_add_f32 v183, 0xf3980000, v184\n\t"
    "v_min_f32 v183, 0, v183\n\t"
    "s_nop 1\n\t"
    "v_mfma_f32_16x16x32_bf16 v[64:67], a[196:199], v[0:3], v[180:183]\n\t"
    "v_mfma_f32_16x16x32_bf16 v[64:67], a[200:203], v[4:7], v[64:67]\n\t"
    "v_mfma_f32_16x16x32_bf16 v[64:67], a[204:207], v[8:11], v[64:67]\n\t"
    "v_mfma_f32_16x16x32_bf16 v[64:67], a[208:211], v[12:15], v[64:67]\n\t"
    "s_nop 7\n\t"
    "v_max3_f32 v176, v176, v64, v65\n\t"
    "v_max3_f32 v176, v176, v66, v67\n\t"
    "v_add_f32 v180, 0x74400000, v184\n\t"
    "v_min_f32 v180, 0, v180\n\t"
    "v_add_f32 v181, 0x743c0000, v184\n\t"
    "v_min_f32 v181, 0, v181\n\t"
    "v_add_f32 v182, 0x74380000, v184\n\t"
    "v_min_f32 v182, 0, v182\n\t"
    "v_add_f32 v183, 0x74340000, v184\n\t"
    "v_min_f32 v183, 0, v183\n\t"
    "s_nop 1\n\t"
    "v_mfma_f32_16x16x32_bf16 v[64:67], a[196:199], v[16:19], v[180:183]\n\t"
    "v_mfma_f32_16x16x32_bf16 v[64:67], a[200:203], v[20:23], v[64:67]\n\t"
    "v_mfma_f32_16x16x32_bf16 v[64:67], a[204:207], v[24:27], v[64:67]\n\t"
    "v_mfma_f32_16x16x32_bf16 v[64:67], a[208:211], v[28:31], v[64:67]\n\t"
    "s_nop 7\n\t"
    "v_max3_f32 v177, v177, v64, v65\n\t"
    "v_max3_f32 v177, v177, v66, v67\n\t"
    "v_add_f32 v180, 0xf3800000, v185\n\t"
    "v_min_f32 v180, 0, v180\n\t"
    "v_add_f32 v181, 0xf3880000, v185\n\t"
    "v_min_f32 v181, 0, v181\n\t"
    "v_add_f32 v182, 0xf3900000, v185\n\t"
    "v_min_f32 v182, 0, v182\n\t"
    "v_add_f32 v183, 0xf3980000, v185\n\t"
    "v_min_f32 v183, 0, v183\n\t"
    "s_nop 1\n\t"
    "v_mfma_f32_16x16x32_bf16 v[64:67], a[196:199], v[32:35], v[180:183]\n\t"
    "v_mfma_f32_16x16x32_bf16 v[64:67], a[200:203], v[36:39], v[64:67]\n\t"
    "v_mfma_f32_16x16x32_bf16 v[64:67], a[204:207], v[40:43], v[64:67]\n\t"
    "v_mfma_f32_16x16x32_bf16 v[64:67], a[208:211], v[44:47], v[64:67]\n\t"
    "s_nop 7\n\t"
    "v_max3_f32 v178, v178, v64, v65\n\t"
    "v_max3_f32 v178, v178, v66, v67\n\t"
    "v_add_f32 v180, 0x74400000, v185\n\t"
    "v_min_f32 v180, 0, v180\n\t"
    "v_add_f32 v181, 0x743c0000, v185\n\t"
    "v_min_f32 v181, 0, v181\n\t"
    "v_add_f32 v182, 0x74380000, v185\n\t"
    "v_min_f32 v182, 0, v182\n\t"
    "v_add_f32 v183, 0x74340000, v185\n\t"
    "v_min_f32 v183, 0, v183\n\t"
    "s_nop 1\n\t"
    "v_mfma_f32_16x16x32_bf16 v[64:67], a[196:199], v[48:51], v[180:183]\n\t"
    "v_mfma_f32_16x16x32_bf16 v[64:67], a[200:203], v[52:55], v[64:67]\n\t"
    "v_mfma_f32_16x16x32_bf16 v[64:67], a[204:207], v[56:59], v[64:67]\n\t"
    "v_mfma_f32_16x16x32_bf16 v[64:67], a[208:211], v[60:63], v[64:67]\n\t"
    "s_nop 7\n\t"
    "v_max3_f32 v179, v179, v64, v65\n\t"
    "v_max3_f32 v179, v179, v66, v67\n\t"
    "v_add_f32 v180, 0xf4000000, v184\n\t"
    "v_min_f32 v180, 0, v180\n\t"
    "v_add_f32 v181, 0xf4040000, v184\n\t"
    "v_min_f32 v181, 0, v181\n\t"
    "v_add_f32 v182, 0xf4080000, v184\n\t"
    "v_min_f32 v182, 0, v182\n\t"
    "v_add_f32 v183, 0xf40c0000, v184\n\t"
    "v_min_f32 v183, 0, v183\n\t"
    "s_nop 1\n\t"
    "v_mfma_f32_16x16x32_bf16 v[64:67], a[212:215], v[0:3], v[180:183]\n\t"
    "v_mfma_f32_16x16x32_bf16 v[64:67], a[216:219], v[4:7], v[64:67]\n\t"
    "v_mfma_f32_16x16x32_bf16 v[64:67], a[220:223], v[8:11], v[64:67]\n\t"
    "v_mfma_f32_16x16x32_bf16 v[64:67], a[224:227], v[12:15], v[64:67]\n\t"
    "s_nop 7\n\t"
    "v_max3_f32 v176, v176, v64, v65\n\t"
    "v_max3_f32 v176, v176, v66, v67\n\t"
    "v_add_f32 v180, 0x74000000, v184\n\t"
    "v_min_f32 v180, 0, v180\n\t"
    "v_add_f32 v181, 0x73f80000, v184\n\t"
    "v_min_f32 v181, 0, v181\n\t"
    "v_add_f32 v182, 0x73f00000, v184\n\t"
    "v_min_f32 v182, 0, v182\n\t"
    "v_add_f32 v183, 0x73e80000, v184\n\t"
    "v_min_f32 v183, 0, v183\n\t"
    "s_nop 1\n\t"
    "v_mfma_f32_16x16x32_bf16 v[64:67], a[212:215], v[16:19], v[180:183]\n\t"
    "v_mfma_f32_16x16x32_bf16 v[64:67], a[216:219], v[20:23], v[64:67]\n\t"
    "v_mfma_f32_16x16x32_bf16 v[64:67], a[220:223], v[24:27], v[64:67]\n\t"
    "v_mfma_f32_16x16x32_bf16 v[64:67], a[224:227], v[28:31], v[64:67]\n\t"
    "s_nop 7\n\t"
    "v_max3_f32 v177, v177, v64, v65\n\t"
    "v_max3_f32 v177, v177, v66, v67\n\t"
    "v_add_f32 v180, 0xf4000000, v185\n\t"
    "v_min_f32 v180, 0, v180\n\t"
    "v_add_f32 v181, 0xf4040000, v185\n\t"
    "v_min_f32 v181, 0, v181\n\t"
    "v_add_f32 v182, 0xf4080000, v185\n\t"
    "v_min_f32 v182, 0, v182\n\t"
    "v_add_f32 v183, 0xf40c0000, v185\n\t"
    "v_min_f32 v183, 0, v183\n\t"
    "s_nop 1\n\t"
    "v_mfma_f32_16x16x32_bf16 v[64:67], a[212:215], v[32:35], v[180:183]\n\t"
    "v_mfma_f32_16x16x32_bf16 v[64:67], a[216:219], v[36:39], v[64:67]\n\t"
    "v_mfma_f32_16x16x32_bf16 v[64:67], a[220:223], v[40:43], v[64:67]\n\t"
    "v_mfma_f32_16x16x32_bf16 v[64:67], a[224:227], v[44:47], v[64:67]\n\t"
    "s_nop 7\n\t"
    "v_max3_f32 v178, v178, v64, v65\n\t"
    "v_max3_f32 v178, v178, v66, v67\n\t"
    "v_add_f32 v180, 0x74000000, v185\n\t"
    "v_min_f32 v180, 0, v180\n\t"
    "v_add_f32 v181, 0x73f80000, v185\n\t"
    "v_min_f32 v181, 0, v181\n\t"
    "v_add_f32 v182, 0x73f00000, v185\n\t"
    "v_min_f32 v182, 0, v182\n\t"
    "v_add_f32 v183, 0x73e80000, v185\n\t"
    "v_min_f32 v183, 0, v183\n\t"
    "s_nop 1\n\t"
    "v_mfma_f32_16x16x32_bf16 v[64:67], a[212:215], v[48:51], v[180:183]\n\t"
    "v_mfma_f32_16x16x32_bf16 v[64:67], a[216:219], v[52:55], v[64:67]\n\t"
    "v_mfma_f32_16x16x32_bf16 v[64:67], a[220:223], v[56:59], v[64:67]\n\t"
    "v_mfma_f32_16x16x32_bf16 v[64:67], a[224:227], v[60:63], v[64:67]\n\t"
    "s_nop 7\n\t"
    "v_max3_f32 v179, v179, v64, v65\n\t"
    "v_max3_f32 v179, v179, v66, v67\n\t"
    "v_add_f32 v180, 0xf4400000, v184\n\t"
    "v_min_f32 v180, 0, v180\n\t"
    "v_add_f32 v181, 0xf4440000, v184\n\t"
    "v_min_f32 v181, 0, v181\n\t"
    "v_add_f32 v182, 0xf4480000, v184\n\t"
    "v_min_f32 v182, 0, v182\n\t"
    "v_add_f32 v183, 0xf44c0000, v184\n\t"
    "v_min_f32 v183, 0, v183\n\t"
    "s_nop 1\n\t"
    "v_mfma_f32_16x16x32_bf16 v[64:67], a[228:231], v[0:3], v[180:183]\n\t"
    "v_mfma_f32_16x16x32_bf16 v[64:67], a[232:235], v[4:7], v[64:67]\n\t"
    "v_mfma_f32_16x16x32_bf16 v[64:67], a[236:239], v[8:11], v[64:67]\n\t"
    "v_mfma_f32_16x16x32_bf16 v[64:67], a[240:243], v[12:15], v[64:67]\n\t"
    "s_nop 7\n\t"
    "v_max3_f32 v176, v176, v64, v65\n\t"
    "v_max3_f32 v176, v176, v66, v67\n\t"
    "v_add_f32 v180, 0x73800000, v184\n\t"
    "v_min_f32 v180, 0, v180\n\t"
    "v_add_f32 v181, 0x73700000, v184\n\t"
    "v_min_f32 v181, 0, v181\n\t"
    "v_add_f32 v182, 0x73600000, v184\n\t"
    "v_min_f32 v182, 0, v182\n\t"
    "v_add_f32 v183, 0x73500000, v184\n\t"
    "v_min_f32 v183, 0, v183\n\t"
    "s_nop 1\n\t"
    "v_mfma_f32_16x16x32_bf16 v[64:67], a[228:231], v[16:19], v[180:183]\n\t"
    "v_mfma_f32_16x16x32_bf16 v[64:67], a[232:235], v[20:23], v[64:67]\n\t"
    "v_mfma_f32_16x16x32_bf16 v[64:67], a[236:239], v[24:27], v[64:67]\n\t"
    "v_mfma_f32_16x16x32_bf16 v[64:67], a[240:243], v[28:31], v[64:67]\n\t"
    "s_nop 7\n\t"
    "v_max3_f32 v177, v177, v64, v65\n\t"
    "v_max3_f32 v177, v177, v66, v67\n\t"
    "v_add_f32 v180, 0xf4400000, v185\n\t"
    "v_min_f32 v180, 0, v180\n\t"
    "v_add_f32 v181, 0xf4440000, v185\n\t"
    "v_min_f32 v181, 0, v181\n\t"
    "v_add_f32 v182, 0xf4480000, v185\n\t"
    "v_min_f32 v182, 0, v182\n\t"
    "v_add_f32 v183, 0xf44c0000, v185\n\t"
    "v_min_f32 v183, 0, v183\n\t"
    "s_nop 1\n\t"
    "v_mfma_f32_16x16x32_bf16 v[64:67], a[228:231], v[32:35], v[180:183]\n\t"
    "v_mfma_f32_16x16x32_bf16 v[64:67], a[232:235], v[36:39], v[64:67]\n\t"
    "v_mfma_f32_16x16x32_bf16 v[64:67], a[236:239], v[40:43], v[64:67]\n\t"
    "v_mfma_f32_16x16x32_bf16 v[64:67], a[240:243], v[44:47], v[64:67]\n\t"
    "s_nop 7\n\t"
    "v_max3_f32 v178, v178, v64, v65\n\t"
    "v_max3_f32 v178, v178, v66, v67\n\t"
    "v_add_f32 v180, 0x73800000, v185\n\t"
    "v_min_f32 v180, 0, v180\n\t"
    "v_add_f32 v181, 0x73700000, v185\n\t"
    "v_min_f32 v181, 0, v181\n\t"
    "v_add_f32 v182, 0x73600000, v185\n\t"
    "v_min_f32 v182, 0, v182\n\t"
    "v_add_f32 v183, 0x73500000, v185\n\t"
    "v_min_f32 v183, 0, v183\n\t"
    "s_nop 1\n\t"
    "v_mfma_f32_16x16x32_bf16 v[64:67], a[228:231], v[48:51], v[180:183]\n\t"
    "v_mfma_f32_16x16x32_bf16 v[64:67], a[232:235], v[52:55], v[64:67]\n\t"
    "v_mfma_f32_16x16x32_bf16 v[64:67], a[236:239], v[56:59], v[64:67]\n\t"
    "v_mfma_f32_16x16x32_bf16 v[64:67], a[240:243], v[60:63], v[64:67]\n\t"
    "s_nop 7\n\t"
    "v_max3_f32 v179, v179, v64, v65\n\t"
    "v_max3_f32 v179, v179, v66, v67\n\t"
    "s_barrier\n\t"
    "s_add_u32 s45, s45, 1\n\t"
    "s_sub_u32 s40, s40, 1\n\t"
    "s_cmp_lg_u32 s40, 0\n\t"
    "s_cbranch_scc1 L_redo_loop\n\t"
    "v_mov_b32 v186, v176\n\t"
    "s_nop 1\n\t"
    "v_permlane16_swap_b32 v176, v186\n\t"
    "v_max_f32 v176, v176, v186\n\t"
    "v_mov_b32 v186, v176\n\t"
    "s_nop 0\n\t"
    "s_nop 0\n\t"
    "v_permlane32_swap_b32 v176, v186\n\t"
    "v_max_f32 v176, v176, v186\n\t"
    "v_sub_f32 v160, 0, v176\n\t"
    "v_sub_f32 v161, 0, v176\n\t"
    "v_sub_f32 v162, 0, v176\n\t"
    "v_sub_f32 v163, 0, v176\n\t"
    "v_mov_b32 v186, v177\n\t"
    "s_nop 1\n\t"
    "v_permlane16_swap_b32 v177, v186\n\t"
    "v_max_f32 v177, v177, v186\n\t"
    "v_mov_b32 v186, v177\n\t"
    "s_nop 0\n\t"
    "s_nop 0\n\t"
    "v_permlane32_swap_b32 v177, v186\n\t"
    "v_max_f32 v177, v177, v186\n\t"
    "v_sub_f32 v164, 0, v177\n\t"
    "v_sub_f32 v165, 0, v177\n\t"
    "v_sub_f32 v166, 0, v177\n\t"
    "v_sub_f32 v167, 0, v177\n\t"
    "v_mov_b32 v186, v178\n\t"
    "s_nop 1\n\t"
    "v_permlane16_swap_b32 v178, v186\n\t"
    "v_max_f32 v178, v178, v186\n\t"
    "v_mov_b32 v186, v178\n\t"
    "s_nop 0\n\t"
    "s_nop 0\n\t"
    "v_permlane32_swap_b32 v178, v186\n\t"
    "v_max_f32 v178, v178, v186\n\t"
    "v_sub_f32 v168, 0, v178\n\t"
    "v_sub_f32 v169, 0, v178\n\t"
    "v_sub_f32 v170, 0, v178\n\t"
    "v_sub_f32 v171, 0, v178\n\t"
    "v_mov_b32 v186, v179\n\t"
    "s_nop 1\n\t"
    "v_permlane16_swap_b32 v179, v186\n\t"
    "v_max_f32 v179, v179, v186\n\t"
    "v_mov_b32 v186, v179\n\t"
    "s_nop 0\n\t"
    "s_nop 0\n\t"
    "v_permlane32_swap_b32 v179, v186\n\t"
    "v_max_f32 v179, v179, v186\n\t"
    "v_sub_f32 v172, 0, v179\n\t"
    "v_sub_f32 v173, 0, v179\n\t"
    "v_sub_f32 v174, 0, v179\n\t"
    "v_sub_f32 v175, 0, v179\n\t"
    "s_mov_b32 s70, s41\n\t"
    "s_add_u32 s71, s41, 0x4000\n\t"
    "s_mov_b32 s68, s41\n\t"
    "s_add_u32 s69, s41, 0x4000\n\t"
    "s_and_b32 m0, s68, 0x1ffff\n\t"
    "s_add_u32 s68, s68, 0x8000\n\t"
    "s_nop 0\n\t"
    "buffer_load_dwordx4 v216, s[20:23], s70 offen offset:0 sc1 lds\n\t"
    "buffer_load_dwordx4 v216, s[20:23], s70 offen offset:1024 sc1 lds\n\t"
    "buffer_load_dwordx4 v216, s[20:23], s70 offen offset:2048 sc1 lds\n\t"
    "buffer_load_dwordx4 v216, s[20:23], s70 offen offset:3072 sc1 lds\n\t"
    "s_add_u32 s70, s70, 0x8000\n\t"
    "s_and_b32 m0, s69, 0x1ffff\n\t"
    "s_add_u32 s69, s69, 0x8000\n\t"
    "s_nop 0\n\t"
    "buffer_load_dwordx4 v216, s[20:23], s71 offen offset:0 sc1 lds\n\t"
    "buffer_load_dwordx4 v216, s[20:23], s71 offen offset:1024 sc1 lds\n\t"
    "buffer_load_dwordx4 v216, s[20:23], s71 offen offset:2048 sc1 lds\n\t"
    "buffer_load_dwordx4 v216, s[20:23], s71 offen offset:3072 sc1 lds\n\t"
    "s_add_u32 s71, s71, 0x8000\n\t"
    "s_and_b32 m0, s68, 0x1ffff\n\t"
    "s_add_u32 s68, s68, 0x8000\n\t"
    "s_nop 0\n\t"
    "buffer_load_dwordx4 v216, s[20:23], s70 offen offset:0 sc1 lds\n\t"
    "buffer_load_dwordx4 v216, s[20:23], s70 offen offset:1024 sc1 lds\n\t"
    "buffer_load_dwordx4 v216, s[20:23], s70 offen offset:2048 sc1 lds\n\t"
    "buffer_load_dwordx4 v216, s[20:23], s70 offen offset:3072 sc1 lds\n\t"
    "s_add_u32 s70, s70, 0x8000\n\t"
    "s_and_b32 m0, s68, 0x1ffff\n\t"
    "s_add_u32 s68, s68, 0x8000\n\t"
    "s_nop 0\n\t"
    "buffer_load_dwordx4 v216, s[20:23], s70 offen offset:0 sc1 lds\n\t"
    "buffer_load_dwordx4 v216, s[20:23], s70 offen offset:1024 sc1 lds\n\t"
    "buffer_load_dwordx4 v216, s[20:23], s70 offen offset:2048 sc1 lds\n\t"
    "buffer_load_dwordx4 v216, s[20:23], s70 offen offset:3072 sc1 lds\n\t"
    "s_add_u32 s70, s70, 0x8000\n\t"
    "s_and_b32 m0, s69, 0x1ffff\n\t"
    "s_add_u32 s69, s69, 0x8000\n\t"
    "s_nop 0\n\t"
    "buffer_load_dwordx4 v216, s[20:23], s71 offen offset:0 sc1 lds\n\t"
    "buffer_load_dwordx4 v216, s[20:23], s71 offen offset:1024 sc1 lds\n\t"
    "buffer_load_dwordx4 v216, s[20:23], s71 offen offset:2048 sc1 lds\n\t"
    "buffer_load_dwordx4 v216, s[20:23], s71 offen offset:3072 sc1 lds\n\t"
    "s_add_u32 s71, s71, 0x8000\n\t"
    "v_mov_b32 v192, v216\n\t"
    "v_add_u32 v193, 0x4000, v216\n\t"
    "s_mov_b64 s[78:79], 0\n\t"
    "s_mov_b32 s51, 0x8000\n\t"
    "s_mov_b32 s52, 0x4000\n\t"
    "s_mov_b32 s45, 0\n\t"
    "s_waitcnt vmcnt(8)\n\t"
    "s_barrier\n\t"
    "ds_read_b128 a[148:151], v192 offset:0\n\t"
    "ds_read_b128 a[152:155], v192 offset:1024\n\t"
    "ds_read_b128 a[156:159], v192 offset:2048\n\t"
    "ds_read_b128 a[160:163], v192 offset:3072\n\t"
    "ds_read_b128 a[164:167], v192 offset:4096\n\t"
    "ds_read_b128 a[168:171], v192 offset:5120\n\t"
    "ds_read_b128 a[172:175], v192 offset:6144\n\t"
    "ds_read_b128 a[176:179], v192 offset:7168\n\t"
    "s_branch L_att\n\t"
    "L_exit:\n\t"
    "L_code_end:\n\t"
    :: [ka] "s"(ka), [wid] "s"(wid), [bid] "s"(blockIdx.x)
    : "memory", "vcc", "scc", "s8", "s9", "s10", "s11", "s12", "s13", "s14", "s15", "s16", "s17", "s18", "s19", "s20", "s21", "s22", "s23", "s24", "s25", "s26", "s27", "s28", "s29", "s30", "s31", "s33", "s34", "s35", "s36", "s37", "s38", "s39", "s40", "s41", "s42", "s43", "s44", "s45", "s46", "s47", "s48", "s49", "s50", "s51", "s52", "s53", "s54", "s55", "s56", "s57", "s58", "s59", "s60", "s61", "s62", "s63", "s64", "s65", "s66", "s67", "s68", "s69", "s70", "s71", "s72", "s73", "s74", "s75", "s76", "s77", "s78", "s79", "s80", "s81", "s82", "s83", "s84", "s85", "s86", "s87", "s88", "s89", "s90", "s91", "s92", "s93", "s94", "s95", "s96", "s97", "s98", "s99", "v0", "v1", "v2", "v3", "v4", "v5", "v6", "v7", "v8", "v9", "v10", "v11", "v12", "v13", "v14", "v15", "v16", "v17", "v18", "v19", "v20", "v21", "v22", "v23", "v24", "v25", "v26", "v27", "v28", "v29", "v30", "v31", "v32", "v33", "v34", "v35", "v36", "v37", "v38", "v39", "v40", "v41", "v42", "v43", "v44", "v45", "v46", "v47", "v48", "v49", "v50", "v51", "v52", "v53", "v54", "v55", "v56", "v57", "v58", "v59", "v60", "v61", "v62", "v63", "v64", "v65", "v66", "v67", "v68", "v69", "v70", "v71", "v72", "v73", "v74", "v75", "v76", "v77", "v78", "v79", "v80", "v81", "v82", "v83", "v84", "v85", "v86", "v87", "v88", "v89", "v90", "v91", "v92", "v93", "v94", "v95", "v96", "v97", "v98", "v99", "v100", "v101", "v102", "v103", "v104", "v105", "v106", "v107", "v108", "v109", "v110", "v111", "v112", "v113", "v114", "v115", "v116", "v117", "v118", "v119", "v120", "v121", "v122", "v123", "v124", "v125", "v126", "v127", "v128", "v129", "v130", "v131", "v132", "v133", "v134", "v135", "v136", "v137", "v138", "v139", "v140", "v141", "v142", "v143", "v144", "v145", "v146", "v147", "v148", "v149", "v150", "v151", "v152", "v153", "v154", "v155", "v156", "v157", "v158", "v159", "v160", "v161", "v162", "v163", "v164", "v165", "v166", "v167", "v168", "v169", "v170", "v171", "v172", "v173", "v174", "v175", "v176", "v177", "v178", "v179", "v180", "v181", "v182", "v183", "v184", "v185", "v186", "v187", "v188", "v189", "v190", "v191", "v192", "v193", "v194", "v195", "v196", "v197", "v198", "v199", "v200", "v201", "v202", "v203", "v204", "v205", "v206", "v207", "v208", "v209", "v210", "v211", "v212", "v213", "v214", "v215", "v216", "v217", "v218", "v219", "v220", "v221", "v222", "v223", "v224", "v225", "v226", "v227", "v228", "v229", "v230", "v231", "v232", "v233", "v234", "v235", "v236", "v237", "v238", "v239", "v240", "v241", "v242", "v243", "v244", "v245", "v246", "v247", "v248", "v249", "v250", "v251", "v252", "v253", "v254", "v255", "a0", "a1", "a2", "a3", "a4", "a5", "a6", "a7", "a8", "a9", "a10", "a11", "a12", "a13", "a14", "a15", "a16", "a17", "a18", "a19", "a20", "a21", "a22", "a23", "a24", "a25", "a26", "a27", "a28", "a29", "a30", "a31", "a32", "a33", "a34", "a35", "a36", "a37", "a38", "a39", "a40", "a41", "a42", "a43", "a44", "a45", "a46", "a47", "a48", "a49", "a50", "a51", "a52", "a53", "a54", "a55", "a56", "a57", "a58", "a59", "a60", "a61", "a62", "a63", "a64", "a65", "a66", "a67", "a68", "a69", "a70", "a71", "a72", "a73", "a74", "a75", "a76", "a77", "a78", "a79", "a80", "a81", "a82", "a83", "a84", "a85", "a86", "a87", "a88", "a89", "a90", "a91", "a92", "a93", "a94", "a95", "a96", "a97", "a98", "a99", "a100", "a101", "a102", "a103", "a104", "a105", "a106", "a107", "a108", "a109", "a110", "a111", "a112", "a113", "a114", "a115", "a116", "a117", "a118", "a119", "a120", "a121", "a122", "a123", "a124", "a125", "a126", "a127", "a128", "a129", "a130", "a131", "a132", "a133", "a134", "a135", "a136", "a137", "a138", "a139", "a140", "a141", "a142", "a143", "a144", "a145", "a146", "a147", "a148", "a149", "a150", "a151", "a152", "a153", "a154", "a155", "a156", "a157", "a158", "a159", "a160", "a161", "a162", "a163", "a164", "a165", "a166", "a167", "a168", "a169", "a170", "a171", "a172", "a173", "a174", "a175", "a176", "a177", "a178", "a179", "a180", "a181", "a182", "a183", "a184", "a185", "a186", "a187", "a188", "a189", "a190", "a191", "a192", "a193", "a194", "a195", "a196", "a197", "a198", "a199", "a200", "a201", "a202", "a203", "a204", "a205", "a206", "a207", "a208", "a209", "a210", "a211", "a212", "a213", "a214", "a215", "a216", "a217", "a218", "a219", "a220", "a221", "a222", "a223", "a224", "a225", "a226", "a227", "a228", "a229", "a230", "a231", "a232", "a233", "a234", "a235", "a236", "a237", "a238", "a239", "a240", "a241", "a242", "a243", "a244", "a245", "a246", "a247", "a248", "a249", "a250", "a251", "a252", "a253", "a254", "a255");
}

constexpr int LDS_BYTES = 163840;
extern "C" void kernel_launch(void* const* d_in, const int* in_sizes, int n_in, void* d_out, int out_size, void* d_ws, size_t ws_size, hipStream_t stream) {
  (void)in_sizes; (void)n_in; (void)out_size; (void)ws_size;
  (void)hipFuncSetAttribute((const void*)gated_mix_fused, hipFuncAttributeMaxDynamicSharedMemorySize, LDS_BYTES);
  hipLaunchKernelGGL(gated_mix_fused, dim3(256), dim3(256), LDS_BYTES, stream, (const void*)d_in[4], (const void*)d_in[6], (const void*)d_in[7], (const void*)d_in[0], (const void*)d_in[9],
                     (const void*)d_in[5], (const void*)d_in[1], (const void*)d_in[8], (const void*)d_in[2], d_ws, d_out);
}
```
